```python
import torch

def _softmax(x, axis=-1):
    return torch.softmax(x, dim=axis)
def _gelu(x):
    return torch.nn.functional.gelu(x, approximate="tanh")
def _silu(x):
    return torch.nn.functional.silu(x)
def _sigmoid(x):
    return torch.sigmoid(x)
def _layernorm(x, axis=-1, eps=1e-5):
    m = x.mean(dim=axis, keepdim=True)
    v = x.var(dim=axis, keepdim=True, unbiased=False)
    return (x - m) / torch.sqrt(v + eps)
def _scatter_add(tgt, idx, src, axis=0):
    return tgt.clone().index_add_(axis, idx, src)
def _segment_sum(src, seg, n):
    out = torch.zeros((n,) + src.shape[1:], dtype=src.dtype)
    return out.index_add_(0, seg, src)
def _pad(x, pw):
    flat = tuple(v for pair in reversed(pw) for v in pair)
    return torch.nn.functional.pad(x, flat)

kernel_name = 'recurrent_scan__mha_block_cached__swiglu_ffn__prompt'

def setup_inputs():

    _w = (lambda a: (lambda t: t.to(torch.bfloat16) if t.is_floating_point() else t)(torch.as_tensor(a)))
    def _A_setup():
        B, Sq, D = 1, 2048, 512
        H, Sk, Dh = 32, 2048, 128
        return {
            "x": _w((lambda *s: torch.randn(*s, dtype=torch.bfloat16) * 0.05)(B, Sq, D)),
            "k_cache": _w((lambda *s: torch.randn(*s, dtype=torch.bfloat16) * 0.8)(B, H, Sk, Dh)),
            "v_cache": _w((lambda *s: torch.randn(*s, dtype=torch.bfloat16) * 0.2)(B, H, Sk, Dh)),
            "wq": _w((lambda *s: torch.randn(*s, dtype=torch.bfloat16) * 0.8)(D, H * Dh) * (D ** -0.5)),
            "wo": _w((lambda *s: torch.randn(*s, dtype=torch.bfloat16) * 0.2)(H * Dh, D) * ((H * Dh) ** -0.5)),
        }
    _A_in = _A_setup()

    def _B_setup():
        B, S, D, F = 1, 2048, 512, 1792
        return {
            "x": _w((lambda *s: torch.randn(*s, dtype=torch.bfloat16) * 0.05)(B, S, D)),
            "w_gate": _w((lambda *s: torch.randn(*s, dtype=torch.bfloat16) * 0.1)(D, F)),
            "w_up": _w((lambda *s: torch.randn(*s, dtype=torch.bfloat16) * 0.1)(D, F)),
            "w_down": _w((lambda *s: torch.randn(*s, dtype=torch.bfloat16) * 0.1)(F, D)),
        }
    _B_in = _B_setup()

    return {
        "A_x": _A_in["x"],
        "A_k_cache": _A_in["k_cache"],
        "A_v_cache": _A_in["v_cache"],
        "A_wq": _A_in["wq"],
        "A_wo": _A_in["wo"],
        "B_x": _B_in["x"],
        "B_w_gate": _B_in["w_gate"],
        "B_w_up": _B_in["w_up"],
        "B_w_down": _B_in["w_down"],
    }

def reference(A_x, A_k_cache, A_v_cache, A_wq, A_wo, B_x, B_w_gate, B_w_up, B_w_down):
    def _A_ref(x, k_cache, v_cache, wq, wo):
        B, Sq, D = x.shape
        H, Dh = 32, 128
        q = (lambda x, *d: x.permute(*d))((x @ wq).reshape(B, Sq, H, Dh), 0, 2, 1, 3)
        scores = torch.einsum("bhqd,bhkd->bhqk", q, k_cache) * (Dh ** -0.5)
        p = _softmax(scores, axis=-1)
        o = torch.einsum("bhqk,bhkd->bhqd", p, v_cache)
        return (lambda x, *d: x.permute(*d))(o, 0, 2, 1, 3).reshape(B, Sq, H * Dh) @ wo

    def _B_ref(x, w_gate, w_up, w_down):
        g = _silu(x @ w_gate)
        u = x @ w_up
        return (g * u) @ w_down

    x = A_x
    xn = _layernorm(x, axis=-1)
    s = _A_ref(xn, A_k_cache, A_v_cache, A_wq, A_wo)
    g = _silu(_B_ref(xn, B_w_gate, B_w_up, B_w_down))
    return x + s * g
```

```cpp
#include <hip/hip_runtime.h>
#include <hip/hip_bf16.h>
#include <stdint.h>
namespace nv {
typedef unsigned short u16;
__device__ __forceinline__ float b2f(u16 x){ return __uint_as_float(((unsigned)x)<<16); }
__device__ __forceinline__ u16 f2b(float x){ unsigned u=__float_as_uint(x); u += 0x7fffu + ((u>>16)&1u); return (u16)(u>>16); }
__device__ __forceinline__ float rb(float x){ return b2f(f2b(x)); }
constexpr int S=2048,D=512,H=32,DH=128,F=1792;
__global__ void ln(const u16* x,u16* xn){
  int row=blockIdx.x; int t=threadIdx.x;
  float v[8]; float s=0;
  for(int i=0;i<8;i++){ v[i]=b2f(x[row*D+t*8+i]); s+=v[i]; }
  for(int o=32;o;o>>=1) s+=__shfl_xor(s,o);
  float mean=s/D; float q=0;
  for(int i=0;i<8;i++){ float d=v[i]-mean; q+=d*d; }
  for(int o=32;o;o>>=1) q+=__shfl_xor(q,o);
  float var=rb(q/D); float m=rb(mean);
  float sd=rb(sqrtf(rb(var+1e-5f)));
  for(int i=0;i<8;i++){ xn[row*D+t*8+i]=f2b(rb(v[i]-m)/sd); }
}
__global__ void gemm(const u16* A,int lda,const u16* B,int ldb,float* C,int ldc,int K){
  __shared__ float sa[16][17], sb[16][17];
  int r=blockIdx.y*16+threadIdx.y, c=blockIdx.x*16+threadIdx.x; float acc=0;
  for(int k0=0;k0<K;k0+=16){
    sa[threadIdx.y][threadIdx.x]=b2f(A[(long)r*lda+k0+threadIdx.x]);
    sb[threadIdx.y][threadIdx.x]=b2f(B[(long)(k0+threadIdx.y)*ldb+c]);
    __syncthreads();
    for(int k=0;k<16;k++) acc+=sa[threadIdx.y][k]*sb[k][threadIdx.x];
    __syncthreads();
  }
  C[(long)r*ldc+c]=acc;
}
__global__ void cvt(const float* a,u16* o,long n){ long i=blockIdx.x*256L+threadIdx.x; if(i<n) o[i]=f2b(a[i]); }
__global__ void hmul(const float* g,const float* u,u16* h,long n){ long i=blockIdx.x*256L+threadIdx.x; if(i<n){ float a=g[i]; float s=a/(1.f+__expf(-a)); h[i]=f2b(s*u[i]); } }
__global__ void attn(const u16* q,const u16* k,const u16* v,u16* o){
  __shared__ float sq[DH]; __shared__ float p[S]; __shared__ float red[256];
  int row=blockIdx.x,h=blockIdx.y,t=threadIdx.x;
  if(t<DH) sq[t]=b2f(q[(long)row*H*DH+h*DH+t]);
  __syncthreads();
  float mx=-1e30f;
  for(int j=t;j<S;j+=256){ const u16* kr=k+((long)h*S+j)*DH; float a=0; for(int d=0;d<DH;d++) a+=sq[d]*b2f(kr[d]); a*=0.08838834764831845f; p[j]=a; mx=fmaxf(mx,a);}
  red[t]=mx; __syncthreads();
  for(int o2=128;o2;o2>>=1){ if(t<o2) red[t]=fmaxf(red[t],red[t+o2]); __syncthreads(); }
  mx=red[0]; __syncthreads();
  float s=0; for(int j=t;j<S;j+=256){ float e=__expf(p[j]-mx); p[j]=e; s+=e; }
  red[t]=s; __syncthreads();
  for(int o2=128;o2;o2>>=1){ if(t<o2) red[t]+=red[t+o2]; __syncthreads(); }
  s=red[0]; __syncthreads();
  int d=t&127, half=t>>7; float a=0;
  for(int j=half*1024;j<half*1024+1024;j++) a+=p[j]*b2f(v[((long)h*S+j)*DH+d]);
  red[t]=a; __syncthreads();
  if(t<128) o[(long)row*H*DH+h*DH+t]=f2b((red[t]+red[t+128])/s);
}
__global__ void fin(const u16* x,const float* s,const float* f,u16* out,long n){ long i=blockIdx.x*256L+threadIdx.x; if(i<n){ float a=f[i]; float g=a/(1.f+__expf(-a)); out[i]=f2b(b2f(x[i])+s[i]*g); } }
struct Bufs{ u16 *xn,*q,*h,*o; float *t0,*t1; };
static inline Bufs carve(void* ws){ char* p=(char*)ws; Bufs b; b.xn=(u16*)p; p+=(size_t)S*D*2; b.q=(u16*)p; p+=(size_t)S*H*DH*2; b.h=(u16*)p; p+=(size_t)S*F*2; b.o=(u16*)p; p+=(size_t)S*H*DH*2; b.t0=(float*)p; p+=(size_t)S*H*DH*4; b.t1=(float*)p; return b; }
static inline void run(void* const* d_in, void* d_out, void* d_ws, hipStream_t st){
  const u16 *x=(const u16*)d_in[0],*k=(const u16*)d_in[1],*v=(const u16*)d_in[2],*wq=(const u16*)d_in[3],*wo=(const u16*)d_in[4],*wg=(const u16*)d_in[6],*wu=(const u16*)d_in[7],*wd=(const u16*)d_in[8];
  Bufs b=carve(d_ws); dim3 tb(16,16);
  ln<<<S,64,0,st>>>(x,b.xn);
  gemm<<<dim3(H*DH/16,S/16),tb,0,st>>>(b.xn,D,wq,H*DH,b.t0,H*DH,D);
  cvt<<<S*H*DH/256,256,0,st>>>(b.t0,b.q,(long)S*H*DH);
  gemm<<<dim3(F/16,S/16),tb,0,st>>>(b.xn,D,wg,F,b.t0,F,D);
  gemm<<<dim3(F/16,S/16),tb,0,st>>>(b.xn,D,wu,F,b.t1,F,D);
  hmul<<<S*F/256,256,0,st>>>(b.t0,b.t1,b.h,(long)S*F);
  attn<<<dim3(S,H),256,0,st>>>(b.q,k,v,b.o);
  gemm<<<dim3(D/16,S/16),tb,0,st>>>(b.o,H*DH,wo,D,b.t0,D,H*DH);
  gemm<<<dim3(D/16,S/16),tb,0,st>>>(b.h,F,wd,D,b.t1,D,F);
  fin<<<S*D/256,256,0,st>>>(x,b.t0,b.t1,(u16*)d_out,(long)S*D);
}
}
extern "C" void kernel_launch(void* const* d_in, const int* in_sizes, int n_in, void* d_out, int out_size, void* d_ws, size_t ws_size, hipStream_t stream) {
  nv::run(d_in,d_out,d_ws,stream);
}
```

```cpp
#include <hip/hip_runtime.h>
#include <hip/hip_bf16.h>
#include <stdint.h>

namespace {
typedef unsigned short u16;
typedef unsigned u32x2 __attribute__((ext_vector_type(2)));
typedef unsigned u32x4 __attribute__((ext_vector_type(4)));
typedef unsigned u32x8 __attribute__((ext_vector_type(8)));
constexpr int SEQ = 2048, DM = 512, NH = 32, DH = 128, FF = 1792;
constexpr size_t WS_FLAG = 0, WS_XN = 8 * 24576, WS_H = WS_XN + (size_t)SEQ * DM * 2, WS_O = WS_H + (size_t)SEQ * FF * 2,
                 WS_PX = WS_O + (size_t)SEQ * NH * DH * 2, WS_END = WS_PX + (size_t)64 * 4 * 65536;

__device__ __forceinline__ u32x4 make_srd(const void* p, unsigned bytes) {
  const unsigned long long a = (unsigned long long)p;
  u32x4 r = {(unsigned)a, (unsigned)(a >> 32) & 0xffffu, bytes, 0x00020000u};
  return r;
}
template <int K> __device__ __forceinline__ void park(unsigned& v, const void* p) {
  const unsigned long long a = (unsigned long long)p;
  asm("v_writelane_b32 %0, %1, %3\n\tv_writelane_b32 %0, %2, %4" : "+v"(v) : "s"((unsigned)a), "s"((unsigned)(a >> 32) & 0xffffu), "i"(2 * K), "i"(2 * K + 1));
}

__global__ __launch_bounds__(256, 1) void fused_block(const u16* __restrict__ x, const u16* __restrict__ kc, const u16* __restrict__ vc,
                                                      const u16* __restrict__ wq, const u16* __restrict__ wo, const u16* __restrict__ wg,
                                                      const u16* __restrict__ wu, const u16* __restrict__ wd, u16* __restrict__ out, char* __restrict__ ws) {
  __shared__ __attribute__((aligned(1024))) char lds[163840];
  const unsigned ldsb = (unsigned)(size_t)lds;
  if (ldsb != 0u) return;
  char *xnf = ws + WS_XN, *hf = ws + WS_H, *of = ws + WS_O, *px = ws + WS_PX;
  const unsigned b = blockIdx.x, xc = b & 7u, hi = (b >> 3) & 3u, head = xc * 4u + hi, jb = b >> 5, mem = b & 31u;
  const unsigned w = __builtin_amdgcn_readfirstlane(threadIdx.x >> 6), lane = threadIdx.x & 63u;
  u32x2 e0; u32x4 x0, x1, cd;
  const u32x4 srdF = make_srd(ws + WS_FLAG + jb * 24576u, 24576u);
  const u32x4 srdXR = make_srd(x + (size_t)(jb * 256u + mem * 8u + w * 2u) * DM, 2048u);
  asm volatile("buffer_load_dwordx2 v[60:61], %4, %6, 0 offen sc1\n\tbuffer_load_dwordx4 v[64:67], %5, %7, 0 offen\n\tbuffer_load_dwordx4 v[68:71], %5, %7, 0 offen offset:1024\n\t"
               "s_getpc_b64 s[16:17]\n.Lkf_pc:\n\ts_and_b32 s17, s17, 0xffff\n\ts_mov_b32 s18, .Lkf_end-.Lkf_pc\n\ts_mov_b32 s19, 0x20000\n\t"
               "buffer_load_dword v44, %5, s[16:19], %8 offen"
               : "={v[60:61]}"(e0), "={v[64:67]}"(x0), "={v[68:71]}"(x1), "={v[44:47]}"(cd)
               : "v"(0u), "v"(lane * 16u), "s"(srdF), "s"(srdXR), "s"(((b >> 3) * 4u + w) * 1024u) : "s16", "s17", "s18", "s19", "memory");
  u32x4 cw = {0u, 0u, 0u, 0u};
  if (w >= 2u) {
    const unsigned r0 = 0u + ((b >> 3) * 2u + (w - 2u)) * (2u * 2u), cx = (xc >> 1) * 448u + (xc & 1u) * 256u, gs = min(7u * 3584u + 512u, (512u - r0) * 3584u - 2u * cx);
    const u32x4 sq = make_srd(wq + xc * 512u + (size_t)r0 * 4096u, 7u * 8192u + 1024u);
    const u32x4 sg = make_srd(wg + cx + (size_t)r0 * FF, gs), su = make_srd(wu + cx + (size_t)r0 * FF, gs);
    asm volatile("s_mov_b32 s16, 0\n\ts_mov_b32 s17, 0\n\t.rept 2\n\t"
                 "buffer_load_dword v40, %1, %3, s16 offen\n\tbuffer_load_dword v40, %2, %4, s17 offen\n\ts_add_u32 s16, s16, 8192\n\t"
                 "buffer_load_dword v40, %1, %3, s16 offen\n\tbuffer_load_dword v40, %2, %5, s17 offen\n\ts_add_u32 s16, s16, 8192\n\ts_add_u32 s17, s17, 7168\n\t.endr"
                 : "+{v[40:43]}"(cw) : "v"(lane * 16u), "v"((lane >> 5) * 3584u + (lane & 31u) * 16u), "s"(sq), "s"(sg), "s"(su) : "s16", "s17", "scc", "memory");
  }
  const unsigned cc = lane & 15u, g = lane >> 4, q = (lane >> 2) & 3u, p = lane & 3u, r8 = 4u * (g & 1u) + q;
  const bool shrt = xc & 1u, up = shrt && (hi & 1u);
  const unsigned colx = (xc >> 1) * 448u + (shrt ? 256u : 0u), col0 = colx + hi * (shrt ? 48u : 64u);
  const unsigned tt0 = jb * 16u + w * 4u;
  u32x8 vva; u32x4 vka, vqa, vga, vkd, vm, vf; u32x2 vn;
#pragma unroll
  for (unsigned i = 0; i < 8; ++i) vva[i] = ldsb + 65536u + (4u * g + q) * 256u + ((i ^ r8) << 5) + p * 8u;
#pragma unroll
  for (unsigned i = 0; i < 4; ++i) {
    vka[i] = ldsb + cc * 256u + 16u * ((4u * i + g) ^ cc);
    vqa[i] = ldsb + 65536u + (4u * g + q) * 256u + (((2u * i + (p >> 1)) ^ r8) << 5) + (p & 1u) * 16u;
    vga[i] = ldsb + (4u * g + q) * 128u + (((up ? (i + 1u) % 3u : i) ^ (r8 >> 1)) << 5) + p * 8u;
    vkd[i] = g * 256u + 16u * (cc ^ (4u * i + g));
  }
  vm[0] = g * 256u + 16u * (cc ^ (g << 1));
  vm[1] = g * 256u + 16u * (cc ^ ((4u + g) << 1));
  vm[2] = g * 8192u + 16u * (cc ^ (g << 1));
  vm[3] = g * 8192u + 16u * (cc ^ ((4u + g) << 1));
  vn[0] = (lane >> 3) * 3584u + 16u * ((lane & 7u) ^ (g << 1));
  vn[1] = lane * 16u;
  vf[0] = ldsb + 131072u + (lane >> 2) * 512u + (lane & 1u) * 256u + ((lane >> 1) & 1u) * 8u + w * 32u;
  vf[1] = threadIdx.x < 8u ? lane * 16u : 0x40000000u;
  vf[2] = lane * 128u; vf[3] = 0u;
  const u32x4 srdK = make_srd(kc + (size_t)head * SEQ * DH, SEQ * DH * 2), srdV = make_srd(vc + (size_t)head * SEQ * DH, SEQ * DH * 2);
  const u32x4 srdWQ = make_srd(wq + head * DH, 511u * 8192u + 256u);
  const unsigned wsz = min(511u * 3584u + 128u, 512u * 3584u - 2u * col0);
  const u32x4 srdWG = make_srd(wg + col0, wsz), srdWU = make_srd((const char*)(wu + col0) - 2048, wsz + 2048u);
  const u32x4 srdX = make_srd(xnf + (size_t)tt0 * 16384u, 65536u);
  const u32x4 srdO = make_srd(of + ((size_t)tt0 * 128u + head * 4u) * 1024u, (3u * 128u + 4u) * 1024u);
  const u32x4 srdH = make_srd(hf + ((size_t)tt0 * 56u + (col0 >> 5)) * 1024u, (3u * 56u + 2u) * 1024u);
  const u32x4 srdXW = make_srd(xnf + (size_t)(jb * 16u + (mem >> 1)) * 16384u + (mem & 1u) * 128u, 16384u - 128u);
  const unsigned sig = xc & 3u, T = jb * 2u + (xc >> 2), N = hi, tl = (xc >> 2) * 4u + N, tile = jb * 8u + tl, wx = (b >> 3) * 4u + w, tt = T * 8u + w * 2u;
  const u32x4 srdPO = make_srd((const char*)wo + (size_t)sig * 524288u + (wx >> 6) * 2097152u + (wx & 63u) * 8192u, 8192u);
  const char* pfd = (const char*)wd + sig * 458752u + min(wx * 3584u, 458752u - 4096u);
  const size_t xo = ((size_t)(T * 128u + sig * 32u) * DM + N * 128u + w * 32u) * 2u;
  unsigned pk = 0u;
  park<0>(pk, hf + (size_t)tt * 57344u + sig * 14336u);
  park<1>(pk, of + (size_t)tt * 131072u + sig * 16384u);
  park<2>(pk, (const char*)wd + sig * 458752u + N * 256u);
  park<3>(pk, (const char*)wo + (size_t)sig * 524288u + N * 256u);
  park<4>(pk, (const char*)x + xo);
  park<5>(pk, (const char*)out + xo);
  park<6>(pk, px + ((size_t)(tile * 4u + w) * 4u + sig) * 16384u);
  park<7>(pk, px + (size_t)(tile * 4u + sig) * 65536u + w * 2048u);

  asm("v_writelane_b32 %0, %1, 18\n\tv_writelane_b32 %0, %2, 19" : "+v"(pk) : "s"(up ? 1024u : 0u), "s"(up ? 8u : 1024u));
  u32x4 sc = {ldsb + 4096u * w, 16u * 8192u * w, 16u * 3584u * w, 4096u * w};
  const u32x4 sd = {head * 128u, sig, mem == 0u ? 0u : 0x40000000u, 16384u + 512u * tl};
  const u32x2 se = {256u + jb * 32u + w * 8u, (xc >> 2) * 2097152u};
  u32x4 sWQ = srdWQ, sWG = srdWG, sWU = srdWU, sX = srdX, sH = srdH, sXW = srdXW;
  asm volatile(R"ASM(
; ---- ln
; (issued by the entry statement)
; (issued by the entry statement)
; (issued by the entry statement)
; (issued by the entry statement)
s_mov_b32 s90, 0
s_mov_b32 s91, 0x4000
s_mov_b32 s92, 0x8000
s_mov_b32 s93, 0xc000
s_mov_b32 s84, 0x3e0293ee
s_mov_b32 s85, 0x3e0293ee
s_mov_b32 s86, 0xbfb8aa3b
s_mov_b32 s87, 0xbfb8aa3b
s_mov_b32 s88, 1.0
s_mov_b32 s89, 1.0
s_mov_b32 s76, s71
s_mov_b32 s77, s71
s_mov_b32 s78, 7
s_mov_b32 s12, 0
s_lshr_b32 s5, s71, 12
s_lshl_b32 s15, s71, 2
s_cmp_ge_u32 s5, 2
s_cbranch_scc1 .Lkf_xw
s_waitcnt vmcnt(1)
.Lkf_xw:
s_waitcnt vmcnt(9)
v_readfirstlane_b32 s94, v60
v_readfirstlane_b32 s95, v61
v_readfirstlane_b32 s20, v64
v_readfirstlane_b32 s22, v68
v_lshlrev_b32_e32 v72, 16, v64
v_and_b32_e32 v73, 0xffff0000, v64
v_lshlrev_b32_e32 v74, 16, v65
v_and_b32_e32 v75, 0xffff0000, v65
v_lshlrev_b32_e32 v76, 16, v66
v_and_b32_e32 v77, 0xffff0000, v66
v_lshlrev_b32_e32 v78, 16, v67
v_and_b32_e32 v79, 0xffff0000, v67
v_lshlrev_b32_e32 v80, 16, v68
v_and_b32_e32 v81, 0xffff0000, v68
v_lshlrev_b32_e32 v82, 16, v69
v_and_b32_e32 v83, 0xffff0000, v69
v_lshlrev_b32_e32 v84, 16, v70
v_and_b32_e32 v85, 0xffff0000, v70
v_lshlrev_b32_e32 v86, 16, v71
v_and_b32_e32 v87, 0xffff0000, v71
s_lshl_b32 s20, s20, 16
s_mov_b32 s21, s20
s_lshl_b32 s22, s22, 16
s_mov_b32 s23, s22
s_mul_i32 s94, s94, 0x19660d
s_mul_i32 s95, s95, 0x15a4e35
s_add_u32 s94, s94, 0x3c6ef35f
s_add_u32 s95, s95, 0x3039
v_pk_add_f32 v[88:89], v[72:73], s[20:21] neg_lo:[0,1] neg_hi:[0,1]
v_pk_add_f32 v[90:91], v[74:75], s[20:21] neg_lo:[0,1] neg_hi:[0,1]
v_pk_add_f32 v[92:93], v[76:77], s[20:21] neg_lo:[0,1] neg_hi:[0,1]
v_pk_add_f32 v[94:95], v[78:79], s[20:21] neg_lo:[0,1] neg_hi:[0,1]
v_pk_add_f32 v[96:97], v[80:81], s[22:23] neg_lo:[0,1] neg_hi:[0,1]
v_pk_add_f32 v[98:99], v[82:83], s[22:23] neg_lo:[0,1] neg_hi:[0,1]
v_pk_add_f32 v[100:101], v[84:85], s[22:23] neg_lo:[0,1] neg_hi:[0,1]
v_pk_add_f32 v[102:103], v[86:87], s[22:23] neg_lo:[0,1] neg_hi:[0,1]
v_pk_add_f32 v[112:113], v[88:89], v[90:91]
v_pk_mul_f32 v[116:117], v[88:89], v[88:89]
v_pk_add_f32 v[114:115], v[96:97], v[98:99]
v_pk_mul_f32 v[118:119], v[96:97], v[96:97]
v_pk_fma_f32 v[116:117], v[90:91], v[90:91], v[116:117]
v_pk_fma_f32 v[118:119], v[98:99], v[98:99], v[118:119]
v_pk_add_f32 v[112:113], v[112:113], v[92:93]
v_pk_fma_f32 v[116:117], v[92:93], v[92:93], v[116:117]
v_pk_add_f32 v[114:115], v[114:115], v[100:101]
v_pk_fma_f32 v[118:119], v[100:101], v[100:101], v[118:119]
v_pk_add_f32 v[112:113], v[112:113], v[94:95]
v_pk_fma_f32 v[116:117], v[94:95], v[94:95], v[116:117]
v_pk_add_f32 v[114:115], v[114:115], v[102:103]
v_pk_fma_f32 v[118:119], v[102:103], v[102:103], v[118:119]
v_add_f32_e32 v104, v112, v113
v_add_f32_e32 v105, v116, v117
v_add_f32_e32 v106, v114, v115
v_add_f32_e32 v107, v118, v119
v_mov_b32_e32 v60, s94
v_mov_b32_e32 v61, s95
v_mov_b32_e32 v62, s94
v_mov_b32_e32 v63, s95
v_add_f32_dpp v104, v104, v104 quad_perm:[1,0,3,2] row_mask:0xf bank_mask:0xf
v_add_f32_dpp v105, v105, v105 quad_perm:[1,0,3,2] row_mask:0xf bank_mask:0xf
v_add_f32_dpp v106, v106, v106 quad_perm:[1,0,3,2] row_mask:0xf bank_mask:0xf
v_add_f32_dpp v107, v107, v107 quad_perm:[1,0,3,2] row_mask:0xf bank_mask:0xf
v_add_f32_dpp v104, v104, v104 quad_perm:[2,3,0,1] row_mask:0xf bank_mask:0xf
v_add_f32_dpp v105, v105, v105 quad_perm:[2,3,0,1] row_mask:0xf bank_mask:0xf
v_add_f32_dpp v106, v106, v106 quad_perm:[2,3,0,1] row_mask:0xf bank_mask:0xf
v_add_f32_dpp v107, v107, v107 quad_perm:[2,3,0,1] row_mask:0xf bank_mask:0xf
v_add_f32_dpp v104, v104, v104 row_ror:4 row_mask:0xf bank_mask:0xf
v_add_f32_dpp v105, v105, v105 row_ror:4 row_mask:0xf bank_mask:0xf
v_add_f32_dpp v106, v106, v106 row_ror:4 row_mask:0xf bank_mask:0xf
v_add_f32_dpp v107, v107, v107 row_ror:4 row_mask:0xf bank_mask:0xf
v_add_f32_dpp v104, v104, v104 row_ror:8 row_mask:0xf bank_mask:0xf
v_add_f32_dpp v105, v105, v105 row_ror:8 row_mask:0xf bank_mask:0xf
v_add_f32_dpp v106, v106, v106 row_ror:8 row_mask:0xf bank_mask:0xf
v_add_f32_dpp v107, v107, v107 row_ror:8 row_mask:0xf bank_mask:0xf
v_mov_b32_e32 v108, v104
v_mov_b32_e32 v109, v105
v_mov_b32_e32 v110, v106
v_mov_b32_e32 v111, v107
v_permlane16_swap_b32_e32 v104, v108
v_permlane16_swap_b32_e32 v105, v109
v_permlane16_swap_b32_e32 v106, v110
v_permlane16_swap_b32_e32 v107, v111
v_add_f32_e32 v104, v104, v108
v_add_f32_e32 v105, v105, v109
v_add_f32_e32 v106, v106, v110
v_add_f32_e32 v107, v107, v111
v_mov_b32_e32 v108, v104
v_mov_b32_e32 v109, v105
v_mov_b32_e32 v110, v106
v_mov_b32_e32 v111, v107
v_permlane32_swap_b32_e32 v104, v108
v_permlane32_swap_b32_e32 v105, v109
v_permlane32_swap_b32_e32 v106, v110
v_permlane32_swap_b32_e32 v107, v111
v_add_f32_e32 v104, v104, v108
v_add_f32_e32 v105, v105, v109
v_add_f32_e32 v106, v106, v110
v_add_f32_e32 v107, v107, v111
v_mul_f32_e32 v104, 0x3b000000, v104
v_mul_f32_e32 v105, 0x3b000000, v105
v_mul_f32_e32 v106, 0x3b000000, v106
v_mul_f32_e32 v107, 0x3b000000, v107
v_fma_f32 v105, -v104, v104, v105
v_fma_f32 v107, -v106, v106, v107
v_add_f32_e32 v104, s20, v104
v_max_f32_e32 v105, 0, v105
v_add_f32_e32 v106, s22, v106
v_max_f32_e32 v107, 0, v107
v_cvt_pk_bf16_f32 v104, v104, v104
v_cvt_pk_bf16_f32 v105, v105, v105
v_cvt_pk_bf16_f32 v106, v106, v106
v_cvt_pk_bf16_f32 v107, v107, v107
v_lshlrev_b32_e32 v104, 16, v104
v_lshlrev_b32_e32 v105, 16, v105
v_lshlrev_b32_e32 v106, 16, v106
v_lshlrev_b32_e32 v107, 16, v107
v_add_f32_e32 v105, 0x3727c5ac, v105
v_add_f32_e32 v107, 0x3727c5ac, v107
v_pk_add_f32 v[88:89], v[72:73], v[104:105] op_sel_hi:[1,0] neg_lo:[0,1] neg_hi:[0,1]
v_pk_add_f32 v[90:91], v[74:75], v[104:105] op_sel_hi:[1,0] neg_lo:[0,1] neg_hi:[0,1]
v_pk_add_f32 v[92:93], v[76:77], v[104:105] op_sel_hi:[1,0] neg_lo:[0,1] neg_hi:[0,1]
v_pk_add_f32 v[94:95], v[78:79], v[104:105] op_sel_hi:[1,0] neg_lo:[0,1] neg_hi:[0,1]
v_pk_add_f32 v[96:97], v[80:81], v[106:107] op_sel_hi:[1,0] neg_lo:[0,1] neg_hi:[0,1]
v_pk_add_f32 v[98:99], v[82:83], v[106:107] op_sel_hi:[1,0] neg_lo:[0,1] neg_hi:[0,1]
v_pk_add_f32 v[100:101], v[84:85], v[106:107] op_sel_hi:[1,0] neg_lo:[0,1] neg_hi:[0,1]
v_pk_add_f32 v[102:103], v[86:87], v[106:107] op_sel_hi:[1,0] neg_lo:[0,1] neg_hi:[0,1]
v_cvt_pk_bf16_f32 v105, v105, v105
v_cvt_pk_bf16_f32 v107, v107, v107
v_lshlrev_b32_e32 v105, 16, v105
v_lshlrev_b32_e32 v107, 16, v107
v_sqrt_f32_e32 v105, v105
v_sqrt_f32_e32 v107, v107
v_cvt_pk_bf16_f32 v64, v88, v89
v_cvt_pk_bf16_f32 v65, v90, v91
v_cvt_pk_bf16_f32 v66, v92, v93
v_cvt_pk_bf16_f32 v67, v94, v95
v_cvt_pk_bf16_f32 v68, v96, v97
v_cvt_pk_bf16_f32 v69, v98, v99
v_cvt_pk_bf16_f32 v70, v100, v101
v_cvt_pk_bf16_f32 v71, v102, v103
v_cvt_pk_bf16_f32 v105, v105, v105
v_cvt_pk_bf16_f32 v107, v107, v107
v_lshlrev_b32_e32 v105, 16, v105
v_lshlrev_b32_e32 v107, 16, v107
v_rcp_f32_e32 v105, v105
v_rcp_f32_e32 v107, v107
v_lshlrev_b32_e32 v88, 16, v64
v_and_b32_e32 v89, 0xffff0000, v64
v_lshlrev_b32_e32 v90, 16, v65
v_and_b32_e32 v91, 0xffff0000, v65
v_lshlrev_b32_e32 v92, 16, v66
v_and_b32_e32 v93, 0xffff0000, v66
v_lshlrev_b32_e32 v94, 16, v67
v_and_b32_e32 v95, 0xffff0000, v67
v_lshlrev_b32_e32 v96, 16, v68
v_and_b32_e32 v97, 0xffff0000, v68
v_lshlrev_b32_e32 v98, 16, v69
v_and_b32_e32 v99, 0xffff0000, v69
v_lshlrev_b32_e32 v100, 16, v70
v_and_b32_e32 v101, 0xffff0000, v70
v_lshlrev_b32_e32 v102, 16, v71
v_and_b32_e32 v103, 0xffff0000, v71
v_pk_mul_f32 v[88:89], v[88:89], v[104:105] op_sel:[0,1] op_sel_hi:[1,1]
v_pk_mul_f32 v[90:91], v[90:91], v[104:105] op_sel:[0,1] op_sel_hi:[1,1]
v_pk_mul_f32 v[92:93], v[92:93], v[104:105] op_sel:[0,1] op_sel_hi:[1,1]
v_pk_mul_f32 v[94:95], v[94:95], v[104:105] op_sel:[0,1] op_sel_hi:[1,1]
v_cvt_pk_bf16_f32 v64, v88, v89
v_cvt_pk_bf16_f32 v65, v90, v91
v_cvt_pk_bf16_f32 v66, v92, v93
v_cvt_pk_bf16_f32 v67, v94, v95
ds_write_b64 v56, v[64:65] offset:0
ds_write_b64 v56, v[66:67] offset:128
v_pk_mul_f32 v[96:97], v[96:97], v[106:107] op_sel:[0,1] op_sel_hi:[1,1]
v_pk_mul_f32 v[98:99], v[98:99], v[106:107] op_sel:[0,1] op_sel_hi:[1,1]
v_pk_mul_f32 v[100:101], v[100:101], v[106:107] op_sel:[0,1] op_sel_hi:[1,1]
v_pk_mul_f32 v[102:103], v[102:103], v[106:107] op_sel:[0,1] op_sel_hi:[1,1]
v_cvt_pk_bf16_f32 v68, v96, v97
v_cvt_pk_bf16_f32 v69, v98, v99
v_cvt_pk_bf16_f32 v70, v100, v101
v_cvt_pk_bf16_f32 v71, v102, v103
ds_write_b64 v56, v[68:69] offset:16
ds_write_b64 v56, v[70:71] offset:144
s_add_u32 s81, s71, 0x20000
v_add_u32_e32 v35, s81, v29
v_and_b32_e32 v36, 0x70, v29
v_lshrrev_b32_e32 v37, 7, v29
v_lshl_add_u32 v36, v37, 8, v36
s_lshl_b32 s79, s71, 1
s_waitcnt lgkmcnt(0)
s_barrier
s_cmp_ge_u32 s5, 2
s_cbranch_scc1 .Lkf_xs
ds_read_b128 v[64:67], v35 offset:0
ds_read_b128 v[68:71], v35 offset:1024
ds_read_b128 v[72:75], v35 offset:2048
ds_read_b128 v[76:79], v35 offset:3072
s_add_u32 s80, s79, 0x1000
s_waitcnt lgkmcnt(0)
buffer_store_dwordx4 v[64:67], v36, s[96:99], s79 offen offset:0 sc1
buffer_store_dwordx4 v[68:71], v36, s[96:99], s79 offen offset:2048 sc1
buffer_store_dwordx4 v[72:75], v36, s[96:99], s80 offen offset:0 sc1
buffer_store_dwordx4 v[76:79], v36, s[96:99], s80 offen offset:2048 sc1
s_waitcnt vmcnt(0)
.Lkf_xs:
s_add_u32 s81, s72, 0x1000
s_mov_b32 s82, 0x1000
s_barrier
buffer_store_dwordx4 v[60:63], v57, s[24:27], s81 offen offset:0 sc1
s_cmp_eq_u32 s5, 0
s_cbranch_scc0 .Lkf_lnw
; ---- spx
.Lkf_spx:
s_mov_b32 s4, 0x20000
buffer_load_dwordx2 v[46:47], v58, s[24:27], s82 offen offset:0 sc1
s_sleep 4
; ---- spxl
.Lkf_spxl:
buffer_load_dwordx2 v[44:45], v58, s[24:27], s82 offen offset:0 sc1
s_waitcnt vmcnt(1)
v_cmp_eq_u64_e32 vcc, s[94:95], v[46:47]
s_nop 0
s_cmp_eq_u32 vcc_lo, -1
s_cbranch_scc1 .Lkf_ln1
buffer_load_dwordx2 v[46:47], v58, s[24:27], s82 offen offset:0 sc1
s_waitcnt vmcnt(1)
v_cmp_eq_u64_e32 vcc, s[94:95], v[44:45]
s_nop 0
s_cmp_eq_u32 vcc_lo, -1
s_cbranch_scc1 .Lkf_ln1
s_sub_u32 s4, s4, 1
s_cbranch_scc0 .Lkf_spxl
s_branch .Lkf_ln1
; ---- lnC
.Lkf_ln1:
buffer_store_dwordx4 v[60:63], v57, s[24:27], s74 offen offset:0 sc1
s_add_u32 s70, s70, 0x70000
s_add_u32 s69, s69, 0x100000
; fetched by the other waves
s_branch .Lkf_ln2
; ---- lnW
.Lkf_lnw:
s_mov_b32 m0, s68
s_add_u32 s79, s70, 0x6c00
buffer_load_dwordx4 v28, s[48:51], s70 offen offset:0 lds
buffer_load_dwordx4 v28, s[48:51], s79 offen offset:1024 lds
buffer_load_dwordx4 v28, s[52:55], s70 offen offset:2048 lds
buffer_load_dwordx4 v28, s[52:55], s79 offen offset:3072 lds
s_add_u32 s70, s70, 0x38000
s_add_u32 m0, s68, 0x10000
s_add_u32 s80, s69, 0x7c00
buffer_load_dwordx4 v26, s[44:47], s69 offen offset:0 lds
s_add_u32 s81, s69, 0xf800
buffer_load_dwordx4 v27, s[44:47], s80 offen offset:1024 lds
s_add_u32 s82, s69, 0x17400
buffer_load_dwordx4 v26, s[44:47], s81 offen offset:2048 lds
buffer_load_dwordx4 v27, s[44:47], s82 offen offset:3072 lds
s_add_u32 s69, s69, 0x80000
s_add_u32 m0, s68, 0x4000
s_add_u32 s79, s70, 0x6c00
buffer_load_dwordx4 v28, s[48:51], s70 offen offset:0 lds
buffer_load_dwordx4 v28, s[48:51], s79 offen offset:1024 lds
buffer_load_dwordx4 v28, s[52:55], s70 offen offset:2048 lds
buffer_load_dwordx4 v28, s[52:55], s79 offen offset:3072 lds
s_add_u32 s70, s70, 0x38000
s_add_u32 m0, s68, 0x14000
s_add_u32 s80, s69, 0x7c00
buffer_load_dwordx4 v26, s[44:47], s69 offen offset:0 lds
s_add_u32 s81, s69, 0xf800
buffer_load_dwordx4 v27, s[44:47], s80 offen offset:1024 lds
s_add_u32 s82, s69, 0x17400
buffer_load_dwordx4 v26, s[44:47], s81 offen offset:2048 lds
buffer_load_dwordx4 v27, s[44:47], s82 offen offset:3072 lds
s_add_u32 s69, s69, 0x80000
s_cmp_eq_u32 s5, 2
s_cbranch_scc1 .Lkf_lnw2
s_cmp_eq_u32 s5, 3
s_cbranch_scc1 .Lkf_ln2
; ---- lnW1
s_sub_u32 m0, s68, 0x1000
s_mov_b32 s79, 0
s_mov_b32 s80, 0x6c00
buffer_load_dwordx4 v28, s[48:51], s79 offen offset:0 lds
buffer_load_dwordx4 v28, s[48:51], s80 offen offset:1024 lds
buffer_load_dwordx4 v28, s[52:55], s79 offen offset:2048 lds
buffer_load_dwordx4 v28, s[52:55], s80 offen offset:3072 lds
s_add_u32 m0, s68, 0x3000
s_mov_b32 s79, 0x38000
s_mov_b32 s80, 0x3ec00
buffer_load_dwordx4 v28, s[48:51], s79 offen offset:0 lds
buffer_load_dwordx4 v28, s[48:51], s80 offen offset:1024 lds
buffer_load_dwordx4 v28, s[52:55], s79 offen offset:2048 lds
buffer_load_dwordx4 v28, s[52:55], s80 offen offset:3072 lds
s_branch .Lkf_ln2
; ---- lnW2
.Lkf_lnw2:
s_add_u32 m0, s68, 0xe000
s_mov_b32 s79, 0
buffer_load_dwordx4 v26, s[44:47], s79 offen offset:0 lds
s_mov_b32 s80, 0x7c00
buffer_load_dwordx4 v27, s[44:47], s80 offen offset:1024 lds
s_mov_b32 s79, 0xf800
buffer_load_dwordx4 v26, s[44:47], s79 offen offset:2048 lds
s_mov_b32 s80, 0x17400
buffer_load_dwordx4 v27, s[44:47], s80 offen offset:3072 lds
s_add_u32 m0, s68, 0x12000
s_mov_b32 s79, 0x80000
buffer_load_dwordx4 v26, s[44:47], s79 offen offset:0 lds
s_mov_b32 s80, 0x87c00
buffer_load_dwordx4 v27, s[44:47], s80 offen offset:1024 lds
s_mov_b32 s79, 0x8f800
buffer_load_dwordx4 v26, s[44:47], s79 offen offset:2048 lds
s_mov_b32 s80, 0x97400
buffer_load_dwordx4 v27, s[44:47], s80 offen offset:3072 lds
; ---- lnD
.Lkf_ln2:
s_barrier
buffer_load_dwordx4 a[32:35], v29, s[56:59], s90 offen offset:0 sc1
buffer_load_dwordx4 a[36:39], v29, s[56:59], s90 offen offset:1024 sc1
buffer_load_dwordx4 a[40:43], v29, s[56:59], s91 offen offset:0 sc1
buffer_load_dwordx4 a[44:47], v29, s[56:59], s91 offen offset:1024 sc1
buffer_load_dwordx4 a[48:51], v29, s[56:59], s92 offen offset:0 sc1
buffer_load_dwordx4 a[52:55], v29, s[56:59], s92 offen offset:1024 sc1
buffer_load_dwordx4 a[56:59], v29, s[56:59], s93 offen offset:0 sc1
buffer_load_dwordx4 a[60:63], v29, s[56:59], s93 offen offset:1024 sc1
buffer_load_dwordx4 a[64:67], v29, s[56:59], s90 offen offset:2048 sc1
buffer_load_dwordx4 a[68:71], v29, s[56:59], s90 offen offset:3072 sc1
s_add_u32 s90, s90, 0x1000
buffer_load_dwordx4 a[72:75], v29, s[56:59], s91 offen offset:2048 sc1
buffer_load_dwordx4 a[76:79], v29, s[56:59], s91 offen offset:3072 sc1
s_add_u32 s91, s91, 0x1000
buffer_load_dwordx4 a[80:83], v29, s[56:59], s92 offen offset:2048 sc1
buffer_load_dwordx4 a[84:87], v29, s[56:59], s92 offen offset:3072 sc1
s_add_u32 s92, s92, 0x1000
buffer_load_dwordx4 a[88:91], v29, s[56:59], s93 offen offset:2048 sc1
buffer_load_dwordx4 a[92:95], v29, s[56:59], s93 offen offset:3072 sc1
s_add_u32 s93, s93, 0x1000
s_waitcnt vmcnt(8)
s_barrier
; ---- pro0
v_mov_b32_e32 v34, 0
v_mov_b32_e32 v35, 0
v_mov_b32_e32 v36, 0
v_mov_b32_e32 v37, 0
ds_read_b64_tr_b16 a[0:1], v16 offset:0
ds_read_b64_tr_b16 a[2:3], v16 offset:4096
ds_read_b64_tr_b16 a[4:5], v16 offset:8192
ds_read_b64_tr_b16 a[6:7], v16 offset:12288
ds_read_b64_tr_b16 a[8:9], v17 offset:0
ds_read_b64_tr_b16 a[10:11], v17 offset:4096
ds_read_b64_tr_b16 a[12:13], v17 offset:8192
ds_read_b64_tr_b16 a[14:15], v17 offset:12288
s_lshl_b32 s21, s100, 13
s_add_u32 s22, s21, 0x8000
s_mul_i32 s23, s100, 0xe00
s_waitcnt lgkmcnt(4)
s_bitcmp1_b32 s73, 0
s_cbranch_scc1 .Lkf_proS
; ---- proL
v_mfma_f32_16x16x32_bf16 v[64:67], a[0:3], a[32:35], 0
ds_read_b64_tr_b16 a[16:17], v18 offset:0
v_mfma_f32_16x16x32_bf16 v[64:67], a[4:7], a[36:39], v[64:67]
ds_read_b64_tr_b16 a[18:19], v18 offset:4096
v_mfma_f32_16x16x32_bf16 v[80:83], a[0:3], a[40:43], 0
ds_read_b64_tr_b16 a[20:21], v18 offset:8192
v_mfma_f32_16x16x32_bf16 v[80:83], a[4:7], a[44:47], v[80:83]
ds_read_b64_tr_b16 a[22:23], v18 offset:12288
v_mfma_f32_16x16x32_bf16 v[96:99], a[0:3], a[48:51], 0
s_add_u32 m0, s68, 0x8000
s_add_u32 s79, s70, 0x6c00
buffer_load_dwordx4 v28, s[48:51], s70 offen offset:0 lds
v_mfma_f32_16x16x32_bf16 v[96:99], a[4:7], a[52:55], v[96:99]
v_mfma_f32_16x16x32_bf16 v[112:115], a[0:3], a[56:59], 0
buffer_load_dwordx4 v28, s[48:51], s79 offen offset:1024 lds
v_mfma_f32_16x16x32_bf16 v[112:115], a[4:7], a[60:63], v[112:115]
s_waitcnt lgkmcnt(4)
v_mfma_f32_16x16x32_bf16 v[68:71], a[8:11], a[32:35], 0
ds_read_b64_tr_b16 a[24:25], v19 offset:0
v_mfma_f32_16x16x32_bf16 v[68:71], a[12:15], a[36:39], v[68:71]
ds_read_b64_tr_b16 a[26:27], v19 offset:4096
v_mfma_f32_16x16x32_bf16 v[84:87], a[8:11], a[40:43], 0
ds_read_b64_tr_b16 a[28:29], v19 offset:8192
v_mfma_f32_16x16x32_bf16 v[84:87], a[12:15], a[44:47], v[84:87]
ds_read_b64_tr_b16 a[30:31], v19 offset:12288
v_mfma_f32_16x16x32_bf16 v[100:103], a[8:11], a[48:51], 0
buffer_load_dwordx4 v28, s[52:55], s70 offen offset:2048 lds
v_mfma_f32_16x16x32_bf16 v[100:103], a[12:15], a[52:55], v[100:103]
v_mfma_f32_16x16x32_bf16 v[116:119], a[8:11], a[56:59], 0
buffer_load_dwordx4 v28, s[52:55], s79 offen offset:3072 lds
s_add_u32 s70, s70, 0x38000
v_mfma_f32_16x16x32_bf16 v[116:119], a[12:15], a[60:63], v[116:119]
s_waitcnt lgkmcnt(4)
v_mfma_f32_16x16x32_bf16 v[72:75], a[16:19], a[32:35], 0
ds_read_b64_tr_b16 a[0:1], v16 offset:2048
v_mfma_f32_16x16x32_bf16 v[72:75], a[20:23], a[36:39], v[72:75]
ds_read_b64_tr_b16 a[2:3], v16 offset:6144
v_mfma_f32_16x16x32_bf16 v[88:91], a[16:19], a[40:43], 0
ds_read_b64_tr_b16 a[4:5], v16 offset:10240
v_mfma_f32_16x16x32_bf16 v[88:91], a[20:23], a[44:47], v[88:91]
ds_read_b64_tr_b16 a[6:7], v16 offset:14336
v_mfma_f32_16x16x32_bf16 v[104:107], a[16:19], a[48:51], 0
s_add_u32 m0, s68, 0x18000
s_add_u32 s80, s69, 0x7c00
buffer_load_dwordx4 v26, s[44:47], s69 offen offset:0 lds
v_mfma_f32_16x16x32_bf16 v[104:107], a[20:23], a[52:55], v[104:107]
v_mfma_f32_16x16x32_bf16 v[120:123], a[16:19], a[56:59], 0
s_add_u32 s81, s69, 0xf800
buffer_load_dwordx4 v27, s[44:47], s80 offen offset:1024 lds
v_mfma_f32_16x16x32_bf16 v[120:123], a[20:23], a[60:63], v[120:123]
s_waitcnt lgkmcnt(4)
v_mfma_f32_16x16x32_bf16 v[76:79], a[24:27], a[32:35], 0
ds_read_b64_tr_b16 a[8:9], v17 offset:2048
v_mfma_f32_16x16x32_bf16 v[76:79], a[28:31], a[36:39], v[76:79]
ds_read_b64_tr_b16 a[10:11], v17 offset:6144
v_mfma_f32_16x16x32_bf16 v[92:95], a[24:27], a[40:43], 0
ds_read_b64_tr_b16 a[12:13], v17 offset:10240
v_mfma_f32_16x16x32_bf16 v[92:95], a[28:31], a[44:47], v[92:95]
ds_read_b64_tr_b16 a[14:15], v17 offset:14336
v_mfma_f32_16x16x32_bf16 v[108:111], a[24:27], a[48:51], 0
s_add_u32 s82, s69, 0x17400
buffer_load_dwordx4 v26, s[44:47], s81 offen offset:2048 lds
v_mfma_f32_16x16x32_bf16 v[108:111], a[28:31], a[52:55], v[108:111]
v_mfma_f32_16x16x32_bf16 v[124:127], a[24:27], a[56:59], 0
buffer_load_dwordx4 v27, s[44:47], s82 offen offset:3072 lds
s_add_u32 s69, s69, 0x80000
v_mfma_f32_16x16x32_bf16 v[124:127], a[28:31], a[60:63], v[124:127]
s_waitcnt lgkmcnt(4)
v_mfma_f32_16x16x32_bf16 a[192:195], a[0:3], a[32:35], 0
ds_read_b64_tr_b16 a[16:17], v18 offset:2048
v_mfma_f32_16x16x32_bf16 a[192:195], a[4:7], a[36:39], a[192:195]
ds_read_b64_tr_b16 a[18:19], v18 offset:6144
v_mfma_f32_16x16x32_bf16 a[208:211], a[0:3], a[40:43], 0
ds_read_b64_tr_b16 a[20:21], v18 offset:10240
v_mfma_f32_16x16x32_bf16 a[208:211], a[4:7], a[44:47], a[208:211]
ds_read_b64_tr_b16 a[22:23], v18 offset:14336
v_mfma_f32_16x16x32_bf16 a[224:227], a[0:3], a[48:51], 0
s_add_u32 m0, s68, 0xc000
s_add_u32 s79, s70, 0x6c00
buffer_load_dwordx4 v28, s[48:51], s70 offen offset:0 lds
v_mfma_f32_16x16x32_bf16 a[224:227], a[4:7], a[52:55], a[224:227]
v_mfma_f32_16x16x32_bf16 a[240:243], a[0:3], a[56:59], 0
buffer_load_dwordx4 v28, s[48:51], s79 offen offset:1024 lds
v_mfma_f32_16x16x32_bf16 a[240:243], a[4:7], a[60:63], a[240:243]
s_waitcnt lgkmcnt(4)
v_mfma_f32_16x16x32_bf16 a[196:199], a[8:11], a[32:35], 0
ds_read_b64_tr_b16 a[24:25], v19 offset:2048
v_mfma_f32_16x16x32_bf16 a[196:199], a[12:15], a[36:39], a[196:199]
ds_read_b64_tr_b16 a[26:27], v19 offset:6144
v_mfma_f32_16x16x32_bf16 a[212:215], a[8:11], a[40:43], 0
ds_read_b64_tr_b16 a[28:29], v19 offset:10240
v_mfma_f32_16x16x32_bf16 a[212:215], a[12:15], a[44:47], a[212:215]
ds_read_b64_tr_b16 a[30:31], v19 offset:14336
v_mfma_f32_16x16x32_bf16 a[228:231], a[8:11], a[48:51], 0
buffer_load_dwordx4 v28, s[52:55], s70 offen offset:2048 lds
v_mfma_f32_16x16x32_bf16 a[228:231], a[12:15], a[52:55], a[228:231]
v_mfma_f32_16x16x32_bf16 a[244:247], a[8:11], a[56:59], 0
buffer_load_dwordx4 v28, s[52:55], s79 offen offset:3072 lds
s_add_u32 s70, s70, 0x38000
v_mfma_f32_16x16x32_bf16 a[244:247], a[12:15], a[60:63], a[244:247]
s_waitcnt lgkmcnt(4)
v_mfma_f32_16x16x32_bf16 a[200:203], a[16:19], a[32:35], 0
ds_read_b64_tr_b16 a[0:1], v12 offset:0
v_mfma_f32_16x16x32_bf16 a[200:203], a[20:23], a[36:39], a[200:203]
ds_read_b64_tr_b16 a[2:3], v12 offset:4096
v_mfma_f32_16x16x32_bf16 a[216:219], a[16:19], a[40:43], 0
ds_read_b64_tr_b16 a[4:5], v12 offset:8192
v_mfma_f32_16x16x32_bf16 a[216:219], a[20:23], a[44:47], a[216:219]
ds_read_b64_tr_b16 a[6:7], v12 offset:12288
v_mfma_f32_16x16x32_bf16 a[232:235], a[16:19], a[48:51], 0
s_add_u32 m0, s68, 0x1c000
s_add_u32 s80, s69, 0x7c00
buffer_load_dwordx4 v26, s[44:47], s69 offen offset:0 lds
v_mfma_f32_16x16x32_bf16 a[232:235], a[20:23], a[52:55], a[232:235]
v_mfma_f32_16x16x32_bf16 a[248:251], a[16:19], a[56:59], 0
s_add_u32 s81, s69, 0xf800
buffer_load_dwordx4 v27, s[44:47], s80 offen offset:1024 lds
v_mfma_f32_16x16x32_bf16 a[248:251], a[20:23], a[60:63], a[248:251]
s_waitcnt lgkmcnt(4)
v_mfma_f32_16x16x32_bf16 a[204:207], a[24:27], a[32:35], 0
ds_read_b64_tr_b16 a[8:9], v12 offset:8
v_mfma_f32_16x16x32_bf16 a[204:207], a[28:31], a[36:39], a[204:207]
ds_read_b64_tr_b16 a[10:11], v12 offset:4104
v_mfma_f32_16x16x32_bf16 a[220:223], a[24:27], a[40:43], 0
ds_read_b64_tr_b16 a[12:13], v12 offset:8200
v_mfma_f32_16x16x32_bf16 a[220:223], a[28:31], a[44:47], a[220:223]
ds_read_b64_tr_b16 a[14:15], v12 offset:12296
v_mfma_f32_16x16x32_bf16 a[236:239], a[24:27], a[48:51], 0
s_add_u32 s82, s69, 0x17400
buffer_load_dwordx4 v26, s[44:47], s81 offen offset:2048 lds
v_mfma_f32_16x16x32_bf16 a[236:239], a[28:31], a[52:55], a[236:239]
v_mfma_f32_16x16x32_bf16 a[252:255], a[24:27], a[56:59], 0
buffer_load_dwordx4 v27, s[44:47], s82 offen offset:3072 lds
s_add_u32 s69, s69, 0x80000
v_mfma_f32_16x16x32_bf16 a[252:255], a[28:31], a[60:63], a[252:255]
s_waitcnt lgkmcnt(4)
v_mfma_f32_16x16x32_bf16 v[128:131], a[0:3], a[32:35], 0
ds_read_b64_tr_b16 a[16:17], v13 offset:0
v_mfma_f32_16x16x32_bf16 v[128:131], a[4:7], a[36:39], v[128:131]
ds_read_b64_tr_b16 a[18:19], v13 offset:4096
v_mfma_f32_16x16x32_bf16 v[160:163], a[0:3], a[40:43], 0
ds_read_b64_tr_b16 a[20:21], v13 offset:8192
v_mfma_f32_16x16x32_bf16 v[160:163], a[4:7], a[44:47], v[160:163]
ds_read_b64_tr_b16 a[22:23], v13 offset:12288
v_mfma_f32_16x16x32_bf16 v[192:195], a[0:3], a[48:51], 0
buffer_load_dword v44, v26, s[44:47], s21 offen offset:0
v_mfma_f32_16x16x32_bf16 v[192:195], a[4:7], a[52:55], v[192:195]
v_mfma_f32_16x16x32_bf16 v[224:227], a[0:3], a[56:59], 0
buffer_load_dword v44, v26, s[44:47], s22 offen offset:0
v_mfma_f32_16x16x32_bf16 v[224:227], a[4:7], a[60:63], v[224:227]
s_waitcnt lgkmcnt(4)
v_mfma_f32_16x16x32_bf16 v[132:135], a[8:11], a[32:35], 0
ds_read_b64_tr_b16 a[24:25], v13 offset:8
v_mfma_f32_16x16x32_bf16 v[132:135], a[12:15], a[36:39], v[132:135]
ds_read_b64_tr_b16 a[26:27], v13 offset:4104
v_mfma_f32_16x16x32_bf16 v[164:167], a[8:11], a[40:43], 0
ds_read_b64_tr_b16 a[28:29], v13 offset:8200
v_mfma_f32_16x16x32_bf16 v[164:167], a[12:15], a[44:47], v[164:167]
ds_read_b64_tr_b16 a[30:31], v13 offset:12296
v_mfma_f32_16x16x32_bf16 v[196:199], a[8:11], a[48:51], 0
buffer_load_dword v44, v28, s[48:51], s23 offen offset:0
v_mfma_f32_16x16x32_bf16 v[196:199], a[12:15], a[52:55], v[196:199]
v_mfma_f32_16x16x32_bf16 v[228:231], a[8:11], a[56:59], 0
buffer_load_dword v44, v28, s[52:55], s23 offen offset:2048
v_mfma_f32_16x16x32_bf16 v[228:231], a[12:15], a[60:63], v[228:231]
s_waitcnt lgkmcnt(4)
v_mfma_f32_16x16x32_bf16 v[136:139], a[16:19], a[32:35], 0
ds_read_b64_tr_b16 a[0:1], v14 offset:0
v_mfma_f32_16x16x32_bf16 v[136:139], a[20:23], a[36:39], v[136:139]
ds_read_b64_tr_b16 a[2:3], v14 offset:4096
v_mfma_f32_16x16x32_bf16 v[168:171], a[16:19], a[40:43], 0
ds_read_b64_tr_b16 a[4:5], v14 offset:8192
v_mfma_f32_16x16x32_bf16 v[168:171], a[20:23], a[44:47], v[168:171]
ds_read_b64_tr_b16 a[6:7], v14 offset:12288
v_mfma_f32_16x16x32_bf16 v[200:203], a[16:19], a[48:51], 0
buffer_load_dwordx4 a[96:99], v29, s[56:59], s90 offen offset:0 sc1
v_mfma_f32_16x16x32_bf16 v[200:203], a[20:23], a[52:55], v[200:203]
v_mfma_f32_16x16x32_bf16 v[232:235], a[16:19], a[56:59], 0
buffer_load_dwordx4 a[100:103], v29, s[56:59], s90 offen offset:1024 sc1
v_mfma_f32_16x16x32_bf16 v[232:235], a[20:23], a[60:63], v[232:235]
s_waitcnt lgkmcnt(4)
v_mfma_f32_16x16x32_bf16 v[140:143], a[24:27], a[32:35], 0
ds_read_b64_tr_b16 a[8:9], v14 offset:8
v_mfma_f32_16x16x32_bf16 v[140:143], a[28:31], a[36:39], v[140:143]
ds_read_b64_tr_b16 a[10:11], v14 offset:4104
v_mfma_f32_16x16x32_bf16 v[172:175], a[24:27], a[40:43], 0
ds_read_b64_tr_b16 a[12:13], v14 offset:8200
v_mfma_f32_16x16x32_bf16 v[172:175], a[28:31], a[44:47], v[172:175]
ds_read_b64_tr_b16 a[14:15], v14 offset:12296
v_mfma_f32_16x16x32_bf16 v[204:207], a[24:27], a[48:51], 0
buffer_load_dwordx4 a[104:107], v29, s[56:59], s91 offen offset:0 sc1
v_mfma_f32_16x16x32_bf16 v[204:207], a[28:31], a[52:55], v[204:207]
v_mfma_f32_16x16x32_bf16 v[236:239], a[24:27], a[56:59], 0
buffer_load_dwordx4 a[108:111], v29, s[56:59], s91 offen offset:1024 sc1
v_mfma_f32_16x16x32_bf16 v[236:239], a[28:31], a[60:63], v[236:239]
s_waitcnt lgkmcnt(4)
v_mfma_f32_16x16x32_bf16 v[144:147], a[0:3], a[32:35], 0
ds_read_b64_tr_b16 a[16:17], v15 offset:0
v_mfma_f32_16x16x32_bf16 v[144:147], a[4:7], a[36:39], v[144:147]
ds_read_b64_tr_b16 a[18:19], v15 offset:4096
v_mfma_f32_16x16x32_bf16 v[176:179], a[0:3], a[40:43], 0
ds_read_b64_tr_b16 a[20:21], v15 offset:8192
v_mfma_f32_16x16x32_bf16 v[176:179], a[4:7], a[44:47], v[176:179]
ds_read_b64_tr_b16 a[22:23], v15 offset:12288
v_mfma_f32_16x16x32_bf16 v[208:211], a[0:3], a[48:51], 0
buffer_load_dwordx4 a[112:115], v29, s[56:59], s92 offen offset:0 sc1
v_mfma_f32_16x16x32_bf16 v[208:211], a[4:7], a[52:55], v[208:211]
v_mfma_f32_16x16x32_bf16 v[240:243], a[0:3], a[56:59], 0
buffer_load_dwordx4 a[116:119], v29, s[56:59], s92 offen offset:1024 sc1
v_mfma_f32_16x16x32_bf16 v[240:243], a[4:7], a[60:63], v[240:243]
s_waitcnt lgkmcnt(4)
v_mfma_f32_16x16x32_bf16 v[148:151], a[8:11], a[32:35], 0
ds_read_b64_tr_b16 a[24:25], v15 offset:8
v_mfma_f32_16x16x32_bf16 v[148:151], a[12:15], a[36:39], v[148:151]
ds_read_b64_tr_b16 a[26:27], v15 offset:4104
v_mfma_f32_16x16x32_bf16 v[180:183], a[8:11], a[40:43], 0
ds_read_b64_tr_b16 a[28:29], v15 offset:8200
v_mfma_f32_16x16x32_bf16 v[180:183], a[12:15], a[44:47], v[180:183]
ds_read_b64_tr_b16 a[30:31], v15 offset:12296
v_mfma_f32_16x16x32_bf16 v[212:215], a[8:11], a[48:51], 0
buffer_load_dwordx4 a[120:123], v29, s[56:59], s93 offen offset:0 sc1
v_mfma_f32_16x16x32_bf16 v[212:215], a[12:15], a[52:55], v[212:215]
v_mfma_f32_16x16x32_bf16 v[244:247], a[8:11], a[56:59], 0
buffer_load_dwordx4 a[124:127], v29, s[56:59], s93 offen offset:1024 sc1
v_mfma_f32_16x16x32_bf16 v[244:247], a[12:15], a[60:63], v[244:247]
s_waitcnt lgkmcnt(4)
v_mfma_f32_16x16x32_bf16 v[152:155], a[16:19], a[32:35], 0
ds_read_b64_tr_b16 a[0:1], v16 offset:16384
v_mfma_f32_16x16x32_bf16 v[152:155], a[20:23], a[36:39], v[152:155]
ds_read_b64_tr_b16 a[2:3], v16 offset:20480
v_mfma_f32_16x16x32_bf16 v[184:187], a[16:19], a[40:43], 0
ds_read_b64_tr_b16 a[4:5], v16 offset:24576
v_mfma_f32_16x16x32_bf16 v[184:187], a[20:23], a[44:47], v[184:187]
ds_read_b64_tr_b16 a[6:7], v16 offset:28672
v_mfma_f32_16x16x32_bf16 v[216:219], a[16:19], a[48:51], 0
v_mfma_f32_16x16x32_bf16 v[216:219], a[20:23], a[52:55], v[216:219]
v_mfma_f32_16x16x32_bf16 v[248:251], a[16:19], a[56:59], 0
v_mfma_f32_16x16x32_bf16 v[248:251], a[20:23], a[60:63], v[248:251]
s_waitcnt lgkmcnt(4)
v_mfma_f32_16x16x32_bf16 v[156:159], a[24:27], a[32:35], 0
ds_read_b64_tr_b16 a[8:9], v17 offset:16384
v_mfma_f32_16x16x32_bf16 v[156:159], a[28:31], a[36:39], v[156:159]
ds_read_b64_tr_b16 a[10:11], v17 offset:20480
v_mfma_f32_16x16x32_bf16 v[188:191], a[24:27], a[40:43], 0
ds_read_b64_tr_b16 a[12:13], v17 offset:24576
v_mfma_f32_16x16x32_bf16 v[188:191], a[28:31], a[44:47], v[188:191]
ds_read_b64_tr_b16 a[14:15], v17 offset:28672
v_mfma_f32_16x16x32_bf16 v[220:223], a[24:27], a[48:51], 0
s_waitcnt vmcnt(20)
v_mfma_f32_16x16x32_bf16 v[220:223], a[28:31], a[52:55], v[220:223]
s_barrier
v_mfma_f32_16x16x32_bf16 v[252:255], a[24:27], a[56:59], 0
s_waitcnt vmcnt(28)
v_mfma_f32_16x16x32_bf16 v[252:255], a[28:31], a[60:63], v[252:255]
s_waitcnt lgkmcnt(4)
v_mfma_f32_16x16x32_bf16 v[64:67], a[0:3], a[64:67], v[64:67]
ds_read_b64_tr_b16 a[16:17], v18 offset:16384
v_mfma_f32_16x16x32_bf16 v[64:67], a[4:7], a[68:71], v[64:67]
ds_read_b64_tr_b16 a[18:19], v18 offset:20480
v_mfma_f32_16x16x32_bf16 v[80:83], a[0:3], a[72:75], v[80:83]
ds_read_b64_tr_b16 a[20:21], v18 offset:24576
v_mfma_f32_16x16x32_bf16 v[80:83], a[4:7], a[76:79], v[80:83]
ds_read_b64_tr_b16 a[22:23], v18 offset:28672
v_mfma_f32_16x16x32_bf16 v[96:99], a[0:3], a[80:83], v[96:99]
s_mov_b32 m0, s68
s_add_u32 s79, s70, 0x6c00
buffer_load_dwordx4 v28, s[48:51], s70 offen offset:0 lds
v_mfma_f32_16x16x32_bf16 v[96:99], a[4:7], a[84:87], v[96:99]
v_mfma_f32_16x16x32_bf16 v[112:115], a[0:3], a[88:91], v[112:115]
buffer_load_dwordx4 v28, s[48:51], s79 offen offset:1024 lds
v_mfma_f32_16x16x32_bf16 v[112:115], a[4:7], a[92:95], v[112:115]
s_waitcnt lgkmcnt(4)
v_mfma_f32_16x16x32_bf16 v[68:71], a[8:11], a[64:67], v[68:71]
ds_read_b64_tr_b16 a[24:25], v19 offset:16384
v_mfma_f32_16x16x32_bf16 v[68:71], a[12:15], a[68:71], v[68:71]
ds_read_b64_tr_b16 a[26:27], v19 offset:20480
v_mfma_f32_16x16x32_bf16 v[84:87], a[8:11], a[72:75], v[84:87]
ds_read_b64_tr_b16 a[28:29], v19 offset:24576
v_mfma_f32_16x16x32_bf16 v[84:87], a[12:15], a[76:79], v[84:87]
ds_read_b64_tr_b16 a[30:31], v19 offset:28672
v_mfma_f32_16x16x32_bf16 v[100:103], a[8:11], a[80:83], v[100:103]
buffer_load_dwordx4 v28, s[52:55], s70 offen offset:2048 lds
v_mfma_f32_16x16x32_bf16 v[100:103], a[12:15], a[84:87], v[100:103]
v_mfma_f32_16x16x32_bf16 v[116:119], a[8:11], a[88:91], v[116:119]
buffer_load_dwordx4 v28, s[52:55], s79 offen offset:3072 lds
s_add_u32 s70, s70, 0x38000
v_mfma_f32_16x16x32_bf16 v[116:119], a[12:15], a[92:95], v[116:119]
s_waitcnt lgkmcnt(4)
v_mfma_f32_16x16x32_bf16 v[72:75], a[16:19], a[64:67], v[72:75]
ds_read_b64_tr_b16 a[0:1], v16 offset:18432
v_mfma_f32_16x16x32_bf16 v[72:75], a[20:23], a[68:71], v[72:75]
ds_read_b64_tr_b16 a[2:3], v16 offset:22528
v_mfma_f32_16x16x32_bf16 v[88:91], a[16:19], a[72:75], v[88:91]
ds_read_b64_tr_b16 a[4:5], v16 offset:26624
v_mfma_f32_16x16x32_bf16 v[88:91], a[20:23], a[76:79], v[88:91]
ds_read_b64_tr_b16 a[6:7], v16 offset:30720
v_mfma_f32_16x16x32_bf16 v[104:107], a[16:19], a[80:83], v[104:107]
s_add_u32 m0, s68, 0x10000
s_add_u32 s80, s69, 0x7c00
buffer_load_dwordx4 v26, s[44:47], s69 offen offset:0 lds
v_mfma_f32_16x16x32_bf16 v[104:107], a[20:23], a[84:87], v[104:107]
v_mfma_f32_16x16x32_bf16 v[120:123], a[16:19], a[88:91], v[120:123]
s_add_u32 s81, s69, 0xf800
buffer_load_dwordx4 v27, s[44:47], s80 offen offset:1024 lds
v_mfma_f32_16x16x32_bf16 v[120:123], a[20:23], a[92:95], v[120:123]
s_waitcnt lgkmcnt(4)
v_mfma_f32_16x16x32_bf16 v[76:79], a[24:27], a[64:67], v[76:79]
ds_read_b64_tr_b16 a[8:9], v17 offset:18432
v_mfma_f32_16x16x32_bf16 v[76:79], a[28:31], a[68:71], v[76:79]
ds_read_b64_tr_b16 a[10:11], v17 offset:22528
v_mfma_f32_16x16x32_bf16 v[92:95], a[24:27], a[72:75], v[92:95]
ds_read_b64_tr_b16 a[12:13], v17 offset:26624
v_mfma_f32_16x16x32_bf16 v[92:95], a[28:31], a[76:79], v[92:95]
ds_read_b64_tr_b16 a[14:15], v17 offset:30720
v_mfma_f32_16x16x32_bf16 v[108:111], a[24:27], a[80:83], v[108:111]
s_add_u32 s82, s69, 0x17400
buffer_load_dwordx4 v26, s[44:47], s81 offen offset:2048 lds
v_mfma_f32_16x16x32_bf16 v[108:111], a[28:31], a[84:87], v[108:111]
v_mfma_f32_16x16x32_bf16 v[124:127], a[24:27], a[88:91], v[124:127]
buffer_load_dwordx4 v27, s[44:47], s82 offen offset:3072 lds
s_add_u32 s69, s69, 0x80000
v_mfma_f32_16x16x32_bf16 v[124:127], a[28:31], a[92:95], v[124:127]
s_waitcnt lgkmcnt(4)
v_mfma_f32_16x16x32_bf16 a[192:195], a[0:3], a[64:67], a[192:195]
ds_read_b64_tr_b16 a[16:17], v18 offset:18432
v_mfma_f32_16x16x32_bf16 a[192:195], a[4:7], a[68:71], a[192:195]
ds_read_b64_tr_b16 a[18:19], v18 offset:22528
v_mfma_f32_16x16x32_bf16 a[208:211], a[0:3], a[72:75], a[208:211]
ds_read_b64_tr_b16 a[20:21], v18 offset:26624
v_mfma_f32_16x16x32_bf16 a[208:211], a[4:7], a[76:79], a[208:211]
ds_read_b64_tr_b16 a[22:23], v18 offset:30720
v_mfma_f32_16x16x32_bf16 a[224:227], a[0:3], a[80:83], a[224:227]
buffer_load_dwordx4 a[128:131], v29, s[56:59], s90 offen offset:2048 sc1
v_mfma_f32_16x16x32_bf16 a[224:227], a[4:7], a[84:87], a[224:227]
v_mfma_f32_16x16x32_bf16 a[240:243], a[0:3], a[88:91], a[240:243]
buffer_load_dwordx4 a[132:135], v29, s[56:59], s90 offen offset:3072 sc1
s_add_u32 s90, s90, 0x1000
v_mfma_f32_16x16x32_bf16 a[240:243], a[4:7], a[92:95], a[240:243]
s_waitcnt lgkmcnt(4)
v_mfma_f32_16x16x32_bf16 a[196:199], a[8:11], a[64:67], a[196:199]
ds_read_b64_tr_b16 a[24:25], v19 offset:18432
v_mfma_f32_16x16x32_bf16 a[196:199], a[12:15], a[68:71], a[196:199]
ds_read_b64_tr_b16 a[26:27], v19 offset:22528
v_mfma_f32_16x16x32_bf16 a[212:215], a[8:11], a[72:75], a[212:215]
ds_read_b64_tr_b16 a[28:29], v19 offset:26624
v_mfma_f32_16x16x32_bf16 a[212:215], a[12:15], a[76:79], a[212:215]
ds_read_b64_tr_b16 a[30:31], v19 offset:30720
v_mfma_f32_16x16x32_bf16 a[228:231], a[8:11], a[80:83], a[228:231]
buffer_load_dwordx4 a[136:139], v29, s[56:59], s91 offen offset:2048 sc1
v_mfma_f32_16x16x32_bf16 a[228:231], a[12:15], a[84:87], a[228:231]
v_mfma_f32_16x16x32_bf16 a[244:247], a[8:11], a[88:91], a[244:247]
buffer_load_dwordx4 a[140:143], v29, s[56:59], s91 offen offset:3072 sc1
s_add_u32 s91, s91, 0x1000
v_mfma_f32_16x16x32_bf16 a[244:247], a[12:15], a[92:95], a[244:247]
s_waitcnt lgkmcnt(4)
v_mfma_f32_16x16x32_bf16 a[200:203], a[16:19], a[64:67], a[200:203]
ds_read_b64_tr_b16 a[0:1], v12 offset:16384
v_mfma_f32_16x16x32_bf16 a[200:203], a[20:23], a[68:71], a[200:203]
ds_read_b64_tr_b16 a[2:3], v12 offset:20480
v_mfma_f32_16x16x32_bf16 a[216:219], a[16:19], a[72:75], a[216:219]
ds_read_b64_tr_b16 a[4:5], v12 offset:24576
v_mfma_f32_16x16x32_bf16 a[216:219], a[20:23], a[76:79], a[216:219]
ds_read_b64_tr_b16 a[6:7], v12 offset:28672
v_mfma_f32_16x16x32_bf16 a[232:235], a[16:19], a[80:83], a[232:235]
buffer_load_dwordx4 a[144:147], v29, s[56:59], s92 offen offset:2048 sc1
v_mfma_f32_16x16x32_bf16 a[232:235], a[20:23], a[84:87], a[232:235]
v_mfma_f32_16x16x32_bf16 a[248:251], a[16:19], a[88:91], a[248:251]
buffer_load_dwordx4 a[148:151], v29, s[56:59], s92 offen offset:3072 sc1
s_add_u32 s92, s92, 0x1000
v_mfma_f32_16x16x32_bf16 a[248:251], a[20:23], a[92:95], a[248:251]
s_waitcnt lgkmcnt(4)
v_mfma_f32_16x16x32_bf16 a[204:207], a[24:27], a[64:67], a[204:207]
ds_read_b64_tr_b16 a[8:9], v12 offset:16392
v_mfma_f32_16x16x32_bf16 a[204:207], a[28:31], a[68:71], a[204:207]
ds_read_b64_tr_b16 a[10:11], v12 offset:20488
v_mfma_f32_16x16x32_bf16 a[220:223], a[24:27], a[72:75], a[220:223]
ds_read_b64_tr_b16 a[12:13], v12 offset:24584
v_mfma_f32_16x16x32_bf16 a[220:223], a[28:31], a[76:79], a[220:223]
ds_read_b64_tr_b16 a[14:15], v12 offset:28680
v_mfma_f32_16x16x32_bf16 a[236:239], a[24:27], a[80:83], a[236:239]
buffer_load_dwordx4 a[152:155], v29, s[56:59], s93 offen offset:2048 sc1
v_mfma_f32_16x16x32_bf16 a[236:239], a[28:31], a[84:87], a[236:239]
v_mfma_f32_16x16x32_bf16 a[252:255], a[24:27], a[88:91], a[252:255]
buffer_load_dwordx4 a[156:159], v29, s[56:59], s93 offen offset:3072 sc1
s_add_u32 s93, s93, 0x1000
v_mfma_f32_16x16x32_bf16 a[252:255], a[28:31], a[92:95], a[252:255]
s_waitcnt lgkmcnt(4)
v_mfma_f32_16x16x32_bf16 v[128:131], a[0:3], a[64:67], v[128:131]
ds_read_b64_tr_b16 a[16:17], v13 offset:16384
v_mfma_f32_16x16x32_bf16 v[128:131], a[4:7], a[68:71], v[128:131]
ds_read_b64_tr_b16 a[18:19], v13 offset:20480
v_mfma_f32_16x16x32_bf16 v[160:163], a[0:3], a[72:75], v[160:163]
ds_read_b64_tr_b16 a[20:21], v13 offset:24576
v_mfma_f32_16x16x32_bf16 v[160:163], a[4:7], a[76:79], v[160:163]
ds_read_b64_tr_b16 a[22:23], v13 offset:28672
v_mfma_f32_16x16x32_bf16 v[192:195], a[0:3], a[80:83], v[192:195]
v_mfma_f32_16x16x32_bf16 v[192:195], a[4:7], a[84:87], v[192:195]
v_mfma_f32_16x16x32_bf16 v[224:227], a[0:3], a[88:91], v[224:227]
v_mfma_f32_16x16x32_bf16 v[224:227], a[4:7], a[92:95], v[224:227]
s_waitcnt lgkmcnt(4)
v_mfma_f32_16x16x32_bf16 v[132:135], a[8:11], a[64:67], v[132:135]
ds_read_b64_tr_b16 a[24:25], v13 offset:16392
v_mfma_f32_16x16x32_bf16 v[132:135], a[12:15], a[68:71], v[132:135]
ds_read_b64_tr_b16 a[26:27], v13 offset:20488
v_mfma_f32_16x16x32_bf16 v[164:167], a[8:11], a[72:75], v[164:167]
ds_read_b64_tr_b16 a[28:29], v13 offset:24584
v_mfma_f32_16x16x32_bf16 v[164:167], a[12:15], a[76:79], v[164:167]
ds_read_b64_tr_b16 a[30:31], v13 offset:28680
v_mfma_f32_16x16x32_bf16 v[196:199], a[8:11], a[80:83], v[196:199]
v_mfma_f32_16x16x32_bf16 v[196:199], a[12:15], a[84:87], v[196:199]
v_mfma_f32_16x16x32_bf16 v[228:231], a[8:11], a[88:91], v[228:231]
v_mfma_f32_16x16x32_bf16 v[228:231], a[12:15], a[92:95], v[228:231]
s_waitcnt lgkmcnt(4)
v_mfma_f32_16x16x32_bf16 v[136:139], a[16:19], a[64:67], v[136:139]
ds_read_b64_tr_b16 a[0:1], v14 offset:16384
v_mfma_f32_16x16x32_bf16 v[136:139], a[20:23], a[68:71], v[136:139]
ds_read_b64_tr_b16 a[2:3], v14 offset:20480
v_mfma_f32_16x16x32_bf16 v[168:171], a[16:19], a[72:75], v[168:171]
ds_read_b64_tr_b16 a[4:5], v14 offset:24576
v_mfma_f32_16x16x32_bf16 v[168:171], a[20:23], a[76:79], v[168:171]
ds_read_b64_tr_b16 a[6:7], v14 offset:28672
v_mfma_f32_16x16x32_bf16 v[200:203], a[16:19], a[80:83], v[200:203]
v_mfma_f32_16x16x32_bf16 v[200:203], a[20:23], a[84:87], v[200:203]
v_mfma_f32_16x16x32_bf16 v[232:235], a[16:19], a[88:91], v[232:235]
v_mfma_f32_16x16x32_bf16 v[232:235], a[20:23], a[92:95], v[232:235]
s_waitcnt lgkmcnt(4)
v_mfma_f32_16x16x32_bf16 v[140:143], a[24:27], a[64:67], v[140:143]
ds_read_b64_tr_b16 a[8:9], v14 offset:16392
v_mfma_f32_16x16x32_bf16 v[140:143], a[28:31], a[68:71], v[140:143]
ds_read_b64_tr_b16 a[10:11], v14 offset:20488
v_mfma_f32_16x16x32_bf16 v[172:175], a[24:27], a[72:75], v[172:175]
ds_read_b64_tr_b16 a[12:13], v14 offset:24584
v_mfma_f32_16x16x32_bf16 v[172:175], a[28:31], a[76:79], v[172:175]
ds_read_b64_tr_b16 a[14:15], v14 offset:28680
v_mfma_f32_16x16x32_bf16 v[204:207], a[24:27], a[80:83], v[204:207]
v_mfma_f32_16x16x32_bf16 v[204:207], a[28:31], a[84:87], v[204:207]
v_mfma_f32_16x16x32_bf16 v[236:239], a[24:27], a[88:91], v[236:239]
v_mfma_f32_16x16x32_bf16 v[236:239], a[28:31], a[92:95], v[236:239]
s_waitcnt lgkmcnt(4)
v_mfma_f32_16x16x32_bf16 v[144:147], a[0:3], a[64:67], v[144:147]
ds_read_b64_tr_b16 a[16:17], v15 offset:16384
v_mfma_f32_16x16x32_bf16 v[144:147], a[4:7], a[68:71], v[144:147]
ds_read_b64_tr_b16 a[18:19], v15 offset:20480
v_mfma_f32_16x16x32_bf16 v[176:179], a[0:3], a[72:75], v[176:179]
ds_read_b64_tr_b16 a[20:21], v15 offset:24576
v_mfma_f32_16x16x32_bf16 v[176:179], a[4:7], a[76:79], v[176:179]
ds_read_b64_tr_b16 a[22:23], v15 offset:28672
v_mfma_f32_16x16x32_bf16 v[208:211], a[0:3], a[80:83], v[208:211]
v_mfma_f32_16x16x32_bf16 v[208:211], a[4:7], a[84:87], v[208:211]
v_mfma_f32_16x16x32_bf16 v[240:243], a[0:3], a[88:91], v[240:243]
v_mfma_f32_16x16x32_bf16 v[240:243], a[4:7], a[92:95], v[240:243]
s_waitcnt lgkmcnt(4)
v_mfma_f32_16x16x32_bf16 v[148:151], a[8:11], a[64:67], v[148:151]
ds_read_b64_tr_b16 a[24:25], v15 offset:16392
v_mfma_f32_16x16x32_bf16 v[148:151], a[12:15], a[68:71], v[148:151]
ds_read_b64_tr_b16 a[26:27], v15 offset:20488
v_mfma_f32_16x16x32_bf16 v[180:183], a[8:11], a[72:75], v[180:183]
ds_read_b64_tr_b16 a[28:29], v15 offset:24584
v_mfma_f32_16x16x32_bf16 v[180:183], a[12:15], a[76:79], v[180:183]
ds_read_b64_tr_b16 a[30:31], v15 offset:28680
v_mfma_f32_16x16x32_bf16 v[212:215], a[8:11], a[80:83], v[212:215]
v_mfma_f32_16x16x32_bf16 v[212:215], a[12:15], a[84:87], v[212:215]
v_mfma_f32_16x16x32_bf16 v[244:247], a[8:11], a[88:91], v[244:247]
v_mfma_f32_16x16x32_bf16 v[244:247], a[12:15], a[92:95], v[244:247]
s_waitcnt lgkmcnt(4)
v_mfma_f32_16x16x32_bf16 v[152:155], a[16:19], a[64:67], v[152:155]
ds_read_b64_tr_b16 a[0:1], v16 offset:32768
v_mfma_f32_16x16x32_bf16 v[152:155], a[20:23], a[68:71], v[152:155]
ds_read_b64_tr_b16 a[2:3], v16 offset:36864
v_mfma_f32_16x16x32_bf16 v[184:187], a[16:19], a[72:75], v[184:187]
ds_read_b64_tr_b16 a[4:5], v16 offset:40960
v_mfma_f32_16x16x32_bf16 v[184:187], a[20:23], a[76:79], v[184:187]
ds_read_b64_tr_b16 a[6:7], v16 offset:45056
v_mfma_f32_16x16x32_bf16 v[216:219], a[16:19], a[80:83], v[216:219]
v_mfma_f32_16x16x32_bf16 v[216:219], a[20:23], a[84:87], v[216:219]
v_mfma_f32_16x16x32_bf16 v[248:251], a[16:19], a[88:91], v[248:251]
v_mfma_f32_16x16x32_bf16 v[248:251], a[20:23], a[92:95], v[248:251]
s_waitcnt lgkmcnt(4)
v_mfma_f32_16x16x32_bf16 v[156:159], a[24:27], a[64:67], v[156:159]
ds_read_b64_tr_b16 a[8:9], v17 offset:32768
v_mfma_f32_16x16x32_bf16 v[156:159], a[28:31], a[68:71], v[156:159]
ds_read_b64_tr_b16 a[10:11], v17 offset:36864
v_mfma_f32_16x16x32_bf16 v[188:191], a[24:27], a[72:75], v[188:191]
ds_read_b64_tr_b16 a[12:13], v17 offset:40960
v_mfma_f32_16x16x32_bf16 v[188:191], a[28:31], a[76:79], v[188:191]
ds_read_b64_tr_b16 a[14:15], v17 offset:45056
v_mfma_f32_16x16x32_bf16 v[220:223], a[24:27], a[80:83], v[220:223]
s_waitcnt vmcnt(28)
v_mfma_f32_16x16x32_bf16 v[220:223], a[28:31], a[84:87], v[220:223]
s_barrier
v_mfma_f32_16x16x32_bf16 v[252:255], a[24:27], a[88:91], v[252:255]
s_waitcnt vmcnt(16)
v_mfma_f32_16x16x32_bf16 v[252:255], a[28:31], a[92:95], v[252:255]
s_waitcnt lgkmcnt(4)
v_mfma_f32_16x16x32_bf16 v[64:67], a[0:3], a[96:99], v[64:67]
ds_read_b64_tr_b16 a[16:17], v18 offset:32768
v_mfma_f32_16x16x32_bf16 v[64:67], a[4:7], a[100:103], v[64:67]
ds_read_b64_tr_b16 a[18:19], v18 offset:36864
v_mfma_f32_16x16x32_bf16 v[80:83], a[0:3], a[104:107], v[80:83]
ds_read_b64_tr_b16 a[20:21], v18 offset:40960
v_mfma_f32_16x16x32_bf16 v[80:83], a[4:7], a[108:111], v[80:83]
ds_read_b64_tr_b16 a[22:23], v18 offset:45056
v_mfma_f32_16x16x32_bf16 v[96:99], a[0:3], a[112:115], v[96:99]
s_add_u32 m0, s68, 0x4000
s_add_u32 s79, s70, 0x6c00
buffer_load_dwordx4 v28, s[48:51], s70 offen offset:0 lds
v_mfma_f32_16x16x32_bf16 v[96:99], a[4:7], a[116:119], v[96:99]
v_mfma_f32_16x16x32_bf16 v[112:115], a[0:3], a[120:123], v[112:115]
buffer_load_dwordx4 v28, s[48:51], s79 offen offset:1024 lds
v_mfma_f32_16x16x32_bf16 v[112:115], a[4:7], a[124:127], v[112:115]
s_waitcnt lgkmcnt(4)
v_mfma_f32_16x16x32_bf16 v[68:71], a[8:11], a[96:99], v[68:71]
ds_read_b64_tr_b16 a[24:25], v19 offset:32768
v_mfma_f32_16x16x32_bf16 v[68:71], a[12:15], a[100:103], v[68:71]
ds_read_b64_tr_b16 a[26:27], v19 offset:36864
v_mfma_f32_16x16x32_bf16 v[84:87], a[8:11], a[104:107], v[84:87]
ds_read_b64_tr_b16 a[28:29], v19 offset:40960
v_mfma_f32_16x16x32_bf16 v[84:87], a[12:15], a[108:111], v[84:87]
ds_read_b64_tr_b16 a[30:31], v19 offset:45056
v_mfma_f32_16x16x32_bf16 v[100:103], a[8:11], a[112:115], v[100:103]
buffer_load_dwordx4 v28, s[52:55], s70 offen offset:2048 lds
v_mfma_f32_16x16x32_bf16 v[100:103], a[12:15], a[116:119], v[100:103]
v_mfma_f32_16x16x32_bf16 v[116:119], a[8:11], a[120:123], v[116:119]
buffer_load_dwordx4 v28, s[52:55], s79 offen offset:3072 lds
s_add_u32 s70, s70, 0x38000
v_mfma_f32_16x16x32_bf16 v[116:119], a[12:15], a[124:127], v[116:119]
s_waitcnt lgkmcnt(4)
v_mfma_f32_16x16x32_bf16 v[72:75], a[16:19], a[96:99], v[72:75]
ds_read_b64_tr_b16 a[0:1], v16 offset:34816
v_mfma_f32_16x16x32_bf16 v[72:75], a[20:23], a[100:103], v[72:75]
ds_read_b64_tr_b16 a[2:3], v16 offset:38912
v_mfma_f32_16x16x32_bf16 v[88:91], a[16:19], a[104:107], v[88:91]
ds_read_b64_tr_b16 a[4:5], v16 offset:43008
v_mfma_f32_16x16x32_bf16 v[88:91], a[20:23], a[108:111], v[88:91]
ds_read_b64_tr_b16 a[6:7], v16 offset:47104
v_mfma_f32_16x16x32_bf16 v[104:107], a[16:19], a[112:115], v[104:107]
s_add_u32 m0, s68, 0x14000
s_add_u32 s80, s69, 0x7c00
buffer_load_dwordx4 v26, s[44:47], s69 offen offset:0 lds
v_mfma_f32_16x16x32_bf16 v[104:107], a[20:23], a[116:119], v[104:107]
v_mfma_f32_16x16x32_bf16 v[120:123], a[16:19], a[120:123], v[120:123]
s_add_u32 s81, s69, 0xf800
buffer_load_dwordx4 v27, s[44:47], s80 offen offset:1024 lds
v_mfma_f32_16x16x32_bf16 v[120:123], a[20:23], a[124:127], v[120:123]
s_waitcnt lgkmcnt(4)
v_mfma_f32_16x16x32_bf16 v[76:79], a[24:27], a[96:99], v[76:79]
ds_read_b64_tr_b16 a[8:9], v17 offset:34816
v_mfma_f32_16x16x32_bf16 v[76:79], a[28:31], a[100:103], v[76:79]
ds_read_b64_tr_b16 a[10:11], v17 offset:38912
v_mfma_f32_16x16x32_bf16 v[92:95], a[24:27], a[104:107], v[92:95]
ds_read_b64_tr_b16 a[12:13], v17 offset:43008
v_mfma_f32_16x16x32_bf16 v[92:95], a[28:31], a[108:111], v[92:95]
ds_read_b64_tr_b16 a[14:15], v17 offset:47104
v_mfma_f32_16x16x32_bf16 v[108:111], a[24:27], a[112:115], v[108:111]
s_add_u32 s82, s69, 0x17400
buffer_load_dwordx4 v26, s[44:47], s81 offen offset:2048 lds
v_mfma_f32_16x16x32_bf16 v[108:111], a[28:31], a[116:119], v[108:111]
v_mfma_f32_16x16x32_bf16 v[124:127], a[24:27], a[120:123], v[124:127]
buffer_load_dwordx4 v27, s[44:47], s82 offen offset:3072 lds
s_add_u32 s69, s69, 0x80000
v_mfma_f32_16x16x32_bf16 v[124:127], a[28:31], a[124:127], v[124:127]
s_waitcnt lgkmcnt(4)
v_mfma_f32_16x16x32_bf16 a[192:195], a[0:3], a[96:99], a[192:195]
ds_read_b64_tr_b16 a[16:17], v18 offset:34816
v_mfma_f32_16x16x32_bf16 a[192:195], a[4:7], a[100:103], a[192:195]
ds_read_b64_tr_b16 a[18:19], v18 offset:38912
v_mfma_f32_16x16x32_bf16 a[208:211], a[0:3], a[104:107], a[208:211]
ds_read_b64_tr_b16 a[20:21], v18 offset:43008
v_mfma_f32_16x16x32_bf16 a[208:211], a[4:7], a[108:111], a[208:211]
ds_read_b64_tr_b16 a[22:23], v18 offset:47104
v_mfma_f32_16x16x32_bf16 a[224:227], a[0:3], a[112:115], a[224:227]
buffer_load_dwordx4 a[32:35], v29, s[56:59], s90 offen offset:0 sc1
v_mfma_f32_16x16x32_bf16 a[224:227], a[4:7], a[116:119], a[224:227]
v_mfma_f32_16x16x32_bf16 a[240:243], a[0:3], a[120:123], a[240:243]
buffer_load_dwordx4 a[36:39], v29, s[56:59], s90 offen offset:1024 sc1
v_mfma_f32_16x16x32_bf16 a[240:243], a[4:7], a[124:127], a[240:243]
s_waitcnt lgkmcnt(4)
v_mfma_f32_16x16x32_bf16 a[196:199], a[8:11], a[96:99], a[196:199]
ds_read_b64_tr_b16 a[24:25], v19 offset:34816
v_mfma_f32_16x16x32_bf16 a[196:199], a[12:15], a[100:103], a[196:199]
ds_read_b64_tr_b16 a[26:27], v19 offset:38912
v_mfma_f32_16x16x32_bf16 a[212:215], a[8:11], a[104:107], a[212:215]
ds_read_b64_tr_b16 a[28:29], v19 offset:43008
v_mfma_f32_16x16x32_bf16 a[212:215], a[12:15], a[108:111], a[212:215]
ds_read_b64_tr_b16 a[30:31], v19 offset:47104
v_mfma_f32_16x16x32_bf16 a[228:231], a[8:11], a[112:115], a[228:231]
buffer_load_dwordx4 a[40:43], v29, s[56:59], s91 offen offset:0 sc1
v_mfma_f32_16x16x32_bf16 a[228:231], a[12:15], a[116:119], a[228:231]
v_mfma_f32_16x16x32_bf16 a[244:247], a[8:11], a[120:123], a[244:247]
buffer_load_dwordx4 a[44:47], v29, s[56:59], s91 offen offset:1024 sc1
v_mfma_f32_16x16x32_bf16 a[244:247], a[12:15], a[124:127], a[244:247]
s_waitcnt lgkmcnt(4)
v_mfma_f32_16x16x32_bf16 a[200:203], a[16:19], a[96:99], a[200:203]
ds_read_b64_tr_b16 a[0:1], v12 offset:32768
v_mfma_f32_16x16x32_bf16 a[200:203], a[20:23], a[100:103], a[200:203]
ds_read_b64_tr_b16 a[2:3], v12 offset:36864
v_mfma_f32_16x16x32_bf16 a[216:219], a[16:19], a[104:107], a[216:219]
ds_read_b64_tr_b16 a[4:5], v12 offset:40960
v_mfma_f32_16x16x32_bf16 a[216:219], a[20:23], a[108:111], a[216:219]
ds_read_b64_tr_b16 a[6:7], v12 offset:45056
v_mfma_f32_16x16x32_bf16 a[232:235], a[16:19], a[112:115], a[232:235]
buffer_load_dwordx4 a[48:51], v29, s[56:59], s92 offen offset:0 sc1
v_mfma_f32_16x16x32_bf16 a[232:235], a[20:23], a[116:119], a[232:235]
v_mfma_f32_16x16x32_bf16 a[248:251], a[16:19], a[120:123], a[248:251]
buffer_load_dwordx4 a[52:55], v29, s[56:59], s92 offen offset:1024 sc1
v_mfma_f32_16x16x32_bf16 a[248:251], a[20:23], a[124:127], a[248:251]
s_waitcnt lgkmcnt(4)
v_mfma_f32_16x16x32_bf16 a[204:207], a[24:27], a[96:99], a[204:207]
ds_read_b64_tr_b16 a[8:9], v12 offset:32776
v_mfma_f32_16x16x32_bf16 a[204:207], a[28:31], a[100:103], a[204:207]
ds_read_b64_tr_b16 a[10:11], v12 offset:36872
v_mfma_f32_16x16x32_bf16 a[220:223], a[24:27], a[104:107], a[220:223]
ds_read_b64_tr_b16 a[12:13], v12 offset:40968
v_mfma_f32_16x16x32_bf16 a[220:223], a[28:31], a[108:111], a[220:223]
ds_read_b64_tr_b16 a[14:15], v12 offset:45064
v_mfma_f32_16x16x32_bf16 a[236:239], a[24:27], a[112:115], a[236:239]
buffer_load_dwordx4 a[56:59], v29, s[56:59], s93 offen offset:0 sc1
v_mfma_f32_16x16x32_bf16 a[236:239], a[28:31], a[116:119], a[236:239]
v_mfma_f32_16x16x32_bf16 a[252:255], a[24:27], a[120:123], a[252:255]
buffer_load_dwordx4 a[60:63], v29, s[56:59], s93 offen offset:1024 sc1
v_mfma_f32_16x16x32_bf16 a[252:255], a[28:31], a[124:127], a[252:255]
s_waitcnt lgkmcnt(4)
v_mfma_f32_16x16x32_bf16 v[128:131], a[0:3], a[96:99], v[128:131]
ds_read_b64_tr_b16 a[16:17], v13 offset:32768
v_mfma_f32_16x16x32_bf16 v[128:131], a[4:7], a[100:103], v[128:131]
ds_read_b64_tr_b16 a[18:19], v13 offset:36864
v_mfma_f32_16x16x32_bf16 v[160:163], a[0:3], a[104:107], v[160:163]
ds_read_b64_tr_b16 a[20:21], v13 offset:40960
v_mfma_f32_16x16x32_bf16 v[160:163], a[4:7], a[108:111], v[160:163]
ds_read_b64_tr_b16 a[22:23], v13 offset:45056
v_mfma_f32_16x16x32_bf16 v[192:195], a[0:3], a[112:115], v[192:195]
v_mfma_f32_16x16x32_bf16 v[192:195], a[4:7], a[116:119], v[192:195]
v_mfma_f32_16x16x32_bf16 v[224:227], a[0:3], a[120:123], v[224:227]
v_mfma_f32_16x16x32_bf16 v[224:227], a[4:7], a[124:127], v[224:227]
s_waitcnt lgkmcnt(4)
v_mfma_f32_16x16x32_bf16 v[132:135], a[8:11], a[96:99], v[132:135]
ds_read_b64_tr_b16 a[24:25], v13 offset:32776
v_mfma_f32_16x16x32_bf16 v[132:135], a[12:15], a[100:103], v[132:135]
ds_read_b64_tr_b16 a[26:27], v13 offset:36872
v_mfma_f32_16x16x32_bf16 v[164:167], a[8:11], a[104:107], v[164:167]
ds_read_b64_tr_b16 a[28:29], v13 offset:40968
v_mfma_f32_16x16x32_bf16 v[164:167], a[12:15], a[108:111], v[164:167]
ds_read_b64_tr_b16 a[30:31], v13 offset:45064
v_mfma_f32_16x16x32_bf16 v[196:199], a[8:11], a[112:115], v[196:199]
v_mfma_f32_16x16x32_bf16 v[196:199], a[12:15], a[116:119], v[196:199]
v_mfma_f32_16x16x32_bf16 v[228:231], a[8:11], a[120:123], v[228:231]
v_mfma_f32_16x16x32_bf16 v[228:231], a[12:15], a[124:127], v[228:231]
s_waitcnt lgkmcnt(4)
v_mfma_f32_16x16x32_bf16 v[136:139], a[16:19], a[96:99], v[136:139]
ds_read_b64_tr_b16 a[0:1], v14 offset:32768
v_mfma_f32_16x16x32_bf16 v[136:139], a[20:23], a[100:103], v[136:139]
ds_read_b64_tr_b16 a[2:3], v14 offset:36864
v_mfma_f32_16x16x32_bf16 v[168:171], a[16:19], a[104:107], v[168:171]
ds_read_b64_tr_b16 a[4:5], v14 offset:40960
v_mfma_f32_16x16x32_bf16 v[168:171], a[20:23], a[108:111], v[168:171]
ds_read_b64_tr_b16 a[6:7], v14 offset:45056
v_mfma_f32_16x16x32_bf16 v[200:203], a[16:19], a[112:115], v[200:203]
v_mfma_f32_16x16x32_bf16 v[200:203], a[20:23], a[116:119], v[200:203]
v_mfma_f32_16x16x32_bf16 v[232:235], a[16:19], a[120:123], v[232:235]
v_mfma_f32_16x16x32_bf16 v[232:235], a[20:23], a[124:127], v[232:235]
s_waitcnt lgkmcnt(4)
v_mfma_f32_16x16x32_bf16 v[140:143], a[24:27], a[96:99], v[140:143]
ds_read_b64_tr_b16 a[8:9], v14 offset:32776
v_mfma_f32_16x16x32_bf16 v[140:143], a[28:31], a[100:103], v[140:143]
ds_read_b64_tr_b16 a[10:11], v14 offset:36872
v_mfma_f32_16x16x32_bf16 v[172:175], a[24:27], a[104:107], v[172:175]
ds_read_b64_tr_b16 a[12:13], v14 offset:40968
v_mfma_f32_16x16x32_bf16 v[172:175], a[28:31], a[108:111], v[172:175]
ds_read_b64_tr_b16 a[14:15], v14 offset:45064
v_mfma_f32_16x16x32_bf16 v[204:207], a[24:27], a[112:115], v[204:207]
v_mfma_f32_16x16x32_bf16 v[204:207], a[28:31], a[116:119], v[204:207]
v_mfma_f32_16x16x32_bf16 v[236:239], a[24:27], a[120:123], v[236:239]
v_mfma_f32_16x16x32_bf16 v[236:239], a[28:31], a[124:127], v[236:239]
s_waitcnt lgkmcnt(4)
v_mfma_f32_16x16x32_bf16 v[144:147], a[0:3], a[96:99], v[144:147]
ds_read_b64_tr_b16 a[16:17], v15 offset:32768
v_mfma_f32_16x16x32_bf16 v[144:147], a[4:7], a[100:103], v[144:147]
ds_read_b64_tr_b16 a[18:19], v15 offset:36864
v_mfma_f32_16x16x32_bf16 v[176:179], a[0:3], a[104:107], v[176:179]
ds_read_b64_tr_b16 a[20:21], v15 offset:40960
v_mfma_f32_16x16x32_bf16 v[176:179], a[4:7], a[108:111], v[176:179]
ds_read_b64_tr_b16 a[22:23], v15 offset:45056
v_mfma_f32_16x16x32_bf16 v[208:211], a[0:3], a[112:115], v[208:211]
v_mfma_f32_16x16x32_bf16 v[208:211], a[4:7], a[116:119], v[208:211]
v_mfma_f32_16x16x32_bf16 v[240:243], a[0:3], a[120:123], v[240:243]
v_mfma_f32_16x16x32_bf16 v[240:243], a[4:7], a[124:127], v[240:243]
s_waitcnt lgkmcnt(4)
v_mfma_f32_16x16x32_bf16 v[148:151], a[8:11], a[96:99], v[148:151]
ds_read_b64_tr_b16 a[24:25], v15 offset:32776
v_mfma_f32_16x16x32_bf16 v[148:151], a[12:15], a[100:103], v[148:151]
ds_read_b64_tr_b16 a[26:27], v15 offset:36872
v_mfma_f32_16x16x32_bf16 v[180:183], a[8:11], a[104:107], v[180:183]
ds_read_b64_tr_b16 a[28:29], v15 offset:40968
v_mfma_f32_16x16x32_bf16 v[180:183], a[12:15], a[108:111], v[180:183]
ds_read_b64_tr_b16 a[30:31], v15 offset:45064
v_mfma_f32_16x16x32_bf16 v[212:215], a[8:11], a[112:115], v[212:215]
v_mfma_f32_16x16x32_bf16 v[212:215], a[12:15], a[116:119], v[212:215]
v_mfma_f32_16x16x32_bf16 v[244:247], a[8:11], a[120:123], v[244:247]
v_mfma_f32_16x16x32_bf16 v[244:247], a[12:15], a[124:127], v[244:247]
s_waitcnt lgkmcnt(4)
v_mfma_f32_16x16x32_bf16 v[152:155], a[16:19], a[96:99], v[152:155]
ds_read_b64_tr_b16 a[0:1], v16 offset:49152
v_mfma_f32_16x16x32_bf16 v[152:155], a[20:23], a[100:103], v[152:155]
ds_read_b64_tr_b16 a[2:3], v16 offset:53248
v_mfma_f32_16x16x32_bf16 v[184:187], a[16:19], a[104:107], v[184:187]
ds_read_b64_tr_b16 a[4:5], v16 offset:57344
v_mfma_f32_16x16x32_bf16 v[184:187], a[20:23], a[108:111], v[184:187]
ds_read_b64_tr_b16 a[6:7], v16 offset:61440
v_mfma_f32_16x16x32_bf16 v[216:219], a[16:19], a[112:115], v[216:219]
v_mfma_f32_16x16x32_bf16 v[216:219], a[20:23], a[116:119], v[216:219]
v_mfma_f32_16x16x32_bf16 v[248:251], a[16:19], a[120:123], v[248:251]
v_mfma_f32_16x16x32_bf16 v[248:251], a[20:23], a[124:127], v[248:251]
s_waitcnt lgkmcnt(4)
v_mfma_f32_16x16x32_bf16 v[156:159], a[24:27], a[96:99], v[156:159]
ds_read_b64_tr_b16 a[8:9], v17 offset:49152
v_mfma_f32_16x16x32_bf16 v[156:159], a[28:31], a[100:103], v[156:159]
ds_read_b64_tr_b16 a[10:11], v17 offset:53248
v_mfma_f32_16x16x32_bf16 v[188:191], a[24:27], a[104:107], v[188:191]
ds_read_b64_tr_b16 a[12:13], v17 offset:57344
v_mfma_f32_16x16x32_bf16 v[188:191], a[28:31], a[108:111], v[188:191]
ds_read_b64_tr_b16 a[14:15], v17 offset:61440
v_mfma_f32_16x16x32_bf16 v[220:223], a[24:27], a[112:115], v[220:223]
s_waitcnt vmcnt(24)
v_mfma_f32_16x16x32_bf16 v[220:223], a[28:31], a[116:119], v[220:223]
s_barrier
v_mfma_f32_16x16x32_bf16 v[252:255], a[24:27], a[120:123], v[252:255]
s_waitcnt vmcnt(16)
v_mfma_f32_16x16x32_bf16 v[252:255], a[28:31], a[124:127], v[252:255]
s_waitcnt lgkmcnt(4)
v_mfma_f32_16x16x32_bf16 v[64:67], a[0:3], a[128:131], v[64:67]
ds_read_b64_tr_b16 a[16:17], v18 offset:49152
v_mfma_f32_16x16x32_bf16 v[64:67], a[4:7], a[132:135], v[64:67]
ds_read_b64_tr_b16 a[18:19], v18 offset:53248
v_mfma_f32_16x16x32_bf16 v[80:83], a[0:3], a[136:139], v[80:83]
ds_read_b64_tr_b16 a[20:21], v18 offset:57344
v_mfma_f32_16x16x32_bf16 v[80:83], a[4:7], a[140:143], v[80:83]
ds_read_b64_tr_b16 a[22:23], v18 offset:61440
v_mfma_f32_16x16x32_bf16 v[96:99], a[0:3], a[144:147], v[96:99]
s_add_u32 m0, s68, 0x8000
s_add_u32 s79, s70, 0x6c00
buffer_load_dwordx4 v28, s[48:51], s70 offen offset:0 lds
v_mfma_f32_16x16x32_bf16 v[96:99], a[4:7], a[148:151], v[96:99]
v_mfma_f32_16x16x32_bf16 v[112:115], a[0:3], a[152:155], v[112:115]
buffer_load_dwordx4 v28, s[48:51], s79 offen offset:1024 lds
v_mfma_f32_16x16x32_bf16 v[112:115], a[4:7], a[156:159], v[112:115]
s_waitcnt lgkmcnt(4)
v_mfma_f32_16x16x32_bf16 v[68:71], a[8:11], a[128:131], v[68:71]
ds_read_b64_tr_b16 a[24:25], v19 offset:49152
v_mfma_f32_16x16x32_bf16 v[68:71], a[12:15], a[132:135], v[68:71]
ds_read_b64_tr_b16 a[26:27], v19 offset:53248
v_mfma_f32_16x16x32_bf16 v[84:87], a[8:11], a[136:139], v[84:87]
ds_read_b64_tr_b16 a[28:29], v19 offset:57344
v_mfma_f32_16x16x32_bf16 v[84:87], a[12:15], a[140:143], v[84:87]
ds_read_b64_tr_b16 a[30:31], v19 offset:61440
v_mfma_f32_16x16x32_bf16 v[100:103], a[8:11], a[144:147], v[100:103]
buffer_load_dwordx4 v28, s[52:55], s70 offen offset:2048 lds
v_mfma_f32_16x16x32_bf16 v[100:103], a[12:15], a[148:151], v[100:103]
v_mfma_f32_16x16x32_bf16 v[116:119], a[8:11], a[152:155], v[116:119]
buffer_load_dwordx4 v28, s[52:55], s79 offen offset:3072 lds
s_add_u32 s70, s70, 0x38000
v_mfma_f32_16x16x32_bf16 v[116:119], a[12:15], a[156:159], v[116:119]
s_waitcnt lgkmcnt(4)
v_mfma_f32_16x16x32_bf16 v[72:75], a[16:19], a[128:131], v[72:75]
ds_read_b64_tr_b16 a[0:1], v16 offset:51200
v_mfma_f32_16x16x32_bf16 v[72:75], a[20:23], a[132:135], v[72:75]
ds_read_b64_tr_b16 a[2:3], v16 offset:55296
v_mfma_f32_16x16x32_bf16 v[88:91], a[16:19], a[136:139], v[88:91]
ds_read_b64_tr_b16 a[4:5], v16 offset:59392
v_mfma_f32_16x16x32_bf16 v[88:91], a[20:23], a[140:143], v[88:91]
ds_read_b64_tr_b16 a[6:7], v16 offset:63488
v_mfma_f32_16x16x32_bf16 v[104:107], a[16:19], a[144:147], v[104:107]
s_add_u32 m0, s68, 0x18000
s_add_u32 s80, s69, 0x7c00
buffer_load_dwordx4 v26, s[44:47], s69 offen offset:0 lds
v_mfma_f32_16x16x32_bf16 v[104:107], a[20:23], a[148:151], v[104:107]
v_mfma_f32_16x16x32_bf16 v[120:123], a[16:19], a[152:155], v[120:123]
s_add_u32 s81, s69, 0xf800
buffer_load_dwordx4 v27, s[44:47], s80 offen offset:1024 lds
v_mfma_f32_16x16x32_bf16 v[120:123], a[20:23], a[156:159], v[120:123]
s_waitcnt lgkmcnt(4)
v_mfma_f32_16x16x32_bf16 v[76:79], a[24:27], a[128:131], v[76:79]
ds_read_b64_tr_b16 a[8:9], v17 offset:51200
v_mfma_f32_16x16x32_bf16 v[76:79], a[28:31], a[132:135], v[76:79]
ds_read_b64_tr_b16 a[10:11], v17 offset:55296
v_mfma_f32_16x16x32_bf16 v[92:95], a[24:27], a[136:139], v[92:95]
ds_read_b64_tr_b16 a[12:13], v17 offset:59392
v_mfma_f32_16x16x32_bf16 v[92:95], a[28:31], a[140:143], v[92:95]
ds_read_b64_tr_b16 a[14:15], v17 offset:63488
v_mfma_f32_16x16x32_bf16 v[108:111], a[24:27], a[144:147], v[108:111]
s_add_u32 s82, s69, 0x17400
buffer_load_dwordx4 v26, s[44:47], s81 offen offset:2048 lds
v_mfma_f32_16x16x32_bf16 v[108:111], a[28:31], a[148:151], v[108:111]
v_mfma_f32_16x16x32_bf16 v[124:127], a[24:27], a[152:155], v[124:127]
buffer_load_dwordx4 v27, s[44:47], s82 offen offset:3072 lds
s_add_u32 s69, s69, 0x80000
v_mfma_f32_16x16x32_bf16 v[124:127], a[28:31], a[156:159], v[124:127]
s_waitcnt lgkmcnt(4)
v_mfma_f32_16x16x32_bf16 a[192:195], a[0:3], a[128:131], a[192:195]
ds_read_b64_tr_b16 a[16:17], v18 offset:51200
v_mfma_f32_16x16x32_bf16 a[192:195], a[4:7], a[132:135], a[192:195]
ds_read_b64_tr_b16 a[18:19], v18 offset:55296
v_mfma_f32_16x16x32_bf16 a[208:211], a[0:3], a[136:139], a[208:211]
ds_read_b64_tr_b16 a[20:21], v18 offset:59392
v_mfma_f32_16x16x32_bf16 a[208:211], a[4:7], a[140:143], a[208:211]
ds_read_b64_tr_b16 a[22:23], v18 offset:63488
v_mfma_f32_16x16x32_bf16 a[224:227], a[0:3], a[144:147], a[224:227]
buffer_load_dwordx4 a[64:67], v29, s[56:59], s90 offen offset:2048 sc1
v_mfma_f32_16x16x32_bf16 a[224:227], a[4:7], a[148:151], a[224:227]
v_mfma_f32_16x16x32_bf16 a[240:243], a[0:3], a[152:155], a[240:243]
buffer_load_dwordx4 a[68:71], v29, s[56:59], s90 offen offset:3072 sc1
s_add_u32 s90, s90, 0x1000
v_mfma_f32_16x16x32_bf16 a[240:243], a[4:7], a[156:159], a[240:243]
s_waitcnt lgkmcnt(4)
v_mfma_f32_16x16x32_bf16 a[196:199], a[8:11], a[128:131], a[196:199]
ds_read_b64_tr_b16 a[24:25], v19 offset:51200
v_mfma_f32_16x16x32_bf16 a[196:199], a[12:15], a[132:135], a[196:199]
ds_read_b64_tr_b16 a[26:27], v19 offset:55296
v_mfma_f32_16x16x32_bf16 a[212:215], a[8:11], a[136:139], a[212:215]
ds_read_b64_tr_b16 a[28:29], v19 offset:59392
v_mfma_f32_16x16x32_bf16 a[212:215], a[12:15], a[140:143], a[212:215]
ds_read_b64_tr_b16 a[30:31], v19 offset:63488
v_mfma_f32_16x16x32_bf16 a[228:231], a[8:11], a[144:147], a[228:231]
buffer_load_dwordx4 a[72:75], v29, s[56:59], s91 offen offset:2048 sc1
v_mfma_f32_16x16x32_bf16 a[228:231], a[12:15], a[148:151], a[228:231]
v_mfma_f32_16x16x32_bf16 a[244:247], a[8:11], a[152:155], a[244:247]
buffer_load_dwordx4 a[76:79], v29, s[56:59], s91 offen offset:3072 sc1
s_add_u32 s91, s91, 0x1000
v_mfma_f32_16x16x32_bf16 a[244:247], a[12:15], a[156:159], a[244:247]
s_waitcnt lgkmcnt(4)
v_mfma_f32_16x16x32_bf16 a[200:203], a[16:19], a[128:131], a[200:203]
ds_read_b64_tr_b16 a[0:1], v12 offset:49152
v_mfma_f32_16x16x32_bf16 a[200:203], a[20:23], a[132:135], a[200:203]
ds_read_b64_tr_b16 a[2:3], v12 offset:53248
v_mfma_f32_16x16x32_bf16 a[216:219], a[16:19], a[136:139], a[216:219]
ds_read_b64_tr_b16 a[4:5], v12 offset:57344
v_mfma_f32_16x16x32_bf16 a[216:219], a[20:23], a[140:143], a[216:219]
ds_read_b64_tr_b16 a[6:7], v12 offset:61440
v_mfma_f32_16x16x32_bf16 a[232:235], a[16:19], a[144:147], a[232:235]
buffer_load_dwordx4 a[80:83], v29, s[56:59], s92 offen offset:2048 sc1
v_mfma_f32_16x16x32_bf16 a[232:235], a[20:23], a[148:151], a[232:235]
v_mfma_f32_16x16x32_bf16 a[248:251], a[16:19], a[152:155], a[248:251]
buffer_load_dwordx4 a[84:87], v29, s[56:59], s92 offen offset:3072 sc1
s_add_u32 s92, s92, 0x1000
v_mfma_f32_16x16x32_bf16 a[248:251], a[20:23], a[156:159], a[248:251]
s_waitcnt lgkmcnt(4)
v_mfma_f32_16x16x32_bf16 a[204:207], a[24:27], a[128:131], a[204:207]
ds_read_b64_tr_b16 a[8:9], v12 offset:49160
v_mfma_f32_16x16x32_bf16 a[204:207], a[28:31], a[132:135], a[204:207]
ds_read_b64_tr_b16 a[10:11], v12 offset:53256
v_mfma_f32_16x16x32_bf16 a[220:223], a[24:27], a[136:139], a[220:223]
ds_read_b64_tr_b16 a[12:13], v12 offset:57352
v_mfma_f32_16x16x32_bf16 a[220:223], a[28:31], a[140:143], a[220:223]
ds_read_b64_tr_b16 a[14:15], v12 offset:61448
v_mfma_f32_16x16x32_bf16 a[236:239], a[24:27], a[144:147], a[236:239]
buffer_load_dwordx4 a[88:91], v29, s[56:59], s93 offen offset:2048 sc1
v_mfma_f32_16x16x32_bf16 a[236:239], a[28:31], a[148:151], a[236:239]
v_mfma_f32_16x16x32_bf16 a[252:255], a[24:27], a[152:155], a[252:255]
buffer_load_dwordx4 a[92:95], v29, s[56:59], s93 offen offset:3072 sc1
s_add_u32 s93, s93, 0x1000
v_mfma_f32_16x16x32_bf16 a[252:255], a[28:31], a[156:159], a[252:255]
s_waitcnt lgkmcnt(4)
v_mfma_f32_16x16x32_bf16 v[128:131], a[0:3], a[128:131], v[128:131]
ds_read_b64_tr_b16 a[16:17], v13 offset:49152
v_mfma_f32_16x16x32_bf16 v[128:131], a[4:7], a[132:135], v[128:131]
ds_read_b64_tr_b16 a[18:19], v13 offset:53248
v_mfma_f32_16x16x32_bf16 v[160:163], a[0:3], a[136:139], v[160:163]
ds_read_b64_tr_b16 a[20:21], v13 offset:57344
v_mfma_f32_16x16x32_bf16 v[160:163], a[4:7], a[140:143], v[160:163]
ds_read_b64_tr_b16 a[22:23], v13 offset:61440
v_mfma_f32_16x16x32_bf16 v[192:195], a[0:3], a[144:147], v[192:195]
buffer_load_dwordx4 a[96:99], v29, s[56:59], s90 offen offset:0 sc1
v_mfma_f32_16x16x32_bf16 v[192:195], a[4:7], a[148:151], v[192:195]
v_mfma_f32_16x16x32_bf16 v[224:227], a[0:3], a[152:155], v[224:227]
buffer_load_dwordx4 a[100:103], v29, s[56:59], s90 offen offset:1024 sc1
v_mfma_f32_16x16x32_bf16 v[224:227], a[4:7], a[156:159], v[224:227]
s_waitcnt lgkmcnt(4)
v_mfma_f32_16x16x32_bf16 v[132:135], a[8:11], a[128:131], v[132:135]
ds_read_b64_tr_b16 a[24:25], v13 offset:49160
v_mfma_f32_16x16x32_bf16 v[132:135], a[12:15], a[132:135], v[132:135]
ds_read_b64_tr_b16 a[26:27], v13 offset:53256
v_mfma_f32_16x16x32_bf16 v[164:167], a[8:11], a[136:139], v[164:167]
ds_read_b64_tr_b16 a[28:29], v13 offset:57352
v_mfma_f32_16x16x32_bf16 v[164:167], a[12:15], a[140:143], v[164:167]
ds_read_b64_tr_b16 a[30:31], v13 offset:61448
v_mfma_f32_16x16x32_bf16 v[196:199], a[8:11], a[144:147], v[196:199]
buffer_load_dwordx4 a[104:107], v29, s[56:59], s91 offen offset:0 sc1
v_mfma_f32_16x16x32_bf16 v[196:199], a[12:15], a[148:151], v[196:199]
v_mfma_f32_16x16x32_bf16 v[228:231], a[8:11], a[152:155], v[228:231]
buffer_load_dwordx4 a[108:111], v29, s[56:59], s91 offen offset:1024 sc1
v_mfma_f32_16x16x32_bf16 v[228:231], a[12:15], a[156:159], v[228:231]
s_waitcnt lgkmcnt(4)
v_mfma_f32_16x16x32_bf16 v[136:139], a[16:19], a[128:131], v[136:139]
ds_read_b64_tr_b16 a[0:1], v14 offset:49152
v_mfma_f32_16x16x32_bf16 v[136:139], a[20:23], a[132:135], v[136:139]
ds_read_b64_tr_b16 a[2:3], v14 offset:53248
v_mfma_f32_16x16x32_bf16 v[168:171], a[16:19], a[136:139], v[168:171]
ds_read_b64_tr_b16 a[4:5], v14 offset:57344
v_mfma_f32_16x16x32_bf16 v[168:171], a[20:23], a[140:143], v[168:171]
ds_read_b64_tr_b16 a[6:7], v14 offset:61440
v_mfma_f32_16x16x32_bf16 v[200:203], a[16:19], a[144:147], v[200:203]
buffer_load_dwordx4 a[112:115], v29, s[56:59], s92 offen offset:0 sc1
v_mfma_f32_16x16x32_bf16 v[200:203], a[20:23], a[148:151], v[200:203]
v_mfma_f32_16x16x32_bf16 v[232:235], a[16:19], a[152:155], v[232:235]
buffer_load_dwordx4 a[116:119], v29, s[56:59], s92 offen offset:1024 sc1
v_mfma_f32_16x16x32_bf16 v[232:235], a[20:23], a[156:159], v[232:235]
s_waitcnt lgkmcnt(4)
v_mfma_f32_16x16x32_bf16 v[140:143], a[24:27], a[128:131], v[140:143]
ds_read_b64_tr_b16 a[8:9], v14 offset:49160
v_mfma_f32_16x16x32_bf16 v[140:143], a[28:31], a[132:135], v[140:143]
ds_read_b64_tr_b16 a[10:11], v14 offset:53256
v_mfma_f32_16x16x32_bf16 v[172:175], a[24:27], a[136:139], v[172:175]
ds_read_b64_tr_b16 a[12:13], v14 offset:57352
v_mfma_f32_16x16x32_bf16 v[172:175], a[28:31], a[140:143], v[172:175]
ds_read_b64_tr_b16 a[14:15], v14 offset:61448
v_mfma_f32_16x16x32_bf16 v[204:207], a[24:27], a[144:147], v[204:207]
buffer_load_dwordx4 a[120:123], v29, s[56:59], s93 offen offset:0 sc1
v_mfma_f32_16x16x32_bf16 v[204:207], a[28:31], a[148:151], v[204:207]
v_mfma_f32_16x16x32_bf16 v[236:239], a[24:27], a[152:155], v[236:239]
buffer_load_dwordx4 a[124:127], v29, s[56:59], s93 offen offset:1024 sc1
v_mfma_f32_16x16x32_bf16 v[236:239], a[28:31], a[156:159], v[236:239]
s_waitcnt lgkmcnt(4)
v_mfma_f32_16x16x32_bf16 v[144:147], a[0:3], a[128:131], v[144:147]
ds_read_b64_tr_b16 a[16:17], v15 offset:49152
v_mfma_f32_16x16x32_bf16 v[144:147], a[4:7], a[132:135], v[144:147]
ds_read_b64_tr_b16 a[18:19], v15 offset:53248
v_mfma_f32_16x16x32_bf16 v[176:179], a[0:3], a[136:139], v[176:179]
ds_read_b64_tr_b16 a[20:21], v15 offset:57344
v_mfma_f32_16x16x32_bf16 v[176:179], a[4:7], a[140:143], v[176:179]
ds_read_b64_tr_b16 a[22:23], v15 offset:61440
v_mfma_f32_16x16x32_bf16 v[208:211], a[0:3], a[144:147], v[208:211]
v_mfma_f32_16x16x32_bf16 v[208:211], a[4:7], a[148:151], v[208:211]
v_mfma_f32_16x16x32_bf16 v[240:243], a[0:3], a[152:155], v[240:243]
v_mfma_f32_16x16x32_bf16 v[240:243], a[4:7], a[156:159], v[240:243]
s_waitcnt lgkmcnt(4)
v_mfma_f32_16x16x32_bf16 v[148:151], a[8:11], a[128:131], v[148:151]
ds_read_b64_tr_b16 a[24:25], v15 offset:49160
v_mfma_f32_16x16x32_bf16 v[148:151], a[12:15], a[132:135], v[148:151]
ds_read_b64_tr_b16 a[26:27], v15 offset:53256
v_mfma_f32_16x16x32_bf16 v[180:183], a[8:11], a[136:139], v[180:183]
ds_read_b64_tr_b16 a[28:29], v15 offset:57352
v_mfma_f32_16x16x32_bf16 v[180:183], a[12:15], a[140:143], v[180:183]
ds_read_b64_tr_b16 a[30:31], v15 offset:61448
v_mfma_f32_16x16x32_bf16 v[212:215], a[8:11], a[144:147], v[212:215]
v_mfma_f32_16x16x32_bf16 v[212:215], a[12:15], a[148:151], v[212:215]
v_mfma_f32_16x16x32_bf16 v[244:247], a[8:11], a[152:155], v[244:247]
v_mfma_f32_16x16x32_bf16 v[244:247], a[12:15], a[156:159], v[244:247]
s_waitcnt lgkmcnt(4)
v_mfma_f32_16x16x32_bf16 v[152:155], a[16:19], a[128:131], v[152:155]
ds_read_b64_tr_b16 a[0:1], v16 offset:0
v_mfma_f32_16x16x32_bf16 v[152:155], a[20:23], a[132:135], v[152:155]
ds_read_b64_tr_b16 a[2:3], v16 offset:4096
v_mfma_f32_16x16x32_bf16 v[184:187], a[16:19], a[136:139], v[184:187]
ds_read_b64_tr_b16 a[4:5], v16 offset:8192
v_mfma_f32_16x16x32_bf16 v[184:187], a[20:23], a[140:143], v[184:187]
ds_read_b64_tr_b16 a[6:7], v16 offset:12288
v_mfma_f32_16x16x32_bf16 v[216:219], a[16:19], a[144:147], v[216:219]
v_mfma_f32_16x16x32_bf16 v[216:219], a[20:23], a[148:151], v[216:219]
v_mfma_f32_16x16x32_bf16 v[248:251], a[16:19], a[152:155], v[248:251]
v_mfma_f32_16x16x32_bf16 v[248:251], a[20:23], a[156:159], v[248:251]
s_waitcnt lgkmcnt(4)
v_mfma_f32_16x16x32_bf16 v[156:159], a[24:27], a[128:131], v[156:159]
ds_read_b64_tr_b16 a[8:9], v17 offset:0
v_mfma_f32_16x16x32_bf16 v[156:159], a[28:31], a[132:135], v[156:159]
ds_read_b64_tr_b16 a[10:11], v17 offset:4096
v_mfma_f32_16x16x32_bf16 v[188:191], a[24:27], a[136:139], v[188:191]
ds_read_b64_tr_b16 a[12:13], v17 offset:8192
v_mfma_f32_16x16x32_bf16 v[188:191], a[28:31], a[140:143], v[188:191]
ds_read_b64_tr_b16 a[14:15], v17 offset:12288
v_mfma_f32_16x16x32_bf16 v[220:223], a[24:27], a[144:147], v[220:223]
s_waitcnt vmcnt(32)
v_mfma_f32_16x16x32_bf16 v[220:223], a[28:31], a[148:151], v[220:223]
s_barrier
v_mfma_f32_16x16x32_bf16 v[252:255], a[24:27], a[152:155], v[252:255]
s_waitcnt vmcnt(24)
v_mfma_f32_16x16x32_bf16 v[252:255], a[28:31], a[156:159], v[252:255]
s_waitcnt lgkmcnt(4)
v_mfma_f32_16x16x32_bf16 v[64:67], a[0:3], a[32:35], v[64:67]
ds_read_b64_tr_b16 a[16:17], v18 offset:0
v_mfma_f32_16x16x32_bf16 v[64:67], a[4:7], a[36:39], v[64:67]
ds_read_b64_tr_b16 a[18:19], v18 offset:4096
v_mfma_f32_16x16x32_bf16 v[80:83], a[0:3], a[40:43], v[80:83]
ds_read_b64_tr_b16 a[20:21], v18 offset:8192
v_mfma_f32_16x16x32_bf16 v[80:83], a[4:7], a[44:47], v[80:83]
ds_read_b64_tr_b16 a[22:23], v18 offset:12288
v_mfma_f32_16x16x32_bf16 v[96:99], a[0:3], a[48:51], v[96:99]
s_add_u32 m0, s68, 0xc000
s_add_u32 s79, s70, 0x6c00
buffer_load_dwordx4 v28, s[48:51], s70 offen offset:0 lds
v_mfma_f32_16x16x32_bf16 v[96:99], a[4:7], a[52:55], v[96:99]
buffer_load_dwordx4 v28, s[48:51], s79 offen offset:1024 lds
v_mfma_f32_16x16x32_bf16 v[112:115], a[0:3], a[56:59], v[112:115]
buffer_load_dwordx4 v28, s[52:55], s70 offen offset:2048 lds
v_mfma_f32_16x16x32_bf16 v[112:115], a[4:7], a[60:63], v[112:115]
s_waitcnt lgkmcnt(4)
v_mfma_f32_16x16x32_bf16 v[68:71], a[8:11], a[32:35], v[68:71]
ds_read_b64_tr_b16 a[24:25], v19 offset:0
v_mfma_f32_16x16x32_bf16 v[68:71], a[12:15], a[36:39], v[68:71]
ds_read_b64_tr_b16 a[26:27], v19 offset:4096
v_mfma_f32_16x16x32_bf16 v[84:87], a[8:11], a[40:43], v[84:87]
ds_read_b64_tr_b16 a[28:29], v19 offset:8192
v_mfma_f32_16x16x32_bf16 v[84:87], a[12:15], a[44:47], v[84:87]
ds_read_b64_tr_b16 a[30:31], v19 offset:12288
v_mfma_f32_16x16x32_bf16 v[100:103], a[8:11], a[48:51], v[100:103]
buffer_load_dwordx4 v28, s[52:55], s79 offen offset:3072 lds
s_add_u32 s70, s70, 0x38000
v_mfma_f32_16x16x32_bf16 v[100:103], a[12:15], a[52:55], v[100:103]
s_add_u32 m0, s68, 0x1c000
s_add_u32 s80, s69, 0x7c00
buffer_load_dwordx4 v26, s[44:47], s69 offen offset:0 lds
v_mfma_f32_16x16x32_bf16 v[116:119], a[8:11], a[56:59], v[116:119]
s_add_u32 s81, s69, 0xf800
buffer_load_dwordx4 v27, s[44:47], s80 offen offset:1024 lds
v_mfma_f32_16x16x32_bf16 v[116:119], a[12:15], a[60:63], v[116:119]
s_waitcnt lgkmcnt(4)
v_mfma_f32_16x16x32_bf16 v[72:75], a[16:19], a[32:35], v[72:75]
ds_read_b64_tr_b16 a[0:1], v16 offset:2048
v_mfma_f32_16x16x32_bf16 v[72:75], a[20:23], a[36:39], v[72:75]
ds_read_b64_tr_b16 a[2:3], v16 offset:6144
v_mfma_f32_16x16x32_bf16 v[88:91], a[16:19], a[40:43], v[88:91]
ds_read_b64_tr_b16 a[4:5], v16 offset:10240
v_mfma_f32_16x16x32_bf16 v[88:91], a[20:23], a[44:47], v[88:91]
ds_read_b64_tr_b16 a[6:7], v16 offset:14336
v_mfma_f32_16x16x32_bf16 v[104:107], a[16:19], a[48:51], v[104:107]
s_add_u32 s82, s69, 0x17400
buffer_load_dwordx4 v26, s[44:47], s81 offen offset:2048 lds
v_mfma_f32_16x16x32_bf16 v[104:107], a[20:23], a[52:55], v[104:107]
v_mfma_f32_16x16x32_bf16 v[120:123], a[16:19], a[56:59], v[120:123]
buffer_load_dwordx4 v27, s[44:47], s82 offen offset:3072 lds
s_add_u32 s69, s69, 0x80000
v_mfma_f32_16x16x32_bf16 v[120:123], a[20:23], a[60:63], v[120:123]
s_waitcnt lgkmcnt(4)
v_mfma_f32_16x16x32_bf16 v[76:79], a[24:27], a[32:35], v[76:79]
ds_read_b64_tr_b16 a[8:9], v17 offset:2048
v_mfma_f32_16x16x32_bf16 v[76:79], a[28:31], a[36:39], v[76:79]
ds_read_b64_tr_b16 a[10:11], v17 offset:6144
v_mfma_f32_16x16x32_bf16 v[92:95], a[24:27], a[40:43], v[92:95]
ds_read_b64_tr_b16 a[12:13], v17 offset:10240
v_mfma_f32_16x16x32_bf16 v[92:95], a[28:31], a[44:47], v[92:95]
ds_read_b64_tr_b16 a[14:15], v17 offset:14336
v_mfma_f32_16x16x32_bf16 v[108:111], a[24:27], a[48:51], v[108:111]
buffer_load_dwordx4 a[128:131], v29, s[56:59], s90 offen offset:2048 sc1
v_mfma_f32_16x16x32_bf16 v[108:111], a[28:31], a[52:55], v[108:111]
v_mfma_f32_16x16x32_bf16 v[124:127], a[24:27], a[56:59], v[124:127]
buffer_load_dwordx4 a[132:135], v29, s[56:59], s90 offen offset:3072 sc1
s_add_u32 s90, s90, 0x1000
v_mfma_f32_16x16x32_bf16 v[124:127], a[28:31], a[60:63], v[124:127]
s_waitcnt lgkmcnt(4)
v_mfma_f32_16x16x32_bf16 a[192:195], a[0:3], a[32:35], a[192:195]
ds_read_b64_tr_b16 a[16:17], v18 offset:2048
v_mfma_f32_16x16x32_bf16 a[192:195], a[4:7], a[36:39], a[192:195]
ds_read_b64_tr_b16 a[18:19], v18 offset:6144
v_mfma_f32_16x16x32_bf16 a[208:211], a[0:3], a[40:43], a[208:211]
ds_read_b64_tr_b16 a[20:21], v18 offset:10240
v_mfma_f32_16x16x32_bf16 a[208:211], a[4:7], a[44:47], a[208:211]
ds_read_b64_tr_b16 a[22:23], v18 offset:14336
v_mfma_f32_16x16x32_bf16 a[224:227], a[0:3], a[48:51], a[224:227]
buffer_load_dwordx4 a[136:139], v29, s[56:59], s91 offen offset:2048 sc1
v_mfma_f32_16x16x32_bf16 a[224:227], a[4:7], a[52:55], a[224:227]
v_mfma_f32_16x16x32_bf16 a[240:243], a[0:3], a[56:59], a[240:243]
buffer_load_dwordx4 a[140:143], v29, s[56:59], s91 offen offset:3072 sc1
s_add_u32 s91, s91, 0x1000
v_mfma_f32_16x16x32_bf16 a[240:243], a[4:7], a[60:63], a[240:243]
s_waitcnt lgkmcnt(4)
v_mfma_f32_16x16x32_bf16 a[196:199], a[8:11], a[32:35], a[196:199]
ds_read_b64_tr_b16 a[24:25], v19 offset:2048
v_mfma_f32_16x16x32_bf16 a[196:199], a[12:15], a[36:39], a[196:199]
ds_read_b64_tr_b16 a[26:27], v19 offset:6144
v_mfma_f32_16x16x32_bf16 a[212:215], a[8:11], a[40:43], a[212:215]
ds_read_b64_tr_b16 a[28:29], v19 offset:10240
v_mfma_f32_16x16x32_bf16 a[212:215], a[12:15], a[44:47], a[212:215]
ds_read_b64_tr_b16 a[30:31], v19 offset:14336
v_mfma_f32_16x16x32_bf16 a[228:231], a[8:11], a[48:51], a[228:231]
buffer_load_dwordx4 a[144:147], v29, s[56:59], s92 offen offset:2048 sc1
v_mfma_f32_16x16x32_bf16 a[228:231], a[12:15], a[52:55], a[228:231]
v_mfma_f32_16x16x32_bf16 a[244:247], a[8:11], a[56:59], a[244:247]
buffer_load_dwordx4 a[148:151], v29, s[56:59], s92 offen offset:3072 sc1
s_add_u32 s92, s92, 0x1000
v_mfma_f32_16x16x32_bf16 a[244:247], a[12:15], a[60:63], a[244:247]
s_waitcnt lgkmcnt(4)
v_mfma_f32_16x16x32_bf16 a[200:203], a[16:19], a[32:35], a[200:203]
ds_read_b64_tr_b16 a[0:1], v16 offset:16384
v_mfma_f32_16x16x32_bf16 a[200:203], a[20:23], a[36:39], a[200:203]
ds_read_b64_tr_b16 a[2:3], v16 offset:20480
v_mfma_f32_16x16x32_bf16 a[216:219], a[16:19], a[40:43], a[216:219]
ds_read_b64_tr_b16 a[4:5], v16 offset:24576
v_mfma_f32_16x16x32_bf16 a[216:219], a[20:23], a[44:47], a[216:219]
ds_read_b64_tr_b16 a[6:7], v16 offset:28672
v_mfma_f32_16x16x32_bf16 a[232:235], a[16:19], a[48:51], a[232:235]
buffer_load_dwordx4 a[152:155], v29, s[56:59], s93 offen offset:2048 sc1
v_mfma_f32_16x16x32_bf16 a[232:235], a[20:23], a[52:55], a[232:235]
v_mfma_f32_16x16x32_bf16 a[248:251], a[16:19], a[56:59], a[248:251]
buffer_load_dwordx4 a[156:159], v29, s[56:59], s93 offen offset:3072 sc1
s_add_u32 s93, s93, 0x1000
v_mfma_f32_16x16x32_bf16 a[248:251], a[20:23], a[60:63], a[248:251]
s_waitcnt lgkmcnt(4)
v_mfma_f32_16x16x32_bf16 a[204:207], a[24:27], a[32:35], a[204:207]
ds_read_b64_tr_b16 a[8:9], v17 offset:16384
v_mfma_f32_16x16x32_bf16 a[204:207], a[28:31], a[36:39], a[204:207]
ds_read_b64_tr_b16 a[10:11], v17 offset:20480
v_mfma_f32_16x16x32_bf16 a[220:223], a[24:27], a[40:43], a[220:223]
ds_read_b64_tr_b16 a[12:13], v17 offset:24576
v_mfma_f32_16x16x32_bf16 a[220:223], a[28:31], a[44:47], a[220:223]
ds_read_b64_tr_b16 a[14:15], v17 offset:28672
v_mfma_f32_16x16x32_bf16 a[236:239], a[24:27], a[48:51], a[236:239]
s_waitcnt vmcnt(32)
v_mfma_f32_16x16x32_bf16 a[236:239], a[28:31], a[52:55], a[236:239]
s_barrier
v_mfma_f32_16x16x32_bf16 a[252:255], a[24:27], a[56:59], a[252:255]
s_waitcnt vmcnt(24)
v_mfma_f32_16x16x32_bf16 a[252:255], a[28:31], a[60:63], a[252:255]
s_waitcnt lgkmcnt(4)
v_mfma_f32_16x16x32_bf16 v[64:67], a[0:3], a[64:67], v[64:67]
ds_read_b64_tr_b16 a[16:17], v18 offset:16384
v_mfma_f32_16x16x32_bf16 v[64:67], a[4:7], a[68:71], v[64:67]
ds_read_b64_tr_b16 a[18:19], v18 offset:20480
v_mfma_f32_16x16x32_bf16 v[80:83], a[0:3], a[72:75], v[80:83]
ds_read_b64_tr_b16 a[20:21], v18 offset:24576
v_mfma_f32_16x16x32_bf16 v[80:83], a[4:7], a[76:79], v[80:83]
ds_read_b64_tr_b16 a[22:23], v18 offset:28672
v_mfma_f32_16x16x32_bf16 v[96:99], a[0:3], a[80:83], v[96:99]
s_mov_b32 m0, s68
s_nop 0
buffer_load_dwordx4 v20, s[36:39], s76 offen offset:0 nt lds
v_mfma_f32_16x16x32_bf16 v[96:99], a[4:7], a[84:87], v[96:99]
v_mfma_f32_16x16x32_bf16 v[112:115], a[0:3], a[88:91], v[112:115]
buffer_load_dwordx4 v21, s[36:39], s76 offen offset:1024 nt lds
v_mfma_f32_16x16x32_bf16 v[112:115], a[4:7], a[92:95], v[112:115]
s_waitcnt lgkmcnt(4)
v_mfma_f32_16x16x32_bf16 v[68:71], a[8:11], a[64:67], v[68:71]
ds_read_b64_tr_b16 a[24:25], v19 offset:16384
v_mfma_f32_16x16x32_bf16 v[68:71], a[12:15], a[68:71], v[68:71]
ds_read_b64_tr_b16 a[26:27], v19 offset:20480
v_mfma_f32_16x16x32_bf16 v[84:87], a[8:11], a[72:75], v[84:87]
ds_read_b64_tr_b16 a[28:29], v19 offset:24576
v_mfma_f32_16x16x32_bf16 v[84:87], a[12:15], a[76:79], v[84:87]
ds_read_b64_tr_b16 a[30:31], v19 offset:28672
v_mfma_f32_16x16x32_bf16 v[100:103], a[8:11], a[80:83], v[100:103]
buffer_load_dwordx4 v22, s[36:39], s76 offen offset:2048 nt lds
v_mfma_f32_16x16x32_bf16 v[100:103], a[12:15], a[84:87], v[100:103]
v_mfma_f32_16x16x32_bf16 v[116:119], a[8:11], a[88:91], v[116:119]
buffer_load_dwordx4 v23, s[36:39], s76 offen offset:3072 nt lds
s_add_u32 s76, s76, 0x4000
v_mfma_f32_16x16x32_bf16 v[116:119], a[12:15], a[92:95], v[116:119]
s_waitcnt lgkmcnt(4)
v_mfma_f32_16x16x32_bf16 v[72:75], a[16:19], a[64:67], v[72:75]
ds_read_b64_tr_b16 a[0:1], v16 offset:18432
v_mfma_f32_16x16x32_bf16 v[72:75], a[20:23], a[68:71], v[72:75]
ds_read_b64_tr_b16 a[2:3], v16 offset:22528
v_mfma_f32_16x16x32_bf16 v[88:91], a[16:19], a[72:75], v[88:91]
ds_read_b64_tr_b16 a[4:5], v16 offset:26624
v_mfma_f32_16x16x32_bf16 v[88:91], a[20:23], a[76:79], v[88:91]
ds_read_b64_tr_b16 a[6:7], v16 offset:30720
v_mfma_f32_16x16x32_bf16 v[104:107], a[16:19], a[80:83], v[104:107]
v_mfma_f32_16x16x32_bf16 v[104:107], a[20:23], a[84:87], v[104:107]
v_mfma_f32_16x16x32_bf16 v[120:123], a[16:19], a[88:91], v[120:123]
v_mfma_f32_16x16x32_bf16 v[120:123], a[20:23], a[92:95], v[120:123]
s_waitcnt lgkmcnt(4)
v_mfma_f32_16x16x32_bf16 v[76:79], a[24:27], a[64:67], v[76:79]
ds_read_b64_tr_b16 a[8:9], v17 offset:18432
v_mfma_f32_16x16x32_bf16 v[76:79], a[28:31], a[68:71], v[76:79]
ds_read_b64_tr_b16 a[10:11], v17 offset:22528
v_mfma_f32_16x16x32_bf16 v[92:95], a[24:27], a[72:75], v[92:95]
ds_read_b64_tr_b16 a[12:13], v17 offset:26624
v_mfma_f32_16x16x32_bf16 v[92:95], a[28:31], a[76:79], v[92:95]
ds_read_b64_tr_b16 a[14:15], v17 offset:30720
v_mfma_f32_16x16x32_bf16 v[108:111], a[24:27], a[80:83], v[108:111]
v_mfma_f32_16x16x32_bf16 v[108:111], a[28:31], a[84:87], v[108:111]
v_mfma_f32_16x16x32_bf16 v[124:127], a[24:27], a[88:91], v[124:127]
v_mfma_f32_16x16x32_bf16 v[124:127], a[28:31], a[92:95], v[124:127]
s_waitcnt lgkmcnt(4)
v_mfma_f32_16x16x32_bf16 a[192:195], a[0:3], a[64:67], a[192:195]
ds_read_b64_tr_b16 a[16:17], v18 offset:18432
v_mfma_f32_16x16x32_bf16 a[192:195], a[4:7], a[68:71], a[192:195]
ds_read_b64_tr_b16 a[18:19], v18 offset:22528
v_mfma_f32_16x16x32_bf16 a[208:211], a[0:3], a[72:75], a[208:211]
ds_read_b64_tr_b16 a[20:21], v18 offset:26624
v_mfma_f32_16x16x32_bf16 a[208:211], a[4:7], a[76:79], a[208:211]
ds_read_b64_tr_b16 a[22:23], v18 offset:30720
v_mfma_f32_16x16x32_bf16 a[224:227], a[0:3], a[80:83], a[224:227]
v_mfma_f32_16x16x32_bf16 a[224:227], a[4:7], a[84:87], a[224:227]
v_mfma_f32_16x16x32_bf16 a[240:243], a[0:3], a[88:91], a[240:243]
v_mfma_f32_16x16x32_bf16 a[240:243], a[4:7], a[92:95], a[240:243]
s_waitcnt lgkmcnt(4)
v_mfma_f32_16x16x32_bf16 a[196:199], a[8:11], a[64:67], a[196:199]
ds_read_b64_tr_b16 a[24:25], v19 offset:18432
v_mfma_f32_16x16x32_bf16 a[196:199], a[12:15], a[68:71], a[196:199]
ds_read_b64_tr_b16 a[26:27], v19 offset:22528
v_mfma_f32_16x16x32_bf16 a[212:215], a[8:11], a[72:75], a[212:215]
ds_read_b64_tr_b16 a[28:29], v19 offset:26624
v_mfma_f32_16x16x32_bf16 a[212:215], a[12:15], a[76:79], a[212:215]
ds_read_b64_tr_b16 a[30:31], v19 offset:30720
v_mfma_f32_16x16x32_bf16 a[228:231], a[8:11], a[80:83], a[228:231]
v_mfma_f32_16x16x32_bf16 a[228:231], a[12:15], a[84:87], a[228:231]
v_mfma_f32_16x16x32_bf16 a[244:247], a[8:11], a[88:91], a[244:247]
v_mfma_f32_16x16x32_bf16 a[244:247], a[12:15], a[92:95], a[244:247]
s_waitcnt lgkmcnt(4)
v_mfma_f32_16x16x32_bf16 a[200:203], a[16:19], a[64:67], a[200:203]
ds_read_b64_tr_b16 a[0:1], v16 offset:32768
v_mfma_f32_16x16x32_bf16 a[200:203], a[20:23], a[68:71], a[200:203]
ds_read_b64_tr_b16 a[2:3], v16 offset:36864
v_mfma_f32_16x16x32_bf16 a[216:219], a[16:19], a[72:75], a[216:219]
ds_read_b64_tr_b16 a[4:5], v16 offset:40960
v_mfma_f32_16x16x32_bf16 a[216:219], a[20:23], a[76:79], a[216:219]
ds_read_b64_tr_b16 a[6:7], v16 offset:45056
v_mfma_f32_16x16x32_bf16 a[232:235], a[16:19], a[80:83], a[232:235]
v_mfma_f32_16x16x32_bf16 a[232:235], a[20:23], a[84:87], a[232:235]
v_mfma_f32_16x16x32_bf16 a[248:251], a[16:19], a[88:91], a[248:251]
v_mfma_f32_16x16x32_bf16 a[248:251], a[20:23], a[92:95], a[248:251]
s_waitcnt lgkmcnt(4)
v_mfma_f32_16x16x32_bf16 a[204:207], a[24:27], a[64:67], a[204:207]
ds_read_b64_tr_b16 a[8:9], v17 offset:32768
v_mfma_f32_16x16x32_bf16 a[204:207], a[28:31], a[68:71], a[204:207]
ds_read_b64_tr_b16 a[10:11], v17 offset:36864
v_mfma_f32_16x16x32_bf16 a[220:223], a[24:27], a[72:75], a[220:223]
ds_read_b64_tr_b16 a[12:13], v17 offset:40960
v_mfma_f32_16x16x32_bf16 a[220:223], a[28:31], a[76:79], a[220:223]
ds_read_b64_tr_b16 a[14:15], v17 offset:45056
v_mfma_f32_16x16x32_bf16 a[236:239], a[24:27], a[80:83], a[236:239]
s_waitcnt vmcnt(16)
v_mfma_f32_16x16x32_bf16 a[236:239], a[28:31], a[84:87], a[236:239]
s_barrier
v_mfma_f32_16x16x32_bf16 a[252:255], a[24:27], a[88:91], a[252:255]
s_waitcnt vmcnt(20)
v_mfma_f32_16x16x32_bf16 a[252:255], a[28:31], a[92:95], a[252:255]
s_waitcnt lgkmcnt(4)
v_mfma_f32_16x16x32_bf16 v[64:67], a[0:3], a[96:99], v[64:67]
ds_read_b64_tr_b16 a[16:17], v18 offset:32768
v_mfma_f32_16x16x32_bf16 v[64:67], a[4:7], a[100:103], v[64:67]
ds_read_b64_tr_b16 a[18:19], v18 offset:36864
v_mfma_f32_16x16x32_bf16 v[80:83], a[0:3], a[104:107], v[80:83]
ds_read_b64_tr_b16 a[20:21], v18 offset:40960
v_mfma_f32_16x16x32_bf16 v[80:83], a[4:7], a[108:111], v[80:83]
ds_read_b64_tr_b16 a[22:23], v18 offset:45056
v_mfma_f32_16x16x32_bf16 v[96:99], a[0:3], a[112:115], v[96:99]
s_add_u32 m0, s68, 0x4000
s_nop 0
buffer_load_dwordx4 v20, s[36:39], s76 offen offset:0 nt lds
v_mfma_f32_16x16x32_bf16 v[96:99], a[4:7], a[116:119], v[96:99]
v_mfma_f32_16x16x32_bf16 v[112:115], a[0:3], a[120:123], v[112:115]
buffer_load_dwordx4 v21, s[36:39], s76 offen offset:1024 nt lds
v_mfma_f32_16x16x32_bf16 v[112:115], a[4:7], a[124:127], v[112:115]
s_waitcnt lgkmcnt(4)
v_mfma_f32_16x16x32_bf16 v[68:71], a[8:11], a[96:99], v[68:71]
ds_read_b64_tr_b16 a[24:25], v19 offset:32768
v_mfma_f32_16x16x32_bf16 v[68:71], a[12:15], a[100:103], v[68:71]
ds_read_b64_tr_b16 a[26:27], v19 offset:36864
v_mfma_f32_16x16x32_bf16 v[84:87], a[8:11], a[104:107], v[84:87]
ds_read_b64_tr_b16 a[28:29], v19 offset:40960
v_mfma_f32_16x16x32_bf16 v[84:87], a[12:15], a[108:111], v[84:87]
ds_read_b64_tr_b16 a[30:31], v19 offset:45056
v_mfma_f32_16x16x32_bf16 v[100:103], a[8:11], a[112:115], v[100:103]
buffer_load_dwordx4 v22, s[36:39], s76 offen offset:2048 nt lds
v_mfma_f32_16x16x32_bf16 v[100:103], a[12:15], a[116:119], v[100:103]
v_mfma_f32_16x16x32_bf16 v[116:119], a[8:11], a[120:123], v[116:119]
buffer_load_dwordx4 v23, s[36:39], s76 offen offset:3072 nt lds
s_add_u32 s76, s76, 0x4000
v_mfma_f32_16x16x32_bf16 v[116:119], a[12:15], a[124:127], v[116:119]
s_waitcnt lgkmcnt(4)
v_mfma_f32_16x16x32_bf16 v[72:75], a[16:19], a[96:99], v[72:75]
ds_read_b64_tr_b16 a[0:1], v16 offset:34816
v_mfma_f32_16x16x32_bf16 v[72:75], a[20:23], a[100:103], v[72:75]
ds_read_b64_tr_b16 a[2:3], v16 offset:38912
v_mfma_f32_16x16x32_bf16 v[88:91], a[16:19], a[104:107], v[88:91]
ds_read_b64_tr_b16 a[4:5], v16 offset:43008
v_mfma_f32_16x16x32_bf16 v[88:91], a[20:23], a[108:111], v[88:91]
ds_read_b64_tr_b16 a[6:7], v16 offset:47104
v_mfma_f32_16x16x32_bf16 v[104:107], a[16:19], a[112:115], v[104:107]
v_mfma_f32_16x16x32_bf16 v[104:107], a[20:23], a[116:119], v[104:107]
v_mfma_f32_16x16x32_bf16 v[120:123], a[16:19], a[120:123], v[120:123]
v_mfma_f32_16x16x32_bf16 v[120:123], a[20:23], a[124:127], v[120:123]
s_waitcnt lgkmcnt(4)
v_mfma_f32_16x16x32_bf16 v[76:79], a[24:27], a[96:99], v[76:79]
ds_read_b64_tr_b16 a[8:9], v17 offset:34816
v_mfma_f32_16x16x32_bf16 v[76:79], a[28:31], a[100:103], v[76:79]
ds_read_b64_tr_b16 a[10:11], v17 offset:38912
v_mfma_f32_16x16x32_bf16 v[92:95], a[24:27], a[104:107], v[92:95]
ds_read_b64_tr_b16 a[12:13], v17 offset:43008
v_mfma_f32_16x16x32_bf16 v[92:95], a[28:31], a[108:111], v[92:95]
ds_read_b64_tr_b16 a[14:15], v17 offset:47104
v_mfma_f32_16x16x32_bf16 v[108:111], a[24:27], a[112:115], v[108:111]
v_mfma_f32_16x16x32_bf16 v[108:111], a[28:31], a[116:119], v[108:111]
v_mfma_f32_16x16x32_bf16 v[124:127], a[24:27], a[120:123], v[124:127]
v_mfma_f32_16x16x32_bf16 v[124:127], a[28:31], a[124:127], v[124:127]
s_waitcnt lgkmcnt(4)
v_mfma_f32_16x16x32_bf16 a[192:195], a[0:3], a[96:99], a[192:195]
ds_read_b64_tr_b16 a[16:17], v18 offset:34816
v_mfma_f32_16x16x32_bf16 a[192:195], a[4:7], a[100:103], a[192:195]
ds_read_b64_tr_b16 a[18:19], v18 offset:38912
v_mfma_f32_16x16x32_bf16 a[208:211], a[0:3], a[104:107], a[208:211]
ds_read_b64_tr_b16 a[20:21], v18 offset:43008
v_mfma_f32_16x16x32_bf16 a[208:211], a[4:7], a[108:111], a[208:211]
ds_read_b64_tr_b16 a[22:23], v18 offset:47104
v_mfma_f32_16x16x32_bf16 a[224:227], a[0:3], a[112:115], a[224:227]
v_mfma_f32_16x16x32_bf16 a[224:227], a[4:7], a[116:119], a[224:227]
v_mfma_f32_16x16x32_bf16 a[240:243], a[0:3], a[120:123], a[240:243]
v_mfma_f32_16x16x32_bf16 a[240:243], a[4:7], a[124:127], a[240:243]
s_waitcnt lgkmcnt(4)
v_mfma_f32_16x16x32_bf16 a[196:199], a[8:11], a[96:99], a[196:199]
ds_read_b64_tr_b16 a[24:25], v19 offset:34816
v_mfma_f32_16x16x32_bf16 a[196:199], a[12:15], a[100:103], a[196:199]
ds_read_b64_tr_b16 a[26:27], v19 offset:38912
v_mfma_f32_16x16x32_bf16 a[212:215], a[8:11], a[104:107], a[212:215]
ds_read_b64_tr_b16 a[28:29], v19 offset:43008
v_mfma_f32_16x16x32_bf16 a[212:215], a[12:15], a[108:111], a[212:215]
ds_read_b64_tr_b16 a[30:31], v19 offset:47104
v_mfma_f32_16x16x32_bf16 a[228:231], a[8:11], a[112:115], a[228:231]
v_mfma_f32_16x16x32_bf16 a[228:231], a[12:15], a[116:119], a[228:231]
v_mfma_f32_16x16x32_bf16 a[244:247], a[8:11], a[120:123], a[244:247]
v_mfma_f32_16x16x32_bf16 a[244:247], a[12:15], a[124:127], a[244:247]
s_waitcnt lgkmcnt(4)
v_mfma_f32_16x16x32_bf16 a[200:203], a[16:19], a[96:99], a[200:203]
ds_read_b64_tr_b16 a[0:1], v16 offset:49152
v_mfma_f32_16x16x32_bf16 a[200:203], a[20:23], a[100:103], a[200:203]
ds_read_b64_tr_b16 a[2:3], v16 offset:53248
v_mfma_f32_16x16x32_bf16 a[216:219], a[16:19], a[104:107], a[216:219]
ds_read_b64_tr_b16 a[4:5], v16 offset:57344
v_mfma_f32_16x16x32_bf16 a[216:219], a[20:23], a[108:111], a[216:219]
ds_read_b64_tr_b16 a[6:7], v16 offset:61440
v_mfma_f32_16x16x32_bf16 a[232:235], a[16:19], a[112:115], a[232:235]
v_mfma_f32_16x16x32_bf16 a[232:235], a[20:23], a[116:119], a[232:235]
v_mfma_f32_16x16x32_bf16 a[248:251], a[16:19], a[120:123], a[248:251]
v_mfma_f32_16x16x32_bf16 a[248:251], a[20:23], a[124:127], a[248:251]
s_waitcnt lgkmcnt(4)
v_mfma_f32_16x16x32_bf16 a[204:207], a[24:27], a[96:99], a[204:207]
ds_read_b64_tr_b16 a[8:9], v17 offset:49152
v_mfma_f32_16x16x32_bf16 a[204:207], a[28:31], a[100:103], a[204:207]
ds_read_b64_tr_b16 a[10:11], v17 offset:53248
v_mfma_f32_16x16x32_bf16 a[220:223], a[24:27], a[104:107], a[220:223]
ds_read_b64_tr_b16 a[12:13], v17 offset:57344
v_mfma_f32_16x16x32_bf16 a[220:223], a[28:31], a[108:111], a[220:223]
ds_read_b64_tr_b16 a[14:15], v17 offset:61440
v_mfma_f32_16x16x32_bf16 a[236:239], a[24:27], a[112:115], a[236:239]
v_mfma_f32_16x16x32_bf16 a[236:239], a[28:31], a[116:119], a[236:239]
s_barrier
v_mfma_f32_16x16x32_bf16 a[252:255], a[24:27], a[120:123], a[252:255]
s_waitcnt vmcnt(8)
v_mfma_f32_16x16x32_bf16 a[252:255], a[28:31], a[124:127], a[252:255]
s_waitcnt lgkmcnt(4)
v_mfma_f32_16x16x32_bf16 v[64:67], a[0:3], a[128:131], v[64:67]
ds_read_b64_tr_b16 a[16:17], v18 offset:49152
v_mfma_f32_16x16x32_bf16 v[64:67], a[4:7], a[132:135], v[64:67]
ds_read_b64_tr_b16 a[18:19], v18 offset:53248
v_mfma_f32_16x16x32_bf16 v[80:83], a[0:3], a[136:139], v[80:83]
ds_read_b64_tr_b16 a[20:21], v18 offset:57344
v_mfma_f32_16x16x32_bf16 v[80:83], a[4:7], a[140:143], v[80:83]
ds_read_b64_tr_b16 a[22:23], v18 offset:61440
v_mfma_f32_16x16x32_bf16 v[96:99], a[0:3], a[144:147], v[96:99]
s_add_u32 m0, s68, 0x8000
s_nop 0
buffer_load_dwordx4 v20, s[36:39], s76 offen offset:0 nt lds
v_mfma_f32_16x16x32_bf16 v[96:99], a[4:7], a[148:151], v[96:99]
v_mfma_f32_16x16x32_bf16 v[112:115], a[0:3], a[152:155], v[112:115]
buffer_load_dwordx4 v21, s[36:39], s76 offen offset:1024 nt lds
v_mfma_f32_16x16x32_bf16 v[112:115], a[4:7], a[156:159], v[112:115]
s_waitcnt lgkmcnt(4)
v_mfma_f32_16x16x32_bf16 v[68:71], a[8:11], a[128:131], v[68:71]
ds_read_b64_tr_b16 a[24:25], v19 offset:49152
v_mfma_f32_16x16x32_bf16 v[68:71], a[12:15], a[132:135], v[68:71]
ds_read_b64_tr_b16 a[26:27], v19 offset:53248
v_mfma_f32_16x16x32_bf16 v[84:87], a[8:11], a[136:139], v[84:87]
ds_read_b64_tr_b16 a[28:29], v19 offset:57344
v_mfma_f32_16x16x32_bf16 v[84:87], a[12:15], a[140:143], v[84:87]
ds_read_b64_tr_b16 a[30:31], v19 offset:61440
v_mfma_f32_16x16x32_bf16 v[100:103], a[8:11], a[144:147], v[100:103]
buffer_load_dwordx4 v22, s[36:39], s76 offen offset:2048 nt lds
v_mfma_f32_16x16x32_bf16 v[100:103], a[12:15], a[148:151], v[100:103]
v_mfma_f32_16x16x32_bf16 v[116:119], a[8:11], a[152:155], v[116:119]
buffer_load_dwordx4 v23, s[36:39], s76 offen offset:3072 nt lds
s_add_u32 s76, s76, 0x4000
v_mfma_f32_16x16x32_bf16 v[116:119], a[12:15], a[156:159], v[116:119]
s_waitcnt lgkmcnt(4)
v_mfma_f32_16x16x32_bf16 v[72:75], a[16:19], a[128:131], v[72:75]
ds_read_b64_tr_b16 a[0:1], v16 offset:51200
v_mfma_f32_16x16x32_bf16 v[72:75], a[20:23], a[132:135], v[72:75]
ds_read_b64_tr_b16 a[2:3], v16 offset:55296
v_mfma_f32_16x16x32_bf16 v[88:91], a[16:19], a[136:139], v[88:91]
ds_read_b64_tr_b16 a[4:5], v16 offset:59392
v_mfma_f32_16x16x32_bf16 v[88:91], a[20:23], a[140:143], v[88:91]
ds_read_b64_tr_b16 a[6:7], v16 offset:63488
v_mfma_f32_16x16x32_bf16 v[104:107], a[16:19], a[144:147], v[104:107]
s_mov_b32 s90, 0
s_mov_b32 s91, 0xe000
s_mov_b32 s92, 0x1c000
s_mov_b32 s93, 0x2a000
v_mfma_f32_16x16x32_bf16 v[104:107], a[20:23], a[148:151], v[104:107]
v_mfma_f32_16x16x32_bf16 v[120:123], a[16:19], a[152:155], v[120:123]
v_mfma_f32_16x16x32_bf16 v[120:123], a[20:23], a[156:159], v[120:123]
s_waitcnt lgkmcnt(4)
v_mfma_f32_16x16x32_bf16 v[76:79], a[24:27], a[128:131], v[76:79]
ds_read_b64_tr_b16 a[8:9], v17 offset:51200
v_mfma_f32_16x16x32_bf16 v[76:79], a[28:31], a[132:135], v[76:79]
ds_read_b64_tr_b16 a[10:11], v17 offset:55296
v_mfma_f32_16x16x32_bf16 v[92:95], a[24:27], a[136:139], v[92:95]
ds_read_b64_tr_b16 a[12:13], v17 offset:59392
v_mfma_f32_16x16x32_bf16 v[92:95], a[28:31], a[140:143], v[92:95]
ds_read_b64_tr_b16 a[14:15], v17 offset:63488
v_mfma_f32_16x16x32_bf16 v[108:111], a[24:27], a[144:147], v[108:111]
v_mfma_f32_16x16x32_bf16 v[108:111], a[28:31], a[148:151], v[108:111]
v_mfma_f32_16x16x32_bf16 v[124:127], a[24:27], a[152:155], v[124:127]
v_mfma_f32_16x16x32_bf16 v[124:127], a[28:31], a[156:159], v[124:127]
s_waitcnt lgkmcnt(4)
v_mfma_f32_16x16x32_bf16 a[192:195], a[0:3], a[128:131], a[192:195]
ds_read_b64_tr_b16 a[16:17], v18 offset:51200
v_mfma_f32_16x16x32_bf16 a[192:195], a[4:7], a[132:135], a[192:195]
ds_read_b64_tr_b16 a[18:19], v18 offset:55296
v_mfma_f32_16x16x32_bf16 a[208:211], a[0:3], a[136:139], a[208:211]
ds_read_b64_tr_b16 a[20:21], v18 offset:59392
v_mfma_f32_16x16x32_bf16 a[208:211], a[4:7], a[140:143], a[208:211]
ds_read_b64_tr_b16 a[22:23], v18 offset:63488
v_mfma_f32_16x16x32_bf16 a[224:227], a[0:3], a[144:147], a[224:227]
v_mfma_f32_16x16x32_bf16 a[224:227], a[4:7], a[148:151], a[224:227]
v_mfma_f32_16x16x32_bf16 a[240:243], a[0:3], a[152:155], a[240:243]
v_mfma_f32_16x16x32_bf16 a[240:243], a[4:7], a[156:159], a[240:243]
s_waitcnt lgkmcnt(4)
v_mfma_f32_16x16x32_bf16 a[196:199], a[8:11], a[128:131], a[196:199]
ds_read_b64_tr_b16 a[24:25], v19 offset:51200
v_mfma_f32_16x16x32_bf16 a[196:199], a[12:15], a[132:135], a[196:199]
ds_read_b64_tr_b16 a[26:27], v19 offset:55296
v_mfma_f32_16x16x32_bf16 a[212:215], a[8:11], a[136:139], a[212:215]
ds_read_b64_tr_b16 a[28:29], v19 offset:59392
v_mfma_f32_16x16x32_bf16 a[212:215], a[12:15], a[140:143], a[212:215]
ds_read_b64_tr_b16 a[30:31], v19 offset:63488
v_mfma_f32_16x16x32_bf16 a[228:231], a[8:11], a[144:147], a[228:231]
v_mfma_f32_16x16x32_bf16 a[228:231], a[12:15], a[148:151], a[228:231]
v_mfma_f32_16x16x32_bf16 a[244:247], a[8:11], a[152:155], a[244:247]
v_mfma_f32_16x16x32_bf16 a[244:247], a[12:15], a[156:159], a[244:247]
s_waitcnt lgkmcnt(4)
v_mfma_f32_16x16x32_bf16 a[200:203], a[16:19], a[128:131], a[200:203]
ds_read_b64_tr_b16 a[0:1], v12 offset:0
v_mfma_f32_16x16x32_bf16 a[200:203], a[20:23], a[132:135], a[200:203]
ds_read_b64_tr_b16 a[2:3], v12 offset:4096
v_mfma_f32_16x16x32_bf16 a[216:219], a[16:19], a[136:139], a[216:219]
ds_read_b64_tr_b16 a[4:5], v12 offset:8192
v_mfma_f32_16x16x32_bf16 a[216:219], a[20:23], a[140:143], a[216:219]
ds_read_b64_tr_b16 a[6:7], v12 offset:12288
v_mfma_f32_16x16x32_bf16 a[232:235], a[16:19], a[144:147], a[232:235]
v_mfma_f32_16x16x32_bf16 a[232:235], a[20:23], a[148:151], a[232:235]
v_mfma_f32_16x16x32_bf16 a[248:251], a[16:19], a[152:155], a[248:251]
v_mfma_f32_16x16x32_bf16 a[248:251], a[20:23], a[156:159], a[248:251]
s_waitcnt lgkmcnt(4)
v_mfma_f32_16x16x32_bf16 a[204:207], a[24:27], a[128:131], a[204:207]
ds_read_b64_tr_b16 a[8:9], v12 offset:8
v_mfma_f32_16x16x32_bf16 a[204:207], a[28:31], a[132:135], a[204:207]
ds_read_b64_tr_b16 a[10:11], v12 offset:4104
v_mfma_f32_16x16x32_bf16 a[220:223], a[24:27], a[136:139], a[220:223]
ds_read_b64_tr_b16 a[12:13], v12 offset:8200
v_mfma_f32_16x16x32_bf16 a[220:223], a[28:31], a[140:143], a[220:223]
ds_read_b64_tr_b16 a[14:15], v12 offset:12296
v_mfma_f32_16x16x32_bf16 a[236:239], a[24:27], a[144:147], a[236:239]
s_waitcnt vmcnt(20)
v_mfma_f32_16x16x32_bf16 a[236:239], a[28:31], a[148:151], a[236:239]
s_barrier
v_mfma_f32_16x16x32_bf16 a[252:255], a[24:27], a[152:155], a[252:255]
v_mfma_f32_16x16x32_bf16 a[252:255], a[28:31], a[156:159], a[252:255]
s_waitcnt lgkmcnt(4)
v_mfma_f32_16x16x32_bf16 v[128:131], a[0:3], a[32:35], v[128:131]
ds_read_b64_tr_b16 a[16:17], v13 offset:0
v_mfma_f32_16x16x32_bf16 v[128:131], a[4:7], a[36:39], v[128:131]
ds_read_b64_tr_b16 a[18:19], v13 offset:4096
v_mfma_f32_16x16x32_bf16 v[160:163], a[0:3], a[40:43], v[160:163]
ds_read_b64_tr_b16 a[20:21], v13 offset:8192
v_mfma_f32_16x16x32_bf16 v[160:163], a[4:7], a[44:47], v[160:163]
ds_read_b64_tr_b16 a[22:23], v13 offset:12288
v_mfma_f32_16x16x32_bf16 v[192:195], a[0:3], a[48:51], v[192:195]
s_add_u32 m0, s68, 0xc000
s_nop 0
buffer_load_dwordx4 v20, s[36:39], s76 offen offset:0 nt lds
v_mul_f32_e32 v38, s86, v64
v_mul_f32_e32 v39, s86, v65
v_mfma_f32_16x16x32_bf16 v[192:195], a[4:7], a[52:55], v[192:195]
v_mul_f32_e32 v40, s86, v66
v_mul_f32_e32 v41, s86, v67
v_mfma_f32_16x16x32_bf16 v[224:227], a[0:3], a[56:59], v[224:227]
buffer_load_dwordx4 v21, s[36:39], s76 offen offset:1024 nt lds
v_exp_f32_e32 v38, v38
v_accvgpr_read_b32 v42, a192
v_mfma_f32_16x16x32_bf16 v[224:227], a[4:7], a[60:63], v[224:227]
s_waitcnt lgkmcnt(4)
v_exp_f32_e32 v39, v39
v_accvgpr_read_b32 v43, a193
v_mfma_f32_16x16x32_bf16 v[132:135], a[8:11], a[32:35], v[132:135]
ds_read_b64_tr_b16 a[24:25], v13 offset:8
v_exp_f32_e32 v40, v40
v_mfma_f32_16x16x32_bf16 v[132:135], a[12:15], a[36:39], v[132:135]
ds_read_b64_tr_b16 a[26:27], v13 offset:4104
v_accvgpr_read_b32 v44, a194
v_exp_f32_e32 v41, v41
v_mfma_f32_16x16x32_bf16 v[164:167], a[8:11], a[40:43], v[164:167]
ds_read_b64_tr_b16 a[28:29], v13 offset:8200
v_accvgpr_read_b32 v45, a195
v_add_f32_e32 v38, 1.0, v38
v_mfma_f32_16x16x32_bf16 v[164:167], a[12:15], a[44:47], v[164:167]
ds_read_b64_tr_b16 a[30:31], v13 offset:12296
v_add_f32_e32 v39, 1.0, v39
v_add_f32_e32 v40, 1.0, v40
v_add_f32_e32 v41, 1.0, v41
v_mfma_f32_16x16x32_bf16 v[196:199], a[8:11], a[48:51], v[196:199]
buffer_load_dwordx4 v22, s[36:39], s76 offen offset:2048 nt lds
v_rcp_f32_e32 v38, v38
v_mfma_f32_16x16x32_bf16 v[196:199], a[12:15], a[52:55], v[196:199]
v_mul_f32_e32 v42, v42, v64
v_rcp_f32_e32 v39, v39
v_mfma_f32_16x16x32_bf16 v[228:231], a[8:11], a[56:59], v[228:231]
buffer_load_dwordx4 v23, s[36:39], s76 offen offset:3072 nt lds
s_add_u32 s76, s76, 0x4000
v_mul_f32_e32 v43, v43, v65
v_mfma_f32_16x16x32_bf16 v[228:231], a[12:15], a[60:63], v[228:231]
s_waitcnt lgkmcnt(4)
v_rcp_f32_e32 v40, v40
v_mul_f32_e32 v44, v44, v66
v_mfma_f32_16x16x32_bf16 v[136:139], a[16:19], a[32:35], v[136:139]
ds_read_b64_tr_b16 a[0:1], v14 offset:0
v_rcp_f32_e32 v41, v41
v_mul_f32_e32 v45, v45, v67
v_mfma_f32_16x16x32_bf16 v[136:139], a[20:23], a[36:39], v[136:139]
ds_read_b64_tr_b16 a[2:3], v14 offset:4096
v_mul_f32_e32 v42, v42, v38
v_mul_f32_e32 v43, v43, v39
v_mul_f32_e32 v44, v44, v40
v_mfma_f32_16x16x32_bf16 v[168:171], a[16:19], a[40:43], v[168:171]
ds_read_b64_tr_b16 a[4:5], v14 offset:8192
v_mul_f32_e32 v45, v45, v41
v_cvt_pk_bf16_f32 v30, v42, v43
v_cvt_pk_bf16_f32 v31, v44, v45
v_mfma_f32_16x16x32_bf16 v[168:171], a[20:23], a[44:47], v[168:171]
ds_read_b64_tr_b16 a[6:7], v14 offset:12288
v_mul_f32_e32 v38, s86, v68
v_mul_f32_e32 v39, s86, v69
v_mfma_f32_16x16x32_bf16 v[200:203], a[16:19], a[48:51], v[200:203]
v_mul_f32_e32 v40, s86, v70
v_mul_f32_e32 v41, s86, v71
v_mfma_f32_16x16x32_bf16 v[200:203], a[20:23], a[52:55], v[200:203]
v_exp_f32_e32 v38, v38
v_accvgpr_read_b32 v42, a196
v_mfma_f32_16x16x32_bf16 v[232:235], a[16:19], a[56:59], v[232:235]
v_exp_f32_e32 v39, v39
v_accvgpr_read_b32 v43, a197
v_mfma_f32_16x16x32_bf16 v[232:235], a[20:23], a[60:63], v[232:235]
s_waitcnt lgkmcnt(4)
v_exp_f32_e32 v40, v40
v_mfma_f32_16x16x32_bf16 v[140:143], a[24:27], a[32:35], v[140:143]
ds_read_b64_tr_b16 a[8:9], v14 offset:8
v_accvgpr_read_b32 v44, a198
v_exp_f32_e32 v41, v41
v_mfma_f32_16x16x32_bf16 v[140:143], a[28:31], a[36:39], v[140:143]
ds_read_b64_tr_b16 a[10:11], v14 offset:4104
v_accvgpr_read_b32 v45, a199
v_add_f32_e32 v38, 1.0, v38
v_mfma_f32_16x16x32_bf16 v[172:175], a[24:27], a[40:43], v[172:175]
ds_read_b64_tr_b16 a[12:13], v14 offset:8200
v_add_f32_e32 v39, 1.0, v39
v_add_f32_e32 v40, 1.0, v40
v_add_f32_e32 v41, 1.0, v41
v_mfma_f32_16x16x32_bf16 v[172:175], a[28:31], a[44:47], v[172:175]
ds_read_b64_tr_b16 a[14:15], v14 offset:12296
v_rcp_f32_e32 v38, v38
v_mfma_f32_16x16x32_bf16 v[204:207], a[24:27], a[48:51], v[204:207]
v_mul_f32_e32 v42, v42, v68
v_rcp_f32_e32 v39, v39
v_mfma_f32_16x16x32_bf16 v[204:207], a[28:31], a[52:55], v[204:207]
v_mul_f32_e32 v43, v43, v69
v_mfma_f32_16x16x32_bf16 v[236:239], a[24:27], a[56:59], v[236:239]
v_rcp_f32_e32 v40, v40
v_mul_f32_e32 v44, v44, v70
v_mfma_f32_16x16x32_bf16 v[236:239], a[28:31], a[60:63], v[236:239]
s_waitcnt lgkmcnt(4)
v_rcp_f32_e32 v41, v41
v_mul_f32_e32 v45, v45, v71
v_mul_f32_e32 v42, v42, v38
v_mfma_f32_16x16x32_bf16 v[144:147], a[0:3], a[32:35], v[144:147]
ds_read_b64_tr_b16 a[16:17], v15 offset:0
v_mul_f32_e32 v43, v43, v39
v_mul_f32_e32 v44, v44, v40
v_mfma_f32_16x16x32_bf16 v[144:147], a[4:7], a[36:39], v[144:147]
ds_read_b64_tr_b16 a[18:19], v15 offset:4096
v_mul_f32_e32 v45, v45, v41
v_cvt_pk_bf16_f32 v32, v42, v43
v_cvt_pk_bf16_f32 v33, v44, v45
v_mfma_f32_16x16x32_bf16 v[176:179], a[0:3], a[40:43], v[176:179]
ds_read_b64_tr_b16 a[20:21], v15 offset:8192
v_mfma_f32_16x16x32_bf16 v[176:179], a[4:7], a[44:47], v[176:179]
ds_read_b64_tr_b16 a[22:23], v15 offset:12288
v_mfma_f32_16x16x32_bf16 v[208:211], a[0:3], a[48:51], v[208:211]
buffer_store_dwordx4 v[30:33], v29, s[64:67], s90 offen offset:0 sc1
v_mul_f32_e32 v38, s86, v72
v_mul_f32_e32 v39, s86, v73
v_mul_f32_e32 v40, s86, v74
v_mul_f32_e32 v41, s86, v75
v_mfma_f32_16x16x32_bf16 v[208:211], a[4:7], a[52:55], v[208:211]
v_exp_f32_e32 v38, v38
v_accvgpr_read_b32 v42, a200
v_mfma_f32_16x16x32_bf16 v[240:243], a[0:3], a[56:59], v[240:243]
v_exp_f32_e32 v39, v39
v_accvgpr_read_b32 v43, a201
v_mfma_f32_16x16x32_bf16 v[240:243], a[4:7], a[60:63], v[240:243]
s_waitcnt lgkmcnt(4)
v_exp_f32_e32 v40, v40
v_accvgpr_read_b32 v44, a202
v_mfma_f32_16x16x32_bf16 v[148:151], a[8:11], a[32:35], v[148:151]
ds_read_b64_tr_b16 a[24:25], v15 offset:8
v_exp_f32_e32 v41, v41
v_mfma_f32_16x16x32_bf16 v[148:151], a[12:15], a[36:39], v[148:151]
ds_read_b64_tr_b16 a[26:27], v15 offset:4104
v_accvgpr_read_b32 v45, a203
v_add_f32_e32 v38, 1.0, v38
v_add_f32_e32 v39, 1.0, v39
v_mfma_f32_16x16x32_bf16 v[180:183], a[8:11], a[40:43], v[180:183]
ds_read_b64_tr_b16 a[28:29], v15 offset:8200
v_add_f32_e32 v40, 1.0, v40
v_add_f32_e32 v41, 1.0, v41
v_mfma_f32_16x16x32_bf16 v[180:183], a[12:15], a[44:47], v[180:183]
ds_read_b64_tr_b16 a[30:31], v15 offset:12296
v_rcp_f32_e32 v38, v38
v_mul_f32_e32 v42, v42, v72
v_mfma_f32_16x16x32_bf16 v[212:215], a[8:11], a[48:51], v[212:215]
v_rcp_f32_e32 v39, v39
v_mul_f32_e32 v43, v43, v73
v_mfma_f32_16x16x32_bf16 v[212:215], a[12:15], a[52:55], v[212:215]
v_rcp_f32_e32 v40, v40
v_mfma_f32_16x16x32_bf16 v[244:247], a[8:11], a[56:59], v[244:247]
v_mul_f32_e32 v44, v44, v74
v_rcp_f32_e32 v41, v41
v_mfma_f32_16x16x32_bf16 v[244:247], a[12:15], a[60:63], v[244:247]
s_waitcnt lgkmcnt(4)
v_mul_f32_e32 v45, v45, v75
v_mul_f32_e32 v42, v42, v38
v_mfma_f32_16x16x32_bf16 v[152:155], a[16:19], a[32:35], v[152:155]
ds_read_b64_tr_b16 a[0:1], v12 offset:16384
v_mul_f32_e32 v43, v43, v39
v_mul_f32_e32 v44, v44, v40
v_mul_f32_e32 v45, v45, v41
v_mfma_f32_16x16x32_bf16 v[152:155], a[20:23], a[36:39], v[152:155]
ds_read_b64_tr_b16 a[2:3], v12 offset:20480
v_cvt_pk_bf16_f32 v50, v42, v43
v_cvt_pk_bf16_f32 v51, v44, v45
v_mfma_f32_16x16x32_bf16 v[184:187], a[16:19], a[40:43], v[184:187]
ds_read_b64_tr_b16 a[4:5], v12 offset:24576
v_mul_f32_e32 v38, s86, v76
v_mul_f32_e32 v39, s86, v77
v_mul_f32_e32 v40, s86, v78
v_mfma_f32_16x16x32_bf16 v[184:187], a[20:23], a[44:47], v[184:187]
ds_read_b64_tr_b16 a[6:7], v12 offset:28672
v_mul_f32_e32 v41, s86, v79
v_mfma_f32_16x16x32_bf16 v[216:219], a[16:19], a[48:51], v[216:219]
v_exp_f32_e32 v38, v38
v_accvgpr_read_b32 v42, a204
v_mfma_f32_16x16x32_bf16 v[216:219], a[20:23], a[52:55], v[216:219]
v_exp_f32_e32 v39, v39
v_accvgpr_read_b32 v43, a205
v_mfma_f32_16x16x32_bf16 v[248:251], a[16:19], a[56:59], v[248:251]
v_exp_f32_e32 v40, v40
v_accvgpr_read_b32 v44, a206
v_mfma_f32_16x16x32_bf16 v[248:251], a[20:23], a[60:63], v[248:251]
s_waitcnt lgkmcnt(4)
v_exp_f32_e32 v41, v41
v_mfma_f32_16x16x32_bf16 v[156:159], a[24:27], a[32:35], v[156:159]
ds_read_b64_tr_b16 a[8:9], v12 offset:16392
v_accvgpr_read_b32 v45, a207
v_add_f32_e32 v38, 1.0, v38
v_add_f32_e32 v39, 1.0, v39
v_mfma_f32_16x16x32_bf16 v[156:159], a[28:31], a[36:39], v[156:159]
ds_read_b64_tr_b16 a[10:11], v12 offset:20488
v_add_f32_e32 v40, 1.0, v40
v_add_f32_e32 v41, 1.0, v41
v_mfma_f32_16x16x32_bf16 v[188:191], a[24:27], a[40:43], v[188:191]
ds_read_b64_tr_b16 a[12:13], v12 offset:24584
v_rcp_f32_e32 v38, v38
v_mul_f32_e32 v42, v42, v76
v_mfma_f32_16x16x32_bf16 v[188:191], a[28:31], a[44:47], v[188:191]
ds_read_b64_tr_b16 a[14:15], v12 offset:28680
v_rcp_f32_e32 v39, v39
v_mul_f32_e32 v43, v43, v77
v_mfma_f32_16x16x32_bf16 v[220:223], a[24:27], a[48:51], v[220:223]
v_rcp_f32_e32 v40, v40
v_mfma_f32_16x16x32_bf16 v[220:223], a[28:31], a[52:55], v[220:223]
s_barrier
v_mul_f32_e32 v44, v44, v78
v_rcp_f32_e32 v41, v41
v_mfma_f32_16x16x32_bf16 v[252:255], a[24:27], a[56:59], v[252:255]
v_mul_f32_e32 v45, v45, v79
v_mul_f32_e32 v42, v42, v38
v_mfma_f32_16x16x32_bf16 v[252:255], a[28:31], a[60:63], v[252:255]
s_waitcnt lgkmcnt(4)
v_mul_f32_e32 v43, v43, v39
v_mul_f32_e32 v44, v44, v40
v_mul_f32_e32 v45, v45, v41
v_mfma_f32_16x16x32_bf16 v[128:131], a[0:3], a[64:67], v[128:131]
ds_read_b64_tr_b16 a[16:17], v13 offset:16384
v_cvt_pk_bf16_f32 v52, v42, v43
v_cvt_pk_bf16_f32 v53, v44, v45
v_mfma_f32_16x16x32_bf16 v[128:131], a[4:7], a[68:71], v[128:131]
ds_read_b64_tr_b16 a[18:19], v13 offset:20480
v_mfma_f32_16x16x32_bf16 v[160:163], a[0:3], a[72:75], v[160:163]
ds_read_b64_tr_b16 a[20:21], v13 offset:24576
v_mfma_f32_16x16x32_bf16 v[160:163], a[4:7], a[76:79], v[160:163]
ds_read_b64_tr_b16 a[22:23], v13 offset:28672
v_mfma_f32_16x16x32_bf16 v[192:195], a[0:3], a[80:83], v[192:195]
s_add_u32 m0, s68, 0x10000
s_nop 0
buffer_load_dwordx4 v24, s[40:43], s77 offen offset:0 nt lds
v_mfma_f32_16x16x32_bf16 v[192:195], a[4:7], a[84:87], v[192:195]
buffer_store_dwordx4 v[50:53], v29, s[64:67], s90 offen offset:1024 sc1
v_mul_f32_e32 v38, s86, v80
v_mul_f32_e32 v39, s86, v81
v_mul_f32_e32 v40, s86, v82
v_mul_f32_e32 v41, s86, v83
v_exp_f32_e32 v38, v38
v_accvgpr_read_b32 v42, a208
v_exp_f32_e32 v39, v39
v_accvgpr_read_b32 v43, a209
v_mfma_f32_16x16x32_bf16 v[224:227], a[0:3], a[88:91], v[224:227]
buffer_load_dwordx4 v25, s[40:43], s77 offen offset:1024 nt lds
v_exp_f32_e32 v40, v40
v_accvgpr_read_b32 v44, a210
v_mfma_f32_16x16x32_bf16 v[224:227], a[4:7], a[92:95], v[224:227]
s_waitcnt lgkmcnt(4)
v_exp_f32_e32 v41, v41
v_accvgpr_read_b32 v45, a211
v_mfma_f32_16x16x32_bf16 v[132:135], a[8:11], a[64:67], v[132:135]
ds_read_b64_tr_b16 a[24:25], v13 offset:16392
v_add_f32_e32 v38, 1.0, v38
v_add_f32_e32 v39, 1.0, v39
v_add_f32_e32 v40, 1.0, v40
v_mfma_f32_16x16x32_bf16 v[132:135], a[12:15], a[68:71], v[132:135]
ds_read_b64_tr_b16 a[26:27], v13 offset:20488
v_add_f32_e32 v41, 1.0, v41
v_mfma_f32_16x16x32_bf16 v[164:167], a[8:11], a[72:75], v[164:167]
ds_read_b64_tr_b16 a[28:29], v13 offset:24584
v_rcp_f32_e32 v38, v38
v_mul_f32_e32 v42, v42, v80
v_mfma_f32_16x16x32_bf16 v[164:167], a[12:15], a[76:79], v[164:167]
ds_read_b64_tr_b16 a[30:31], v13 offset:28680
v_rcp_f32_e32 v39, v39
v_mul_f32_e32 v43, v43, v81
v_mfma_f32_16x16x32_bf16 v[196:199], a[8:11], a[80:83], v[196:199]
buffer_load_dwordx4 v24, s[40:43], s77 offen offset:2048 nt lds
v_rcp_f32_e32 v40, v40
v_mul_f32_e32 v44, v44, v82
v_mfma_f32_16x16x32_bf16 v[196:199], a[12:15], a[84:87], v[196:199]
v_rcp_f32_e32 v41, v41
v_mfma_f32_16x16x32_bf16 v[228:231], a[8:11], a[88:91], v[228:231]
buffer_load_dwordx4 v25, s[40:43], s77 offen offset:3072 nt lds
s_add_u32 s77, s77, 0x4000
v_mul_f32_e32 v45, v45, v83
v_mul_f32_e32 v42, v42, v38
v_mul_f32_e32 v43, v43, v39
v_mfma_f32_16x16x32_bf16 v[228:231], a[12:15], a[92:95], v[228:231]
s_waitcnt lgkmcnt(4)
v_mul_f32_e32 v44, v44, v40
v_mul_f32_e32 v45, v45, v41
v_mfma_f32_16x16x32_bf16 v[136:139], a[16:19], a[64:67], v[136:139]
ds_read_b64_tr_b16 a[0:1], v14 offset:16384
v_cvt_pk_bf16_f32 v30, v42, v43
v_cvt_pk_bf16_f32 v31, v44, v45
v_mul_f32_e32 v38, s86, v84
v_mfma_f32_16x16x32_bf16 v[136:139], a[20:23], a[68:71], v[136:139]
ds_read_b64_tr_b16 a[2:3], v14 offset:20480
v_mul_f32_e32 v39, s86, v85
v_mul_f32_e32 v40, s86, v86
v_mfma_f32_16x16x32_bf16 v[168:171], a[16:19], a[72:75], v[168:171]
ds_read_b64_tr_b16 a[4:5], v14 offset:24576
v_mul_f32_e32 v41, s86, v87
v_exp_f32_e32 v38, v38
v_mfma_f32_16x16x32_bf16 v[168:171], a[20:23], a[76:79], v[168:171]
ds_read_b64_tr_b16 a[6:7], v14 offset:28672
v_accvgpr_read_b32 v42, a212
v_mfma_f32_16x16x32_bf16 v[200:203], a[16:19], a[80:83], v[200:203]
v_exp_f32_e32 v39, v39
v_accvgpr_read_b32 v43, a213
v_mfma_f32_16x16x32_bf16 v[200:203], a[20:23], a[84:87], v[200:203]
v_exp_f32_e32 v40, v40
v_accvgpr_read_b32 v44, a214
v_mfma_f32_16x16x32_bf16 v[232:235], a[16:19], a[88:91], v[232:235]
v_exp_f32_e32 v41, v41
v_accvgpr_read_b32 v45, a215
v_mfma_f32_16x16x32_bf16 v[232:235], a[20:23], a[92:95], v[232:235]
s_waitcnt lgkmcnt(4)
v_add_f32_e32 v38, 1.0, v38
v_add_f32_e32 v39, 1.0, v39
v_add_f32_e32 v40, 1.0, v40
v_mfma_f32_16x16x32_bf16 v[140:143], a[24:27], a[64:67], v[140:143]
ds_read_b64_tr_b16 a[8:9], v14 offset:16392
v_add_f32_e32 v41, 1.0, v41
v_mfma_f32_16x16x32_bf16 v[140:143], a[28:31], a[68:71], v[140:143]
ds_read_b64_tr_b16 a[10:11], v14 offset:20488
v_rcp_f32_e32 v38, v38
v_mul_f32_e32 v42, v42, v84
v_mfma_f32_16x16x32_bf16 v[172:175], a[24:27], a[72:75], v[172:175]
ds_read_b64_tr_b16 a[12:13], v14 offset:24584
v_rcp_f32_e32 v39, v39
v_mul_f32_e32 v43, v43, v85
v_mfma_f32_16x16x32_bf16 v[172:175], a[28:31], a[76:79], v[172:175]
ds_read_b64_tr_b16 a[14:15], v14 offset:28680
v_rcp_f32_e32 v40, v40
v_mul_f32_e32 v44, v44, v86
v_mfma_f32_16x16x32_bf16 v[204:207], a[24:27], a[80:83], v[204:207]
v_rcp_f32_e32 v41, v41
v_mfma_f32_16x16x32_bf16 v[204:207], a[28:31], a[84:87], v[204:207]
v_mul_f32_e32 v45, v45, v87
v_mul_f32_e32 v42, v42, v38
v_mul_f32_e32 v43, v43, v39
v_mfma_f32_16x16x32_bf16 v[236:239], a[24:27], a[88:91], v[236:239]
v_mul_f32_e32 v44, v44, v40
v_mul_f32_e32 v45, v45, v41
v_mfma_f32_16x16x32_bf16 v[236:239], a[28:31], a[92:95], v[236:239]
s_waitcnt lgkmcnt(4)
v_cvt_pk_bf16_f32 v32, v42, v43
v_cvt_pk_bf16_f32 v33, v44, v45
v_mfma_f32_16x16x32_bf16 v[144:147], a[0:3], a[64:67], v[144:147]
ds_read_b64_tr_b16 a[16:17], v15 offset:16384
v_mfma_f32_16x16x32_bf16 v[144:147], a[4:7], a[68:71], v[144:147]
ds_read_b64_tr_b16 a[18:19], v15 offset:20480
v_mfma_f32_16x16x32_bf16 v[176:179], a[0:3], a[72:75], v[176:179]
ds_read_b64_tr_b16 a[20:21], v15 offset:24576
v_mfma_f32_16x16x32_bf16 v[176:179], a[4:7], a[76:79], v[176:179]
ds_read_b64_tr_b16 a[22:23], v15 offset:28672
v_mfma_f32_16x16x32_bf16 v[208:211], a[0:3], a[80:83], v[208:211]
buffer_store_dwordx4 v[30:33], v29, s[64:67], s91 offen offset:0 sc1
v_mul_f32_e32 v38, s86, v88
v_mul_f32_e32 v39, s86, v89
v_mul_f32_e32 v40, s86, v90
v_mul_f32_e32 v41, s86, v91
v_exp_f32_e32 v38, v38
v_accvgpr_read_b32 v42, a216
v_exp_f32_e32 v39, v39
v_accvgpr_read_b32 v43, a217
v_exp_f32_e32 v40, v40
v_mfma_f32_16x16x32_bf16 v[208:211], a[4:7], a[84:87], v[208:211]
v_accvgpr_read_b32 v44, a218
v_mfma_f32_16x16x32_bf16 v[240:243], a[0:3], a[88:91], v[240:243]
v_exp_f32_e32 v41, v41
v_accvgpr_read_b32 v45, a219
v_add_f32_e32 v38, 1.0, v38
v_mfma_f32_16x16x32_bf16 v[240:243], a[4:7], a[92:95], v[240:243]
s_waitcnt lgkmcnt(4)
v_add_f32_e32 v39, 1.0, v39
v_add_f32_e32 v40, 1.0, v40
v_mfma_f32_16x16x32_bf16 v[148:151], a[8:11], a[64:67], v[148:151]
ds_read_b64_tr_b16 a[24:25], v15 offset:16392
v_add_f32_e32 v41, 1.0, v41
v_rcp_f32_e32 v38, v38
v_mfma_f32_16x16x32_bf16 v[148:151], a[12:15], a[68:71], v[148:151]
ds_read_b64_tr_b16 a[26:27], v15 offset:20488
v_mul_f32_e32 v42, v42, v88
v_mfma_f32_16x16x32_bf16 v[180:183], a[8:11], a[72:75], v[180:183]
ds_read_b64_tr_b16 a[28:29], v15 offset:24584
v_rcp_f32_e32 v39, v39
v_mul_f32_e32 v43, v43, v89
v_mfma_f32_16x16x32_bf16 v[180:183], a[12:15], a[76:79], v[180:183]
ds_read_b64_tr_b16 a[30:31], v15 offset:28680
v_rcp_f32_e32 v40, v40
v_mul_f32_e32 v44, v44, v90
v_mfma_f32_16x16x32_bf16 v[212:215], a[8:11], a[80:83], v[212:215]
v_rcp_f32_e32 v41, v41
v_mul_f32_e32 v45, v45, v91
v_mfma_f32_16x16x32_bf16 v[212:215], a[12:15], a[84:87], v[212:215]
v_mul_f32_e32 v42, v42, v38
v_mul_f32_e32 v43, v43, v39
v_mfma_f32_16x16x32_bf16 v[244:247], a[8:11], a[88:91], v[244:247]
v_mul_f32_e32 v44, v44, v40
v_mul_f32_e32 v45, v45, v41
v_cvt_pk_bf16_f32 v50, v42, v43
v_mfma_f32_16x16x32_bf16 v[244:247], a[12:15], a[92:95], v[244:247]
s_waitcnt lgkmcnt(4)
v_cvt_pk_bf16_f32 v51, v44, v45
v_mul_f32_e32 v38, s86, v92
v_mfma_f32_16x16x32_bf16 v[152:155], a[16:19], a[64:67], v[152:155]
ds_read_b64_tr_b16 a[0:1], v12 offset:32768
v_mul_f32_e32 v39, s86, v93
v_mul_f32_e32 v40, s86, v94
v_mul_f32_e32 v41, s86, v95
v_mfma_f32_16x16x32_bf16 v[152:155], a[20:23], a[68:71], v[152:155]
ds_read_b64_tr_b16 a[2:3], v12 offset:36864
v_exp_f32_e32 v38, v38
v_accvgpr_read_b32 v42, a220
v_mfma_f32_16x16x32_bf16 v[184:187], a[16:19], a[72:75], v[184:187]
ds_read_b64_tr_b16 a[4:5], v12 offset:40960
v_exp_f32_e32 v39, v39
v_mfma_f32_16x16x32_bf16 v[184:187], a[20:23], a[76:79], v[184:187]
ds_read_b64_tr_b16 a[6:7], v12 offset:45056
v_accvgpr_read_b32 v43, a221
v_exp_f32_e32 v40, v40
v_mfma_f32_16x16x32_bf16 v[216:219], a[16:19], a[80:83], v[216:219]
v_accvgpr_read_b32 v44, a222
v_mfma_f32_16x16x32_bf16 v[216:219], a[20:23], a[84:87], v[216:219]
v_exp_f32_e32 v41, v41
v_accvgpr_read_b32 v45, a223
v_add_f32_e32 v38, 1.0, v38
v_mfma_f32_16x16x32_bf16 v[248:251], a[16:19], a[88:91], v[248:251]
v_add_f32_e32 v39, 1.0, v39
v_add_f32_e32 v40, 1.0, v40
v_mfma_f32_16x16x32_bf16 v[248:251], a[20:23], a[92:95], v[248:251]
s_waitcnt lgkmcnt(4)
v_add_f32_e32 v41, 1.0, v41
v_rcp_f32_e32 v38, v38
v_mfma_f32_16x16x32_bf16 v[156:159], a[24:27], a[64:67], v[156:159]
ds_read_b64_tr_b16 a[8:9], v12 offset:32776
v_mul_f32_e32 v42, v42, v92
v_mfma_f32_16x16x32_bf16 v[156:159], a[28:31], a[68:71], v[156:159]
ds_read_b64_tr_b16 a[10:11], v12 offset:36872
v_rcp_f32_e32 v39, v39
v_mul_f32_e32 v43, v43, v93
v_mfma_f32_16x16x32_bf16 v[188:191], a[24:27], a[72:75], v[188:191]
ds_read_b64_tr_b16 a[12:13], v12 offset:40968
v_rcp_f32_e32 v40, v40
v_mul_f32_e32 v44, v44, v94
v_mfma_f32_16x16x32_bf16 v[188:191], a[28:31], a[76:79], v[188:191]
ds_read_b64_tr_b16 a[14:15], v12 offset:45064
v_rcp_f32_e32 v41, v41
v_mul_f32_e32 v45, v45, v95
v_mfma_f32_16x16x32_bf16 v[220:223], a[24:27], a[80:83], v[220:223]
v_mul_f32_e32 v42, v42, v38
v_mul_f32_e32 v43, v43, v39
v_mfma_f32_16x16x32_bf16 v[220:223], a[28:31], a[84:87], v[220:223]
s_barrier
v_mul_f32_e32 v44, v44, v40
v_mul_f32_e32 v45, v45, v41
v_cvt_pk_bf16_f32 v52, v42, v43
v_mfma_f32_16x16x32_bf16 v[252:255], a[24:27], a[88:91], v[252:255]
v_cvt_pk_bf16_f32 v53, v44, v45
v_mfma_f32_16x16x32_bf16 v[252:255], a[28:31], a[92:95], v[252:255]
s_waitcnt lgkmcnt(4)
v_mfma_f32_16x16x32_bf16 v[128:131], a[0:3], a[96:99], v[128:131]
ds_read_b64_tr_b16 a[16:17], v13 offset:32768
v_mfma_f32_16x16x32_bf16 v[128:131], a[4:7], a[100:103], v[128:131]
ds_read_b64_tr_b16 a[18:19], v13 offset:36864
v_mfma_f32_16x16x32_bf16 v[160:163], a[0:3], a[104:107], v[160:163]
ds_read_b64_tr_b16 a[20:21], v13 offset:40960
v_mfma_f32_16x16x32_bf16 v[160:163], a[4:7], a[108:111], v[160:163]
ds_read_b64_tr_b16 a[22:23], v13 offset:45056
v_mfma_f32_16x16x32_bf16 v[192:195], a[0:3], a[112:115], v[192:195]
s_add_u32 m0, s68, 0x14000
s_nop 0
buffer_load_dwordx4 v24, s[40:43], s77 offen offset:0 nt lds
v_mfma_f32_16x16x32_bf16 v[192:195], a[4:7], a[116:119], v[192:195]
buffer_store_dwordx4 v[50:53], v29, s[64:67], s91 offen offset:1024 sc1
v_mul_f32_e32 v38, s86, v96
v_mul_f32_e32 v39, s86, v97
v_mul_f32_e32 v40, s86, v98
v_mul_f32_e32 v41, s86, v99
v_exp_f32_e32 v38, v38
v_accvgpr_read_b32 v42, a224
v_exp_f32_e32 v39, v39
v_accvgpr_read_b32 v43, a225
v_exp_f32_e32 v40, v40
v_accvgpr_read_b32 v44, a226
v_exp_f32_e32 v41, v41
v_accvgpr_read_b32 v45, a227
v_add_f32_e32 v38, 1.0, v38
v_mfma_f32_16x16x32_bf16 v[224:227], a[0:3], a[120:123], v[224:227]
buffer_load_dwordx4 v25, s[40:43], s77 offen offset:1024 nt lds
v_add_f32_e32 v39, 1.0, v39
v_add_f32_e32 v40, 1.0, v40
v_add_f32_e32 v41, 1.0, v41
v_mfma_f32_16x16x32_bf16 v[224:227], a[4:7], a[124:127], v[224:227]
s_waitcnt lgkmcnt(4)
v_rcp_f32_e32 v38, v38
v_mfma_f32_16x16x32_bf16 v[132:135], a[8:11], a[96:99], v[132:135]
ds_read_b64_tr_b16 a[24:25], v13 offset:32776
v_mul_f32_e32 v42, v42, v96
v_rcp_f32_e32 v39, v39
v_mfma_f32_16x16x32_bf16 v[132:135], a[12:15], a[100:103], v[132:135]
ds_read_b64_tr_b16 a[26:27], v13 offset:36872
v_mul_f32_e32 v43, v43, v97
v_mfma_f32_16x16x32_bf16 v[164:167], a[8:11], a[104:107], v[164:167]
ds_read_b64_tr_b16 a[28:29], v13 offset:40968
v_rcp_f32_e32 v40, v40
v_mul_f32_e32 v44, v44, v98
v_mfma_f32_16x16x32_bf16 v[164:167], a[12:15], a[108:111], v[164:167]
ds_read_b64_tr_b16 a[30:31], v13 offset:45064
v_rcp_f32_e32 v41, v41
v_mul_f32_e32 v45, v45, v99
v_mfma_f32_16x16x32_bf16 v[196:199], a[8:11], a[112:115], v[196:199]
buffer_load_dwordx4 v24, s[40:43], s77 offen offset:2048 nt lds
v_mul_f32_e32 v42, v42, v38
v_mul_f32_e32 v43, v43, v39
v_mul_f32_e32 v44, v44, v40
v_mfma_f32_16x16x32_bf16 v[196:199], a[12:15], a[116:119], v[196:199]
v_mul_f32_e32 v45, v45, v41
v_cvt_pk_bf16_f32 v30, v42, v43
v_cvt_pk_bf16_f32 v31, v44, v45
v_mfma_f32_16x16x32_bf16 v[228:231], a[8:11], a[120:123], v[228:231]
buffer_load_dwordx4 v25, s[40:43], s77 offen offset:3072 nt lds
s_add_u32 s77, s77, 0x4000
v_mul_f32_e32 v38, s86, v100
v_mul_f32_e32 v39, s86, v101
v_mfma_f32_16x16x32_bf16 v[228:231], a[12:15], a[124:127], v[228:231]
s_waitcnt lgkmcnt(4)
v_mul_f32_e32 v40, s86, v102
v_mul_f32_e32 v41, s86, v103
v_mfma_f32_16x16x32_bf16 v[136:139], a[16:19], a[96:99], v[136:139]
ds_read_b64_tr_b16 a[0:1], v14 offset:32768
v_exp_f32_e32 v38, v38
v_accvgpr_read_b32 v42, a228
v_mfma_f32_16x16x32_bf16 v[136:139], a[20:23], a[100:103], v[136:139]
ds_read_b64_tr_b16 a[2:3], v14 offset:36864
v_exp_f32_e32 v39, v39
v_accvgpr_read_b32 v43, a229
v_mfma_f32_16x16x32_bf16 v[168:171], a[16:19], a[104:107], v[168:171]
ds_read_b64_tr_b16 a[4:5], v14 offset:40960
v_exp_f32_e32 v40, v40
v_mfma_f32_16x16x32_bf16 v[168:171], a[20:23], a[108:111], v[168:171]
ds_read_b64_tr_b16 a[6:7], v14 offset:45056
v_accvgpr_read_b32 v44, a230
v_exp_f32_e32 v41, v41
v_mfma_f32_16x16x32_bf16 v[200:203], a[16:19], a[112:115], v[200:203]
v_accvgpr_read_b32 v45, a231
v_add_f32_e32 v38, 1.0, v38
v_mfma_f32_16x16x32_bf16 v[200:203], a[20:23], a[116:119], v[200:203]
v_add_f32_e32 v39, 1.0, v39
v_add_f32_e32 v40, 1.0, v40
v_add_f32_e32 v41, 1.0, v41
v_mfma_f32_16x16x32_bf16 v[232:235], a[16:19], a[120:123], v[232:235]
v_rcp_f32_e32 v38, v38
v_mfma_f32_16x16x32_bf16 v[232:235], a[20:23], a[124:127], v[232:235]
s_waitcnt lgkmcnt(4)
v_mul_f32_e32 v42, v42, v100
v_rcp_f32_e32 v39, v39
v_mfma_f32_16x16x32_bf16 v[140:143], a[24:27], a[96:99], v[140:143]
ds_read_b64_tr_b16 a[8:9], v14 offset:32776
v_mul_f32_e32 v43, v43, v101
v_mfma_f32_16x16x32_bf16 v[140:143], a[28:31], a[100:103], v[140:143]
ds_read_b64_tr_b16 a[10:11], v14 offset:36872
v_rcp_f32_e32 v40, v40
v_mul_f32_e32 v44, v44, v102
v_mfma_f32_16x16x32_bf16 v[172:175], a[24:27], a[104:107], v[172:175]
ds_read_b64_tr_b16 a[12:13], v14 offset:40968
v_rcp_f32_e32 v41, v41
v_mul_f32_e32 v45, v45, v103
v_mul_f32_e32 v42, v42, v38
v_mfma_f32_16x16x32_bf16 v[172:175], a[28:31], a[108:111], v[172:175]
ds_read_b64_tr_b16 a[14:15], v14 offset:45064
v_mul_f32_e32 v43, v43, v39
v_mul_f32_e32 v44, v44, v40
v_mfma_f32_16x16x32_bf16 v[204:207], a[24:27], a[112:115], v[204:207]
v_mul_f32_e32 v45, v45, v41
v_cvt_pk_bf16_f32 v32, v42, v43
v_cvt_pk_bf16_f32 v33, v44, v45
v_mfma_f32_16x16x32_bf16 v[204:207], a[28:31], a[116:119], v[204:207]
buffer_store_dwordx4 v[30:33], v29, s[64:67], s92 offen offset:0 sc1
v_mfma_f32_16x16x32_bf16 v[236:239], a[24:27], a[120:123], v[236:239]
v_mul_f32_e32 v38, s86, v104
v_mul_f32_e32 v39, s86, v105
v_mul_f32_e32 v40, s86, v106
v_mfma_f32_16x16x32_bf16 v[236:239], a[28:31], a[124:127], v[236:239]
s_waitcnt lgkmcnt(4)
v_mul_f32_e32 v41, s86, v107
v_mfma_f32_16x16x32_bf16 v[144:147], a[0:3], a[96:99], v[144:147]
ds_read_b64_tr_b16 a[16:17], v15 offset:32768
v_exp_f32_e32 v38, v38
v_accvgpr_read_b32 v42, a232
v_mfma_f32_16x16x32_bf16 v[144:147], a[4:7], a[100:103], v[144:147]
ds_read_b64_tr_b16 a[18:19], v15 offset:36864
v_exp_f32_e32 v39, v39
v_accvgpr_read_b32 v43, a233
v_mfma_f32_16x16x32_bf16 v[176:179], a[0:3], a[104:107], v[176:179]
ds_read_b64_tr_b16 a[20:21], v15 offset:40960
v_exp_f32_e32 v40, v40
v_accvgpr_read_b32 v44, a234
v_mfma_f32_16x16x32_bf16 v[176:179], a[4:7], a[108:111], v[176:179]
ds_read_b64_tr_b16 a[22:23], v15 offset:45056
v_exp_f32_e32 v41, v41
v_mfma_f32_16x16x32_bf16 v[208:211], a[0:3], a[112:115], v[208:211]
v_accvgpr_read_b32 v45, a235
v_add_f32_e32 v38, 1.0, v38
v_add_f32_e32 v39, 1.0, v39
v_mfma_f32_16x16x32_bf16 v[208:211], a[4:7], a[116:119], v[208:211]
v_add_f32_e32 v40, 1.0, v40
v_add_f32_e32 v41, 1.0, v41
v_mfma_f32_16x16x32_bf16 v[240:243], a[0:3], a[120:123], v[240:243]
v_rcp_f32_e32 v38, v38
v_mul_f32_e32 v42, v42, v104
v_mfma_f32_16x16x32_bf16 v[240:243], a[4:7], a[124:127], v[240:243]
s_waitcnt lgkmcnt(4)
v_rcp_f32_e32 v39, v39
v_mul_f32_e32 v43, v43, v105
v_mfma_f32_16x16x32_bf16 v[148:151], a[8:11], a[96:99], v[148:151]
ds_read_b64_tr_b16 a[24:25], v15 offset:32776
v_rcp_f32_e32 v40, v40
v_mfma_f32_16x16x32_bf16 v[148:151], a[12:15], a[100:103], v[148:151]
ds_read_b64_tr_b16 a[26:27], v15 offset:36872
v_mul_f32_e32 v44, v44, v106
v_rcp_f32_e32 v41, v41
v_mfma_f32_16x16x32_bf16 v[180:183], a[8:11], a[104:107], v[180:183]
ds_read_b64_tr_b16 a[28:29], v15 offset:40968
v_mul_f32_e32 v45, v45, v107
v_mul_f32_e32 v42, v42, v38
v_mfma_f32_16x16x32_bf16 v[180:183], a[12:15], a[108:111], v[180:183]
ds_read_b64_tr_b16 a[30:31], v15 offset:45064
v_mul_f32_e32 v43, v43, v39
v_mul_f32_e32 v44, v44, v40
v_mul_f32_e32 v45, v45, v41
v_mfma_f32_16x16x32_bf16 v[212:215], a[8:11], a[112:115], v[212:215]
v_cvt_pk_bf16_f32 v50, v42, v43
v_cvt_pk_bf16_f32 v51, v44, v45
v_mfma_f32_16x16x32_bf16 v[212:215], a[12:15], a[116:119], v[212:215]
v_mul_f32_e32 v38, s86, v108
v_mul_f32_e32 v39, s86, v109
v_mul_f32_e32 v40, s86, v110
v_mfma_f32_16x16x32_bf16 v[244:247], a[8:11], a[120:123], v[244:247]
v_mul_f32_e32 v41, s86, v111
v_mfma_f32_16x16x32_bf16 v[244:247], a[12:15], a[124:127], v[244:247]
s_waitcnt lgkmcnt(4)
v_exp_f32_e32 v38, v38
v_accvgpr_read_b32 v42, a236
v_mfma_f32_16x16x32_bf16 v[152:155], a[16:19], a[96:99], v[152:155]
ds_read_b64_tr_b16 a[0:1], v12 offset:49152
v_exp_f32_e32 v39, v39
v_accvgpr_read_b32 v43, a237
v_mfma_f32_16x16x32_bf16 v[152:155], a[20:23], a[100:103], v[152:155]
ds_read_b64_tr_b16 a[2:3], v12 offset:53248
v_exp_f32_e32 v40, v40
v_accvgpr_read_b32 v44, a238
v_mfma_f32_16x16x32_bf16 v[184:187], a[16:19], a[104:107], v[184:187]
ds_read_b64_tr_b16 a[4:5], v12 offset:57344
v_exp_f32_e32 v41, v41
v_mfma_f32_16x16x32_bf16 v[184:187], a[20:23], a[108:111], v[184:187]
ds_read_b64_tr_b16 a[6:7], v12 offset:61440
v_accvgpr_read_b32 v45, a239
v_add_f32_e32 v38, 1.0, v38
v_add_f32_e32 v39, 1.0, v39
v_mfma_f32_16x16x32_bf16 v[216:219], a[16:19], a[112:115], v[216:219]
v_add_f32_e32 v40, 1.0, v40
v_add_f32_e32 v41, 1.0, v41
v_mfma_f32_16x16x32_bf16 v[216:219], a[20:23], a[116:119], v[216:219]
v_rcp_f32_e32 v38, v38
v_mul_f32_e32 v42, v42, v108
v_mfma_f32_16x16x32_bf16 v[248:251], a[16:19], a[120:123], v[248:251]
v_rcp_f32_e32 v39, v39
v_mul_f32_e32 v43, v43, v109
v_mfma_f32_16x16x32_bf16 v[248:251], a[20:23], a[124:127], v[248:251]
s_waitcnt lgkmcnt(4)
v_rcp_f32_e32 v40, v40
v_mfma_f32_16x16x32_bf16 v[156:159], a[24:27], a[96:99], v[156:159]
ds_read_b64_tr_b16 a[8:9], v12 offset:49160
v_mul_f32_e32 v44, v44, v110
v_rcp_f32_e32 v41, v41
v_mfma_f32_16x16x32_bf16 v[156:159], a[28:31], a[100:103], v[156:159]
ds_read_b64_tr_b16 a[10:11], v12 offset:53256
v_mul_f32_e32 v45, v45, v111
v_mul_f32_e32 v42, v42, v38
v_mfma_f32_16x16x32_bf16 v[188:191], a[24:27], a[104:107], v[188:191]
ds_read_b64_tr_b16 a[12:13], v12 offset:57352
v_mul_f32_e32 v43, v43, v39
v_mul_f32_e32 v44, v44, v40
v_mul_f32_e32 v45, v45, v41
v_mfma_f32_16x16x32_bf16 v[188:191], a[28:31], a[108:111], v[188:191]
ds_read_b64_tr_b16 a[14:15], v12 offset:61448
v_cvt_pk_bf16_f32 v52, v42, v43
v_cvt_pk_bf16_f32 v53, v44, v45
v_mfma_f32_16x16x32_bf16 v[220:223], a[24:27], a[112:115], v[220:223]
buffer_store_dwordx4 v[50:53], v29, s[64:67], s92 offen offset:1024 sc1
v_mul_f32_e32 v38, s86, v112
v_mfma_f32_16x16x32_bf16 v[220:223], a[28:31], a[116:119], v[220:223]
s_barrier
v_mul_f32_e32 v39, s86, v113
v_mul_f32_e32 v40, s86, v114
v_mfma_f32_16x16x32_bf16 v[252:255], a[24:27], a[120:123], v[252:255]
v_mul_f32_e32 v41, s86, v115
v_exp_f32_e32 v38, v38
v_mfma_f32_16x16x32_bf16 v[252:255], a[28:31], a[124:127], v[252:255]
s_waitcnt lgkmcnt(4)
v_accvgpr_read_b32 v42, a240
v_mfma_f32_16x16x32_bf16 v[128:131], a[0:3], a[128:131], v[128:131]
ds_read_b64_tr_b16 a[16:17], v13 offset:49152
v_exp_f32_e32 v39, v39
v_accvgpr_read_b32 v43, a241
v_mfma_f32_16x16x32_bf16 v[128:131], a[4:7], a[132:135], v[128:131]
ds_read_b64_tr_b16 a[18:19], v13 offset:53248
v_exp_f32_e32 v40, v40
v_accvgpr_read_b32 v44, a242
v_mfma_f32_16x16x32_bf16 v[160:163], a[0:3], a[136:139], v[160:163]
ds_read_b64_tr_b16 a[20:21], v13 offset:57344
v_exp_f32_e32 v41, v41
v_accvgpr_read_b32 v45, a243
v_mfma_f32_16x16x32_bf16 v[160:163], a[4:7], a[140:143], v[160:163]
ds_read_b64_tr_b16 a[22:23], v13 offset:61440
v_add_f32_e32 v38, 1.0, v38
v_add_f32_e32 v39, 1.0, v39
v_add_f32_e32 v40, 1.0, v40
v_mfma_f32_16x16x32_bf16 v[192:195], a[0:3], a[144:147], v[192:195]
s_add_u32 m0, s68, 0x18000
s_nop 0
buffer_load_dwordx4 v24, s[40:43], s77 offen offset:0 nt lds
v_add_f32_e32 v41, 1.0, v41
v_mfma_f32_16x16x32_bf16 v[192:195], a[4:7], a[148:151], v[192:195]
v_rcp_f32_e32 v38, v38
v_mul_f32_e32 v42, v42, v112
v_mfma_f32_16x16x32_bf16 v[224:227], a[0:3], a[152:155], v[224:227]
buffer_load_dwordx4 v25, s[40:43], s77 offen offset:1024 nt lds
v_rcp_f32_e32 v39, v39
v_mul_f32_e32 v43, v43, v113
v_mfma_f32_16x16x32_bf16 v[224:227], a[4:7], a[156:159], v[224:227]
s_waitcnt lgkmcnt(4)
v_rcp_f32_e32 v40, v40
v_mul_f32_e32 v44, v44, v114
v_mfma_f32_16x16x32_bf16 v[132:135], a[8:11], a[128:131], v[132:135]
ds_read_b64_tr_b16 a[24:25], v13 offset:49160
v_rcp_f32_e32 v41, v41
v_mfma_f32_16x16x32_bf16 v[132:135], a[12:15], a[132:135], v[132:135]
ds_read_b64_tr_b16 a[26:27], v13 offset:53256
v_mul_f32_e32 v45, v45, v115
v_mul_f32_e32 v42, v42, v38
v_mul_f32_e32 v43, v43, v39
v_mfma_f32_16x16x32_bf16 v[164:167], a[8:11], a[136:139], v[164:167]
ds_read_b64_tr_b16 a[28:29], v13 offset:57352
v_mul_f32_e32 v44, v44, v40
v_mul_f32_e32 v45, v45, v41
v_mfma_f32_16x16x32_bf16 v[164:167], a[12:15], a[140:143], v[164:167]
ds_read_b64_tr_b16 a[30:31], v13 offset:61448
v_cvt_pk_bf16_f32 v30, v42, v43
v_cvt_pk_bf16_f32 v31, v44, v45
v_mul_f32_e32 v38, s86, v116
v_mfma_f32_16x16x32_bf16 v[196:199], a[8:11], a[144:147], v[196:199]
buffer_load_dwordx4 v24, s[40:43], s77 offen offset:2048 nt lds
v_mul_f32_e32 v39, s86, v117
v_mul_f32_e32 v40, s86, v118
v_mfma_f32_16x16x32_bf16 v[196:199], a[12:15], a[148:151], v[196:199]
v_mul_f32_e32 v41, s86, v119
v_exp_f32_e32 v38, v38
v_mfma_f32_16x16x32_bf16 v[228:231], a[8:11], a[152:155], v[228:231]
buffer_load_dwordx4 v25, s[40:43], s77 offen offset:3072 nt lds
s_add_u32 s77, s77, 0x4000
v_accvgpr_read_b32 v42, a244
v_mfma_f32_16x16x32_bf16 v[228:231], a[12:15], a[156:159], v[228:231]
s_waitcnt lgkmcnt(4)
v_exp_f32_e32 v39, v39
v_accvgpr_read_b32 v43, a245
v_mfma_f32_16x16x32_bf16 v[136:139], a[16:19], a[128:131], v[136:139]
ds_read_b64_tr_b16 a[0:1], v14 offset:49152
v_exp_f32_e32 v40, v40
v_accvgpr_read_b32 v44, a246
v_mfma_f32_16x16x32_bf16 v[136:139], a[20:23], a[132:135], v[136:139]
ds_read_b64_tr_b16 a[2:3], v14 offset:53248
v_exp_f32_e32 v41, v41
v_accvgpr_read_b32 v45, a247
v_mfma_f32_16x16x32_bf16 v[168:171], a[16:19], a[136:139], v[168:171]
ds_read_b64_tr_b16 a[4:5], v14 offset:57344
v_add_f32_e32 v38, 1.0, v38
v_add_f32_e32 v39, 1.0, v39
v_add_f32_e32 v40, 1.0, v40
v_mfma_f32_16x16x32_bf16 v[168:171], a[20:23], a[140:143], v[168:171]
ds_read_b64_tr_b16 a[6:7], v14 offset:61440
v_add_f32_e32 v41, 1.0, v41
v_mfma_f32_16x16x32_bf16 v[200:203], a[16:19], a[144:147], v[200:203]
v_rcp_f32_e32 v38, v38
v_mul_f32_e32 v42, v42, v116
v_mfma_f32_16x16x32_bf16 v[200:203], a[20:23], a[148:151], v[200:203]
v_rcp_f32_e32 v39, v39
v_mul_f32_e32 v43, v43, v117
v_mfma_f32_16x16x32_bf16 v[232:235], a[16:19], a[152:155], v[232:235]
v_rcp_f32_e32 v40, v40
v_mul_f32_e32 v44, v44, v118
v_mfma_f32_16x16x32_bf16 v[232:235], a[20:23], a[156:159], v[232:235]
s_waitcnt lgkmcnt(4)
v_rcp_f32_e32 v41, v41
v_mfma_f32_16x16x32_bf16 v[140:143], a[24:27], a[128:131], v[140:143]
ds_read_b64_tr_b16 a[8:9], v14 offset:49160
v_mul_f32_e32 v45, v45, v119
v_mul_f32_e32 v42, v42, v38
v_mul_f32_e32 v43, v43, v39
v_mfma_f32_16x16x32_bf16 v[140:143], a[28:31], a[132:135], v[140:143]
ds_read_b64_tr_b16 a[10:11], v14 offset:53256
v_mul_f32_e32 v44, v44, v40
v_mul_f32_e32 v45, v45, v41
v_mfma_f32_16x16x32_bf16 v[172:175], a[24:27], a[136:139], v[172:175]
ds_read_b64_tr_b16 a[12:13], v14 offset:57352
v_cvt_pk_bf16_f32 v32, v42, v43
v_cvt_pk_bf16_f32 v33, v44, v45
v_mfma_f32_16x16x32_bf16 v[172:175], a[28:31], a[140:143], v[172:175]
ds_read_b64_tr_b16 a[14:15], v14 offset:61448
v_mfma_f32_16x16x32_bf16 v[204:207], a[24:27], a[144:147], v[204:207]
buffer_store_dwordx4 v[30:33], v29, s[64:67], s93 offen offset:0 sc1
v_mul_f32_e32 v38, s86, v120
v_mul_f32_e32 v39, s86, v121
v_mul_f32_e32 v40, s86, v122
v_mul_f32_e32 v41, s86, v123
v_mfma_f32_16x16x32_bf16 v[204:207], a[28:31], a[148:151], v[204:207]
v_exp_f32_e32 v38, v38
v_mfma_f32_16x16x32_bf16 v[236:239], a[24:27], a[152:155], v[236:239]
v_accvgpr_read_b32 v42, a248
v_exp_f32_e32 v39, v39
v_mfma_f32_16x16x32_bf16 v[236:239], a[28:31], a[156:159], v[236:239]
s_waitcnt lgkmcnt(4)
v_accvgpr_read_b32 v43, a249
v_exp_f32_e32 v40, v40
v_mfma_f32_16x16x32_bf16 v[144:147], a[0:3], a[128:131], v[144:147]
ds_read_b64_tr_b16 a[16:17], v15 offset:49152
v_accvgpr_read_b32 v44, a250
v_mfma_f32_16x16x32_bf16 v[144:147], a[4:7], a[132:135], v[144:147]
ds_read_b64_tr_b16 a[18:19], v15 offset:53248
v_exp_f32_e32 v41, v41
v_accvgpr_read_b32 v45, a251
v_add_f32_e32 v38, 1.0, v38
v_mfma_f32_16x16x32_bf16 v[176:179], a[0:3], a[136:139], v[176:179]
ds_read_b64_tr_b16 a[20:21], v15 offset:57344
v_add_f32_e32 v39, 1.0, v39
v_add_f32_e32 v40, 1.0, v40
v_mfma_f32_16x16x32_bf16 v[176:179], a[4:7], a[140:143], v[176:179]
ds_read_b64_tr_b16 a[22:23], v15 offset:61440
v_add_f32_e32 v41, 1.0, v41
v_rcp_f32_e32 v38, v38
v_mfma_f32_16x16x32_bf16 v[208:211], a[0:3], a[144:147], v[208:211]
v_mul_f32_e32 v42, v42, v120
v_mfma_f32_16x16x32_bf16 v[208:211], a[4:7], a[148:151], v[208:211]
v_rcp_f32_e32 v39, v39
v_mul_f32_e32 v43, v43, v121
v_mfma_f32_16x16x32_bf16 v[240:243], a[0:3], a[152:155], v[240:243]
v_rcp_f32_e32 v40, v40
v_mul_f32_e32 v44, v44, v122
v_mfma_f32_16x16x32_bf16 v[240:243], a[4:7], a[156:159], v[240:243]
s_waitcnt lgkmcnt(4)
v_rcp_f32_e32 v41, v41
v_mul_f32_e32 v45, v45, v123
v_mfma_f32_16x16x32_bf16 v[148:151], a[8:11], a[128:131], v[148:151]
ds_read_b64_tr_b16 a[24:25], v15 offset:49160
v_mul_f32_e32 v42, v42, v38
v_mul_f32_e32 v43, v43, v39
v_mfma_f32_16x16x32_bf16 v[148:151], a[12:15], a[132:135], v[148:151]
ds_read_b64_tr_b16 a[26:27], v15 offset:53256
v_mul_f32_e32 v44, v44, v40
v_mul_f32_e32 v45, v45, v41
v_cvt_pk_bf16_f32 v50, v42, v43
v_mfma_f32_16x16x32_bf16 v[180:183], a[8:11], a[136:139], v[180:183]
ds_read_b64_tr_b16 a[28:29], v15 offset:57352
v_cvt_pk_bf16_f32 v51, v44, v45
v_mul_f32_e32 v38, s86, v124
v_mfma_f32_16x16x32_bf16 v[180:183], a[12:15], a[140:143], v[180:183]
ds_read_b64_tr_b16 a[30:31], v15 offset:61448
v_mul_f32_e32 v39, s86, v125
v_mul_f32_e32 v40, s86, v126
v_mul_f32_e32 v41, s86, v127
v_mfma_f32_16x16x32_bf16 v[212:215], a[8:11], a[144:147], v[212:215]
v_exp_f32_e32 v38, v38
v_accvgpr_read_b32 v42, a252
v_mfma_f32_16x16x32_bf16 v[212:215], a[12:15], a[148:151], v[212:215]
v_exp_f32_e32 v39, v39
v_mfma_f32_16x16x32_bf16 v[244:247], a[8:11], a[152:155], v[244:247]
v_accvgpr_read_b32 v43, a253
v_exp_f32_e32 v40, v40
v_mfma_f32_16x16x32_bf16 v[244:247], a[12:15], a[156:159], v[244:247]
s_waitcnt lgkmcnt(4)
v_accvgpr_read_b32 v44, a254
v_mfma_f32_16x16x32_bf16 v[152:155], a[16:19], a[128:131], v[152:155]
v_exp_f32_e32 v41, v41
v_accvgpr_read_b32 v45, a255
v_add_f32_e32 v38, 1.0, v38
v_mfma_f32_16x16x32_bf16 v[152:155], a[20:23], a[132:135], v[152:155]
v_add_f32_e32 v39, 1.0, v39
v_add_f32_e32 v40, 1.0, v40
v_mfma_f32_16x16x32_bf16 v[184:187], a[16:19], a[136:139], v[184:187]
v_add_f32_e32 v41, 1.0, v41
v_rcp_f32_e32 v38, v38
v_mfma_f32_16x16x32_bf16 v[184:187], a[20:23], a[140:143], v[184:187]
v_mul_f32_e32 v42, v42, v124
v_mfma_f32_16x16x32_bf16 v[216:219], a[16:19], a[144:147], v[216:219]
v_rcp_f32_e32 v39, v39
v_mul_f32_e32 v43, v43, v125
v_mfma_f32_16x16x32_bf16 v[216:219], a[20:23], a[148:151], v[216:219]
v_rcp_f32_e32 v40, v40
v_mul_f32_e32 v44, v44, v126
v_mfma_f32_16x16x32_bf16 v[248:251], a[16:19], a[152:155], v[248:251]
v_rcp_f32_e32 v41, v41
v_mul_f32_e32 v45, v45, v127
v_mfma_f32_16x16x32_bf16 v[248:251], a[20:23], a[156:159], v[248:251]
s_waitcnt lgkmcnt(0)
v_mul_f32_e32 v42, v42, v38
v_mul_f32_e32 v43, v43, v39
v_mfma_f32_16x16x32_bf16 v[156:159], a[24:27], a[128:131], v[156:159]
v_mul_f32_e32 v44, v44, v40
v_mul_f32_e32 v45, v45, v41
v_cvt_pk_bf16_f32 v52, v42, v43
v_mfma_f32_16x16x32_bf16 v[156:159], a[28:31], a[132:135], v[156:159]
v_cvt_pk_bf16_f32 v53, v44, v45
v_mfma_f32_16x16x32_bf16 v[188:191], a[24:27], a[136:139], v[188:191]
v_mfma_f32_16x16x32_bf16 v[188:191], a[28:31], a[140:143], v[188:191]
v_mfma_f32_16x16x32_bf16 v[220:223], a[24:27], a[144:147], v[220:223]
s_waitcnt vmcnt(13)
v_mfma_f32_16x16x32_bf16 v[220:223], a[28:31], a[148:151], v[220:223]
s_barrier
v_mfma_f32_16x16x32_bf16 v[252:255], a[24:27], a[152:155], v[252:255]
buffer_store_dwordx4 v[50:53], v29, s[64:67], s93 offen offset:1024 sc1
v_mfma_f32_16x16x32_bf16 v[252:255], a[28:31], a[156:159], v[252:255]
; ---- cvtL
ds_read_b128 a[128:131], v8 offset:0
ds_read_b128 a[132:135], v9 offset:0
ds_read_b128 a[136:139], v10 offset:0
ds_read_b128 a[140:143], v11 offset:0
ds_read_b128 a[144:147], v8 offset:4096
v_mov_b32_e32 v30, 0
v_mov_b32_e32 v31, 0
v_mov_b32_e32 v32, 0
v_mov_b32_e32 v33, 0
v_pk_mul_f32 v[128:129], v[128:129], s[84:85]
v_pk_mul_f32 v[130:131], v[130:131], s[84:85]
v_pk_mul_f32 v[132:133], v[132:133], s[84:85]
v_pk_mul_f32 v[134:135], v[134:135], s[84:85]
v_cvt_pk_bf16_f32 v128, v128, v129
v_cvt_pk_bf16_f32 v129, v130, v131
v_cvt_pk_bf16_f32 v130, v132, v133
v_cvt_pk_bf16_f32 v131, v134, v135
v_pk_mul_f32 v[136:137], v[136:137], s[84:85]
v_pk_mul_f32 v[138:139], v[138:139], s[84:85]
v_pk_mul_f32 v[140:141], v[140:141], s[84:85]
v_pk_mul_f32 v[142:143], v[142:143], s[84:85]
v_cvt_pk_bf16_f32 v132, v136, v137
v_cvt_pk_bf16_f32 v133, v138, v139
v_cvt_pk_bf16_f32 v134, v140, v141
v_cvt_pk_bf16_f32 v135, v142, v143
v_pk_mul_f32 v[144:145], v[144:145], s[84:85]
v_pk_mul_f32 v[146:147], v[146:147], s[84:85]
v_pk_mul_f32 v[148:149], v[148:149], s[84:85]
v_pk_mul_f32 v[150:151], v[150:151], s[84:85]
v_cvt_pk_bf16_f32 v136, v144, v145
v_cvt_pk_bf16_f32 v137, v146, v147
v_cvt_pk_bf16_f32 v138, v148, v149
v_cvt_pk_bf16_f32 v139, v150, v151
v_pk_mul_f32 v[152:153], v[152:153], s[84:85]
v_pk_mul_f32 v[154:155], v[154:155], s[84:85]
v_pk_mul_f32 v[156:157], v[156:157], s[84:85]
v_pk_mul_f32 v[158:159], v[158:159], s[84:85]
v_cvt_pk_bf16_f32 v140, v152, v153
v_cvt_pk_bf16_f32 v141, v154, v155
v_cvt_pk_bf16_f32 v142, v156, v157
v_cvt_pk_bf16_f32 v143, v158, v159
v_pk_mul_f32 v[160:161], v[160:161], s[84:85]
v_pk_mul_f32 v[162:163], v[162:163], s[84:85]
v_pk_mul_f32 v[164:165], v[164:165], s[84:85]
v_pk_mul_f32 v[166:167], v[166:167], s[84:85]
v_cvt_pk_bf16_f32 v144, v160, v161
v_cvt_pk_bf16_f32 v145, v162, v163
v_cvt_pk_bf16_f32 v146, v164, v165
v_cvt_pk_bf16_f32 v147, v166, v167
v_pk_mul_f32 v[168:169], v[168:169], s[84:85]
v_pk_mul_f32 v[170:171], v[170:171], s[84:85]
v_pk_mul_f32 v[172:173], v[172:173], s[84:85]
v_pk_mul_f32 v[174:175], v[174:175], s[84:85]
v_cvt_pk_bf16_f32 v148, v168, v169
v_cvt_pk_bf16_f32 v149, v170, v171
v_cvt_pk_bf16_f32 v150, v172, v173
v_cvt_pk_bf16_f32 v151, v174, v175
v_pk_mul_f32 v[176:177], v[176:177], s[84:85]
v_pk_mul_f32 v[178:179], v[178:179], s[84:85]
v_pk_mul_f32 v[180:181], v[180:181], s[84:85]
v_pk_mul_f32 v[182:183], v[182:183], s[84:85]
v_cvt_pk_bf16_f32 v152, v176, v177
v_cvt_pk_bf16_f32 v153, v178, v179
v_cvt_pk_bf16_f32 v154, v180, v181
v_cvt_pk_bf16_f32 v155, v182, v183
v_pk_mul_f32 v[184:185], v[184:185], s[84:85]
v_pk_mul_f32 v[186:187], v[186:187], s[84:85]
v_pk_mul_f32 v[188:189], v[188:189], s[84:85]
v_pk_mul_f32 v[190:191], v[190:191], s[84:85]
v_cvt_pk_bf16_f32 v156, v184, v185
v_cvt_pk_bf16_f32 v157, v186, v187
v_cvt_pk_bf16_f32 v158, v188, v189
v_cvt_pk_bf16_f32 v159, v190, v191
v_pk_mul_f32 v[192:193], v[192:193], s[84:85]
v_pk_mul_f32 v[194:195], v[194:195], s[84:85]
v_pk_mul_f32 v[196:197], v[196:197], s[84:85]
v_pk_mul_f32 v[198:199], v[198:199], s[84:85]
v_cvt_pk_bf16_f32 v160, v192, v193
v_cvt_pk_bf16_f32 v161, v194, v195
v_cvt_pk_bf16_f32 v162, v196, v197
v_cvt_pk_bf16_f32 v163, v198, v199
v_pk_mul_f32 v[200:201], v[200:201], s[84:85]
v_pk_mul_f32 v[202:203], v[202:203], s[84:85]
v_pk_mul_f32 v[204:205], v[204:205], s[84:85]
v_pk_mul_f32 v[206:207], v[206:207], s[84:85]
v_cvt_pk_bf16_f32 v164, v200, v201
v_cvt_pk_bf16_f32 v165, v202, v203
v_cvt_pk_bf16_f32 v166, v204, v205
v_cvt_pk_bf16_f32 v167, v206, v207
v_pk_mul_f32 v[208:209], v[208:209], s[84:85]
v_pk_mul_f32 v[210:211], v[210:211], s[84:85]
v_pk_mul_f32 v[212:213], v[212:213], s[84:85]
v_pk_mul_f32 v[214:215], v[214:215], s[84:85]
v_cvt_pk_bf16_f32 v168, v208, v209
v_cvt_pk_bf16_f32 v169, v210, v211
v_cvt_pk_bf16_f32 v170, v212, v213
v_cvt_pk_bf16_f32 v171, v214, v215
v_pk_mul_f32 v[216:217], v[216:217], s[84:85]
v_pk_mul_f32 v[218:219], v[218:219], s[84:85]
v_pk_mul_f32 v[220:221], v[220:221], s[84:85]
v_pk_mul_f32 v[222:223], v[222:223], s[84:85]
v_cvt_pk_bf16_f32 v172, v216, v217
v_cvt_pk_bf16_f32 v173, v218, v219
v_cvt_pk_bf16_f32 v174, v220, v221
v_cvt_pk_bf16_f32 v175, v222, v223
v_pk_mul_f32 v[224:225], v[224:225], s[84:85]
v_pk_mul_f32 v[226:227], v[226:227], s[84:85]
v_pk_mul_f32 v[228:229], v[228:229], s[84:85]
v_pk_mul_f32 v[230:231], v[230:231], s[84:85]
v_cvt_pk_bf16_f32 v176, v224, v225
v_cvt_pk_bf16_f32 v177, v226, v227
v_cvt_pk_bf16_f32 v178, v228, v229
v_cvt_pk_bf16_f32 v179, v230, v231
v_pk_mul_f32 v[232:233], v[232:233], s[84:85]
v_pk_mul_f32 v[234:235], v[234:235], s[84:85]
v_pk_mul_f32 v[236:237], v[236:237], s[84:85]
v_pk_mul_f32 v[238:239], v[238:239], s[84:85]
v_cvt_pk_bf16_f32 v180, v232, v233
v_cvt_pk_bf16_f32 v181, v234, v235
v_cvt_pk_bf16_f32 v182, v236, v237
v_cvt_pk_bf16_f32 v183, v238, v239
v_pk_mul_f32 v[240:241], v[240:241], s[84:85]
v_pk_mul_f32 v[242:243], v[242:243], s[84:85]
v_pk_mul_f32 v[244:245], v[244:245], s[84:85]
v_pk_mul_f32 v[246:247], v[246:247], s[84:85]
v_cvt_pk_bf16_f32 v184, v240, v241
v_cvt_pk_bf16_f32 v185, v242, v243
v_cvt_pk_bf16_f32 v186, v244, v245
v_cvt_pk_bf16_f32 v187, v246, v247
v_pk_mul_f32 v[248:249], v[248:249], s[84:85]
v_pk_mul_f32 v[250:251], v[250:251], s[84:85]
v_pk_mul_f32 v[252:253], v[252:253], s[84:85]
v_pk_mul_f32 v[254:255], v[254:255], s[84:85]
v_cvt_pk_bf16_f32 v188, v248, v249
v_cvt_pk_bf16_f32 v189, v250, v251
v_cvt_pk_bf16_f32 v190, v252, v253
v_cvt_pk_bf16_f32 v191, v254, v255
v_mfma_f32_32x32x16_bf16 a[0:15], v[34:37], v[34:37], 0
v_mfma_f32_32x32x16_bf16 a[16:31], v[34:37], v[34:37], 0
v_mfma_f32_32x32x16_bf16 a[32:47], v[34:37], v[34:37], 0
v_mfma_f32_32x32x16_bf16 a[48:63], v[34:37], v[34:37], 0
v_mfma_f32_32x32x16_bf16 a[64:79], v[34:37], v[34:37], 0
v_mfma_f32_32x32x16_bf16 a[80:95], v[34:37], v[34:37], 0
v_mfma_f32_32x32x16_bf16 a[96:111], v[34:37], v[34:37], 0
v_mfma_f32_32x32x16_bf16 a[112:127], v[34:37], v[34:37], 0
s_branch .Lkf_cvt
; ---- proS
.Lkf_proS:
v_mfma_f32_16x16x32_bf16 v[64:67], a[0:3], a[32:35], 0
ds_read_b64_tr_b16 a[16:17], v18 offset:0
v_mfma_f32_16x16x32_bf16 v[64:67], a[4:7], a[36:39], v[64:67]
ds_read_b64_tr_b16 a[18:19], v18 offset:4096
v_mfma_f32_16x16x32_bf16 v[80:83], a[0:3], a[40:43], 0
ds_read_b64_tr_b16 a[20:21], v18 offset:8192
v_mfma_f32_16x16x32_bf16 v[80:83], a[4:7], a[44:47], v[80:83]
ds_read_b64_tr_b16 a[22:23], v18 offset:12288
v_mfma_f32_16x16x32_bf16 v[96:99], a[0:3], a[48:51], 0
s_add_u32 m0, s68, 0x8000
s_add_u32 s79, s70, 0x6c00
buffer_load_dwordx4 v28, s[48:51], s70 offen offset:0 lds
v_mfma_f32_16x16x32_bf16 v[96:99], a[4:7], a[52:55], v[96:99]
buffer_load_dwordx4 v28, s[48:51], s79 offen offset:1024 lds
v_mfma_f32_16x16x32_bf16 v[112:115], a[0:3], a[56:59], 0
buffer_load_dwordx4 v28, s[52:55], s70 offen offset:2048 lds
v_mfma_f32_16x16x32_bf16 v[112:115], a[4:7], a[60:63], v[112:115]
s_waitcnt lgkmcnt(4)
v_mfma_f32_16x16x32_bf16 v[68:71], a[8:11], a[32:35], 0
ds_read_b64_tr_b16 a[24:25], v16 offset:2048
v_mfma_f32_16x16x32_bf16 v[68:71], a[12:15], a[36:39], v[68:71]
ds_read_b64_tr_b16 a[26:27], v16 offset:6144
v_mfma_f32_16x16x32_bf16 v[84:87], a[8:11], a[40:43], 0
ds_read_b64_tr_b16 a[28:29], v16 offset:10240
v_mfma_f32_16x16x32_bf16 v[84:87], a[12:15], a[44:47], v[84:87]
ds_read_b64_tr_b16 a[30:31], v16 offset:14336
v_mfma_f32_16x16x32_bf16 v[100:103], a[8:11], a[48:51], 0
buffer_load_dwordx4 v28, s[52:55], s79 offen offset:3072 lds
s_add_u32 s70, s70, 0x38000
v_mfma_f32_16x16x32_bf16 v[100:103], a[12:15], a[52:55], v[100:103]
s_add_u32 m0, s68, 0x18000
s_add_u32 s80, s69, 0x7c00
buffer_load_dwordx4 v26, s[44:47], s69 offen offset:0 lds
v_mfma_f32_16x16x32_bf16 v[116:119], a[8:11], a[56:59], 0
s_add_u32 s81, s69, 0xf800
buffer_load_dwordx4 v27, s[44:47], s80 offen offset:1024 lds
v_mfma_f32_16x16x32_bf16 v[116:119], a[12:15], a[60:63], v[116:119]
s_waitcnt lgkmcnt(4)
v_mfma_f32_16x16x32_bf16 v[72:75], a[16:19], a[32:35], 0
ds_read_b64_tr_b16 a[0:1], v17 offset:2048
v_mfma_f32_16x16x32_bf16 v[72:75], a[20:23], a[36:39], v[72:75]
ds_read_b64_tr_b16 a[2:3], v17 offset:6144
v_mfma_f32_16x16x32_bf16 v[88:91], a[16:19], a[40:43], 0
ds_read_b64_tr_b16 a[4:5], v17 offset:10240
v_mfma_f32_16x16x32_bf16 v[88:91], a[20:23], a[44:47], v[88:91]
ds_read_b64_tr_b16 a[6:7], v17 offset:14336
v_mfma_f32_16x16x32_bf16 v[104:107], a[16:19], a[48:51], 0
s_add_u32 s82, s69, 0x17400
buffer_load_dwordx4 v26, s[44:47], s81 offen offset:2048 lds
v_mfma_f32_16x16x32_bf16 v[104:107], a[20:23], a[52:55], v[104:107]
v_mfma_f32_16x16x32_bf16 v[120:123], a[16:19], a[56:59], 0
buffer_load_dwordx4 v27, s[44:47], s82 offen offset:3072 lds
s_add_u32 s69, s69, 0x80000
v_mfma_f32_16x16x32_bf16 v[120:123], a[20:23], a[60:63], v[120:123]
s_waitcnt lgkmcnt(4)
v_mfma_f32_16x16x32_bf16 a[192:195], a[24:27], a[32:35], 0
ds_read_b64_tr_b16 a[8:9], v18 offset:2048
v_mfma_f32_16x16x32_bf16 a[192:195], a[28:31], a[36:39], a[192:195]
ds_read_b64_tr_b16 a[10:11], v18 offset:6144
v_mfma_f32_16x16x32_bf16 a[208:211], a[24:27], a[40:43], 0
ds_read_b64_tr_b16 a[12:13], v18 offset:10240
v_mfma_f32_16x16x32_bf16 a[208:211], a[28:31], a[44:47], a[208:211]
ds_read_b64_tr_b16 a[14:15], v18 offset:14336
v_mfma_f32_16x16x32_bf16 a[224:227], a[24:27], a[48:51], 0
s_add_u32 m0, s68, 0xc000
s_add_u32 s79, s70, 0x6c00
buffer_load_dwordx4 v28, s[48:51], s70 offen offset:0 lds
v_mfma_f32_16x16x32_bf16 a[224:227], a[28:31], a[52:55], a[224:227]
v_mfma_f32_16x16x32_bf16 a[240:243], a[24:27], a[56:59], 0
buffer_load_dwordx4 v28, s[48:51], s79 offen offset:1024 lds
v_mfma_f32_16x16x32_bf16 a[240:243], a[28:31], a[60:63], a[240:243]
s_waitcnt lgkmcnt(4)
v_mfma_f32_16x16x32_bf16 a[196:199], a[0:3], a[32:35], 0
ds_read_b64_tr_b16 a[16:17], v12 offset:0
v_mfma_f32_16x16x32_bf16 a[196:199], a[4:7], a[36:39], a[196:199]
ds_read_b64_tr_b16 a[18:19], v12 offset:4096
v_mfma_f32_16x16x32_bf16 a[212:215], a[0:3], a[40:43], 0
ds_read_b64_tr_b16 a[20:21], v12 offset:8192
v_mfma_f32_16x16x32_bf16 a[212:215], a[4:7], a[44:47], a[212:215]
ds_read_b64_tr_b16 a[22:23], v12 offset:12288
v_mfma_f32_16x16x32_bf16 a[228:231], a[0:3], a[48:51], 0
buffer_load_dwordx4 v28, s[52:55], s70 offen offset:2048 lds
v_mfma_f32_16x16x32_bf16 a[228:231], a[4:7], a[52:55], a[228:231]
v_mfma_f32_16x16x32_bf16 a[244:247], a[0:3], a[56:59], 0
buffer_load_dwordx4 v28, s[52:55], s79 offen offset:3072 lds
s_add_u32 s70, s70, 0x38000
v_mfma_f32_16x16x32_bf16 a[244:247], a[4:7], a[60:63], a[244:247]
s_waitcnt lgkmcnt(4)
v_mfma_f32_16x16x32_bf16 a[200:203], a[8:11], a[32:35], 0
ds_read_b64_tr_b16 a[24:25], v12 offset:8
v_mfma_f32_16x16x32_bf16 a[200:203], a[12:15], a[36:39], a[200:203]
ds_read_b64_tr_b16 a[26:27], v12 offset:4104
v_mfma_f32_16x16x32_bf16 a[216:219], a[8:11], a[40:43], 0
ds_read_b64_tr_b16 a[28:29], v12 offset:8200
v_mfma_f32_16x16x32_bf16 a[216:219], a[12:15], a[44:47], a[216:219]
ds_read_b64_tr_b16 a[30:31], v12 offset:12296
v_mfma_f32_16x16x32_bf16 a[232:235], a[8:11], a[48:51], 0
s_add_u32 m0, s68, 0x1c000
s_add_u32 s80, s69, 0x7c00
buffer_load_dwordx4 v26, s[44:47], s69 offen offset:0 lds
v_mfma_f32_16x16x32_bf16 a[232:235], a[12:15], a[52:55], a[232:235]
v_mfma_f32_16x16x32_bf16 a[248:251], a[8:11], a[56:59], 0
s_add_u32 s81, s69, 0xf800
buffer_load_dwordx4 v27, s[44:47], s80 offen offset:1024 lds
v_mfma_f32_16x16x32_bf16 a[248:251], a[12:15], a[60:63], a[248:251]
s_waitcnt lgkmcnt(4)
v_mfma_f32_16x16x32_bf16 v[128:131], a[16:19], a[32:35], 0
ds_read_b64_tr_b16 a[0:1], v13 offset:0
v_mfma_f32_16x16x32_bf16 v[128:131], a[20:23], a[36:39], v[128:131]
ds_read_b64_tr_b16 a[2:3], v13 offset:4096
v_mfma_f32_16x16x32_bf16 v[160:163], a[16:19], a[40:43], 0
ds_read_b64_tr_b16 a[4:5], v13 offset:8192
v_mfma_f32_16x16x32_bf16 v[160:163], a[20:23], a[44:47], v[160:163]
ds_read_b64_tr_b16 a[6:7], v13 offset:12288
v_mfma_f32_16x16x32_bf16 v[192:195], a[16:19], a[48:51], 0
s_add_u32 s82, s69, 0x17400
buffer_load_dwordx4 v26, s[44:47], s81 offen offset:2048 lds
v_mfma_f32_16x16x32_bf16 v[192:195], a[20:23], a[52:55], v[192:195]
v_mfma_f32_16x16x32_bf16 v[224:227], a[16:19], a[56:59], 0
buffer_load_dwordx4 v27, s[44:47], s82 offen offset:3072 lds
s_add_u32 s69, s69, 0x80000
v_mfma_f32_16x16x32_bf16 v[224:227], a[20:23], a[60:63], v[224:227]
s_waitcnt lgkmcnt(4)
v_mfma_f32_16x16x32_bf16 v[132:135], a[24:27], a[32:35], 0
ds_read_b64_tr_b16 a[8:9], v13 offset:8
v_mfma_f32_16x16x32_bf16 v[132:135], a[28:31], a[36:39], v[132:135]
ds_read_b64_tr_b16 a[10:11], v13 offset:4104
v_mfma_f32_16x16x32_bf16 v[164:167], a[24:27], a[40:43], 0
ds_read_b64_tr_b16 a[12:13], v13 offset:8200
v_mfma_f32_16x16x32_bf16 v[164:167], a[28:31], a[44:47], v[164:167]
ds_read_b64_tr_b16 a[14:15], v13 offset:12296
v_mfma_f32_16x16x32_bf16 v[196:199], a[24:27], a[48:51], 0
buffer_load_dword v44, v26, s[44:47], s21 offen offset:0
v_mfma_f32_16x16x32_bf16 v[196:199], a[28:31], a[52:55], v[196:199]
v_mfma_f32_16x16x32_bf16 v[228:231], a[24:27], a[56:59], 0
buffer_load_dword v44, v26, s[44:47], s22 offen offset:0
v_mfma_f32_16x16x32_bf16 v[228:231], a[28:31], a[60:63], v[228:231]
s_waitcnt lgkmcnt(4)
v_mfma_f32_16x16x32_bf16 v[136:139], a[0:3], a[32:35], 0
ds_read_b64_tr_b16 a[16:17], v14 offset:0
v_mfma_f32_16x16x32_bf16 v[136:139], a[4:7], a[36:39], v[136:139]
ds_read_b64_tr_b16 a[18:19], v14 offset:4096
v_mfma_f32_16x16x32_bf16 v[168:171], a[0:3], a[40:43], 0
ds_read_b64_tr_b16 a[20:21], v14 offset:8192
v_mfma_f32_16x16x32_bf16 v[168:171], a[4:7], a[44:47], v[168:171]
ds_read_b64_tr_b16 a[22:23], v14 offset:12288
v_mfma_f32_16x16x32_bf16 v[200:203], a[0:3], a[48:51], 0
buffer_load_dword v44, v28, s[48:51], s23 offen offset:0
v_mfma_f32_16x16x32_bf16 v[200:203], a[4:7], a[52:55], v[200:203]
v_mfma_f32_16x16x32_bf16 v[232:235], a[0:3], a[56:59], 0
buffer_load_dword v44, v28, s[52:55], s23 offen offset:2048
v_mfma_f32_16x16x32_bf16 v[232:235], a[4:7], a[60:63], v[232:235]
s_waitcnt lgkmcnt(4)
v_mfma_f32_16x16x32_bf16 v[140:143], a[8:11], a[32:35], 0
ds_read_b64_tr_b16 a[24:25], v14 offset:8
v_mfma_f32_16x16x32_bf16 v[140:143], a[12:15], a[36:39], v[140:143]
ds_read_b64_tr_b16 a[26:27], v14 offset:4104
v_mfma_f32_16x16x32_bf16 v[172:175], a[8:11], a[40:43], 0
ds_read_b64_tr_b16 a[28:29], v14 offset:8200
v_mfma_f32_16x16x32_bf16 v[172:175], a[12:15], a[44:47], v[172:175]
ds_read_b64_tr_b16 a[30:31], v14 offset:12296
v_mfma_f32_16x16x32_bf16 v[204:207], a[8:11], a[48:51], 0
buffer_load_dwordx4 a[96:99], v29, s[56:59], s90 offen offset:0 sc1
v_mfma_f32_16x16x32_bf16 v[204:207], a[12:15], a[52:55], v[204:207]
v_mfma_f32_16x16x32_bf16 v[236:239], a[8:11], a[56:59], 0
buffer_load_dwordx4 a[100:103], v29, s[56:59], s90 offen offset:1024 sc1
v_mfma_f32_16x16x32_bf16 v[236:239], a[12:15], a[60:63], v[236:239]
s_waitcnt lgkmcnt(4)
v_mfma_f32_16x16x32_bf16 v[144:147], a[16:19], a[32:35], 0
ds_read_b64_tr_b16 a[0:1], v15 offset:0
v_mfma_f32_16x16x32_bf16 v[144:147], a[20:23], a[36:39], v[144:147]
ds_read_b64_tr_b16 a[2:3], v15 offset:4096
v_mfma_f32_16x16x32_bf16 v[176:179], a[16:19], a[40:43], 0
ds_read_b64_tr_b16 a[4:5], v15 offset:8192
v_mfma_f32_16x16x32_bf16 v[176:179], a[20:23], a[44:47], v[176:179]
ds_read_b64_tr_b16 a[6:7], v15 offset:12288
v_mfma_f32_16x16x32_bf16 v[208:211], a[16:19], a[48:51], 0
buffer_load_dwordx4 a[104:107], v29, s[56:59], s91 offen offset:0 sc1
v_mfma_f32_16x16x32_bf16 v[208:211], a[20:23], a[52:55], v[208:211]
v_mfma_f32_16x16x32_bf16 v[240:243], a[16:19], a[56:59], 0
buffer_load_dwordx4 a[108:111], v29, s[56:59], s91 offen offset:1024 sc1
v_mfma_f32_16x16x32_bf16 v[240:243], a[20:23], a[60:63], v[240:243]
s_waitcnt lgkmcnt(4)
v_mfma_f32_16x16x32_bf16 v[148:151], a[24:27], a[32:35], 0
ds_read_b64_tr_b16 a[8:9], v15 offset:8
v_mfma_f32_16x16x32_bf16 v[148:151], a[28:31], a[36:39], v[148:151]
ds_read_b64_tr_b16 a[10:11], v15 offset:4104
v_mfma_f32_16x16x32_bf16 v[180:183], a[24:27], a[40:43], 0
ds_read_b64_tr_b16 a[12:13], v15 offset:8200
v_mfma_f32_16x16x32_bf16 v[180:183], a[28:31], a[44:47], v[180:183]
ds_read_b64_tr_b16 a[14:15], v15 offset:12296
v_mfma_f32_16x16x32_bf16 v[212:215], a[24:27], a[48:51], 0
buffer_load_dwordx4 a[112:115], v29, s[56:59], s92 offen offset:0 sc1
v_mfma_f32_16x16x32_bf16 v[212:215], a[28:31], a[52:55], v[212:215]
v_mfma_f32_16x16x32_bf16 v[244:247], a[24:27], a[56:59], 0
buffer_load_dwordx4 a[116:119], v29, s[56:59], s92 offen offset:1024 sc1
v_mfma_f32_16x16x32_bf16 v[244:247], a[28:31], a[60:63], v[244:247]
s_waitcnt lgkmcnt(4)
v_mfma_f32_16x16x32_bf16 v[152:155], a[0:3], a[32:35], 0
ds_read_b64_tr_b16 a[16:17], v16 offset:16384
v_mfma_f32_16x16x32_bf16 v[152:155], a[4:7], a[36:39], v[152:155]
ds_read_b64_tr_b16 a[18:19], v16 offset:20480
v_mfma_f32_16x16x32_bf16 v[184:187], a[0:3], a[40:43], 0
ds_read_b64_tr_b16 a[20:21], v16 offset:24576
v_mfma_f32_16x16x32_bf16 v[184:187], a[4:7], a[44:47], v[184:187]
ds_read_b64_tr_b16 a[22:23], v16 offset:28672
v_mfma_f32_16x16x32_bf16 v[216:219], a[0:3], a[48:51], 0
buffer_load_dwordx4 a[120:123], v29, s[56:59], s93 offen offset:0 sc1
v_mfma_f32_16x16x32_bf16 v[216:219], a[4:7], a[52:55], v[216:219]
v_mfma_f32_16x16x32_bf16 v[248:251], a[0:3], a[56:59], 0
buffer_load_dwordx4 a[124:127], v29, s[56:59], s93 offen offset:1024 sc1
v_mfma_f32_16x16x32_bf16 v[248:251], a[4:7], a[60:63], v[248:251]
s_waitcnt lgkmcnt(4)
v_mfma_f32_16x16x32_bf16 v[156:159], a[8:11], a[32:35], 0
ds_read_b64_tr_b16 a[24:25], v17 offset:16384
v_mfma_f32_16x16x32_bf16 v[156:159], a[12:15], a[36:39], v[156:159]
ds_read_b64_tr_b16 a[26:27], v17 offset:20480
v_mfma_f32_16x16x32_bf16 v[188:191], a[8:11], a[40:43], 0
ds_read_b64_tr_b16 a[28:29], v17 offset:24576
v_mfma_f32_16x16x32_bf16 v[188:191], a[12:15], a[44:47], v[188:191]
ds_read_b64_tr_b16 a[30:31], v17 offset:28672
v_mfma_f32_16x16x32_bf16 v[220:223], a[8:11], a[48:51], 0
s_waitcnt vmcnt(20)
v_mfma_f32_16x16x32_bf16 v[220:223], a[12:15], a[52:55], v[220:223]
s_barrier
v_mfma_f32_16x16x32_bf16 v[252:255], a[8:11], a[56:59], 0
s_waitcnt vmcnt(28)
v_mfma_f32_16x16x32_bf16 v[252:255], a[12:15], a[60:63], v[252:255]
s_waitcnt lgkmcnt(4)
v_mfma_f32_16x16x32_bf16 v[64:67], a[16:19], a[64:67], v[64:67]
ds_read_b64_tr_b16 a[0:1], v18 offset:16384
v_mfma_f32_16x16x32_bf16 v[64:67], a[20:23], a[68:71], v[64:67]
ds_read_b64_tr_b16 a[2:3], v18 offset:20480
v_mfma_f32_16x16x32_bf16 v[80:83], a[16:19], a[72:75], v[80:83]
ds_read_b64_tr_b16 a[4:5], v18 offset:24576
v_mfma_f32_16x16x32_bf16 v[80:83], a[20:23], a[76:79], v[80:83]
ds_read_b64_tr_b16 a[6:7], v18 offset:28672
v_mfma_f32_16x16x32_bf16 v[96:99], a[16:19], a[80:83], v[96:99]
s_mov_b32 m0, s68
s_add_u32 s79, s70, 0x6c00
buffer_load_dwordx4 v28, s[48:51], s70 offen offset:0 lds
v_mfma_f32_16x16x32_bf16 v[96:99], a[20:23], a[84:87], v[96:99]
v_mfma_f32_16x16x32_bf16 v[112:115], a[16:19], a[88:91], v[112:115]
buffer_load_dwordx4 v28, s[48:51], s79 offen offset:1024 lds
v_mfma_f32_16x16x32_bf16 v[112:115], a[20:23], a[92:95], v[112:115]
s_waitcnt lgkmcnt(4)
v_mfma_f32_16x16x32_bf16 v[68:71], a[24:27], a[64:67], v[68:71]
ds_read_b64_tr_b16 a[8:9], v16 offset:18432
v_mfma_f32_16x16x32_bf16 v[68:71], a[28:31], a[68:71], v[68:71]
ds_read_b64_tr_b16 a[10:11], v16 offset:22528
v_mfma_f32_16x16x32_bf16 v[84:87], a[24:27], a[72:75], v[84:87]
ds_read_b64_tr_b16 a[12:13], v16 offset:26624
v_mfma_f32_16x16x32_bf16 v[84:87], a[28:31], a[76:79], v[84:87]
ds_read_b64_tr_b16 a[14:15], v16 offset:30720
v_mfma_f32_16x16x32_bf16 v[100:103], a[24:27], a[80:83], v[100:103]
buffer_load_dwordx4 v28, s[52:55], s70 offen offset:2048 lds
v_mfma_f32_16x16x32_bf16 v[100:103], a[28:31], a[84:87], v[100:103]
v_mfma_f32_16x16x32_bf16 v[116:119], a[24:27], a[88:91], v[116:119]
buffer_load_dwordx4 v28, s[52:55], s79 offen offset:3072 lds
s_add_u32 s70, s70, 0x38000
v_mfma_f32_16x16x32_bf16 v[116:119], a[28:31], a[92:95], v[116:119]
s_waitcnt lgkmcnt(4)
v_mfma_f32_16x16x32_bf16 v[72:75], a[0:3], a[64:67], v[72:75]
ds_read_b64_tr_b16 a[16:17], v17 offset:18432
v_mfma_f32_16x16x32_bf16 v[72:75], a[4:7], a[68:71], v[72:75]
ds_read_b64_tr_b16 a[18:19], v17 offset:22528
v_mfma_f32_16x16x32_bf16 v[88:91], a[0:3], a[72:75], v[88:91]
ds_read_b64_tr_b16 a[20:21], v17 offset:26624
v_mfma_f32_16x16x32_bf16 v[88:91], a[4:7], a[76:79], v[88:91]
ds_read_b64_tr_b16 a[22:23], v17 offset:30720
v_mfma_f32_16x16x32_bf16 v[104:107], a[0:3], a[80:83], v[104:107]
s_add_u32 m0, s68, 0x10000
s_add_u32 s80, s69, 0x7c00
buffer_load_dwordx4 v26, s[44:47], s69 offen offset:0 lds
v_mfma_f32_16x16x32_bf16 v[104:107], a[4:7], a[84:87], v[104:107]
v_mfma_f32_16x16x32_bf16 v[120:123], a[0:3], a[88:91], v[120:123]
s_add_u32 s81, s69, 0xf800
buffer_load_dwordx4 v27, s[44:47], s80 offen offset:1024 lds
v_mfma_f32_16x16x32_bf16 v[120:123], a[4:7], a[92:95], v[120:123]
s_waitcnt lgkmcnt(4)
v_mfma_f32_16x16x32_bf16 a[192:195], a[8:11], a[64:67], a[192:195]
ds_read_b64_tr_b16 a[24:25], v18 offset:18432
v_mfma_f32_16x16x32_bf16 a[192:195], a[12:15], a[68:71], a[192:195]
ds_read_b64_tr_b16 a[26:27], v18 offset:22528
v_mfma_f32_16x16x32_bf16 a[208:211], a[8:11], a[72:75], a[208:211]
ds_read_b64_tr_b16 a[28:29], v18 offset:26624
v_mfma_f32_16x16x32_bf16 a[208:211], a[12:15], a[76:79], a[208:211]
ds_read_b64_tr_b16 a[30:31], v18 offset:30720
v_mfma_f32_16x16x32_bf16 a[224:227], a[8:11], a[80:83], a[224:227]
s_add_u32 s82, s69, 0x17400
buffer_load_dwordx4 v26, s[44:47], s81 offen offset:2048 lds
v_mfma_f32_16x16x32_bf16 a[224:227], a[12:15], a[84:87], a[224:227]
v_mfma_f32_16x16x32_bf16 a[240:243], a[8:11], a[88:91], a[240:243]
buffer_load_dwordx4 v27, s[44:47], s82 offen offset:3072 lds
s_add_u32 s69, s69, 0x80000
v_mfma_f32_16x16x32_bf16 a[240:243], a[12:15], a[92:95], a[240:243]
s_waitcnt lgkmcnt(4)
v_mfma_f32_16x16x32_bf16 a[196:199], a[16:19], a[64:67], a[196:199]
ds_read_b64_tr_b16 a[0:1], v12 offset:16384
v_mfma_f32_16x16x32_bf16 a[196:199], a[20:23], a[68:71], a[196:199]
ds_read_b64_tr_b16 a[2:3], v12 offset:20480
v_mfma_f32_16x16x32_bf16 a[212:215], a[16:19], a[72:75], a[212:215]
ds_read_b64_tr_b16 a[4:5], v12 offset:24576
v_mfma_f32_16x16x32_bf16 a[212:215], a[20:23], a[76:79], a[212:215]
ds_read_b64_tr_b16 a[6:7], v12 offset:28672
v_mfma_f32_16x16x32_bf16 a[228:231], a[16:19], a[80:83], a[228:231]
buffer_load_dwordx4 a[128:131], v29, s[56:59], s90 offen offset:2048 sc1
v_mfma_f32_16x16x32_bf16 a[228:231], a[20:23], a[84:87], a[228:231]
v_mfma_f32_16x16x32_bf16 a[244:247], a[16:19], a[88:91], a[244:247]
buffer_load_dwordx4 a[132:135], v29, s[56:59], s90 offen offset:3072 sc1
s_add_u32 s90, s90, 0x1000
v_mfma_f32_16x16x32_bf16 a[244:247], a[20:23], a[92:95], a[244:247]
s_waitcnt lgkmcnt(4)
v_mfma_f32_16x16x32_bf16 a[200:203], a[24:27], a[64:67], a[200:203]
ds_read_b64_tr_b16 a[8:9], v12 offset:16392
v_mfma_f32_16x16x32_bf16 a[200:203], a[28:31], a[68:71], a[200:203]
ds_read_b64_tr_b16 a[10:11], v12 offset:20488
v_mfma_f32_16x16x32_bf16 a[216:219], a[24:27], a[72:75], a[216:219]
ds_read_b64_tr_b16 a[12:13], v12 offset:24584
v_mfma_f32_16x16x32_bf16 a[216:219], a[28:31], a[76:79], a[216:219]
ds_read_b64_tr_b16 a[14:15], v12 offset:28680
v_mfma_f32_16x16x32_bf16 a[232:235], a[24:27], a[80:83], a[232:235]
buffer_load_dwordx4 a[136:139], v29, s[56:59], s91 offen offset:2048 sc1
v_mfma_f32_16x16x32_bf16 a[232:235], a[28:31], a[84:87], a[232:235]
v_mfma_f32_16x16x32_bf16 a[248:251], a[24:27], a[88:91], a[248:251]
buffer_load_dwordx4 a[140:143], v29, s[56:59], s91 offen offset:3072 sc1
s_add_u32 s91, s91, 0x1000
v_mfma_f32_16x16x32_bf16 a[248:251], a[28:31], a[92:95], a[248:251]
s_waitcnt lgkmcnt(4)
v_mfma_f32_16x16x32_bf16 v[128:131], a[0:3], a[64:67], v[128:131]
ds_read_b64_tr_b16 a[16:17], v13 offset:16384
v_mfma_f32_16x16x32_bf16 v[128:131], a[4:7], a[68:71], v[128:131]
ds_read_b64_tr_b16 a[18:19], v13 offset:20480
v_mfma_f32_16x16x32_bf16 v[160:163], a[0:3], a[72:75], v[160:163]
ds_read_b64_tr_b16 a[20:21], v13 offset:24576
v_mfma_f32_16x16x32_bf16 v[160:163], a[4:7], a[76:79], v[160:163]
ds_read_b64_tr_b16 a[22:23], v13 offset:28672
v_mfma_f32_16x16x32_bf16 v[192:195], a[0:3], a[80:83], v[192:195]
buffer_load_dwordx4 a[144:147], v29, s[56:59], s92 offen offset:2048 sc1
v_mfma_f32_16x16x32_bf16 v[192:195], a[4:7], a[84:87], v[192:195]
v_mfma_f32_16x16x32_bf16 v[224:227], a[0:3], a[88:91], v[224:227]
buffer_load_dwordx4 a[148:151], v29, s[56:59], s92 offen offset:3072 sc1
s_add_u32 s92, s92, 0x1000
v_mfma_f32_16x16x32_bf16 v[224:227], a[4:7], a[92:95], v[224:227]
s_waitcnt lgkmcnt(4)
v_mfma_f32_16x16x32_bf16 v[132:135], a[8:11], a[64:67], v[132:135]
ds_read_b64_tr_b16 a[24:25], v13 offset:16392
v_mfma_f32_16x16x32_bf16 v[132:135], a[12:15], a[68:71], v[132:135]
ds_read_b64_tr_b16 a[26:27], v13 offset:20488
v_mfma_f32_16x16x32_bf16 v[164:167], a[8:11], a[72:75], v[164:167]
ds_read_b64_tr_b16 a[28:29], v13 offset:24584
v_mfma_f32_16x16x32_bf16 v[164:167], a[12:15], a[76:79], v[164:167]
ds_read_b64_tr_b16 a[30:31], v13 offset:28680
v_mfma_f32_16x16x32_bf16 v[196:199], a[8:11], a[80:83], v[196:199]
buffer_load_dwordx4 a[152:155], v29, s[56:59], s93 offen offset:2048 sc1
v_mfma_f32_16x16x32_bf16 v[196:199], a[12:15], a[84:87], v[196:199]
v_mfma_f32_16x16x32_bf16 v[228:231], a[8:11], a[88:91], v[228:231]
buffer_load_dwordx4 a[156:159], v29, s[56:59], s93 offen offset:3072 sc1
s_add_u32 s93, s93, 0x1000
v_mfma_f32_16x16x32_bf16 v[228:231], a[12:15], a[92:95], v[228:231]
s_waitcnt lgkmcnt(4)
v_mfma_f32_16x16x32_bf16 v[136:139], a[16:19], a[64:67], v[136:139]
ds_read_b64_tr_b16 a[0:1], v14 offset:16384
v_mfma_f32_16x16x32_bf16 v[136:139], a[20:23], a[68:71], v[136:139]
ds_read_b64_tr_b16 a[2:3], v14 offset:20480
v_mfma_f32_16x16x32_bf16 v[168:171], a[16:19], a[72:75], v[168:171]
ds_read_b64_tr_b16 a[4:5], v14 offset:24576
v_mfma_f32_16x16x32_bf16 v[168:171], a[20:23], a[76:79], v[168:171]
ds_read_b64_tr_b16 a[6:7], v14 offset:28672
v_mfma_f32_16x16x32_bf16 v[200:203], a[16:19], a[80:83], v[200:203]
v_mfma_f32_16x16x32_bf16 v[200:203], a[20:23], a[84:87], v[200:203]
v_mfma_f32_16x16x32_bf16 v[232:235], a[16:19], a[88:91], v[232:235]
v_mfma_f32_16x16x32_bf16 v[232:235], a[20:23], a[92:95], v[232:235]
s_waitcnt lgkmcnt(4)
v_mfma_f32_16x16x32_bf16 v[140:143], a[24:27], a[64:67], v[140:143]
ds_read_b64_tr_b16 a[8:9], v14 offset:16392
v_mfma_f32_16x16x32_bf16 v[140:143], a[28:31], a[68:71], v[140:143]
ds_read_b64_tr_b16 a[10:11], v14 offset:20488
v_mfma_f32_16x16x32_bf16 v[172:175], a[24:27], a[72:75], v[172:175]
ds_read_b64_tr_b16 a[12:13], v14 offset:24584
v_mfma_f32_16x16x32_bf16 v[172:175], a[28:31], a[76:79], v[172:175]
ds_read_b64_tr_b16 a[14:15], v14 offset:28680
v_mfma_f32_16x16x32_bf16 v[204:207], a[24:27], a[80:83], v[204:207]
v_mfma_f32_16x16x32_bf16 v[204:207], a[28:31], a[84:87], v[204:207]
v_mfma_f32_16x16x32_bf16 v[236:239], a[24:27], a[88:91], v[236:239]
v_mfma_f32_16x16x32_bf16 v[236:239], a[28:31], a[92:95], v[236:239]
s_waitcnt lgkmcnt(4)
v_mfma_f32_16x16x32_bf16 v[144:147], a[0:3], a[64:67], v[144:147]
ds_read_b64_tr_b16 a[16:17], v15 offset:16384
v_mfma_f32_16x16x32_bf16 v[144:147], a[4:7], a[68:71], v[144:147]
ds_read_b64_tr_b16 a[18:19], v15 offset:20480
v_mfma_f32_16x16x32_bf16 v[176:179], a[0:3], a[72:75], v[176:179]
ds_read_b64_tr_b16 a[20:21], v15 offset:24576
v_mfma_f32_16x16x32_bf16 v[176:179], a[4:7], a[76:79], v[176:179]
ds_read_b64_tr_b16 a[22:23], v15 offset:28672
v_mfma_f32_16x16x32_bf16 v[208:211], a[0:3], a[80:83], v[208:211]
v_mfma_f32_16x16x32_bf16 v[208:211], a[4:7], a[84:87], v[208:211]
v_mfma_f32_16x16x32_bf16 v[240:243], a[0:3], a[88:91], v[240:243]
v_mfma_f32_16x16x32_bf16 v[240:243], a[4:7], a[92:95], v[240:243]
s_waitcnt lgkmcnt(4)
v_mfma_f32_16x16x32_bf16 v[148:151], a[8:11], a[64:67], v[148:151]
ds_read_b64_tr_b16 a[24:25], v15 offset:16392
v_mfma_f32_16x16x32_bf16 v[148:151], a[12:15], a[68:71], v[148:151]
ds_read_b64_tr_b16 a[26:27], v15 offset:20488
v_mfma_f32_16x16x32_bf16 v[180:183], a[8:11], a[72:75], v[180:183]
ds_read_b64_tr_b16 a[28:29], v15 offset:24584
v_mfma_f32_16x16x32_bf16 v[180:183], a[12:15], a[76:79], v[180:183]
ds_read_b64_tr_b16 a[30:31], v15 offset:28680
v_mfma_f32_16x16x32_bf16 v[212:215], a[8:11], a[80:83], v[212:215]
v_mfma_f32_16x16x32_bf16 v[212:215], a[12:15], a[84:87], v[212:215]
v_mfma_f32_16x16x32_bf16 v[244:247], a[8:11], a[88:91], v[244:247]
v_mfma_f32_16x16x32_bf16 v[244:247], a[12:15], a[92:95], v[244:247]
s_waitcnt lgkmcnt(4)
v_mfma_f32_16x16x32_bf16 v[152:155], a[16:19], a[64:67], v[152:155]
ds_read_b64_tr_b16 a[0:1], v16 offset:32768
v_mfma_f32_16x16x32_bf16 v[152:155], a[20:23], a[68:71], v[152:155]
ds_read_b64_tr_b16 a[2:3], v16 offset:36864
v_mfma_f32_16x16x32_bf16 v[184:187], a[16:19], a[72:75], v[184:187]
ds_read_b64_tr_b16 a[4:5], v16 offset:40960
v_mfma_f32_16x16x32_bf16 v[184:187], a[20:23], a[76:79], v[184:187]
ds_read_b64_tr_b16 a[6:7], v16 offset:45056
v_mfma_f32_16x16x32_bf16 v[216:219], a[16:19], a[80:83], v[216:219]
v_mfma_f32_16x16x32_bf16 v[216:219], a[20:23], a[84:87], v[216:219]
v_mfma_f32_16x16x32_bf16 v[248:251], a[16:19], a[88:91], v[248:251]
v_mfma_f32_16x16x32_bf16 v[248:251], a[20:23], a[92:95], v[248:251]
s_waitcnt lgkmcnt(4)
v_mfma_f32_16x16x32_bf16 v[156:159], a[24:27], a[64:67], v[156:159]
ds_read_b64_tr_b16 a[8:9], v17 offset:32768
v_mfma_f32_16x16x32_bf16 v[156:159], a[28:31], a[68:71], v[156:159]
ds_read_b64_tr_b16 a[10:11], v17 offset:36864
v_mfma_f32_16x16x32_bf16 v[188:191], a[24:27], a[72:75], v[188:191]
ds_read_b64_tr_b16 a[12:13], v17 offset:40960
v_mfma_f32_16x16x32_bf16 v[188:191], a[28:31], a[76:79], v[188:191]
ds_read_b64_tr_b16 a[14:15], v17 offset:45056
v_mfma_f32_16x16x32_bf16 v[220:223], a[24:27], a[80:83], v[220:223]
s_waitcnt vmcnt(28)
v_mfma_f32_16x16x32_bf16 v[220:223], a[28:31], a[84:87], v[220:223]
s_barrier
v_mfma_f32_16x16x32_bf16 v[252:255], a[24:27], a[88:91], v[252:255]
s_waitcnt vmcnt(16)
v_mfma_f32_16x16x32_bf16 v[252:255], a[28:31], a[92:95], v[252:255]
s_waitcnt lgkmcnt(4)
v_mfma_f32_16x16x32_bf16 v[64:67], a[0:3], a[96:99], v[64:67]
ds_read_b64_tr_b16 a[16:17], v18 offset:32768
v_mfma_f32_16x16x32_bf16 v[64:67], a[4:7], a[100:103], v[64:67]
ds_read_b64_tr_b16 a[18:19], v18 offset:36864
v_mfma_f32_16x16x32_bf16 v[80:83], a[0:3], a[104:107], v[80:83]
ds_read_b64_tr_b16 a[20:21], v18 offset:40960
v_mfma_f32_16x16x32_bf16 v[80:83], a[4:7], a[108:111], v[80:83]
ds_read_b64_tr_b16 a[22:23], v18 offset:45056
v_mfma_f32_16x16x32_bf16 v[96:99], a[0:3], a[112:115], v[96:99]
s_add_u32 m0, s68, 0x4000
s_add_u32 s79, s70, 0x6c00
buffer_load_dwordx4 v28, s[48:51], s70 offen offset:0 lds
v_mfma_f32_16x16x32_bf16 v[96:99], a[4:7], a[116:119], v[96:99]
v_mfma_f32_16x16x32_bf16 v[112:115], a[0:3], a[120:123], v[112:115]
buffer_load_dwordx4 v28, s[48:51], s79 offen offset:1024 lds
v_mfma_f32_16x16x32_bf16 v[112:115], a[4:7], a[124:127], v[112:115]
s_waitcnt lgkmcnt(4)
v_mfma_f32_16x16x32_bf16 v[68:71], a[8:11], a[96:99], v[68:71]
ds_read_b64_tr_b16 a[24:25], v16 offset:34816
v_mfma_f32_16x16x32_bf16 v[68:71], a[12:15], a[100:103], v[68:71]
ds_read_b64_tr_b16 a[26:27], v16 offset:38912
v_mfma_f32_16x16x32_bf16 v[84:87], a[8:11], a[104:107], v[84:87]
ds_read_b64_tr_b16 a[28:29], v16 offset:43008
v_mfma_f32_16x16x32_bf16 v[84:87], a[12:15], a[108:111], v[84:87]
ds_read_b64_tr_b16 a[30:31], v16 offset:47104
v_mfma_f32_16x16x32_bf16 v[100:103], a[8:11], a[112:115], v[100:103]
buffer_load_dwordx4 v28, s[52:55], s70 offen offset:2048 lds
v_mfma_f32_16x16x32_bf16 v[100:103], a[12:15], a[116:119], v[100:103]
v_mfma_f32_16x16x32_bf16 v[116:119], a[8:11], a[120:123], v[116:119]
buffer_load_dwordx4 v28, s[52:55], s79 offen offset:3072 lds
s_add_u32 s70, s70, 0x38000
v_mfma_f32_16x16x32_bf16 v[116:119], a[12:15], a[124:127], v[116:119]
s_waitcnt lgkmcnt(4)
v_mfma_f32_16x16x32_bf16 v[72:75], a[16:19], a[96:99], v[72:75]
ds_read_b64_tr_b16 a[0:1], v17 offset:34816
v_mfma_f32_16x16x32_bf16 v[72:75], a[20:23], a[100:103], v[72:75]
ds_read_b64_tr_b16 a[2:3], v17 offset:38912
v_mfma_f32_16x16x32_bf16 v[88:91], a[16:19], a[104:107], v[88:91]
ds_read_b64_tr_b16 a[4:5], v17 offset:43008
v_mfma_f32_16x16x32_bf16 v[88:91], a[20:23], a[108:111], v[88:91]
ds_read_b64_tr_b16 a[6:7], v17 offset:47104
v_mfma_f32_16x16x32_bf16 v[104:107], a[16:19], a[112:115], v[104:107]
s_add_u32 m0, s68, 0x14000
s_add_u32 s80, s69, 0x7c00
buffer_load_dwordx4 v26, s[44:47], s69 offen offset:0 lds
v_mfma_f32_16x16x32_bf16 v[104:107], a[20:23], a[116:119], v[104:107]
v_mfma_f32_16x16x32_bf16 v[120:123], a[16:19], a[120:123], v[120:123]
s_add_u32 s81, s69, 0xf800
buffer_load_dwordx4 v27, s[44:47], s80 offen offset:1024 lds
v_mfma_f32_16x16x32_bf16 v[120:123], a[20:23], a[124:127], v[120:123]
s_waitcnt lgkmcnt(4)
v_mfma_f32_16x16x32_bf16 a[192:195], a[24:27], a[96:99], a[192:195]
ds_read_b64_tr_b16 a[8:9], v18 offset:34816
v_mfma_f32_16x16x32_bf16 a[192:195], a[28:31], a[100:103], a[192:195]
ds_read_b64_tr_b16 a[10:11], v18 offset:38912
v_mfma_f32_16x16x32_bf16 a[208:211], a[24:27], a[104:107], a[208:211]
ds_read_b64_tr_b16 a[12:13], v18 offset:43008
v_mfma_f32_16x16x32_bf16 a[208:211], a[28:31], a[108:111], a[208:211]
ds_read_b64_tr_b16 a[14:15], v18 offset:47104
v_mfma_f32_16x16x32_bf16 a[224:227], a[24:27], a[112:115], a[224:227]
s_add_u32 s82, s69, 0x17400
buffer_load_dwordx4 v26, s[44:47], s81 offen offset:2048 lds
v_mfma_f32_16x16x32_bf16 a[224:227], a[28:31], a[116:119], a[224:227]
v_mfma_f32_16x16x32_bf16 a[240:243], a[24:27], a[120:123], a[240:243]
buffer_load_dwordx4 v27, s[44:47], s82 offen offset:3072 lds
s_add_u32 s69, s69, 0x80000
v_mfma_f32_16x16x32_bf16 a[240:243], a[28:31], a[124:127], a[240:243]
s_waitcnt lgkmcnt(4)
v_mfma_f32_16x16x32_bf16 a[196:199], a[0:3], a[96:99], a[196:199]
ds_read_b64_tr_b16 a[16:17], v12 offset:32768
v_mfma_f32_16x16x32_bf16 a[196:199], a[4:7], a[100:103], a[196:199]
ds_read_b64_tr_b16 a[18:19], v12 offset:36864
v_mfma_f32_16x16x32_bf16 a[212:215], a[0:3], a[104:107], a[212:215]
ds_read_b64_tr_b16 a[20:21], v12 offset:40960
v_mfma_f32_16x16x32_bf16 a[212:215], a[4:7], a[108:111], a[212:215]
ds_read_b64_tr_b16 a[22:23], v12 offset:45056
v_mfma_f32_16x16x32_bf16 a[228:231], a[0:3], a[112:115], a[228:231]
buffer_load_dwordx4 a[32:35], v29, s[56:59], s90 offen offset:0 sc1
v_mfma_f32_16x16x32_bf16 a[228:231], a[4:7], a[116:119], a[228:231]
v_mfma_f32_16x16x32_bf16 a[244:247], a[0:3], a[120:123], a[244:247]
buffer_load_dwordx4 a[36:39], v29, s[56:59], s90 offen offset:1024 sc1
v_mfma_f32_16x16x32_bf16 a[244:247], a[4:7], a[124:127], a[244:247]
s_waitcnt lgkmcnt(4)
v_mfma_f32_16x16x32_bf16 a[200:203], a[8:11], a[96:99], a[200:203]
ds_read_b64_tr_b16 a[24:25], v12 offset:32776
v_mfma_f32_16x16x32_bf16 a[200:203], a[12:15], a[100:103], a[200:203]
ds_read_b64_tr_b16 a[26:27], v12 offset:36872
v_mfma_f32_16x16x32_bf16 a[216:219], a[8:11], a[104:107], a[216:219]
ds_read_b64_tr_b16 a[28:29], v12 offset:40968
v_mfma_f32_16x16x32_bf16 a[216:219], a[12:15], a[108:111], a[216:219]
ds_read_b64_tr_b16 a[30:31], v12 offset:45064
v_mfma_f32_16x16x32_bf16 a[232:235], a[8:11], a[112:115], a[232:235]
buffer_load_dwordx4 a[40:43], v29, s[56:59], s91 offen offset:0 sc1
v_mfma_f32_16x16x32_bf16 a[232:235], a[12:15], a[116:119], a[232:235]
v_mfma_f32_16x16x32_bf16 a[248:251], a[8:11], a[120:123], a[248:251]
buffer_load_dwordx4 a[44:47], v29, s[56:59], s91 offen offset:1024 sc1
v_mfma_f32_16x16x32_bf16 a[248:251], a[12:15], a[124:127], a[248:251]
s_waitcnt lgkmcnt(4)
v_mfma_f32_16x16x32_bf16 v[128:131], a[16:19], a[96:99], v[128:131]
ds_read_b64_tr_b16 a[0:1], v13 offset:32768
v_mfma_f32_16x16x32_bf16 v[128:131], a[20:23], a[100:103], v[128:131]
ds_read_b64_tr_b16 a[2:3], v13 offset:36864
v_mfma_f32_16x16x32_bf16 v[160:163], a[16:19], a[104:107], v[160:163]
ds_read_b64_tr_b16 a[4:5], v13 offset:40960
v_mfma_f32_16x16x32_bf16 v[160:163], a[20:23], a[108:111], v[160:163]
ds_read_b64_tr_b16 a[6:7], v13 offset:45056
v_mfma_f32_16x16x32_bf16 v[192:195], a[16:19], a[112:115], v[192:195]
buffer_load_dwordx4 a[48:51], v29, s[56:59], s92 offen offset:0 sc1
v_mfma_f32_16x16x32_bf16 v[192:195], a[20:23], a[116:119], v[192:195]
v_mfma_f32_16x16x32_bf16 v[224:227], a[16:19], a[120:123], v[224:227]
buffer_load_dwordx4 a[52:55], v29, s[56:59], s92 offen offset:1024 sc1
v_mfma_f32_16x16x32_bf16 v[224:227], a[20:23], a[124:127], v[224:227]
s_waitcnt lgkmcnt(4)
v_mfma_f32_16x16x32_bf16 v[132:135], a[24:27], a[96:99], v[132:135]
ds_read_b64_tr_b16 a[8:9], v13 offset:32776
v_mfma_f32_16x16x32_bf16 v[132:135], a[28:31], a[100:103], v[132:135]
ds_read_b64_tr_b16 a[10:11], v13 offset:36872
v_mfma_f32_16x16x32_bf16 v[164:167], a[24:27], a[104:107], v[164:167]
ds_read_b64_tr_b16 a[12:13], v13 offset:40968
v_mfma_f32_16x16x32_bf16 v[164:167], a[28:31], a[108:111], v[164:167]
ds_read_b64_tr_b16 a[14:15], v13 offset:45064
v_mfma_f32_16x16x32_bf16 v[196:199], a[24:27], a[112:115], v[196:199]
buffer_load_dwordx4 a[56:59], v29, s[56:59], s93 offen offset:0 sc1
v_mfma_f32_16x16x32_bf16 v[196:199], a[28:31], a[116:119], v[196:199]
v_mfma_f32_16x16x32_bf16 v[228:231], a[24:27], a[120:123], v[228:231]
buffer_load_dwordx4 a[60:63], v29, s[56:59], s93 offen offset:1024 sc1
v_mfma_f32_16x16x32_bf16 v[228:231], a[28:31], a[124:127], v[228:231]
s_waitcnt lgkmcnt(4)
v_mfma_f32_16x16x32_bf16 v[136:139], a[0:3], a[96:99], v[136:139]
ds_read_b64_tr_b16 a[16:17], v14 offset:32768
v_mfma_f32_16x16x32_bf16 v[136:139], a[4:7], a[100:103], v[136:139]
ds_read_b64_tr_b16 a[18:19], v14 offset:36864
v_mfma_f32_16x16x32_bf16 v[168:171], a[0:3], a[104:107], v[168:171]
ds_read_b64_tr_b16 a[20:21], v14 offset:40960
v_mfma_f32_16x16x32_bf16 v[168:171], a[4:7], a[108:111], v[168:171]
ds_read_b64_tr_b16 a[22:23], v14 offset:45056
v_mfma_f32_16x16x32_bf16 v[200:203], a[0:3], a[112:115], v[200:203]
v_mfma_f32_16x16x32_bf16 v[200:203], a[4:7], a[116:119], v[200:203]
v_mfma_f32_16x16x32_bf16 v[232:235], a[0:3], a[120:123], v[232:235]
v_mfma_f32_16x16x32_bf16 v[232:235], a[4:7], a[124:127], v[232:235]
s_waitcnt lgkmcnt(4)
v_mfma_f32_16x16x32_bf16 v[140:143], a[8:11], a[96:99], v[140:143]
ds_read_b64_tr_b16 a[24:25], v14 offset:32776
v_mfma_f32_16x16x32_bf16 v[140:143], a[12:15], a[100:103], v[140:143]
ds_read_b64_tr_b16 a[26:27], v14 offset:36872
v_mfma_f32_16x16x32_bf16 v[172:175], a[8:11], a[104:107], v[172:175]
ds_read_b64_tr_b16 a[28:29], v14 offset:40968
v_mfma_f32_16x16x32_bf16 v[172:175], a[12:15], a[108:111], v[172:175]
ds_read_b64_tr_b16 a[30:31], v14 offset:45064
v_mfma_f32_16x16x32_bf16 v[204:207], a[8:11], a[112:115], v[204:207]
v_mfma_f32_16x16x32_bf16 v[204:207], a[12:15], a[116:119], v[204:207]
v_mfma_f32_16x16x32_bf16 v[236:239], a[8:11], a[120:123], v[236:239]
v_mfma_f32_16x16x32_bf16 v[236:239], a[12:15], a[124:127], v[236:239]
s_waitcnt lgkmcnt(4)
v_mfma_f32_16x16x32_bf16 v[144:147], a[16:19], a[96:99], v[144:147]
ds_read_b64_tr_b16 a[0:1], v15 offset:32768
v_mfma_f32_16x16x32_bf16 v[144:147], a[20:23], a[100:103], v[144:147]
ds_read_b64_tr_b16 a[2:3], v15 offset:36864
v_mfma_f32_16x16x32_bf16 v[176:179], a[16:19], a[104:107], v[176:179]
ds_read_b64_tr_b16 a[4:5], v15 offset:40960
v_mfma_f32_16x16x32_bf16 v[176:179], a[20:23], a[108:111], v[176:179]
ds_read_b64_tr_b16 a[6:7], v15 offset:45056
v_mfma_f32_16x16x32_bf16 v[208:211], a[16:19], a[112:115], v[208:211]
v_mfma_f32_16x16x32_bf16 v[208:211], a[20:23], a[116:119], v[208:211]
v_mfma_f32_16x16x32_bf16 v[240:243], a[16:19], a[120:123], v[240:243]
v_mfma_f32_16x16x32_bf16 v[240:243], a[20:23], a[124:127], v[240:243]
s_waitcnt lgkmcnt(4)
v_mfma_f32_16x16x32_bf16 v[148:151], a[24:27], a[96:99], v[148:151]
ds_read_b64_tr_b16 a[8:9], v15 offset:32776
v_mfma_f32_16x16x32_bf16 v[148:151], a[28:31], a[100:103], v[148:151]
ds_read_b64_tr_b16 a[10:11], v15 offset:36872
v_mfma_f32_16x16x32_bf16 v[180:183], a[24:27], a[104:107], v[180:183]
ds_read_b64_tr_b16 a[12:13], v15 offset:40968
v_mfma_f32_16x16x32_bf16 v[180:183], a[28:31], a[108:111], v[180:183]
ds_read_b64_tr_b16 a[14:15], v15 offset:45064
v_mfma_f32_16x16x32_bf16 v[212:215], a[24:27], a[112:115], v[212:215]
v_mfma_f32_16x16x32_bf16 v[212:215], a[28:31], a[116:119], v[212:215]
v_mfma_f32_16x16x32_bf16 v[244:247], a[24:27], a[120:123], v[244:247]
v_mfma_f32_16x16x32_bf16 v[244:247], a[28:31], a[124:127], v[244:247]
s_waitcnt lgkmcnt(4)
v_mfma_f32_16x16x32_bf16 v[152:155], a[0:3], a[96:99], v[152:155]
ds_read_b64_tr_b16 a[16:17], v16 offset:49152
v_mfma_f32_16x16x32_bf16 v[152:155], a[4:7], a[100:103], v[152:155]
ds_read_b64_tr_b16 a[18:19], v16 offset:53248
v_mfma_f32_16x16x32_bf16 v[184:187], a[0:3], a[104:107], v[184:187]
ds_read_b64_tr_b16 a[20:21], v16 offset:57344
v_mfma_f32_16x16x32_bf16 v[184:187], a[4:7], a[108:111], v[184:187]
ds_read_b64_tr_b16 a[22:23], v16 offset:61440
v_mfma_f32_16x16x32_bf16 v[216:219], a[0:3], a[112:115], v[216:219]
v_mfma_f32_16x16x32_bf16 v[216:219], a[4:7], a[116:119], v[216:219]
v_mfma_f32_16x16x32_bf16 v[248:251], a[0:3], a[120:123], v[248:251]
v_mfma_f32_16x16x32_bf16 v[248:251], a[4:7], a[124:127], v[248:251]
s_waitcnt lgkmcnt(4)
v_mfma_f32_16x16x32_bf16 v[156:159], a[8:11], a[96:99], v[156:159]
ds_read_b64_tr_b16 a[24:25], v17 offset:49152
v_mfma_f32_16x16x32_bf16 v[156:159], a[12:15], a[100:103], v[156:159]
ds_read_b64_tr_b16 a[26:27], v17 offset:53248
v_mfma_f32_16x16x32_bf16 v[188:191], a[8:11], a[104:107], v[188:191]
ds_read_b64_tr_b16 a[28:29], v17 offset:57344
v_mfma_f32_16x16x32_bf16 v[188:191], a[12:15], a[108:111], v[188:191]
ds_read_b64_tr_b16 a[30:31], v17 offset:61440
v_mfma_f32_16x16x32_bf16 v[220:223], a[8:11], a[112:115], v[220:223]
s_waitcnt vmcnt(24)
v_mfma_f32_16x16x32_bf16 v[220:223], a[12:15], a[116:119], v[220:223]
s_barrier
v_mfma_f32_16x16x32_bf16 v[252:255], a[8:11], a[120:123], v[252:255]
s_waitcnt vmcnt(16)
v_mfma_f32_16x16x32_bf16 v[252:255], a[12:15], a[124:127], v[252:255]
s_waitcnt lgkmcnt(4)
v_mfma_f32_16x16x32_bf16 v[64:67], a[16:19], a[128:131], v[64:67]
ds_read_b64_tr_b16 a[0:1], v18 offset:49152
v_mfma_f32_16x16x32_bf16 v[64:67], a[20:23], a[132:135], v[64:67]
ds_read_b64_tr_b16 a[2:3], v18 offset:53248
v_mfma_f32_16x16x32_bf16 v[80:83], a[16:19], a[136:139], v[80:83]
ds_read_b64_tr_b16 a[4:5], v18 offset:57344
v_mfma_f32_16x16x32_bf16 v[80:83], a[20:23], a[140:143], v[80:83]
ds_read_b64_tr_b16 a[6:7], v18 offset:61440
v_mfma_f32_16x16x32_bf16 v[96:99], a[16:19], a[144:147], v[96:99]
s_add_u32 m0, s68, 0x8000
s_add_u32 s79, s70, 0x6c00
buffer_load_dwordx4 v28, s[48:51], s70 offen offset:0 lds
v_mfma_f32_16x16x32_bf16 v[96:99], a[20:23], a[148:151], v[96:99]
v_mfma_f32_16x16x32_bf16 v[112:115], a[16:19], a[152:155], v[112:115]
buffer_load_dwordx4 v28, s[48:51], s79 offen offset:1024 lds
v_mfma_f32_16x16x32_bf16 v[112:115], a[20:23], a[156:159], v[112:115]
s_waitcnt lgkmcnt(4)
v_mfma_f32_16x16x32_bf16 v[68:71], a[24:27], a[128:131], v[68:71]
ds_read_b64_tr_b16 a[8:9], v16 offset:51200
v_mfma_f32_16x16x32_bf16 v[68:71], a[28:31], a[132:135], v[68:71]
ds_read_b64_tr_b16 a[10:11], v16 offset:55296
v_mfma_f32_16x16x32_bf16 v[84:87], a[24:27], a[136:139], v[84:87]
ds_read_b64_tr_b16 a[12:13], v16 offset:59392
v_mfma_f32_16x16x32_bf16 v[84:87], a[28:31], a[140:143], v[84:87]
ds_read_b64_tr_b16 a[14:15], v16 offset:63488
v_mfma_f32_16x16x32_bf16 v[100:103], a[24:27], a[144:147], v[100:103]
buffer_load_dwordx4 v28, s[52:55], s70 offen offset:2048 lds
v_mfma_f32_16x16x32_bf16 v[100:103], a[28:31], a[148:151], v[100:103]
v_mfma_f32_16x16x32_bf16 v[116:119], a[24:27], a[152:155], v[116:119]
buffer_load_dwordx4 v28, s[52:55], s79 offen offset:3072 lds
s_add_u32 s70, s70, 0x38000
v_mfma_f32_16x16x32_bf16 v[116:119], a[28:31], a[156:159], v[116:119]
s_waitcnt lgkmcnt(4)
v_mfma_f32_16x16x32_bf16 v[72:75], a[0:3], a[128:131], v[72:75]
ds_read_b64_tr_b16 a[16:17], v17 offset:51200
v_mfma_f32_16x16x32_bf16 v[72:75], a[4:7], a[132:135], v[72:75]
ds_read_b64_tr_b16 a[18:19], v17 offset:55296
v_mfma_f32_16x16x32_bf16 v[88:91], a[0:3], a[136:139], v[88:91]
ds_read_b64_tr_b16 a[20:21], v17 offset:59392
v_mfma_f32_16x16x32_bf16 v[88:91], a[4:7], a[140:143], v[88:91]
ds_read_b64_tr_b16 a[22:23], v17 offset:63488
v_mfma_f32_16x16x32_bf16 v[104:107], a[0:3], a[144:147], v[104:107]
s_add_u32 m0, s68, 0x18000
s_add_u32 s80, s69, 0x7c00
buffer_load_dwordx4 v26, s[44:47], s69 offen offset:0 lds
v_mfma_f32_16x16x32_bf16 v[104:107], a[4:7], a[148:151], v[104:107]
v_mfma_f32_16x16x32_bf16 v[120:123], a[0:3], a[152:155], v[120:123]
s_add_u32 s81, s69, 0xf800
buffer_load_dwordx4 v27, s[44:47], s80 offen offset:1024 lds
v_mfma_f32_16x16x32_bf16 v[120:123], a[4:7], a[156:159], v[120:123]
s_waitcnt lgkmcnt(4)
v_mfma_f32_16x16x32_bf16 a[192:195], a[8:11], a[128:131], a[192:195]
ds_read_b64_tr_b16 a[24:25], v18 offset:51200
v_mfma_f32_16x16x32_bf16 a[192:195], a[12:15], a[132:135], a[192:195]
ds_read_b64_tr_b16 a[26:27], v18 offset:55296
v_mfma_f32_16x16x32_bf16 a[208:211], a[8:11], a[136:139], a[208:211]
ds_read_b64_tr_b16 a[28:29], v18 offset:59392
v_mfma_f32_16x16x32_bf16 a[208:211], a[12:15], a[140:143], a[208:211]
ds_read_b64_tr_b16 a[30:31], v18 offset:63488
v_mfma_f32_16x16x32_bf16 a[224:227], a[8:11], a[144:147], a[224:227]
s_add_u32 s82, s69, 0x17400
buffer_load_dwordx4 v26, s[44:47], s81 offen offset:2048 lds
v_mfma_f32_16x16x32_bf16 a[224:227], a[12:15], a[148:151], a[224:227]
v_mfma_f32_16x16x32_bf16 a[240:243], a[8:11], a[152:155], a[240:243]
buffer_load_dwordx4 v27, s[44:47], s82 offen offset:3072 lds
s_add_u32 s69, s69, 0x80000
v_mfma_f32_16x16x32_bf16 a[240:243], a[12:15], a[156:159], a[240:243]
s_waitcnt lgkmcnt(4)
v_mfma_f32_16x16x32_bf16 a[196:199], a[16:19], a[128:131], a[196:199]
ds_read_b64_tr_b16 a[0:1], v12 offset:49152
v_mfma_f32_16x16x32_bf16 a[196:199], a[20:23], a[132:135], a[196:199]
ds_read_b64_tr_b16 a[2:3], v12 offset:53248
v_mfma_f32_16x16x32_bf16 a[212:215], a[16:19], a[136:139], a[212:215]
ds_read_b64_tr_b16 a[4:5], v12 offset:57344
v_mfma_f32_16x16x32_bf16 a[212:215], a[20:23], a[140:143], a[212:215]
ds_read_b64_tr_b16 a[6:7], v12 offset:61440
v_mfma_f32_16x16x32_bf16 a[228:231], a[16:19], a[144:147], a[228:231]
buffer_load_dwordx4 a[64:67], v29, s[56:59], s90 offen offset:2048 sc1
v_mfma_f32_16x16x32_bf16 a[228:231], a[20:23], a[148:151], a[228:231]
v_mfma_f32_16x16x32_bf16 a[244:247], a[16:19], a[152:155], a[244:247]
buffer_load_dwordx4 a[68:71], v29, s[56:59], s90 offen offset:3072 sc1
s_add_u32 s90, s90, 0x1000
v_mfma_f32_16x16x32_bf16 a[244:247], a[20:23], a[156:159], a[244:247]
s_waitcnt lgkmcnt(4)
v_mfma_f32_16x16x32_bf16 a[200:203], a[24:27], a[128:131], a[200:203]
ds_read_b64_tr_b16 a[8:9], v12 offset:49160
v_mfma_f32_16x16x32_bf16 a[200:203], a[28:31], a[132:135], a[200:203]
ds_read_b64_tr_b16 a[10:11], v12 offset:53256
v_mfma_f32_16x16x32_bf16 a[216:219], a[24:27], a[136:139], a[216:219]
ds_read_b64_tr_b16 a[12:13], v12 offset:57352
v_mfma_f32_16x16x32_bf16 a[216:219], a[28:31], a[140:143], a[216:219]
ds_read_b64_tr_b16 a[14:15], v12 offset:61448
v_mfma_f32_16x16x32_bf16 a[232:235], a[24:27], a[144:147], a[232:235]
buffer_load_dwordx4 a[72:75], v29, s[56:59], s91 offen offset:2048 sc1
v_mfma_f32_16x16x32_bf16 a[232:235], a[28:31], a[148:151], a[232:235]
v_mfma_f32_16x16x32_bf16 a[248:251], a[24:27], a[152:155], a[248:251]
buffer_load_dwordx4 a[76:79], v29, s[56:59], s91 offen offset:3072 sc1
s_add_u32 s91, s91, 0x1000
v_mfma_f32_16x16x32_bf16 a[248:251], a[28:31], a[156:159], a[248:251]
s_waitcnt lgkmcnt(4)
v_mfma_f32_16x16x32_bf16 v[128:131], a[0:3], a[128:131], v[128:131]
ds_read_b64_tr_b16 a[16:17], v13 offset:49152
v_mfma_f32_16x16x32_bf16 v[128:131], a[4:7], a[132:135], v[128:131]
ds_read_b64_tr_b16 a[18:19], v13 offset:53248
v_mfma_f32_16x16x32_bf16 v[160:163], a[0:3], a[136:139], v[160:163]
ds_read_b64_tr_b16 a[20:21], v13 offset:57344
v_mfma_f32_16x16x32_bf16 v[160:163], a[4:7], a[140:143], v[160:163]
ds_read_b64_tr_b16 a[22:23], v13 offset:61440
v_mfma_f32_16x16x32_bf16 v[192:195], a[0:3], a[144:147], v[192:195]
buffer_load_dwordx4 a[80:83], v29, s[56:59], s92 offen offset:2048 sc1
v_mfma_f32_16x16x32_bf16 v[192:195], a[4:7], a[148:151], v[192:195]
v_mfma_f32_16x16x32_bf16 v[224:227], a[0:3], a[152:155], v[224:227]
buffer_load_dwordx4 a[84:87], v29, s[56:59], s92 offen offset:3072 sc1
s_add_u32 s92, s92, 0x1000
v_mfma_f32_16x16x32_bf16 v[224:227], a[4:7], a[156:159], v[224:227]
s_waitcnt lgkmcnt(4)
v_mfma_f32_16x16x32_bf16 v[132:135], a[8:11], a[128:131], v[132:135]
ds_read_b64_tr_b16 a[24:25], v13 offset:49160
v_mfma_f32_16x16x32_bf16 v[132:135], a[12:15], a[132:135], v[132:135]
ds_read_b64_tr_b16 a[26:27], v13 offset:53256
v_mfma_f32_16x16x32_bf16 v[164:167], a[8:11], a[136:139], v[164:167]
ds_read_b64_tr_b16 a[28:29], v13 offset:57352
v_mfma_f32_16x16x32_bf16 v[164:167], a[12:15], a[140:143], v[164:167]
ds_read_b64_tr_b16 a[30:31], v13 offset:61448
v_mfma_f32_16x16x32_bf16 v[196:199], a[8:11], a[144:147], v[196:199]
buffer_load_dwordx4 a[88:91], v29, s[56:59], s93 offen offset:2048 sc1
v_mfma_f32_16x16x32_bf16 v[196:199], a[12:15], a[148:151], v[196:199]
v_mfma_f32_16x16x32_bf16 v[228:231], a[8:11], a[152:155], v[228:231]
buffer_load_dwordx4 a[92:95], v29, s[56:59], s93 offen offset:3072 sc1
s_add_u32 s93, s93, 0x1000
v_mfma_f32_16x16x32_bf16 v[228:231], a[12:15], a[156:159], v[228:231]
s_waitcnt lgkmcnt(4)
v_mfma_f32_16x16x32_bf16 v[136:139], a[16:19], a[128:131], v[136:139]
ds_read_b64_tr_b16 a[0:1], v14 offset:49152
v_mfma_f32_16x16x32_bf16 v[136:139], a[20:23], a[132:135], v[136:139]
ds_read_b64_tr_b16 a[2:3], v14 offset:53248
v_mfma_f32_16x16x32_bf16 v[168:171], a[16:19], a[136:139], v[168:171]
ds_read_b64_tr_b16 a[4:5], v14 offset:57344
v_mfma_f32_16x16x32_bf16 v[168:171], a[20:23], a[140:143], v[168:171]
ds_read_b64_tr_b16 a[6:7], v14 offset:61440
v_mfma_f32_16x16x32_bf16 v[200:203], a[16:19], a[144:147], v[200:203]
buffer_load_dwordx4 a[96:99], v29, s[56:59], s90 offen offset:0 sc1
v_mfma_f32_16x16x32_bf16 v[200:203], a[20:23], a[148:151], v[200:203]
v_mfma_f32_16x16x32_bf16 v[232:235], a[16:19], a[152:155], v[232:235]
buffer_load_dwordx4 a[100:103], v29, s[56:59], s90 offen offset:1024 sc1
v_mfma_f32_16x16x32_bf16 v[232:235], a[20:23], a[156:159], v[232:235]
s_waitcnt lgkmcnt(4)
v_mfma_f32_16x16x32_bf16 v[140:143], a[24:27], a[128:131], v[140:143]
ds_read_b64_tr_b16 a[8:9], v14 offset:49160
v_mfma_f32_16x16x32_bf16 v[140:143], a[28:31], a[132:135], v[140:143]
ds_read_b64_tr_b16 a[10:11], v14 offset:53256
v_mfma_f32_16x16x32_bf16 v[172:175], a[24:27], a[136:139], v[172:175]
ds_read_b64_tr_b16 a[12:13], v14 offset:57352
v_mfma_f32_16x16x32_bf16 v[172:175], a[28:31], a[140:143], v[172:175]
ds_read_b64_tr_b16 a[14:15], v14 offset:61448
v_mfma_f32_16x16x32_bf16 v[204:207], a[24:27], a[144:147], v[204:207]
buffer_load_dwordx4 a[104:107], v29, s[56:59], s91 offen offset:0 sc1
v_mfma_f32_16x16x32_bf16 v[204:207], a[28:31], a[148:151], v[204:207]
v_mfma_f32_16x16x32_bf16 v[236:239], a[24:27], a[152:155], v[236:239]
buffer_load_dwordx4 a[108:111], v29, s[56:59], s91 offen offset:1024 sc1
v_mfma_f32_16x16x32_bf16 v[236:239], a[28:31], a[156:159], v[236:239]
s_waitcnt lgkmcnt(4)
v_mfma_f32_16x16x32_bf16 v[144:147], a[0:3], a[128:131], v[144:147]
ds_read_b64_tr_b16 a[16:17], v15 offset:49152
v_mfma_f32_16x16x32_bf16 v[144:147], a[4:7], a[132:135], v[144:147]
ds_read_b64_tr_b16 a[18:19], v15 offset:53248
v_mfma_f32_16x16x32_bf16 v[176:179], a[0:3], a[136:139], v[176:179]
ds_read_b64_tr_b16 a[20:21], v15 offset:57344
v_mfma_f32_16x16x32_bf16 v[176:179], a[4:7], a[140:143], v[176:179]
ds_read_b64_tr_b16 a[22:23], v15 offset:61440
v_mfma_f32_16x16x32_bf16 v[208:211], a[0:3], a[144:147], v[208:211]
buffer_load_dwordx4 a[112:115], v29, s[56:59], s92 offen offset:0 sc1
v_mfma_f32_16x16x32_bf16 v[208:211], a[4:7], a[148:151], v[208:211]
v_mfma_f32_16x16x32_bf16 v[240:243], a[0:3], a[152:155], v[240:243]
buffer_load_dwordx4 a[116:119], v29, s[56:59], s92 offen offset:1024 sc1
v_mfma_f32_16x16x32_bf16 v[240:243], a[4:7], a[156:159], v[240:243]
s_waitcnt lgkmcnt(4)
v_mfma_f32_16x16x32_bf16 v[148:151], a[8:11], a[128:131], v[148:151]
ds_read_b64_tr_b16 a[24:25], v15 offset:49160
v_mfma_f32_16x16x32_bf16 v[148:151], a[12:15], a[132:135], v[148:151]
ds_read_b64_tr_b16 a[26:27], v15 offset:53256
v_mfma_f32_16x16x32_bf16 v[180:183], a[8:11], a[136:139], v[180:183]
ds_read_b64_tr_b16 a[28:29], v15 offset:57352
v_mfma_f32_16x16x32_bf16 v[180:183], a[12:15], a[140:143], v[180:183]
ds_read_b64_tr_b16 a[30:31], v15 offset:61448
v_mfma_f32_16x16x32_bf16 v[212:215], a[8:11], a[144:147], v[212:215]
buffer_load_dwordx4 a[120:123], v29, s[56:59], s93 offen offset:0 sc1
v_mfma_f32_16x16x32_bf16 v[212:215], a[12:15], a[148:151], v[212:215]
v_mfma_f32_16x16x32_bf16 v[244:247], a[8:11], a[152:155], v[244:247]
buffer_load_dwordx4 a[124:127], v29, s[56:59], s93 offen offset:1024 sc1
v_mfma_f32_16x16x32_bf16 v[244:247], a[12:15], a[156:159], v[244:247]
s_waitcnt lgkmcnt(4)
v_mfma_f32_16x16x32_bf16 v[152:155], a[16:19], a[128:131], v[152:155]
ds_read_b64_tr_b16 a[0:1], v16 offset:0
v_mfma_f32_16x16x32_bf16 v[152:155], a[20:23], a[132:135], v[152:155]
ds_read_b64_tr_b16 a[2:3], v16 offset:4096
v_mfma_f32_16x16x32_bf16 v[184:187], a[16:19], a[136:139], v[184:187]
ds_read_b64_tr_b16 a[4:5], v16 offset:8192
v_mfma_f32_16x16x32_bf16 v[184:187], a[20:23], a[140:143], v[184:187]
ds_read_b64_tr_b16 a[6:7], v16 offset:12288
v_mfma_f32_16x16x32_bf16 v[216:219], a[16:19], a[144:147], v[216:219]
v_mfma_f32_16x16x32_bf16 v[216:219], a[20:23], a[148:151], v[216:219]
v_mfma_f32_16x16x32_bf16 v[248:251], a[16:19], a[152:155], v[248:251]
v_mfma_f32_16x16x32_bf16 v[248:251], a[20:23], a[156:159], v[248:251]
s_waitcnt lgkmcnt(4)
v_mfma_f32_16x16x32_bf16 v[156:159], a[24:27], a[128:131], v[156:159]
ds_read_b64_tr_b16 a[8:9], v17 offset:0
v_mfma_f32_16x16x32_bf16 v[156:159], a[28:31], a[132:135], v[156:159]
ds_read_b64_tr_b16 a[10:11], v17 offset:4096
v_mfma_f32_16x16x32_bf16 v[188:191], a[24:27], a[136:139], v[188:191]
ds_read_b64_tr_b16 a[12:13], v17 offset:8192
v_mfma_f32_16x16x32_bf16 v[188:191], a[28:31], a[140:143], v[188:191]
ds_read_b64_tr_b16 a[14:15], v17 offset:12288
v_mfma_f32_16x16x32_bf16 v[220:223], a[24:27], a[144:147], v[220:223]
s_waitcnt vmcnt(32)
v_mfma_f32_16x16x32_bf16 v[220:223], a[28:31], a[148:151], v[220:223]
s_barrier
v_mfma_f32_16x16x32_bf16 v[252:255], a[24:27], a[152:155], v[252:255]
s_waitcnt vmcnt(24)
v_mfma_f32_16x16x32_bf16 v[252:255], a[28:31], a[156:159], v[252:255]
s_waitcnt lgkmcnt(4)
v_mfma_f32_16x16x32_bf16 v[64:67], a[0:3], a[32:35], v[64:67]
ds_read_b64_tr_b16 a[16:17], v18 offset:0
v_mfma_f32_16x16x32_bf16 v[64:67], a[4:7], a[36:39], v[64:67]
ds_read_b64_tr_b16 a[18:19], v18 offset:4096
v_mfma_f32_16x16x32_bf16 v[80:83], a[0:3], a[40:43], v[80:83]
ds_read_b64_tr_b16 a[20:21], v18 offset:8192
v_mfma_f32_16x16x32_bf16 v[80:83], a[4:7], a[44:47], v[80:83]
ds_read_b64_tr_b16 a[22:23], v18 offset:12288
v_mfma_f32_16x16x32_bf16 v[96:99], a[0:3], a[48:51], v[96:99]
s_add_u32 m0, s68, 0xc000
s_add_u32 s79, s70, 0x6c00
buffer_load_dwordx4 v28, s[48:51], s70 offen offset:0 lds
v_mfma_f32_16x16x32_bf16 v[96:99], a[4:7], a[52:55], v[96:99]
buffer_load_dwordx4 v28, s[48:51], s79 offen offset:1024 lds
v_mfma_f32_16x16x32_bf16 v[112:115], a[0:3], a[56:59], v[112:115]
buffer_load_dwordx4 v28, s[52:55], s70 offen offset:2048 lds
v_mfma_f32_16x16x32_bf16 v[112:115], a[4:7], a[60:63], v[112:115]
s_waitcnt lgkmcnt(4)
v_mfma_f32_16x16x32_bf16 v[68:71], a[8:11], a[32:35], v[68:71]
ds_read_b64_tr_b16 a[24:25], v16 offset:2048
v_mfma_f32_16x16x32_bf16 v[68:71], a[12:15], a[36:39], v[68:71]
ds_read_b64_tr_b16 a[26:27], v16 offset:6144
v_mfma_f32_16x16x32_bf16 v[84:87], a[8:11], a[40:43], v[84:87]
ds_read_b64_tr_b16 a[28:29], v16 offset:10240
v_mfma_f32_16x16x32_bf16 v[84:87], a[12:15], a[44:47], v[84:87]
ds_read_b64_tr_b16 a[30:31], v16 offset:14336
v_mfma_f32_16x16x32_bf16 v[100:103], a[8:11], a[48:51], v[100:103]
buffer_load_dwordx4 v28, s[52:55], s79 offen offset:3072 lds
s_add_u32 s70, s70, 0x38000
v_mfma_f32_16x16x32_bf16 v[100:103], a[12:15], a[52:55], v[100:103]
s_add_u32 m0, s68, 0x1c000
s_add_u32 s80, s69, 0x7c00
buffer_load_dwordx4 v26, s[44:47], s69 offen offset:0 lds
v_mfma_f32_16x16x32_bf16 v[116:119], a[8:11], a[56:59], v[116:119]
s_add_u32 s81, s69, 0xf800
buffer_load_dwordx4 v27, s[44:47], s80 offen offset:1024 lds
v_mfma_f32_16x16x32_bf16 v[116:119], a[12:15], a[60:63], v[116:119]
s_waitcnt lgkmcnt(4)
v_mfma_f32_16x16x32_bf16 v[72:75], a[16:19], a[32:35], v[72:75]
ds_read_b64_tr_b16 a[0:1], v17 offset:2048
v_mfma_f32_16x16x32_bf16 v[72:75], a[20:23], a[36:39], v[72:75]
ds_read_b64_tr_b16 a[2:3], v17 offset:6144
v_mfma_f32_16x16x32_bf16 v[88:91], a[16:19], a[40:43], v[88:91]
ds_read_b64_tr_b16 a[4:5], v17 offset:10240
v_mfma_f32_16x16x32_bf16 v[88:91], a[20:23], a[44:47], v[88:91]
ds_read_b64_tr_b16 a[6:7], v17 offset:14336
v_mfma_f32_16x16x32_bf16 v[104:107], a[16:19], a[48:51], v[104:107]
s_add_u32 s82, s69, 0x17400
buffer_load_dwordx4 v26, s[44:47], s81 offen offset:2048 lds
v_mfma_f32_16x16x32_bf16 v[104:107], a[20:23], a[52:55], v[104:107]
buffer_load_dwordx4 v27, s[44:47], s82 offen offset:3072 lds
s_add_u32 s69, s69, 0x80000
v_mfma_f32_16x16x32_bf16 v[120:123], a[16:19], a[56:59], v[120:123]
buffer_load_dwordx4 a[128:131], v29, s[56:59], s90 offen offset:2048 sc1
v_mfma_f32_16x16x32_bf16 v[120:123], a[20:23], a[60:63], v[120:123]
s_waitcnt lgkmcnt(4)
v_mfma_f32_16x16x32_bf16 a[192:195], a[24:27], a[32:35], a[192:195]
ds_read_b64_tr_b16 a[8:9], v18 offset:2048
v_mfma_f32_16x16x32_bf16 a[192:195], a[28:31], a[36:39], a[192:195]
ds_read_b64_tr_b16 a[10:11], v18 offset:6144
v_mfma_f32_16x16x32_bf16 a[208:211], a[24:27], a[40:43], a[208:211]
ds_read_b64_tr_b16 a[12:13], v18 offset:10240
v_mfma_f32_16x16x32_bf16 a[208:211], a[28:31], a[44:47], a[208:211]
ds_read_b64_tr_b16 a[14:15], v18 offset:14336
v_mfma_f32_16x16x32_bf16 a[224:227], a[24:27], a[48:51], a[224:227]
buffer_load_dwordx4 a[132:135], v29, s[56:59], s90 offen offset:3072 sc1
s_add_u32 s90, s90, 0x1000
v_mfma_f32_16x16x32_bf16 a[224:227], a[28:31], a[52:55], a[224:227]
buffer_load_dwordx4 a[136:139], v29, s[56:59], s91 offen offset:2048 sc1
v_mfma_f32_16x16x32_bf16 a[240:243], a[24:27], a[56:59], a[240:243]
buffer_load_dwordx4 a[140:143], v29, s[56:59], s91 offen offset:3072 sc1
s_add_u32 s91, s91, 0x1000
v_mfma_f32_16x16x32_bf16 a[240:243], a[28:31], a[60:63], a[240:243]
s_waitcnt lgkmcnt(4)
v_mfma_f32_16x16x32_bf16 a[196:199], a[0:3], a[32:35], a[196:199]
ds_read_b64_tr_b16 a[16:17], v16 offset:16384
v_mfma_f32_16x16x32_bf16 a[196:199], a[4:7], a[36:39], a[196:199]
ds_read_b64_tr_b16 a[18:19], v16 offset:20480
v_mfma_f32_16x16x32_bf16 a[212:215], a[0:3], a[40:43], a[212:215]
ds_read_b64_tr_b16 a[20:21], v16 offset:24576
v_mfma_f32_16x16x32_bf16 a[212:215], a[4:7], a[44:47], a[212:215]
ds_read_b64_tr_b16 a[22:23], v16 offset:28672
v_mfma_f32_16x16x32_bf16 a[228:231], a[0:3], a[48:51], a[228:231]
buffer_load_dwordx4 a[144:147], v29, s[56:59], s92 offen offset:2048 sc1
v_mfma_f32_16x16x32_bf16 a[228:231], a[4:7], a[52:55], a[228:231]
buffer_load_dwordx4 a[148:151], v29, s[56:59], s92 offen offset:3072 sc1
s_add_u32 s92, s92, 0x1000
v_mfma_f32_16x16x32_bf16 a[244:247], a[0:3], a[56:59], a[244:247]
buffer_load_dwordx4 a[152:155], v29, s[56:59], s93 offen offset:2048 sc1
v_mfma_f32_16x16x32_bf16 a[244:247], a[4:7], a[60:63], a[244:247]
s_waitcnt lgkmcnt(4)
v_mfma_f32_16x16x32_bf16 a[200:203], a[8:11], a[32:35], a[200:203]
ds_read_b64_tr_b16 a[24:25], v17 offset:16384
v_mfma_f32_16x16x32_bf16 a[200:203], a[12:15], a[36:39], a[200:203]
ds_read_b64_tr_b16 a[26:27], v17 offset:20480
v_mfma_f32_16x16x32_bf16 a[216:219], a[8:11], a[40:43], a[216:219]
ds_read_b64_tr_b16 a[28:29], v17 offset:24576
v_mfma_f32_16x16x32_bf16 a[216:219], a[12:15], a[44:47], a[216:219]
ds_read_b64_tr_b16 a[30:31], v17 offset:28672
v_mfma_f32_16x16x32_bf16 a[232:235], a[8:11], a[48:51], a[232:235]
s_waitcnt vmcnt(31)
v_mfma_f32_16x16x32_bf16 a[232:235], a[12:15], a[52:55], a[232:235]
s_barrier
v_mfma_f32_16x16x32_bf16 a[248:251], a[8:11], a[56:59], a[248:251]
s_waitcnt vmcnt(23)
v_mfma_f32_16x16x32_bf16 a[248:251], a[12:15], a[60:63], a[248:251]
s_waitcnt lgkmcnt(4)
v_mfma_f32_16x16x32_bf16 v[64:67], a[16:19], a[64:67], v[64:67]
ds_read_b64_tr_b16 a[0:1], v18 offset:16384
v_mfma_f32_16x16x32_bf16 v[64:67], a[20:23], a[68:71], v[64:67]
ds_read_b64_tr_b16 a[2:3], v18 offset:20480
v_mfma_f32_16x16x32_bf16 v[80:83], a[16:19], a[72:75], v[80:83]
ds_read_b64_tr_b16 a[4:5], v18 offset:24576
v_mfma_f32_16x16x32_bf16 v[80:83], a[20:23], a[76:79], v[80:83]
ds_read_b64_tr_b16 a[6:7], v18 offset:28672
v_mfma_f32_16x16x32_bf16 v[96:99], a[16:19], a[80:83], v[96:99]
buffer_load_dwordx4 a[156:159], v29, s[56:59], s93 offen offset:3072 sc1
s_add_u32 s93, s93, 0x1000
v_mfma_f32_16x16x32_bf16 v[96:99], a[20:23], a[84:87], v[96:99]
v_mfma_f32_16x16x32_bf16 v[112:115], a[16:19], a[88:91], v[112:115]
s_mov_b32 m0, s68
s_nop 0
buffer_load_dwordx4 v20, s[36:39], s76 offen offset:0 nt lds
v_mfma_f32_16x16x32_bf16 v[112:115], a[20:23], a[92:95], v[112:115]
s_waitcnt lgkmcnt(4)
v_mfma_f32_16x16x32_bf16 v[68:71], a[24:27], a[64:67], v[68:71]
ds_read_b64_tr_b16 a[8:9], v16 offset:18432
v_mfma_f32_16x16x32_bf16 v[68:71], a[28:31], a[68:71], v[68:71]
ds_read_b64_tr_b16 a[10:11], v16 offset:22528
v_mfma_f32_16x16x32_bf16 v[84:87], a[24:27], a[72:75], v[84:87]
ds_read_b64_tr_b16 a[12:13], v16 offset:26624
v_mfma_f32_16x16x32_bf16 v[84:87], a[28:31], a[76:79], v[84:87]
ds_read_b64_tr_b16 a[14:15], v16 offset:30720
v_mfma_f32_16x16x32_bf16 v[100:103], a[24:27], a[80:83], v[100:103]
buffer_load_dwordx4 v21, s[36:39], s76 offen offset:1024 nt lds
v_mfma_f32_16x16x32_bf16 v[100:103], a[28:31], a[84:87], v[100:103]
v_mfma_f32_16x16x32_bf16 v[116:119], a[24:27], a[88:91], v[116:119]
buffer_load_dwordx4 v22, s[36:39], s76 offen offset:2048 nt lds
v_mfma_f32_16x16x32_bf16 v[116:119], a[28:31], a[92:95], v[116:119]
s_waitcnt lgkmcnt(4)
v_mfma_f32_16x16x32_bf16 v[72:75], a[0:3], a[64:67], v[72:75]
ds_read_b64_tr_b16 a[16:17], v17 offset:18432
v_mfma_f32_16x16x32_bf16 v[72:75], a[4:7], a[68:71], v[72:75]
ds_read_b64_tr_b16 a[18:19], v17 offset:22528
v_mfma_f32_16x16x32_bf16 v[88:91], a[0:3], a[72:75], v[88:91]
ds_read_b64_tr_b16 a[20:21], v17 offset:26624
v_mfma_f32_16x16x32_bf16 v[88:91], a[4:7], a[76:79], v[88:91]
ds_read_b64_tr_b16 a[22:23], v17 offset:30720
v_mfma_f32_16x16x32_bf16 v[104:107], a[0:3], a[80:83], v[104:107]
buffer_load_dwordx4 v23, s[36:39], s76 offen offset:3072 nt lds
s_add_u32 s76, s76, 0x4000
v_mfma_f32_16x16x32_bf16 v[104:107], a[4:7], a[84:87], v[104:107]
v_mfma_f32_16x16x32_bf16 v[120:123], a[0:3], a[88:91], v[120:123]
v_mfma_f32_16x16x32_bf16 v[120:123], a[4:7], a[92:95], v[120:123]
s_waitcnt lgkmcnt(4)
v_mfma_f32_16x16x32_bf16 a[192:195], a[8:11], a[64:67], a[192:195]
ds_read_b64_tr_b16 a[24:25], v18 offset:18432
v_mfma_f32_16x16x32_bf16 a[192:195], a[12:15], a[68:71], a[192:195]
ds_read_b64_tr_b16 a[26:27], v18 offset:22528
v_mfma_f32_16x16x32_bf16 a[208:211], a[8:11], a[72:75], a[208:211]
ds_read_b64_tr_b16 a[28:29], v18 offset:26624
v_mfma_f32_16x16x32_bf16 a[208:211], a[12:15], a[76:79], a[208:211]
ds_read_b64_tr_b16 a[30:31], v18 offset:30720
v_mfma_f32_16x16x32_bf16 a[224:227], a[8:11], a[80:83], a[224:227]
v_mfma_f32_16x16x32_bf16 a[224:227], a[12:15], a[84:87], a[224:227]
v_mfma_f32_16x16x32_bf16 a[240:243], a[8:11], a[88:91], a[240:243]
v_mfma_f32_16x16x32_bf16 a[240:243], a[12:15], a[92:95], a[240:243]
s_waitcnt lgkmcnt(4)
v_mfma_f32_16x16x32_bf16 a[196:199], a[16:19], a[64:67], a[196:199]
ds_read_b64_tr_b16 a[0:1], v16 offset:32768
v_mfma_f32_16x16x32_bf16 a[196:199], a[20:23], a[68:71], a[196:199]
ds_read_b64_tr_b16 a[2:3], v16 offset:36864
v_mfma_f32_16x16x32_bf16 a[212:215], a[16:19], a[72:75], a[212:215]
ds_read_b64_tr_b16 a[4:5], v16 offset:40960
v_mfma_f32_16x16x32_bf16 a[212:215], a[20:23], a[76:79], a[212:215]
ds_read_b64_tr_b16 a[6:7], v16 offset:45056
v_mfma_f32_16x16x32_bf16 a[228:231], a[16:19], a[80:83], a[228:231]
v_mfma_f32_16x16x32_bf16 a[228:231], a[20:23], a[84:87], a[228:231]
v_mfma_f32_16x16x32_bf16 a[244:247], a[16:19], a[88:91], a[244:247]
v_mfma_f32_16x16x32_bf16 a[244:247], a[20:23], a[92:95], a[244:247]
s_waitcnt lgkmcnt(4)
v_mfma_f32_16x16x32_bf16 a[200:203], a[24:27], a[64:67], a[200:203]
ds_read_b64_tr_b16 a[8:9], v17 offset:32768
v_mfma_f32_16x16x32_bf16 a[200:203], a[28:31], a[68:71], a[200:203]
ds_read_b64_tr_b16 a[10:11], v17 offset:36864
v_mfma_f32_16x16x32_bf16 a[216:219], a[24:27], a[72:75], a[216:219]
ds_read_b64_tr_b16 a[12:13], v17 offset:40960
v_mfma_f32_16x16x32_bf16 a[216:219], a[28:31], a[76:79], a[216:219]
ds_read_b64_tr_b16 a[14:15], v17 offset:45056
v_mfma_f32_16x16x32_bf16 a[232:235], a[24:27], a[80:83], a[232:235]
s_waitcnt vmcnt(16)
v_mfma_f32_16x16x32_bf16 a[232:235], a[28:31], a[84:87], a[232:235]
s_barrier
v_mfma_f32_16x16x32_bf16 a[248:251], a[24:27], a[88:91], a[248:251]
s_waitcnt vmcnt(20)
v_mfma_f32_16x16x32_bf16 a[248:251], a[28:31], a[92:95], a[248:251]
s_waitcnt lgkmcnt(4)
v_mfma_f32_16x16x32_bf16 v[64:67], a[0:3], a[96:99], v[64:67]
ds_read_b64_tr_b16 a[16:17], v18 offset:32768
v_mfma_f32_16x16x32_bf16 v[64:67], a[4:7], a[100:103], v[64:67]
ds_read_b64_tr_b16 a[18:19], v18 offset:36864
v_mfma_f32_16x16x32_bf16 v[80:83], a[0:3], a[104:107], v[80:83]
ds_read_b64_tr_b16 a[20:21], v18 offset:40960
v_mfma_f32_16x16x32_bf16 v[80:83], a[4:7], a[108:111], v[80:83]
ds_read_b64_tr_b16 a[22:23], v18 offset:45056
v_mfma_f32_16x16x32_bf16 v[96:99], a[0:3], a[112:115], v[96:99]
s_add_u32 m0, s68, 0x4000
s_nop 0
buffer_load_dwordx4 v20, s[36:39], s76 offen offset:0 nt lds
v_mfma_f32_16x16x32_bf16 v[96:99], a[4:7], a[116:119], v[96:99]
v_mfma_f32_16x16x32_bf16 v[112:115], a[0:3], a[120:123], v[112:115]
buffer_load_dwordx4 v21, s[36:39], s76 offen offset:1024 nt lds
v_mfma_f32_16x16x32_bf16 v[112:115], a[4:7], a[124:127], v[112:115]
s_waitcnt lgkmcnt(4)
v_mfma_f32_16x16x32_bf16 v[68:71], a[8:11], a[96:99], v[68:71]
ds_read_b64_tr_b16 a[24:25], v16 offset:34816
v_mfma_f32_16x16x32_bf16 v[68:71], a[12:15], a[100:103], v[68:71]
ds_read_b64_tr_b16 a[26:27], v16 offset:38912
v_mfma_f32_16x16x32_bf16 v[84:87], a[8:11], a[104:107], v[84:87]
ds_read_b64_tr_b16 a[28:29], v16 offset:43008
v_mfma_f32_16x16x32_bf16 v[84:87], a[12:15], a[108:111], v[84:87]
ds_read_b64_tr_b16 a[30:31], v16 offset:47104
v_mfma_f32_16x16x32_bf16 v[100:103], a[8:11], a[112:115], v[100:103]
buffer_load_dwordx4 v22, s[36:39], s76 offen offset:2048 nt lds
v_mfma_f32_16x16x32_bf16 v[100:103], a[12:15], a[116:119], v[100:103]
v_mfma_f32_16x16x32_bf16 v[116:119], a[8:11], a[120:123], v[116:119]
buffer_load_dwordx4 v23, s[36:39], s76 offen offset:3072 nt lds
s_add_u32 s76, s76, 0x4000
v_mfma_f32_16x16x32_bf16 v[116:119], a[12:15], a[124:127], v[116:119]
s_waitcnt lgkmcnt(4)
v_mfma_f32_16x16x32_bf16 v[72:75], a[16:19], a[96:99], v[72:75]
ds_read_b64_tr_b16 a[0:1], v17 offset:34816
v_mfma_f32_16x16x32_bf16 v[72:75], a[20:23], a[100:103], v[72:75]
ds_read_b64_tr_b16 a[2:3], v17 offset:38912
v_mfma_f32_16x16x32_bf16 v[88:91], a[16:19], a[104:107], v[88:91]
ds_read_b64_tr_b16 a[4:5], v17 offset:43008
v_mfma_f32_16x16x32_bf16 v[88:91], a[20:23], a[108:111], v[88:91]
ds_read_b64_tr_b16 a[6:7], v17 offset:47104
v_mfma_f32_16x16x32_bf16 v[104:107], a[16:19], a[112:115], v[104:107]
v_mfma_f32_16x16x32_bf16 v[104:107], a[20:23], a[116:119], v[104:107]
v_mfma_f32_16x16x32_bf16 v[120:123], a[16:19], a[120:123], v[120:123]
v_mfma_f32_16x16x32_bf16 v[120:123], a[20:23], a[124:127], v[120:123]
s_waitcnt lgkmcnt(4)
v_mfma_f32_16x16x32_bf16 a[192:195], a[24:27], a[96:99], a[192:195]
ds_read_b64_tr_b16 a[8:9], v18 offset:34816
v_mfma_f32_16x16x32_bf16 a[192:195], a[28:31], a[100:103], a[192:195]
ds_read_b64_tr_b16 a[10:11], v18 offset:38912
v_mfma_f32_16x16x32_bf16 a[208:211], a[24:27], a[104:107], a[208:211]
ds_read_b64_tr_b16 a[12:13], v18 offset:43008
v_mfma_f32_16x16x32_bf16 a[208:211], a[28:31], a[108:111], a[208:211]
ds_read_b64_tr_b16 a[14:15], v18 offset:47104
v_mfma_f32_16x16x32_bf16 a[224:227], a[24:27], a[112:115], a[224:227]
v_mfma_f32_16x16x32_bf16 a[224:227], a[28:31], a[116:119], a[224:227]
v_mfma_f32_16x16x32_bf16 a[240:243], a[24:27], a[120:123], a[240:243]
v_mfma_f32_16x16x32_bf16 a[240:243], a[28:31], a[124:127], a[240:243]
s_waitcnt lgkmcnt(4)
v_mfma_f32_16x16x32_bf16 a[196:199], a[0:3], a[96:99], a[196:199]
ds_read_b64_tr_b16 a[16:17], v16 offset:49152
v_mfma_f32_16x16x32_bf16 a[196:199], a[4:7], a[100:103], a[196:199]
ds_read_b64_tr_b16 a[18:19], v16 offset:53248
v_mfma_f32_16x16x32_bf16 a[212:215], a[0:3], a[104:107], a[212:215]
ds_read_b64_tr_b16 a[20:21], v16 offset:57344
v_mfma_f32_16x16x32_bf16 a[212:215], a[4:7], a[108:111], a[212:215]
ds_read_b64_tr_b16 a[22:23], v16 offset:61440
v_mfma_f32_16x16x32_bf16 a[228:231], a[0:3], a[112:115], a[228:231]
v_mfma_f32_16x16x32_bf16 a[228:231], a[4:7], a[116:119], a[228:231]
v_mfma_f32_16x16x32_bf16 a[244:247], a[0:3], a[120:123], a[244:247]
v_mfma_f32_16x16x32_bf16 a[244:247], a[4:7], a[124:127], a[244:247]
s_waitcnt lgkmcnt(4)
v_mfma_f32_16x16x32_bf16 a[200:203], a[8:11], a[96:99], a[200:203]
ds_read_b64_tr_b16 a[24:25], v17 offset:49152
v_mfma_f32_16x16x32_bf16 a[200:203], a[12:15], a[100:103], a[200:203]
ds_read_b64_tr_b16 a[26:27], v17 offset:53248
v_mfma_f32_16x16x32_bf16 a[216:219], a[8:11], a[104:107], a[216:219]
ds_read_b64_tr_b16 a[28:29], v17 offset:57344
v_mfma_f32_16x16x32_bf16 a[216:219], a[12:15], a[108:111], a[216:219]
ds_read_b64_tr_b16 a[30:31], v17 offset:61440
v_mfma_f32_16x16x32_bf16 a[232:235], a[8:11], a[112:115], a[232:235]
v_mfma_f32_16x16x32_bf16 a[232:235], a[12:15], a[116:119], a[232:235]
s_barrier
v_mfma_f32_16x16x32_bf16 a[248:251], a[8:11], a[120:123], a[248:251]
s_waitcnt vmcnt(8)
v_mfma_f32_16x16x32_bf16 a[248:251], a[12:15], a[124:127], a[248:251]
s_waitcnt lgkmcnt(4)
v_mfma_f32_16x16x32_bf16 v[64:67], a[16:19], a[128:131], v[64:67]
ds_read_b64_tr_b16 a[0:1], v18 offset:49152
v_mfma_f32_16x16x32_bf16 v[64:67], a[20:23], a[132:135], v[64:67]
ds_read_b64_tr_b16 a[2:3], v18 offset:53248
v_mfma_f32_16x16x32_bf16 v[80:83], a[16:19], a[136:139], v[80:83]
ds_read_b64_tr_b16 a[4:5], v18 offset:57344
v_mfma_f32_16x16x32_bf16 v[80:83], a[20:23], a[140:143], v[80:83]
ds_read_b64_tr_b16 a[6:7], v18 offset:61440
v_mfma_f32_16x16x32_bf16 v[96:99], a[16:19], a[144:147], v[96:99]
s_add_u32 m0, s68, 0x8000
s_nop 0
buffer_load_dwordx4 v20, s[36:39], s76 offen offset:0 nt lds
v_mfma_f32_16x16x32_bf16 v[96:99], a[20:23], a[148:151], v[96:99]
v_mfma_f32_16x16x32_bf16 v[112:115], a[16:19], a[152:155], v[112:115]
buffer_load_dwordx4 v21, s[36:39], s76 offen offset:1024 nt lds
v_mfma_f32_16x16x32_bf16 v[112:115], a[20:23], a[156:159], v[112:115]
s_waitcnt lgkmcnt(4)
v_mfma_f32_16x16x32_bf16 v[68:71], a[24:27], a[128:131], v[68:71]
ds_read_b64_tr_b16 a[8:9], v16 offset:51200
v_mfma_f32_16x16x32_bf16 v[68:71], a[28:31], a[132:135], v[68:71]
ds_read_b64_tr_b16 a[10:11], v16 offset:55296
v_mfma_f32_16x16x32_bf16 v[84:87], a[24:27], a[136:139], v[84:87]
ds_read_b64_tr_b16 a[12:13], v16 offset:59392
v_mfma_f32_16x16x32_bf16 v[84:87], a[28:31], a[140:143], v[84:87]
ds_read_b64_tr_b16 a[14:15], v16 offset:63488
v_mfma_f32_16x16x32_bf16 v[100:103], a[24:27], a[144:147], v[100:103]
buffer_load_dwordx4 v22, s[36:39], s76 offen offset:2048 nt lds
v_mfma_f32_16x16x32_bf16 v[100:103], a[28:31], a[148:151], v[100:103]
v_mfma_f32_16x16x32_bf16 v[116:119], a[24:27], a[152:155], v[116:119]
buffer_load_dwordx4 v23, s[36:39], s76 offen offset:3072 nt lds
s_add_u32 s76, s76, 0x4000
v_mfma_f32_16x16x32_bf16 v[116:119], a[28:31], a[156:159], v[116:119]
s_waitcnt lgkmcnt(4)
v_mfma_f32_16x16x32_bf16 v[72:75], a[0:3], a[128:131], v[72:75]
ds_read_b64_tr_b16 a[16:17], v17 offset:51200
v_mfma_f32_16x16x32_bf16 v[72:75], a[4:7], a[132:135], v[72:75]
ds_read_b64_tr_b16 a[18:19], v17 offset:55296
v_mfma_f32_16x16x32_bf16 v[88:91], a[0:3], a[136:139], v[88:91]
ds_read_b64_tr_b16 a[20:21], v17 offset:59392
v_mfma_f32_16x16x32_bf16 v[88:91], a[4:7], a[140:143], v[88:91]
ds_read_b64_tr_b16 a[22:23], v17 offset:63488
v_mfma_f32_16x16x32_bf16 v[104:107], a[0:3], a[144:147], v[104:107]
s_mov_b32 s90, 0
s_mov_b32 s91, 0xe000
s_mov_b32 s92, 0x1c000
s_mov_b32 s93, 0x2a000
v_mfma_f32_16x16x32_bf16 v[104:107], a[4:7], a[148:151], v[104:107]
v_mfma_f32_16x16x32_bf16 v[120:123], a[0:3], a[152:155], v[120:123]
v_mfma_f32_16x16x32_bf16 v[120:123], a[4:7], a[156:159], v[120:123]
s_waitcnt lgkmcnt(4)
v_mfma_f32_16x16x32_bf16 a[192:195], a[8:11], a[128:131], a[192:195]
ds_read_b64_tr_b16 a[24:25], v18 offset:51200
v_mfma_f32_16x16x32_bf16 a[192:195], a[12:15], a[132:135], a[192:195]
ds_read_b64_tr_b16 a[26:27], v18 offset:55296
v_mfma_f32_16x16x32_bf16 a[208:211], a[8:11], a[136:139], a[208:211]
ds_read_b64_tr_b16 a[28:29], v18 offset:59392
v_mfma_f32_16x16x32_bf16 a[208:211], a[12:15], a[140:143], a[208:211]
ds_read_b64_tr_b16 a[30:31], v18 offset:63488
v_mfma_f32_16x16x32_bf16 a[224:227], a[8:11], a[144:147], a[224:227]
v_mfma_f32_16x16x32_bf16 a[224:227], a[12:15], a[148:151], a[224:227]
v_mfma_f32_16x16x32_bf16 a[240:243], a[8:11], a[152:155], a[240:243]
v_mfma_f32_16x16x32_bf16 a[240:243], a[12:15], a[156:159], a[240:243]
s_waitcnt lgkmcnt(4)
v_mfma_f32_16x16x32_bf16 a[196:199], a[16:19], a[128:131], a[196:199]
ds_read_b64_tr_b16 a[0:1], v12 offset:0
v_mfma_f32_16x16x32_bf16 a[196:199], a[20:23], a[132:135], a[196:199]
ds_read_b64_tr_b16 a[2:3], v12 offset:4096
v_mfma_f32_16x16x32_bf16 a[212:215], a[16:19], a[136:139], a[212:215]
ds_read_b64_tr_b16 a[4:5], v12 offset:8192
v_mfma_f32_16x16x32_bf16 a[212:215], a[20:23], a[140:143], a[212:215]
ds_read_b64_tr_b16 a[6:7], v12 offset:12288
v_mfma_f32_16x16x32_bf16 a[228:231], a[16:19], a[144:147], a[228:231]
v_mfma_f32_16x16x32_bf16 a[228:231], a[20:23], a[148:151], a[228:231]
v_mfma_f32_16x16x32_bf16 a[244:247], a[16:19], a[152:155], a[244:247]
v_mfma_f32_16x16x32_bf16 a[244:247], a[20:23], a[156:159], a[244:247]
s_waitcnt lgkmcnt(4)
v_mfma_f32_16x16x32_bf16 a[200:203], a[24:27], a[128:131], a[200:203]
ds_read_b64_tr_b16 a[8:9], v12 offset:8
v_mfma_f32_16x16x32_bf16 a[200:203], a[28:31], a[132:135], a[200:203]
ds_read_b64_tr_b16 a[10:11], v12 offset:4104
v_mfma_f32_16x16x32_bf16 a[216:219], a[24:27], a[136:139], a[216:219]
ds_read_b64_tr_b16 a[12:13], v12 offset:8200
v_mfma_f32_16x16x32_bf16 a[216:219], a[28:31], a[140:143], a[216:219]
ds_read_b64_tr_b16 a[14:15], v12 offset:12296
v_mfma_f32_16x16x32_bf16 a[232:235], a[24:27], a[144:147], a[232:235]
s_waitcnt vmcnt(20)
v_mfma_f32_16x16x32_bf16 a[232:235], a[28:31], a[148:151], a[232:235]
s_barrier
v_mfma_f32_16x16x32_bf16 a[248:251], a[24:27], a[152:155], a[248:251]
v_mfma_f32_16x16x32_bf16 a[248:251], a[28:31], a[156:159], a[248:251]
s_waitcnt lgkmcnt(4)
v_mfma_f32_16x16x32_bf16 v[128:131], a[0:3], a[32:35], v[128:131]
ds_read_b64_tr_b16 a[16:17], v13 offset:0
v_mfma_f32_16x16x32_bf16 v[128:131], a[4:7], a[36:39], v[128:131]
ds_read_b64_tr_b16 a[18:19], v13 offset:4096
v_mfma_f32_16x16x32_bf16 v[160:163], a[0:3], a[40:43], v[160:163]
ds_read_b64_tr_b16 a[20:21], v13 offset:8192
v_mfma_f32_16x16x32_bf16 v[160:163], a[4:7], a[44:47], v[160:163]
ds_read_b64_tr_b16 a[22:23], v13 offset:12288
v_mfma_f32_16x16x32_bf16 v[192:195], a[0:3], a[48:51], v[192:195]
s_add_u32 m0, s68, 0xc000
s_nop 0
buffer_load_dwordx4 v20, s[36:39], s76 offen offset:0 nt lds
v_readlane_b32 s79, v49, 18
v_mfma_f32_16x16x32_bf16 v[192:195], a[4:7], a[52:55], v[192:195]
v_readlane_b32 s80, v49, 19
v_add_u32_e32 v46, s79, v29
v_mfma_f32_16x16x32_bf16 v[224:227], a[0:3], a[56:59], v[224:227]
buffer_load_dwordx4 v21, s[36:39], s76 offen offset:1024 nt lds
v_add_u32_e32 v47, s80, v29
v_mul_f32_e32 v38, s86, v64
v_mfma_f32_16x16x32_bf16 v[224:227], a[4:7], a[60:63], v[224:227]
s_waitcnt lgkmcnt(4)
v_mul_f32_e32 v39, s86, v65
v_mul_f32_e32 v40, s86, v66
v_mfma_f32_16x16x32_bf16 v[132:135], a[8:11], a[32:35], v[132:135]
ds_read_b64_tr_b16 a[24:25], v13 offset:8
v_mul_f32_e32 v41, s86, v67
v_mfma_f32_16x16x32_bf16 v[132:135], a[12:15], a[36:39], v[132:135]
ds_read_b64_tr_b16 a[26:27], v13 offset:4104
v_exp_f32_e32 v38, v38
v_accvgpr_read_b32 v42, a192
v_mfma_f32_16x16x32_bf16 v[164:167], a[8:11], a[40:43], v[164:167]
ds_read_b64_tr_b16 a[28:29], v13 offset:8200
v_exp_f32_e32 v39, v39
v_mfma_f32_16x16x32_bf16 v[164:167], a[12:15], a[44:47], v[164:167]
ds_read_b64_tr_b16 a[30:31], v13 offset:12296
v_accvgpr_read_b32 v43, a193
v_mfma_f32_16x16x32_bf16 v[196:199], a[8:11], a[48:51], v[196:199]
buffer_load_dwordx4 v22, s[36:39], s76 offen offset:2048 nt lds
v_exp_f32_e32 v40, v40
v_accvgpr_read_b32 v44, a194
v_mfma_f32_16x16x32_bf16 v[196:199], a[12:15], a[52:55], v[196:199]
v_exp_f32_e32 v41, v41
v_mfma_f32_16x16x32_bf16 v[228:231], a[8:11], a[56:59], v[228:231]
buffer_load_dwordx4 v23, s[36:39], s76 offen offset:3072 nt lds
s_add_u32 s76, s76, 0x4000
v_accvgpr_read_b32 v45, a195
v_add_f32_e32 v38, 1.0, v38
v_mfma_f32_16x16x32_bf16 v[228:231], a[12:15], a[60:63], v[228:231]
s_waitcnt lgkmcnt(4)
v_add_f32_e32 v39, 1.0, v39
v_add_f32_e32 v40, 1.0, v40
v_mfma_f32_16x16x32_bf16 v[136:139], a[16:19], a[32:35], v[136:139]
ds_read_b64_tr_b16 a[0:1], v14 offset:0
v_add_f32_e32 v41, 1.0, v41
v_mfma_f32_16x16x32_bf16 v[136:139], a[20:23], a[36:39], v[136:139]
ds_read_b64_tr_b16 a[2:3], v14 offset:4096
v_rcp_f32_e32 v38, v38
v_mul_f32_e32 v42, v42, v64
v_mfma_f32_16x16x32_bf16 v[168:171], a[16:19], a[40:43], v[168:171]
ds_read_b64_tr_b16 a[4:5], v14 offset:8192
v_rcp_f32_e32 v39, v39
v_mfma_f32_16x16x32_bf16 v[168:171], a[20:23], a[44:47], v[168:171]
ds_read_b64_tr_b16 a[6:7], v14 offset:12288
v_mul_f32_e32 v43, v43, v65
v_mfma_f32_16x16x32_bf16 v[200:203], a[16:19], a[48:51], v[200:203]
v_rcp_f32_e32 v40, v40
v_mfma_f32_16x16x32_bf16 v[200:203], a[20:23], a[52:55], v[200:203]
v_mul_f32_e32 v44, v44, v66
v_mfma_f32_16x16x32_bf16 v[232:235], a[16:19], a[56:59], v[232:235]
v_rcp_f32_e32 v41, v41
v_mul_f32_e32 v45, v45, v67
v_mfma_f32_16x16x32_bf16 v[232:235], a[20:23], a[60:63], v[232:235]
s_waitcnt lgkmcnt(4)
v_mul_f32_e32 v42, v42, v38
v_mul_f32_e32 v43, v43, v39
v_mfma_f32_16x16x32_bf16 v[140:143], a[24:27], a[32:35], v[140:143]
ds_read_b64_tr_b16 a[8:9], v14 offset:8
v_mul_f32_e32 v44, v44, v40
v_mul_f32_e32 v45, v45, v41
v_mfma_f32_16x16x32_bf16 v[140:143], a[28:31], a[36:39], v[140:143]
ds_read_b64_tr_b16 a[10:11], v14 offset:4104
v_cvt_pk_bf16_f32 v30, v42, v43
v_cvt_pk_bf16_f32 v31, v44, v45
v_mfma_f32_16x16x32_bf16 v[172:175], a[24:27], a[40:43], v[172:175]
ds_read_b64_tr_b16 a[12:13], v14 offset:8200
v_mul_f32_e32 v38, s86, v68
v_mul_f32_e32 v39, s86, v69
v_mfma_f32_16x16x32_bf16 v[172:175], a[28:31], a[44:47], v[172:175]
ds_read_b64_tr_b16 a[14:15], v14 offset:12296
v_mul_f32_e32 v40, s86, v70
v_mul_f32_e32 v41, s86, v71
v_mfma_f32_16x16x32_bf16 v[204:207], a[24:27], a[48:51], v[204:207]
v_exp_f32_e32 v38, v38
v_mfma_f32_16x16x32_bf16 v[204:207], a[28:31], a[52:55], v[204:207]
v_accvgpr_read_b32 v42, a196
v_mfma_f32_16x16x32_bf16 v[236:239], a[24:27], a[56:59], v[236:239]
v_exp_f32_e32 v39, v39
v_accvgpr_read_b32 v43, a197
v_mfma_f32_16x16x32_bf16 v[236:239], a[28:31], a[60:63], v[236:239]
s_waitcnt lgkmcnt(4)
v_exp_f32_e32 v40, v40
v_mfma_f32_16x16x32_bf16 v[144:147], a[0:3], a[32:35], v[144:147]
ds_read_b64_tr_b16 a[16:17], v15 offset:0
v_accvgpr_read_b32 v44, a198
v_mfma_f32_16x16x32_bf16 v[144:147], a[4:7], a[36:39], v[144:147]
ds_read_b64_tr_b16 a[18:19], v15 offset:4096
v_exp_f32_e32 v41, v41
v_accvgpr_read_b32 v45, a199
v_mfma_f32_16x16x32_bf16 v[176:179], a[0:3], a[40:43], v[176:179]
ds_read_b64_tr_b16 a[20:21], v15 offset:8192
v_add_f32_e32 v38, 1.0, v38
v_mfma_f32_16x16x32_bf16 v[176:179], a[4:7], a[44:47], v[176:179]
ds_read_b64_tr_b16 a[22:23], v15 offset:12288
v_add_f32_e32 v39, 1.0, v39
v_add_f32_e32 v40, 1.0, v40
v_mfma_f32_16x16x32_bf16 v[208:211], a[0:3], a[48:51], v[208:211]
v_add_f32_e32 v41, 1.0, v41
v_mfma_f32_16x16x32_bf16 v[208:211], a[4:7], a[52:55], v[208:211]
v_rcp_f32_e32 v38, v38
v_mul_f32_e32 v42, v42, v68
v_mfma_f32_16x16x32_bf16 v[240:243], a[0:3], a[56:59], v[240:243]
v_rcp_f32_e32 v39, v39
v_mfma_f32_16x16x32_bf16 v[240:243], a[4:7], a[60:63], v[240:243]
s_waitcnt lgkmcnt(4)
v_mul_f32_e32 v43, v43, v69
v_mfma_f32_16x16x32_bf16 v[148:151], a[8:11], a[32:35], v[148:151]
ds_read_b64_tr_b16 a[24:25], v15 offset:8
v_rcp_f32_e32 v40, v40
v_mul_f32_e32 v44, v44, v70
v_mfma_f32_16x16x32_bf16 v[148:151], a[12:15], a[36:39], v[148:151]
ds_read_b64_tr_b16 a[26:27], v15 offset:4104
v_rcp_f32_e32 v41, v41
v_mfma_f32_16x16x32_bf16 v[180:183], a[8:11], a[40:43], v[180:183]
ds_read_b64_tr_b16 a[28:29], v15 offset:8200
v_mul_f32_e32 v45, v45, v71
v_mul_f32_e32 v42, v42, v38
v_mfma_f32_16x16x32_bf16 v[180:183], a[12:15], a[44:47], v[180:183]
ds_read_b64_tr_b16 a[30:31], v15 offset:12296
v_mul_f32_e32 v43, v43, v39
v_mul_f32_e32 v44, v44, v40
v_mfma_f32_16x16x32_bf16 v[212:215], a[8:11], a[48:51], v[212:215]
v_mul_f32_e32 v45, v45, v41
v_cvt_pk_bf16_f32 v32, v42, v43
v_mfma_f32_16x16x32_bf16 v[212:215], a[12:15], a[52:55], v[212:215]
v_cvt_pk_bf16_f32 v33, v44, v45
v_mfma_f32_16x16x32_bf16 v[244:247], a[8:11], a[56:59], v[244:247]
buffer_store_dwordx4 v[30:33], v46, s[64:67], s90 offen offset:0 sc1
v_mul_f32_e32 v38, s86, v72
v_mfma_f32_16x16x32_bf16 v[244:247], a[12:15], a[60:63], v[244:247]
s_waitcnt lgkmcnt(4)
v_mul_f32_e32 v39, s86, v73
v_mul_f32_e32 v40, s86, v74
v_mfma_f32_16x16x32_bf16 v[152:155], a[16:19], a[32:35], v[152:155]
ds_read_b64_tr_b16 a[0:1], v12 offset:16384
v_mul_f32_e32 v41, s86, v75
v_mfma_f32_16x16x32_bf16 v[152:155], a[20:23], a[36:39], v[152:155]
ds_read_b64_tr_b16 a[2:3], v12 offset:20480
v_exp_f32_e32 v38, v38
v_accvgpr_read_b32 v42, a200
v_mfma_f32_16x16x32_bf16 v[184:187], a[16:19], a[40:43], v[184:187]
ds_read_b64_tr_b16 a[4:5], v12 offset:24576
v_mfma_f32_16x16x32_bf16 v[184:187], a[20:23], a[44:47], v[184:187]
ds_read_b64_tr_b16 a[6:7], v12 offset:28672
v_exp_f32_e32 v39, v39
v_accvgpr_read_b32 v43, a201
v_mfma_f32_16x16x32_bf16 v[216:219], a[16:19], a[48:51], v[216:219]
v_exp_f32_e32 v40, v40
v_mfma_f32_16x16x32_bf16 v[216:219], a[20:23], a[52:55], v[216:219]
v_accvgpr_read_b32 v44, a202
v_mfma_f32_16x16x32_bf16 v[248:251], a[16:19], a[56:59], v[248:251]
v_exp_f32_e32 v41, v41
v_accvgpr_read_b32 v45, a203
v_mfma_f32_16x16x32_bf16 v[248:251], a[20:23], a[60:63], v[248:251]
s_waitcnt lgkmcnt(4)
v_add_f32_e32 v38, 1.0, v38
v_add_f32_e32 v39, 1.0, v39
v_mfma_f32_16x16x32_bf16 v[156:159], a[24:27], a[32:35], v[156:159]
ds_read_b64_tr_b16 a[8:9], v12 offset:16392
v_add_f32_e32 v40, 1.0, v40
v_add_f32_e32 v41, 1.0, v41
v_mfma_f32_16x16x32_bf16 v[156:159], a[28:31], a[36:39], v[156:159]
ds_read_b64_tr_b16 a[10:11], v12 offset:20488
v_rcp_f32_e32 v38, v38
v_mfma_f32_16x16x32_bf16 v[188:191], a[24:27], a[40:43], v[188:191]
ds_read_b64_tr_b16 a[12:13], v12 offset:24584
v_mul_f32_e32 v42, v42, v72
v_mfma_f32_16x16x32_bf16 v[188:191], a[28:31], a[44:47], v[188:191]
ds_read_b64_tr_b16 a[14:15], v12 offset:28680
v_rcp_f32_e32 v39, v39
v_mul_f32_e32 v43, v43, v73
v_mfma_f32_16x16x32_bf16 v[220:223], a[24:27], a[48:51], v[220:223]
v_rcp_f32_e32 v40, v40
v_mfma_f32_16x16x32_bf16 v[220:223], a[28:31], a[52:55], v[220:223]
s_barrier
v_mul_f32_e32 v44, v44, v74
v_mfma_f32_16x16x32_bf16 v[252:255], a[24:27], a[56:59], v[252:255]
v_rcp_f32_e32 v41, v41
v_mul_f32_e32 v45, v45, v75
v_mfma_f32_16x16x32_bf16 v[252:255], a[28:31], a[60:63], v[252:255]
s_waitcnt lgkmcnt(4)
v_mul_f32_e32 v42, v42, v38
v_mul_f32_e32 v43, v43, v39
v_mfma_f32_16x16x32_bf16 v[128:131], a[0:3], a[64:67], v[128:131]
ds_read_b64_tr_b16 a[16:17], v13 offset:16384
v_mul_f32_e32 v44, v44, v40
v_mul_f32_e32 v45, v45, v41
v_mfma_f32_16x16x32_bf16 v[128:131], a[4:7], a[68:71], v[128:131]
ds_read_b64_tr_b16 a[18:19], v13 offset:20480
v_cvt_pk_bf16_f32 v50, v42, v43
v_mfma_f32_16x16x32_bf16 v[160:163], a[0:3], a[72:75], v[160:163]
ds_read_b64_tr_b16 a[20:21], v13 offset:24576
v_cvt_pk_bf16_f32 v51, v44, v45
v_mfma_f32_16x16x32_bf16 v[160:163], a[4:7], a[76:79], v[160:163]
ds_read_b64_tr_b16 a[22:23], v13 offset:28672
v_mfma_f32_16x16x32_bf16 v[192:195], a[0:3], a[80:83], v[192:195]
s_add_u32 m0, s68, 0x10000
s_nop 0
buffer_load_dwordx4 v24, s[40:43], s77 offen offset:0 nt lds
v_mfma_f32_16x16x32_bf16 v[192:195], a[4:7], a[84:87], v[192:195]
buffer_store_dwordx2 v[50:51], v47, s[64:67], s90 offen offset:0 sc1
v_mul_f32_e32 v38, s86, v80
v_mul_f32_e32 v39, s86, v81
v_mul_f32_e32 v40, s86, v82
v_mul_f32_e32 v41, s86, v83
v_mfma_f32_16x16x32_bf16 v[224:227], a[0:3], a[88:91], v[224:227]
buffer_load_dwordx4 v25, s[40:43], s77 offen offset:1024 nt lds
v_exp_f32_e32 v38, v38
v_accvgpr_read_b32 v42, a208
v_mfma_f32_16x16x32_bf16 v[224:227], a[4:7], a[92:95], v[224:227]
s_waitcnt lgkmcnt(4)
v_exp_f32_e32 v39, v39
v_mfma_f32_16x16x32_bf16 v[132:135], a[8:11], a[64:67], v[132:135]
ds_read_b64_tr_b16 a[24:25], v13 offset:16392
v_accvgpr_read_b32 v43, a209
v_mfma_f32_16x16x32_bf16 v[132:135], a[12:15], a[68:71], v[132:135]
ds_read_b64_tr_b16 a[26:27], v13 offset:20488
v_exp_f32_e32 v40, v40
v_accvgpr_read_b32 v44, a210
v_mfma_f32_16x16x32_bf16 v[164:167], a[8:11], a[72:75], v[164:167]
ds_read_b64_tr_b16 a[28:29], v13 offset:24584
v_exp_f32_e32 v41, v41
v_mfma_f32_16x16x32_bf16 v[164:167], a[12:15], a[76:79], v[164:167]
ds_read_b64_tr_b16 a[30:31], v13 offset:28680
v_accvgpr_read_b32 v45, a211
v_add_f32_e32 v38, 1.0, v38
v_mfma_f32_16x16x32_bf16 v[196:199], a[8:11], a[80:83], v[196:199]
buffer_load_dwordx4 v24, s[40:43], s77 offen offset:2048 nt lds
v_add_f32_e32 v39, 1.0, v39
v_add_f32_e32 v40, 1.0, v40
v_mfma_f32_16x16x32_bf16 v[196:199], a[12:15], a[84:87], v[196:199]
v_add_f32_e32 v41, 1.0, v41
v_mfma_f32_16x16x32_bf16 v[228:231], a[8:11], a[88:91], v[228:231]
buffer_load_dwordx4 v25, s[40:43], s77 offen offset:3072 nt lds
s_add_u32 s77, s77, 0x4000
v_rcp_f32_e32 v38, v38
v_mul_f32_e32 v42, v42, v80
v_mfma_f32_16x16x32_bf16 v[228:231], a[12:15], a[92:95], v[228:231]
s_waitcnt lgkmcnt(4)
v_rcp_f32_e32 v39, v39
v_mfma_f32_16x16x32_bf16 v[136:139], a[16:19], a[64:67], v[136:139]
ds_read_b64_tr_b16 a[0:1], v14 offset:16384
v_mul_f32_e32 v43, v43, v81
v_mfma_f32_16x16x32_bf16 v[136:139], a[20:23], a[68:71], v[136:139]
ds_read_b64_tr_b16 a[2:3], v14 offset:20480
v_rcp_f32_e32 v40, v40
v_mfma_f32_16x16x32_bf16 v[168:171], a[16:19], a[72:75], v[168:171]
ds_read_b64_tr_b16 a[4:5], v14 offset:24576
v_mul_f32_e32 v44, v44, v82
v_mfma_f32_16x16x32_bf16 v[168:171], a[20:23], a[76:79], v[168:171]
ds_read_b64_tr_b16 a[6:7], v14 offset:28672
v_rcp_f32_e32 v41, v41
v_mul_f32_e32 v45, v45, v83
v_mfma_f32_16x16x32_bf16 v[200:203], a[16:19], a[80:83], v[200:203]
v_mul_f32_e32 v42, v42, v38
v_mul_f32_e32 v43, v43, v39
v_mfma_f32_16x16x32_bf16 v[200:203], a[20:23], a[84:87], v[200:203]
v_mul_f32_e32 v44, v44, v40
v_mul_f32_e32 v45, v45, v41
v_mfma_f32_16x16x32_bf16 v[232:235], a[16:19], a[88:91], v[232:235]
v_cvt_pk_bf16_f32 v30, v42, v43
v_cvt_pk_bf16_f32 v31, v44, v45
v_mfma_f32_16x16x32_bf16 v[232:235], a[20:23], a[92:95], v[232:235]
s_waitcnt lgkmcnt(4)
v_mul_f32_e32 v38, s86, v84
v_mul_f32_e32 v39, s86, v85
v_mfma_f32_16x16x32_bf16 v[140:143], a[24:27], a[64:67], v[140:143]
ds_read_b64_tr_b16 a[8:9], v14 offset:16392
v_mul_f32_e32 v40, s86, v86
v_mul_f32_e32 v41, s86, v87
v_mfma_f32_16x16x32_bf16 v[140:143], a[28:31], a[68:71], v[140:143]
ds_read_b64_tr_b16 a[10:11], v14 offset:20488
v_exp_f32_e32 v38, v38
v_mfma_f32_16x16x32_bf16 v[172:175], a[24:27], a[72:75], v[172:175]
ds_read_b64_tr_b16 a[12:13], v14 offset:24584
v_accvgpr_read_b32 v42, a212
v_mfma_f32_16x16x32_bf16 v[172:175], a[28:31], a[76:79], v[172:175]
ds_read_b64_tr_b16 a[14:15], v14 offset:28680
v_exp_f32_e32 v39, v39
v_accvgpr_read_b32 v43, a213
v_mfma_f32_16x16x32_bf16 v[204:207], a[24:27], a[80:83], v[204:207]
v_exp_f32_e32 v40, v40
v_mfma_f32_16x16x32_bf16 v[204:207], a[28:31], a[84:87], v[204:207]
v_accvgpr_read_b32 v44, a214
v_mfma_f32_16x16x32_bf16 v[236:239], a[24:27], a[88:91], v[236:239]
v_exp_f32_e32 v41, v41
v_accvgpr_read_b32 v45, a215
v_mfma_f32_16x16x32_bf16 v[236:239], a[28:31], a[92:95], v[236:239]
s_waitcnt lgkmcnt(4)
v_add_f32_e32 v38, 1.0, v38
v_add_f32_e32 v39, 1.0, v39
v_mfma_f32_16x16x32_bf16 v[144:147], a[0:3], a[64:67], v[144:147]
ds_read_b64_tr_b16 a[16:17], v15 offset:16384
v_add_f32_e32 v40, 1.0, v40
v_mfma_f32_16x16x32_bf16 v[144:147], a[4:7], a[68:71], v[144:147]
ds_read_b64_tr_b16 a[18:19], v15 offset:20480
v_add_f32_e32 v41, 1.0, v41
v_mfma_f32_16x16x32_bf16 v[176:179], a[0:3], a[72:75], v[176:179]
ds_read_b64_tr_b16 a[20:21], v15 offset:24576
v_rcp_f32_e32 v38, v38
v_mul_f32_e32 v42, v42, v84
v_mfma_f32_16x16x32_bf16 v[176:179], a[4:7], a[76:79], v[176:179]
ds_read_b64_tr_b16 a[22:23], v15 offset:28672
v_rcp_f32_e32 v39, v39
v_mfma_f32_16x16x32_bf16 v[208:211], a[0:3], a[80:83], v[208:211]
v_mul_f32_e32 v43, v43, v85
v_mfma_f32_16x16x32_bf16 v[208:211], a[4:7], a[84:87], v[208:211]
v_rcp_f32_e32 v40, v40
v_mul_f32_e32 v44, v44, v86
v_mfma_f32_16x16x32_bf16 v[240:243], a[0:3], a[88:91], v[240:243]
v_rcp_f32_e32 v41, v41
v_mfma_f32_16x16x32_bf16 v[240:243], a[4:7], a[92:95], v[240:243]
s_waitcnt lgkmcnt(4)
v_mul_f32_e32 v45, v45, v87
v_mul_f32_e32 v42, v42, v38
v_mfma_f32_16x16x32_bf16 v[148:151], a[8:11], a[64:67], v[148:151]
ds_read_b64_tr_b16 a[24:25], v15 offset:16392
v_mul_f32_e32 v43, v43, v39
v_mul_f32_e32 v44, v44, v40
v_mfma_f32_16x16x32_bf16 v[148:151], a[12:15], a[68:71], v[148:151]
ds_read_b64_tr_b16 a[26:27], v15 offset:20488
v_mul_f32_e32 v45, v45, v41
v_cvt_pk_bf16_f32 v32, v42, v43
v_mfma_f32_16x16x32_bf16 v[180:183], a[8:11], a[72:75], v[180:183]
ds_read_b64_tr_b16 a[28:29], v15 offset:24584
v_cvt_pk_bf16_f32 v33, v44, v45
v_mfma_f32_16x16x32_bf16 v[180:183], a[12:15], a[76:79], v[180:183]
ds_read_b64_tr_b16 a[30:31], v15 offset:28680
v_mfma_f32_16x16x32_bf16 v[212:215], a[8:11], a[80:83], v[212:215]
buffer_store_dwordx4 v[30:33], v46, s[64:67], s91 offen offset:0 sc1
v_mul_f32_e32 v38, s86, v88
v_mul_f32_e32 v39, s86, v89
v_mul_f32_e32 v40, s86, v90
v_mfma_f32_16x16x32_bf16 v[212:215], a[12:15], a[84:87], v[212:215]
v_mul_f32_e32 v41, s86, v91
v_mfma_f32_16x16x32_bf16 v[244:247], a[8:11], a[88:91], v[244:247]
v_exp_f32_e32 v38, v38
v_accvgpr_read_b32 v42, a216
v_mfma_f32_16x16x32_bf16 v[244:247], a[12:15], a[92:95], v[244:247]
s_waitcnt lgkmcnt(4)
v_mfma_f32_16x16x32_bf16 v[152:155], a[16:19], a[64:67], v[152:155]
ds_read_b64_tr_b16 a[0:1], v12 offset:32768
v_exp_f32_e32 v39, v39
v_accvgpr_read_b32 v43, a217
v_mfma_f32_16x16x32_bf16 v[152:155], a[20:23], a[68:71], v[152:155]
ds_read_b64_tr_b16 a[2:3], v12 offset:36864
v_exp_f32_e32 v40, v40
v_mfma_f32_16x16x32_bf16 v[184:187], a[16:19], a[72:75], v[184:187]
ds_read_b64_tr_b16 a[4:5], v12 offset:40960
v_accvgpr_read_b32 v44, a218
v_mfma_f32_16x16x32_bf16 v[184:187], a[20:23], a[76:79], v[184:187]
ds_read_b64_tr_b16 a[6:7], v12 offset:45056
v_exp_f32_e32 v41, v41
v_accvgpr_read_b32 v45, a219
v_mfma_f32_16x16x32_bf16 v[216:219], a[16:19], a[80:83], v[216:219]
v_add_f32_e32 v38, 1.0, v38
v_add_f32_e32 v39, 1.0, v39
v_mfma_f32_16x16x32_bf16 v[216:219], a[20:23], a[84:87], v[216:219]
v_add_f32_e32 v40, 1.0, v40
v_add_f32_e32 v41, 1.0, v41
v_mfma_f32_16x16x32_bf16 v[248:251], a[16:19], a[88:91], v[248:251]
v_rcp_f32_e32 v38, v38
v_mfma_f32_16x16x32_bf16 v[248:251], a[20:23], a[92:95], v[248:251]
s_waitcnt lgkmcnt(4)
v_mul_f32_e32 v42, v42, v88
v_mfma_f32_16x16x32_bf16 v[156:159], a[24:27], a[64:67], v[156:159]
ds_read_b64_tr_b16 a[8:9], v12 offset:32776
v_rcp_f32_e32 v39, v39
v_mul_f32_e32 v43, v43, v89
v_mfma_f32_16x16x32_bf16 v[156:159], a[28:31], a[68:71], v[156:159]
ds_read_b64_tr_b16 a[10:11], v12 offset:36872
v_rcp_f32_e32 v40, v40
v_mfma_f32_16x16x32_bf16 v[188:191], a[24:27], a[72:75], v[188:191]
ds_read_b64_tr_b16 a[12:13], v12 offset:40968
v_mul_f32_e32 v44, v44, v90
v_mfma_f32_16x16x32_bf16 v[188:191], a[28:31], a[76:79], v[188:191]
ds_read_b64_tr_b16 a[14:15], v12 offset:45064
v_rcp_f32_e32 v41, v41
v_mul_f32_e32 v45, v45, v91
v_mfma_f32_16x16x32_bf16 v[220:223], a[24:27], a[80:83], v[220:223]
v_mul_f32_e32 v42, v42, v38
v_mul_f32_e32 v43, v43, v39
v_mfma_f32_16x16x32_bf16 v[220:223], a[28:31], a[84:87], v[220:223]
s_barrier
v_mul_f32_e32 v44, v44, v40
v_mul_f32_e32 v45, v45, v41
v_mfma_f32_16x16x32_bf16 v[252:255], a[24:27], a[88:91], v[252:255]
v_cvt_pk_bf16_f32 v50, v42, v43
v_cvt_pk_bf16_f32 v51, v44, v45
v_mfma_f32_16x16x32_bf16 v[252:255], a[28:31], a[92:95], v[252:255]
s_waitcnt lgkmcnt(4)
v_mfma_f32_16x16x32_bf16 v[128:131], a[0:3], a[96:99], v[128:131]
ds_read_b64_tr_b16 a[16:17], v13 offset:32768
v_mfma_f32_16x16x32_bf16 v[128:131], a[4:7], a[100:103], v[128:131]
ds_read_b64_tr_b16 a[18:19], v13 offset:36864
v_mfma_f32_16x16x32_bf16 v[160:163], a[0:3], a[104:107], v[160:163]
ds_read_b64_tr_b16 a[20:21], v13 offset:40960
v_mfma_f32_16x16x32_bf16 v[160:163], a[4:7], a[108:111], v[160:163]
ds_read_b64_tr_b16 a[22:23], v13 offset:45056
v_mfma_f32_16x16x32_bf16 v[192:195], a[0:3], a[112:115], v[192:195]
s_add_u32 m0, s68, 0x14000
s_nop 0
buffer_load_dwordx4 v24, s[40:43], s77 offen offset:0 nt lds
v_mfma_f32_16x16x32_bf16 v[192:195], a[4:7], a[116:119], v[192:195]
buffer_store_dwordx2 v[50:51], v47, s[64:67], s91 offen offset:0 sc1
v_mul_f32_e32 v38, s86, v96
v_mul_f32_e32 v39, s86, v97
v_mul_f32_e32 v40, s86, v98
v_mul_f32_e32 v41, s86, v99
v_exp_f32_e32 v38, v38
v_accvgpr_read_b32 v42, a224
v_exp_f32_e32 v39, v39
v_accvgpr_read_b32 v43, a225
v_mfma_f32_16x16x32_bf16 v[224:227], a[0:3], a[120:123], v[224:227]
buffer_load_dwordx4 v25, s[40:43], s77 offen offset:1024 nt lds
v_exp_f32_e32 v40, v40
v_accvgpr_read_b32 v44, a226
v_mfma_f32_16x16x32_bf16 v[224:227], a[4:7], a[124:127], v[224:227]
s_waitcnt lgkmcnt(4)
v_exp_f32_e32 v41, v41
v_mfma_f32_16x16x32_bf16 v[132:135], a[8:11], a[96:99], v[132:135]
ds_read_b64_tr_b16 a[24:25], v13 offset:32776
v_accvgpr_read_b32 v45, a227
v_add_f32_e32 v38, 1.0, v38
v_mfma_f32_16x16x32_bf16 v[132:135], a[12:15], a[100:103], v[132:135]
ds_read_b64_tr_b16 a[26:27], v13 offset:36872
v_add_f32_e32 v39, 1.0, v39
v_add_f32_e32 v40, 1.0, v40
v_mfma_f32_16x16x32_bf16 v[164:167], a[8:11], a[104:107], v[164:167]
ds_read_b64_tr_b16 a[28:29], v13 offset:40968
v_add_f32_e32 v41, 1.0, v41
v_mfma_f32_16x16x32_bf16 v[164:167], a[12:15], a[108:111], v[164:167]
ds_read_b64_tr_b16 a[30:31], v13 offset:45064
v_rcp_f32_e32 v38, v38
v_mul_f32_e32 v42, v42, v96
v_mfma_f32_16x16x32_bf16 v[196:199], a[8:11], a[112:115], v[196:199]
buffer_load_dwordx4 v24, s[40:43], s77 offen offset:2048 nt lds
v_rcp_f32_e32 v39, v39
v_mfma_f32_16x16x32_bf16 v[196:199], a[12:15], a[116:119], v[196:199]
v_mul_f32_e32 v43, v43, v97
v_mfma_f32_16x16x32_bf16 v[228:231], a[8:11], a[120:123], v[228:231]
buffer_load_dwordx4 v25, s[40:43], s77 offen offset:3072 nt lds
s_add_u32 s77, s77, 0x4000
v_rcp_f32_e32 v40, v40
v_mfma_f32_16x16x32_bf16 v[228:231], a[12:15], a[124:127], v[228:231]
s_waitcnt lgkmcnt(4)
v_mul_f32_e32 v44, v44, v98
v_mfma_f32_16x16x32_bf16 v[136:139], a[16:19], a[96:99], v[136:139]
ds_read_b64_tr_b16 a[0:1], v14 offset:32768
v_rcp_f32_e32 v41, v41
v_mul_f32_e32 v45, v45, v99
v_mfma_f32_16x16x32_bf16 v[136:139], a[20:23], a[100:103], v[136:139]
ds_read_b64_tr_b16 a[2:3], v14 offset:36864
v_mul_f32_e32 v42, v42, v38
v_mul_f32_e32 v43, v43, v39
v_mfma_f32_16x16x32_bf16 v[168:171], a[16:19], a[104:107], v[168:171]
ds_read_b64_tr_b16 a[4:5], v14 offset:40960
v_mul_f32_e32 v44, v44, v40
v_mul_f32_e32 v45, v45, v41
v_mfma_f32_16x16x32_bf16 v[168:171], a[20:23], a[108:111], v[168:171]
ds_read_b64_tr_b16 a[6:7], v14 offset:45056
v_cvt_pk_bf16_f32 v30, v42, v43
v_cvt_pk_bf16_f32 v31, v44, v45
v_mfma_f32_16x16x32_bf16 v[200:203], a[16:19], a[112:115], v[200:203]
v_mul_f32_e32 v38, s86, v100
v_mul_f32_e32 v39, s86, v101
v_mfma_f32_16x16x32_bf16 v[200:203], a[20:23], a[116:119], v[200:203]
v_mul_f32_e32 v40, s86, v102
v_mul_f32_e32 v41, s86, v103
v_mfma_f32_16x16x32_bf16 v[232:235], a[16:19], a[120:123], v[232:235]
v_exp_f32_e32 v38, v38
v_mfma_f32_16x16x32_bf16 v[232:235], a[20:23], a[124:127], v[232:235]
s_waitcnt lgkmcnt(4)
v_accvgpr_read_b32 v42, a228
v_mfma_f32_16x16x32_bf16 v[140:143], a[24:27], a[96:99], v[140:143]
ds_read_b64_tr_b16 a[8:9], v14 offset:32776
v_exp_f32_e32 v39, v39
v_accvgpr_read_b32 v43, a229
v_mfma_f32_16x16x32_bf16 v[140:143], a[28:31], a[100:103], v[140:143]
ds_read_b64_tr_b16 a[10:11], v14 offset:36872
v_exp_f32_e32 v40, v40
v_mfma_f32_16x16x32_bf16 v[172:175], a[24:27], a[104:107], v[172:175]
ds_read_b64_tr_b16 a[12:13], v14 offset:40968
v_accvgpr_read_b32 v44, a230
v_mfma_f32_16x16x32_bf16 v[172:175], a[28:31], a[108:111], v[172:175]
ds_read_b64_tr_b16 a[14:15], v14 offset:45064
v_exp_f32_e32 v41, v41
v_accvgpr_read_b32 v45, a231
v_mfma_f32_16x16x32_bf16 v[204:207], a[24:27], a[112:115], v[204:207]
v_add_f32_e32 v38, 1.0, v38
v_add_f32_e32 v39, 1.0, v39
v_mfma_f32_16x16x32_bf16 v[204:207], a[28:31], a[116:119], v[204:207]
v_add_f32_e32 v40, 1.0, v40
v_mfma_f32_16x16x32_bf16 v[236:239], a[24:27], a[120:123], v[236:239]
v_add_f32_e32 v41, 1.0, v41
v_mfma_f32_16x16x32_bf16 v[236:239], a[28:31], a[124:127], v[236:239]
s_waitcnt lgkmcnt(4)
v_rcp_f32_e32 v38, v38
v_mul_f32_e32 v42, v42, v100
v_mfma_f32_16x16x32_bf16 v[144:147], a[0:3], a[96:99], v[144:147]
ds_read_b64_tr_b16 a[16:17], v15 offset:32768
v_rcp_f32_e32 v39, v39
v_mfma_f32_16x16x32_bf16 v[144:147], a[4:7], a[100:103], v[144:147]
ds_read_b64_tr_b16 a[18:19], v15 offset:36864
v_mul_f32_e32 v43, v43, v101
v_mfma_f32_16x16x32_bf16 v[176:179], a[0:3], a[104:107], v[176:179]
ds_read_b64_tr_b16 a[20:21], v15 offset:40960
v_rcp_f32_e32 v40, v40
v_mul_f32_e32 v44, v44, v102
v_mfma_f32_16x16x32_bf16 v[176:179], a[4:7], a[108:111], v[176:179]
ds_read_b64_tr_b16 a[22:23], v15 offset:45056
v_rcp_f32_e32 v41, v41
v_mfma_f32_16x16x32_bf16 v[208:211], a[0:3], a[112:115], v[208:211]
v_mul_f32_e32 v45, v45, v103
v_mul_f32_e32 v42, v42, v38
v_mfma_f32_16x16x32_bf16 v[208:211], a[4:7], a[116:119], v[208:211]
v_mul_f32_e32 v43, v43, v39
v_mul_f32_e32 v44, v44, v40
v_mfma_f32_16x16x32_bf16 v[240:243], a[0:3], a[120:123], v[240:243]
v_mul_f32_e32 v45, v45, v41
v_cvt_pk_bf16_f32 v32, v42, v43
v_mfma_f32_16x16x32_bf16 v[240:243], a[4:7], a[124:127], v[240:243]
s_waitcnt lgkmcnt(4)
v_cvt_pk_bf16_f32 v33, v44, v45
v_mfma_f32_16x16x32_bf16 v[148:151], a[8:11], a[96:99], v[148:151]
ds_read_b64_tr_b16 a[24:25], v15 offset:32776
v_mfma_f32_16x16x32_bf16 v[148:151], a[12:15], a[100:103], v[148:151]
ds_read_b64_tr_b16 a[26:27], v15 offset:36872
v_mfma_f32_16x16x32_bf16 v[180:183], a[8:11], a[104:107], v[180:183]
ds_read_b64_tr_b16 a[28:29], v15 offset:40968
v_mfma_f32_16x16x32_bf16 v[180:183], a[12:15], a[108:111], v[180:183]
ds_read_b64_tr_b16 a[30:31], v15 offset:45064
v_mfma_f32_16x16x32_bf16 v[212:215], a[8:11], a[112:115], v[212:215]
buffer_store_dwordx4 v[30:33], v46, s[64:67], s92 offen offset:0 sc1
v_mul_f32_e32 v38, s86, v104
v_mul_f32_e32 v39, s86, v105
v_mul_f32_e32 v40, s86, v106
v_mul_f32_e32 v41, s86, v107
v_exp_f32_e32 v38, v38
v_accvgpr_read_b32 v42, a232
v_exp_f32_e32 v39, v39
v_mfma_f32_16x16x32_bf16 v[212:215], a[12:15], a[116:119], v[212:215]
v_accvgpr_read_b32 v43, a233
v_mfma_f32_16x16x32_bf16 v[244:247], a[8:11], a[120:123], v[244:247]
v_exp_f32_e32 v40, v40
v_mfma_f32_16x16x32_bf16 v[244:247], a[12:15], a[124:127], v[244:247]
s_waitcnt lgkmcnt(4)
v_accvgpr_read_b32 v44, a234
v_mfma_f32_16x16x32_bf16 v[152:155], a[16:19], a[96:99], v[152:155]
ds_read_b64_tr_b16 a[0:1], v12 offset:49152
v_exp_f32_e32 v41, v41
v_accvgpr_read_b32 v45, a235
v_mfma_f32_16x16x32_bf16 v[152:155], a[20:23], a[100:103], v[152:155]
ds_read_b64_tr_b16 a[2:3], v12 offset:53248
v_add_f32_e32 v38, 1.0, v38
v_add_f32_e32 v39, 1.0, v39
v_mfma_f32_16x16x32_bf16 v[184:187], a[16:19], a[104:107], v[184:187]
ds_read_b64_tr_b16 a[4:5], v12 offset:57344
v_add_f32_e32 v40, 1.0, v40
v_add_f32_e32 v41, 1.0, v41
v_mfma_f32_16x16x32_bf16 v[184:187], a[20:23], a[108:111], v[184:187]
ds_read_b64_tr_b16 a[6:7], v12 offset:61440
v_rcp_f32_e32 v38, v38
v_mfma_f32_16x16x32_bf16 v[216:219], a[16:19], a[112:115], v[216:219]
v_mul_f32_e32 v42, v42, v104
v_mfma_f32_16x16x32_bf16 v[216:219], a[20:23], a[116:119], v[216:219]
v_rcp_f32_e32 v39, v39
v_mul_f32_e32 v43, v43, v105
v_mfma_f32_16x16x32_bf16 v[248:251], a[16:19], a[120:123], v[248:251]
v_rcp_f32_e32 v40, v40
v_mfma_f32_16x16x32_bf16 v[248:251], a[20:23], a[124:127], v[248:251]
s_waitcnt lgkmcnt(4)
v_mul_f32_e32 v44, v44, v106
v_mfma_f32_16x16x32_bf16 v[156:159], a[24:27], a[96:99], v[156:159]
ds_read_b64_tr_b16 a[8:9], v12 offset:49160
v_rcp_f32_e32 v41, v41
v_mul_f32_e32 v45, v45, v107
v_mfma_f32_16x16x32_bf16 v[156:159], a[28:31], a[100:103], v[156:159]
ds_read_b64_tr_b16 a[10:11], v12 offset:53256
v_mul_f32_e32 v42, v42, v38
v_mul_f32_e32 v43, v43, v39
v_mfma_f32_16x16x32_bf16 v[188:191], a[24:27], a[104:107], v[188:191]
ds_read_b64_tr_b16 a[12:13], v12 offset:57352
v_mul_f32_e32 v44, v44, v40
v_mul_f32_e32 v45, v45, v41
v_mfma_f32_16x16x32_bf16 v[188:191], a[28:31], a[108:111], v[188:191]
ds_read_b64_tr_b16 a[14:15], v12 offset:61448
v_cvt_pk_bf16_f32 v50, v42, v43
v_cvt_pk_bf16_f32 v51, v44, v45
v_mfma_f32_16x16x32_bf16 v[220:223], a[24:27], a[112:115], v[220:223]
v_mfma_f32_16x16x32_bf16 v[220:223], a[28:31], a[116:119], v[220:223]
s_barrier
v_mfma_f32_16x16x32_bf16 v[252:255], a[24:27], a[120:123], v[252:255]
buffer_store_dwordx2 v[50:51], v47, s[64:67], s92 offen offset:0 sc1
v_mul_f32_e32 v38, s86, v112
v_mul_f32_e32 v39, s86, v113
v_mul_f32_e32 v40, s86, v114
v_mfma_f32_16x16x32_bf16 v[252:255], a[28:31], a[124:127], v[252:255]
s_waitcnt lgkmcnt(4)
v_mul_f32_e32 v41, s86, v115
v_mfma_f32_16x16x32_bf16 v[128:131], a[0:3], a[128:131], v[128:131]
ds_read_b64_tr_b16 a[16:17], v13 offset:49152
v_exp_f32_e32 v38, v38
v_accvgpr_read_b32 v42, a240
v_mfma_f32_16x16x32_bf16 v[128:131], a[4:7], a[132:135], v[128:131]
ds_read_b64_tr_b16 a[18:19], v13 offset:53248
v_exp_f32_e32 v39, v39
v_mfma_f32_16x16x32_bf16 v[160:163], a[0:3], a[136:139], v[160:163]
ds_read_b64_tr_b16 a[20:21], v13 offset:57344
v_accvgpr_read_b32 v43, a241
v_mfma_f32_16x16x32_bf16 v[160:163], a[4:7], a[140:143], v[160:163]
ds_read_b64_tr_b16 a[22:23], v13 offset:61440
v_exp_f32_e32 v40, v40
v_accvgpr_read_b32 v44, a242
v_mfma_f32_16x16x32_bf16 v[192:195], a[0:3], a[144:147], v[192:195]
s_add_u32 m0, s68, 0x18000
s_nop 0
buffer_load_dwordx4 v24, s[40:43], s77 offen offset:0 nt lds
v_exp_f32_e32 v41, v41
v_mfma_f32_16x16x32_bf16 v[192:195], a[4:7], a[148:151], v[192:195]
v_accvgpr_read_b32 v45, a243
v_add_f32_e32 v38, 1.0, v38
v_mfma_f32_16x16x32_bf16 v[224:227], a[0:3], a[152:155], v[224:227]
buffer_load_dwordx4 v25, s[40:43], s77 offen offset:1024 nt lds
v_add_f32_e32 v39, 1.0, v39
v_add_f32_e32 v40, 1.0, v40
v_mfma_f32_16x16x32_bf16 v[224:227], a[4:7], a[156:159], v[224:227]
s_waitcnt lgkmcnt(4)
v_add_f32_e32 v41, 1.0, v41
v_mfma_f32_16x16x32_bf16 v[132:135], a[8:11], a[128:131], v[132:135]
ds_read_b64_tr_b16 a[24:25], v13 offset:49160
v_rcp_f32_e32 v38, v38
v_mul_f32_e32 v42, v42, v112
v_mfma_f32_16x16x32_bf16 v[132:135], a[12:15], a[132:135], v[132:135]
ds_read_b64_tr_b16 a[26:27], v13 offset:53256
v_rcp_f32_e32 v39, v39
v_mfma_f32_16x16x32_bf16 v[164:167], a[8:11], a[136:139], v[164:167]
ds_read_b64_tr_b16 a[28:29], v13 offset:57352
v_mul_f32_e32 v43, v43, v113
v_mfma_f32_16x16x32_bf16 v[164:167], a[12:15], a[140:143], v[164:167]
ds_read_b64_tr_b16 a[30:31], v13 offset:61448
v_rcp_f32_e32 v40, v40
v_mfma_f32_16x16x32_bf16 v[196:199], a[8:11], a[144:147], v[196:199]
buffer_load_dwordx4 v24, s[40:43], s77 offen offset:2048 nt lds
v_mul_f32_e32 v44, v44, v114
v_mfma_f32_16x16x32_bf16 v[196:199], a[12:15], a[148:151], v[196:199]
v_rcp_f32_e32 v41, v41
v_mul_f32_e32 v45, v45, v115
v_mfma_f32_16x16x32_bf16 v[228:231], a[8:11], a[152:155], v[228:231]
buffer_load_dwordx4 v25, s[40:43], s77 offen offset:3072 nt lds
s_add_u32 s77, s77, 0x4000
v_mul_f32_e32 v42, v42, v38
v_mul_f32_e32 v43, v43, v39
v_mfma_f32_16x16x32_bf16 v[228:231], a[12:15], a[156:159], v[228:231]
s_waitcnt lgkmcnt(4)
v_mul_f32_e32 v44, v44, v40
v_mul_f32_e32 v45, v45, v41
v_mfma_f32_16x16x32_bf16 v[136:139], a[16:19], a[128:131], v[136:139]
ds_read_b64_tr_b16 a[0:1], v14 offset:49152
v_cvt_pk_bf16_f32 v30, v42, v43
v_cvt_pk_bf16_f32 v31, v44, v45
v_mfma_f32_16x16x32_bf16 v[136:139], a[20:23], a[132:135], v[136:139]
ds_read_b64_tr_b16 a[2:3], v14 offset:53248
v_mul_f32_e32 v38, s86, v116
v_mul_f32_e32 v39, s86, v117
v_mfma_f32_16x16x32_bf16 v[168:171], a[16:19], a[136:139], v[168:171]
ds_read_b64_tr_b16 a[4:5], v14 offset:57344
v_mul_f32_e32 v40, s86, v118
v_mul_f32_e32 v41, s86, v119
v_mfma_f32_16x16x32_bf16 v[168:171], a[20:23], a[140:143], v[168:171]
ds_read_b64_tr_b16 a[6:7], v14 offset:61440
v_exp_f32_e32 v38, v38
v_mfma_f32_16x16x32_bf16 v[200:203], a[16:19], a[144:147], v[200:203]
v_accvgpr_read_b32 v42, a244
v_mfma_f32_16x16x32_bf16 v[200:203], a[20:23], a[148:151], v[200:203]
v_exp_f32_e32 v39, v39
v_accvgpr_read_b32 v43, a245
v_mfma_f32_16x16x32_bf16 v[232:235], a[16:19], a[152:155], v[232:235]
v_exp_f32_e32 v40, v40
v_mfma_f32_16x16x32_bf16 v[232:235], a[20:23], a[156:159], v[232:235]
s_waitcnt lgkmcnt(4)
v_accvgpr_read_b32 v44, a246
v_mfma_f32_16x16x32_bf16 v[140:143], a[24:27], a[128:131], v[140:143]
ds_read_b64_tr_b16 a[8:9], v14 offset:49160
v_exp_f32_e32 v41, v41
v_accvgpr_read_b32 v45, a247
v_mfma_f32_16x16x32_bf16 v[140:143], a[28:31], a[132:135], v[140:143]
ds_read_b64_tr_b16 a[10:11], v14 offset:53256
v_add_f32_e32 v38, 1.0, v38
v_add_f32_e32 v39, 1.0, v39
v_mfma_f32_16x16x32_bf16 v[172:175], a[24:27], a[136:139], v[172:175]
ds_read_b64_tr_b16 a[12:13], v14 offset:57352
v_add_f32_e32 v40, 1.0, v40
v_add_f32_e32 v41, 1.0, v41
v_mfma_f32_16x16x32_bf16 v[172:175], a[28:31], a[140:143], v[172:175]
ds_read_b64_tr_b16 a[14:15], v14 offset:61448
v_mfma_f32_16x16x32_bf16 v[204:207], a[24:27], a[144:147], v[204:207]
v_rcp_f32_e32 v38, v38
v_mul_f32_e32 v42, v42, v116
v_mfma_f32_16x16x32_bf16 v[204:207], a[28:31], a[148:151], v[204:207]
v_rcp_f32_e32 v39, v39
v_mfma_f32_16x16x32_bf16 v[236:239], a[24:27], a[152:155], v[236:239]
v_mul_f32_e32 v43, v43, v117
v_mfma_f32_16x16x32_bf16 v[236:239], a[28:31], a[156:159], v[236:239]
s_waitcnt lgkmcnt(4)
v_rcp_f32_e32 v40, v40
v_mul_f32_e32 v44, v44, v118
v_mfma_f32_16x16x32_bf16 v[144:147], a[0:3], a[128:131], v[144:147]
ds_read_b64_tr_b16 a[16:17], v15 offset:49152
v_rcp_f32_e32 v41, v41
v_mfma_f32_16x16x32_bf16 v[144:147], a[4:7], a[132:135], v[144:147]
ds_read_b64_tr_b16 a[18:19], v15 offset:53248
v_mul_f32_e32 v45, v45, v119
v_mul_f32_e32 v42, v42, v38
v_mfma_f32_16x16x32_bf16 v[176:179], a[0:3], a[136:139], v[176:179]
ds_read_b64_tr_b16 a[20:21], v15 offset:57344
v_mul_f32_e32 v43, v43, v39
v_mul_f32_e32 v44, v44, v40
v_mfma_f32_16x16x32_bf16 v[176:179], a[4:7], a[140:143], v[176:179]
ds_read_b64_tr_b16 a[22:23], v15 offset:61440
v_mul_f32_e32 v45, v45, v41
v_cvt_pk_bf16_f32 v32, v42, v43
v_mfma_f32_16x16x32_bf16 v[208:211], a[0:3], a[144:147], v[208:211]
v_cvt_pk_bf16_f32 v33, v44, v45
v_mfma_f32_16x16x32_bf16 v[208:211], a[4:7], a[148:151], v[208:211]
buffer_store_dwordx4 v[30:33], v46, s[64:67], s93 offen offset:0 sc1
v_mul_f32_e32 v38, s86, v120
v_mfma_f32_16x16x32_bf16 v[240:243], a[0:3], a[152:155], v[240:243]
v_mul_f32_e32 v39, s86, v121
v_mul_f32_e32 v40, s86, v122
v_mfma_f32_16x16x32_bf16 v[240:243], a[4:7], a[156:159], v[240:243]
s_waitcnt lgkmcnt(4)
v_mul_f32_e32 v41, s86, v123
v_mfma_f32_16x16x32_bf16 v[148:151], a[8:11], a[128:131], v[148:151]
ds_read_b64_tr_b16 a[24:25], v15 offset:49160
v_exp_f32_e32 v38, v38
v_accvgpr_read_b32 v42, a248
v_mfma_f32_16x16x32_bf16 v[148:151], a[12:15], a[132:135], v[148:151]
ds_read_b64_tr_b16 a[26:27], v15 offset:53256
v_exp_f32_e32 v39, v39
v_mfma_f32_16x16x32_bf16 v[180:183], a[8:11], a[136:139], v[180:183]
ds_read_b64_tr_b16 a[28:29], v15 offset:57352
v_accvgpr_read_b32 v43, a249
v_mfma_f32_16x16x32_bf16 v[180:183], a[12:15], a[140:143], v[180:183]
ds_read_b64_tr_b16 a[30:31], v15 offset:61448
v_exp_f32_e32 v40, v40
v_mfma_f32_16x16x32_bf16 v[212:215], a[8:11], a[144:147], v[212:215]
v_accvgpr_read_b32 v44, a250
v_mfma_f32_16x16x32_bf16 v[212:215], a[12:15], a[148:151], v[212:215]
v_exp_f32_e32 v41, v41
v_accvgpr_read_b32 v45, a251
v_mfma_f32_16x16x32_bf16 v[244:247], a[8:11], a[152:155], v[244:247]
v_add_f32_e32 v38, 1.0, v38
v_add_f32_e32 v39, 1.0, v39
v_mfma_f32_16x16x32_bf16 v[244:247], a[12:15], a[156:159], v[244:247]
s_waitcnt lgkmcnt(4)
v_add_f32_e32 v40, 1.0, v40
v_add_f32_e32 v41, 1.0, v41
v_mfma_f32_16x16x32_bf16 v[152:155], a[16:19], a[128:131], v[152:155]
v_rcp_f32_e32 v38, v38
v_mfma_f32_16x16x32_bf16 v[152:155], a[20:23], a[132:135], v[152:155]
v_mul_f32_e32 v42, v42, v120
v_mfma_f32_16x16x32_bf16 v[184:187], a[16:19], a[136:139], v[184:187]
v_rcp_f32_e32 v39, v39
v_mul_f32_e32 v43, v43, v121
v_mfma_f32_16x16x32_bf16 v[184:187], a[20:23], a[140:143], v[184:187]
v_rcp_f32_e32 v40, v40
v_mfma_f32_16x16x32_bf16 v[216:219], a[16:19], a[144:147], v[216:219]
v_mul_f32_e32 v44, v44, v122
v_mfma_f32_16x16x32_bf16 v[216:219], a[20:23], a[148:151], v[216:219]
v_rcp_f32_e32 v41, v41
v_mul_f32_e32 v45, v45, v123
v_mfma_f32_16x16x32_bf16 v[248:251], a[16:19], a[152:155], v[248:251]
v_mul_f32_e32 v42, v42, v38
v_mul_f32_e32 v43, v43, v39
v_mfma_f32_16x16x32_bf16 v[248:251], a[20:23], a[156:159], v[248:251]
s_waitcnt lgkmcnt(0)
v_mul_f32_e32 v44, v44, v40
v_mul_f32_e32 v45, v45, v41
v_mfma_f32_16x16x32_bf16 v[156:159], a[24:27], a[128:131], v[156:159]
v_cvt_pk_bf16_f32 v50, v42, v43
v_cvt_pk_bf16_f32 v51, v44, v45
v_mfma_f32_16x16x32_bf16 v[156:159], a[28:31], a[132:135], v[156:159]
v_mfma_f32_16x16x32_bf16 v[188:191], a[24:27], a[136:139], v[188:191]
v_mfma_f32_16x16x32_bf16 v[188:191], a[28:31], a[140:143], v[188:191]
v_mfma_f32_16x16x32_bf16 v[220:223], a[24:27], a[144:147], v[220:223]
s_waitcnt vmcnt(13)
v_mfma_f32_16x16x32_bf16 v[220:223], a[28:31], a[148:151], v[220:223]
s_barrier
v_mfma_f32_16x16x32_bf16 v[252:255], a[24:27], a[152:155], v[252:255]
buffer_store_dwordx2 v[50:51], v47, s[64:67], s93 offen offset:0 sc1
v_mfma_f32_16x16x32_bf16 v[252:255], a[28:31], a[156:159], v[252:255]
; ---- cvtS
ds_read_b128 a[128:131], v8 offset:0
ds_read_b128 a[132:135], v9 offset:0
ds_read_b128 a[136:139], v10 offset:0
ds_read_b128 a[140:143], v11 offset:0
ds_read_b128 a[144:147], v8 offset:4096
v_mov_b32_e32 v30, 0
v_mov_b32_e32 v31, 0
v_mov_b32_e32 v32, 0
v_mov_b32_e32 v33, 0
v_pk_mul_f32 v[128:129], v[128:129], s[84:85]
v_pk_mul_f32 v[130:131], v[130:131], s[84:85]
v_pk_mul_f32 v[132:133], v[132:133], s[84:85]
v_pk_mul_f32 v[134:135], v[134:135], s[84:85]
v_cvt_pk_bf16_f32 v128, v128, v129
v_cvt_pk_bf16_f32 v129, v130, v131
v_cvt_pk_bf16_f32 v130, v132, v133
v_cvt_pk_bf16_f32 v131, v134, v135
v_pk_mul_f32 v[136:137], v[136:137], s[84:85]
v_pk_mul_f32 v[138:139], v[138:139], s[84:85]
v_pk_mul_f32 v[140:141], v[140:141], s[84:85]
v_pk_mul_f32 v[142:143], v[142:143], s[84:85]
v_cvt_pk_bf16_f32 v132, v136, v137
v_cvt_pk_bf16_f32 v133, v138, v139
v_cvt_pk_bf16_f32 v134, v140, v141
v_cvt_pk_bf16_f32 v135, v142, v143
v_pk_mul_f32 v[144:145], v[144:145], s[84:85]
v_pk_mul_f32 v[146:147], v[146:147], s[84:85]
v_pk_mul_f32 v[148:149], v[148:149], s[84:85]
v_pk_mul_f32 v[150:151], v[150:151], s[84:85]
v_cvt_pk_bf16_f32 v136, v144, v145
v_cvt_pk_bf16_f32 v137, v146, v147
v_cvt_pk_bf16_f32 v138, v148, v149
v_cvt_pk_bf16_f32 v139, v150, v151
v_pk_mul_f32 v[152:153], v[152:153], s[84:85]
v_pk_mul_f32 v[154:155], v[154:155], s[84:85]
v_pk_mul_f32 v[156:157], v[156:157], s[84:85]
v_pk_mul_f32 v[158:159], v[158:159], s[84:85]
v_cvt_pk_bf16_f32 v140, v152, v153
v_cvt_pk_bf16_f32 v141, v154, v155
v_cvt_pk_bf16_f32 v142, v156, v157
v_cvt_pk_bf16_f32 v143, v158, v159
v_pk_mul_f32 v[160:161], v[160:161], s[84:85]
v_pk_mul_f32 v[162:163], v[162:163], s[84:85]
v_pk_mul_f32 v[164:165], v[164:165], s[84:85]
v_pk_mul_f32 v[166:167], v[166:167], s[84:85]
v_cvt_pk_bf16_f32 v144, v160, v161
v_cvt_pk_bf16_f32 v145, v162, v163
v_cvt_pk_bf16_f32 v146, v164, v165
v_cvt_pk_bf16_f32 v147, v166, v167
v_pk_mul_f32 v[168:169], v[168:169], s[84:85]
v_pk_mul_f32 v[170:171], v[170:171], s[84:85]
v_pk_mul_f32 v[172:173], v[172:173], s[84:85]
v_pk_mul_f32 v[174:175], v[174:175], s[84:85]
v_cvt_pk_bf16_f32 v148, v168, v169
v_cvt_pk_bf16_f32 v149, v170, v171
v_cvt_pk_bf16_f32 v150, v172, v173
v_cvt_pk_bf16_f32 v151, v174, v175
v_pk_mul_f32 v[176:177], v[176:177], s[84:85]
v_pk_mul_f32 v[178:179], v[178:179], s[84:85]
v_pk_mul_f32 v[180:181], v[180:181], s[84:85]
v_pk_mul_f32 v[182:183], v[182:183], s[84:85]
v_cvt_pk_bf16_f32 v152, v176, v177
v_cvt_pk_bf16_f32 v153, v178, v179
v_cvt_pk_bf16_f32 v154, v180, v181
v_cvt_pk_bf16_f32 v155, v182, v183
v_pk_mul_f32 v[184:185], v[184:185], s[84:85]
v_pk_mul_f32 v[186:187], v[186:187], s[84:85]
v_pk_mul_f32 v[188:189], v[188:189], s[84:85]
v_pk_mul_f32 v[190:191], v[190:191], s[84:85]
v_cvt_pk_bf16_f32 v156, v184, v185
v_cvt_pk_bf16_f32 v157, v186, v187
v_cvt_pk_bf16_f32 v158, v188, v189
v_cvt_pk_bf16_f32 v159, v190, v191
v_pk_mul_f32 v[192:193], v[192:193], s[84:85]
v_pk_mul_f32 v[194:195], v[194:195], s[84:85]
v_pk_mul_f32 v[196:197], v[196:197], s[84:85]
v_pk_mul_f32 v[198:199], v[198:199], s[84:85]
v_cvt_pk_bf16_f32 v160, v192, v193
v_cvt_pk_bf16_f32 v161, v194, v195
v_cvt_pk_bf16_f32 v162, v196, v197
v_cvt_pk_bf16_f32 v163, v198, v199
v_pk_mul_f32 v[200:201], v[200:201], s[84:85]
v_pk_mul_f32 v[202:203], v[202:203], s[84:85]
v_pk_mul_f32 v[204:205], v[204:205], s[84:85]
v_pk_mul_f32 v[206:207], v[206:207], s[84:85]
v_cvt_pk_bf16_f32 v164, v200, v201
v_cvt_pk_bf16_f32 v165, v202, v203
v_cvt_pk_bf16_f32 v166, v204, v205
v_cvt_pk_bf16_f32 v167, v206, v207
v_pk_mul_f32 v[208:209], v[208:209], s[84:85]
v_pk_mul_f32 v[210:211], v[210:211], s[84:85]
v_pk_mul_f32 v[212:213], v[212:213], s[84:85]
v_pk_mul_f32 v[214:215], v[214:215], s[84:85]
v_cvt_pk_bf16_f32 v168, v208, v209
v_cvt_pk_bf16_f32 v169, v210, v211
v_cvt_pk_bf16_f32 v170, v212, v213
v_cvt_pk_bf16_f32 v171, v214, v215
v_pk_mul_f32 v[216:217], v[216:217], s[84:85]
v_pk_mul_f32 v[218:219], v[218:219], s[84:85]
v_pk_mul_f32 v[220:221], v[220:221], s[84:85]
v_pk_mul_f32 v[222:223], v[222:223], s[84:85]
v_cvt_pk_bf16_f32 v172, v216, v217
v_cvt_pk_bf16_f32 v173, v218, v219
v_cvt_pk_bf16_f32 v174, v220, v221
v_cvt_pk_bf16_f32 v175, v222, v223
v_pk_mul_f32 v[224:225], v[224:225], s[84:85]
v_pk_mul_f32 v[226:227], v[226:227], s[84:85]
v_pk_mul_f32 v[228:229], v[228:229], s[84:85]
v_pk_mul_f32 v[230:231], v[230:231], s[84:85]
v_cvt_pk_bf16_f32 v176, v224, v225
v_cvt_pk_bf16_f32 v177, v226, v227
v_cvt_pk_bf16_f32 v178, v228, v229
v_cvt_pk_bf16_f32 v179, v230, v231
v_pk_mul_f32 v[232:233], v[232:233], s[84:85]
v_pk_mul_f32 v[234:235], v[234:235], s[84:85]
v_pk_mul_f32 v[236:237], v[236:237], s[84:85]
v_pk_mul_f32 v[238:239], v[238:239], s[84:85]
v_cvt_pk_bf16_f32 v180, v232, v233
v_cvt_pk_bf16_f32 v181, v234, v235
v_cvt_pk_bf16_f32 v182, v236, v237
v_cvt_pk_bf16_f32 v183, v238, v239
v_pk_mul_f32 v[240:241], v[240:241], s[84:85]
v_pk_mul_f32 v[242:243], v[242:243], s[84:85]
v_pk_mul_f32 v[244:245], v[244:245], s[84:85]
v_pk_mul_f32 v[246:247], v[246:247], s[84:85]
v_cvt_pk_bf16_f32 v184, v240, v241
v_cvt_pk_bf16_f32 v185, v242, v243
v_cvt_pk_bf16_f32 v186, v244, v245
v_cvt_pk_bf16_f32 v187, v246, v247
v_pk_mul_f32 v[248:249], v[248:249], s[84:85]
v_pk_mul_f32 v[250:251], v[250:251], s[84:85]
v_pk_mul_f32 v[252:253], v[252:253], s[84:85]
v_pk_mul_f32 v[254:255], v[254:255], s[84:85]
v_cvt_pk_bf16_f32 v188, v248, v249
v_cvt_pk_bf16_f32 v189, v250, v251
v_cvt_pk_bf16_f32 v190, v252, v253
v_cvt_pk_bf16_f32 v191, v254, v255
v_mfma_f32_32x32x16_bf16 a[0:15], v[34:37], v[34:37], 0
v_mfma_f32_32x32x16_bf16 a[16:31], v[34:37], v[34:37], 0
v_mfma_f32_32x32x16_bf16 a[32:47], v[34:37], v[34:37], 0
v_mfma_f32_32x32x16_bf16 a[48:63], v[34:37], v[34:37], 0
v_mfma_f32_32x32x16_bf16 a[64:79], v[34:37], v[34:37], 0
v_mfma_f32_32x32x16_bf16 a[80:95], v[34:37], v[34:37], 0
v_mfma_f32_32x32x16_bf16 a[96:111], v[34:37], v[34:37], 0
v_mfma_f32_32x32x16_bf16 a[112:127], v[34:37], v[34:37], 0
; ---- cvt
.Lkf_cvt:
global_load_dword v48, v29, s[6:7] offset:0
global_load_dword v48, v29, s[6:7] offset:1024
global_load_dword v48, v29, s[6:7] offset:2048
global_load_dword v48, v29, s[6:7] offset:3072
s_xor_b32 s7, s101, 0x200000
s_bfe_u32 s6, s75, 0x20009
s_lshl_b32 s6, s6, 17
v_readlane_b32 s44, v49, 0
v_readlane_b32 s45, v49, 1
s_mov_b32 s46, 0x11800
s_mov_b32 s47, 0x20000
v_readlane_b32 s48, v49, 2
v_readlane_b32 s49, v49, 3
s_mov_b32 s50, 0x34000
s_mov_b32 s51, 0x20000
v_readlane_b32 s52, v49, 4
v_readlane_b32 s53, v49, 5
s_mov_b32 s54, 0x6fd00
s_mov_b32 s55, 0x20000
v_readlane_b32 s56, v49, 6
v_readlane_b32 s57, v49, 7
s_mov_b32 s58, 0x27fd00
s_mov_b32 s59, 0x20000
v_readlane_b32 s16, v49, 8
v_readlane_b32 s17, v49, 9
s_mov_b32 s18, 0x7c40
s_mov_b32 s19, 0x20000
v_readlane_b32 s96, v49, 10
v_readlane_b32 s97, v49, 11
s_mov_b32 s98, 0x7c40
s_mov_b32 s99, 0x20000
v_readlane_b32 s64, v49, 12
v_readlane_b32 s65, v49, 13
s_mov_b32 s66, 0x4000
s_mov_b32 s67, 0x20000
v_readlane_b32 s28, v49, 14
v_readlane_b32 s29, v49, 15
s_mov_b32 s30, 0x10000
s_mov_b32 s31, 0x20000
s_cmp_eq_u32 s5, s73
s_cselect_b32 s66, 0, s66
v_lshrrev_b32_e32 v38, 8, v29
v_bfe_u32 v39, v29, 6, 2
v_sub_u32_e32 v40, v39, v38
s_mov_b32 s79, 0x300
v_mul_lo_u32 v40, v40, s79
v_add_u32_e32 v120, v40, v0
v_add_u32_e32 v121, v40, v1
v_add_u32_e32 v122, v40, v2
v_add_u32_e32 v123, v40, v3
v_add_u32_e32 v124, v40, v4
v_add_u32_e32 v125, v40, v5
v_add_u32_e32 v126, v40, v6
v_add_u32_e32 v127, v40, v7
v_subrev_u32_e32 v112, 0x10000, v120
v_add_u32_e32 v120, 0x10000, v120
v_subrev_u32_e32 v113, 0x10000, v121
v_add_u32_e32 v121, 0x10000, v121
v_subrev_u32_e32 v114, 0x10000, v122
v_add_u32_e32 v122, 0x10000, v122
v_subrev_u32_e32 v115, 0x10000, v123
v_add_u32_e32 v123, 0x10000, v123
v_subrev_u32_e32 v116, 0x10000, v124
v_add_u32_e32 v124, 0x10000, v124
v_subrev_u32_e32 v117, 0x10000, v125
v_add_u32_e32 v125, 0x10000, v125
v_subrev_u32_e32 v118, 0x10000, v126
v_add_u32_e32 v126, 0x10000, v126
v_subrev_u32_e32 v119, 0x10000, v127
v_add_u32_e32 v127, 0x10000, v127
v_and_b32_e32 v39, 1, v38
v_lshlrev_b32_e32 v39, 7, v39
v_and_b32_e32 v40, 0xf0, v29
v_xor_b32_e32 v40, v39, v40
v_lshl_add_u32 v40, v38, 12, v40
v_xor_b32_e32 v52, 0, v40
v_xor_b32_e32 v53, 32, v40
v_xor_b32_e32 v54, 64, v40
v_xor_b32_e32 v55, 0x60, v40
v_and_b32_e32 v50, 0xf0, v29
v_lshlrev_b32_e32 v50, 6, v50
v_lshl_add_u32 v50, v38, 3, v50
v_add_u32_e32 v51, s68, v29
s_lshl_b32 s82, s73, 10
s_add_u32 s82, s82, 0x2000
s_add_u32 s81, s72, 0x2000
v_and_b32_e32 v39, 0xf0, v29
v_cmp_eq_u32_e32 vcc, 0, v39
v_mov_b32_e32 v40, 0x3f803f80
s_nop 0
v_cndmask_b32_e32 v34, 0, v40, vcc
v_cndmask_b32_e32 v35, 0, v40, vcc
v_cndmask_b32_e32 v36, 0, v40, vcc
v_cndmask_b32_e32 v37, 0, v40, vcc
v_mov_b32_e32 v38, 0
v_mov_b32_e32 v39, 0
v_mov_b32_e32 v40, 0
v_mov_b32_e32 v41, 0
v_mov_b32_e32 v42, 0
v_mov_b32_e32 v43, 0
v_mov_b32_e32 v44, 0
v_mov_b32_e32 v45, 0
s_waitcnt lgkmcnt(1)
; ---- first
v_mfma_f32_16x16x32_bf16 v[64:67], a[128:131], v[128:131], 0
v_mfma_f32_16x16x32_bf16 v[64:67], a[132:135], v[132:135], v[64:67]
v_mfma_f32_16x16x32_bf16 v[64:67], a[136:139], v[136:139], v[64:67]
v_mfma_f32_16x16x32_bf16 v[64:67], a[140:143], v[140:143], v[64:67]
ds_read_b128 a[148:151], v9 offset:4096
v_mfma_f32_16x16x32_bf16 v[68:71], a[128:131], v[144:147], 0
s_nop 5
v_exp_f32_e32 v64, v64
v_mfma_f32_16x16x32_bf16 v[68:71], a[132:135], v[148:151], v[68:71]
v_mfma_f32_16x16x32_bf16 v[68:71], a[136:139], v[152:155], v[68:71]
v_exp_f32_e32 v65, v65
v_mfma_f32_16x16x32_bf16 v[68:71], a[140:143], v[156:159], v[68:71]
v_cvt_pk_bf16_f32 v192, v64, v65
v_exp_f32_e32 v66, v66
ds_read_b128 a[152:155], v10 offset:4096
v_add_f32_e32 v30, v30, v64
v_exp_f32_e32 v67, v67
s_nop 0
v_cvt_pk_bf16_f32 v193, v66, v67
v_add_f32_e32 v30, v30, v65
v_mfma_f32_16x16x32_bf16 v[72:75], a[128:131], v[160:163], 0
v_exp_f32_e32 v68, v68
v_mfma_f32_16x16x32_bf16 v[72:75], a[132:135], v[164:167], v[72:75]
v_add_f32_e32 v30, v30, v66
v_mfma_f32_16x16x32_bf16 v[72:75], a[136:139], v[168:171], v[72:75]
v_exp_f32_e32 v69, v69
v_mfma_f32_16x16x32_bf16 v[72:75], a[140:143], v[172:175], v[72:75]
v_cvt_pk_bf16_f32 v200, v68, v69
v_exp_f32_e32 v70, v70
ds_read_b128 a[156:159], v11 offset:4096
v_add_f32_e32 v30, v30, v67
v_exp_f32_e32 v71, v71
v_mfma_f32_16x16x32_bf16 v[76:79], a[128:131], v[176:179], 0
s_nop 1
v_exp_f32_e32 v72, v72
v_mfma_f32_16x16x32_bf16 v[76:79], a[132:135], v[180:183], v[76:79]
v_cvt_pk_bf16_f32 v201, v70, v71
v_mfma_f32_16x16x32_bf16 v[76:79], a[136:139], v[184:187], v[76:79]
v_exp_f32_e32 v73, v73
v_mfma_f32_16x16x32_bf16 v[76:79], a[140:143], v[188:191], v[76:79]
v_cvt_pk_bf16_f32 v208, v72, v73
v_exp_f32_e32 v74, v74
ds_read_b128 a[128:131], v8 offset:8192
v_add_f32_e32 v32, v32, v72
s_waitcnt lgkmcnt(1)
v_exp_f32_e32 v75, v75
s_nop 0
v_cvt_pk_bf16_f32 v209, v74, v75
v_add_f32_e32 v32, v32, v73
v_mfma_f32_16x16x32_bf16 v[64:67], a[144:147], v[128:131], 0
v_exp_f32_e32 v76, v76
v_mfma_f32_16x16x32_bf16 v[64:67], a[148:151], v[132:135], v[64:67]
v_add_f32_e32 v32, v32, v74
v_mfma_f32_16x16x32_bf16 v[64:67], a[152:155], v[136:139], v[64:67]
v_exp_f32_e32 v77, v77
v_mfma_f32_16x16x32_bf16 v[64:67], a[156:159], v[140:143], v[64:67]
v_cvt_pk_bf16_f32 v216, v76, v77
v_exp_f32_e32 v78, v78
ds_read_b128 a[132:135], v9 offset:8192
v_add_f32_e32 v32, v32, v75
v_exp_f32_e32 v79, v79
v_mfma_f32_16x16x32_bf16 v[68:71], a[144:147], v[144:147], 0
s_nop 1
v_exp_f32_e32 v64, v64
v_mfma_f32_16x16x32_bf16 v[68:71], a[148:151], v[148:151], v[68:71]
v_cvt_pk_bf16_f32 v217, v78, v79
v_mfma_f32_16x16x32_bf16 v[68:71], a[152:155], v[152:155], v[68:71]
v_exp_f32_e32 v65, v65
v_mfma_f32_16x16x32_bf16 v[68:71], a[156:159], v[156:159], v[68:71]
v_cvt_pk_bf16_f32 v194, v64, v65
v_exp_f32_e32 v66, v66
ds_read_b128 a[136:139], v10 offset:8192
v_add_f32_e32 v30, v30, v64
v_exp_f32_e32 v67, v67
s_nop 0
v_cvt_pk_bf16_f32 v195, v66, v67
v_add_f32_e32 v30, v30, v65
v_mfma_f32_16x16x32_bf16 v[72:75], a[144:147], v[160:163], 0
v_exp_f32_e32 v68, v68
v_mfma_f32_16x16x32_bf16 v[72:75], a[148:151], v[164:167], v[72:75]
v_add_f32_e32 v30, v30, v66
v_mfma_f32_16x16x32_bf16 v[72:75], a[152:155], v[168:171], v[72:75]
v_exp_f32_e32 v69, v69
v_mfma_f32_16x16x32_bf16 v[72:75], a[156:159], v[172:175], v[72:75]
v_cvt_pk_bf16_f32 v202, v68, v69
v_exp_f32_e32 v70, v70
ds_read_b128 a[140:143], v11 offset:8192
v_add_f32_e32 v30, v30, v67
v_exp_f32_e32 v71, v71
v_mfma_f32_16x16x32_bf16 v[76:79], a[144:147], v[176:179], 0
s_nop 1
v_exp_f32_e32 v72, v72
v_mfma_f32_16x16x32_bf16 v[76:79], a[148:151], v[180:183], v[76:79]
v_cvt_pk_bf16_f32 v203, v70, v71
v_mfma_f32_16x16x32_bf16 v[76:79], a[152:155], v[184:187], v[76:79]
v_exp_f32_e32 v73, v73
v_mfma_f32_16x16x32_bf16 v[76:79], a[156:159], v[188:191], v[76:79]
v_cvt_pk_bf16_f32 v210, v72, v73
v_exp_f32_e32 v74, v74
ds_read_b128 a[144:147], v8 offset:12288
v_add_f32_e32 v32, v32, v72
s_waitcnt lgkmcnt(1)
v_exp_f32_e32 v75, v75
s_nop 0
v_cvt_pk_bf16_f32 v211, v74, v75
v_add_f32_e32 v32, v32, v73
v_mfma_f32_16x16x32_bf16 v[64:67], a[128:131], v[128:131], 0
v_exp_f32_e32 v76, v76
v_mfma_f32_16x16x32_bf16 v[64:67], a[132:135], v[132:135], v[64:67]
v_add_f32_e32 v32, v32, v74
v_mfma_f32_16x16x32_bf16 v[64:67], a[136:139], v[136:139], v[64:67]
v_exp_f32_e32 v77, v77
v_mfma_f32_16x16x32_bf16 v[64:67], a[140:143], v[140:143], v[64:67]
v_cvt_pk_bf16_f32 v218, v76, v77
v_exp_f32_e32 v78, v78
ds_read_b128 a[148:151], v9 offset:12288
v_add_f32_e32 v32, v32, v75
v_exp_f32_e32 v79, v79
v_mfma_f32_16x16x32_bf16 v[68:71], a[128:131], v[144:147], 0
s_nop 1
v_exp_f32_e32 v64, v64
v_mfma_f32_16x16x32_bf16 v[68:71], a[132:135], v[148:151], v[68:71]
v_cvt_pk_bf16_f32 v219, v78, v79
v_mfma_f32_16x16x32_bf16 v[68:71], a[136:139], v[152:155], v[68:71]
v_exp_f32_e32 v65, v65
v_mfma_f32_16x16x32_bf16 v[68:71], a[140:143], v[156:159], v[68:71]
v_cvt_pk_bf16_f32 v196, v64, v65
v_exp_f32_e32 v66, v66
ds_read_b128 a[152:155], v10 offset:12288
v_add_f32_e32 v30, v30, v64
v_exp_f32_e32 v67, v67
s_nop 0
v_cvt_pk_bf16_f32 v197, v66, v67
v_add_f32_e32 v30, v30, v65
v_mfma_f32_16x16x32_bf16 v[72:75], a[128:131], v[160:163], 0
v_exp_f32_e32 v68, v68
v_mfma_f32_16x16x32_bf16 v[72:75], a[132:135], v[164:167], v[72:75]
v_add_f32_e32 v30, v30, v66
v_mfma_f32_16x16x32_bf16 v[72:75], a[136:139], v[168:171], v[72:75]
v_exp_f32_e32 v69, v69
v_mfma_f32_16x16x32_bf16 v[72:75], a[140:143], v[172:175], v[72:75]
v_cvt_pk_bf16_f32 v204, v68, v69
v_exp_f32_e32 v70, v70
ds_read_b128 a[156:159], v11 offset:12288
v_add_f32_e32 v30, v30, v67
v_exp_f32_e32 v71, v71
v_mfma_f32_16x16x32_bf16 v[76:79], a[128:131], v[176:179], 0
s_nop 1
v_exp_f32_e32 v72, v72
v_mfma_f32_16x16x32_bf16 v[76:79], a[132:135], v[180:183], v[76:79]
v_cvt_pk_bf16_f32 v205, v70, v71
v_mfma_f32_16x16x32_bf16 v[76:79], a[136:139], v[184:187], v[76:79]
v_exp_f32_e32 v73, v73
v_mfma_f32_16x16x32_bf16 v[76:79], a[140:143], v[188:191], v[76:79]
v_cvt_pk_bf16_f32 v212, v72, v73
v_exp_f32_e32 v74, v74
ds_read_b128 a[128:131], v8 offset:16384
v_add_f32_e32 v32, v32, v72
s_waitcnt lgkmcnt(1)
v_exp_f32_e32 v75, v75
s_nop 0
v_cvt_pk_bf16_f32 v213, v74, v75
v_add_f32_e32 v32, v32, v73
v_mfma_f32_16x16x32_bf16 v[64:67], a[144:147], v[128:131], 0
v_exp_f32_e32 v76, v76
v_mfma_f32_16x16x32_bf16 v[64:67], a[148:151], v[132:135], v[64:67]
ds_read_b64_tr_b16 a[160:161], v0 offset:0
v_add_f32_e32 v32, v32, v74
v_mfma_f32_16x16x32_bf16 v[64:67], a[152:155], v[136:139], v[64:67]
v_exp_f32_e32 v77, v77
v_mfma_f32_16x16x32_bf16 v[64:67], a[156:159], v[140:143], v[64:67]
ds_read_b64_tr_b16 a[162:163], v0 offset:4096
v_cvt_pk_bf16_f32 v220, v76, v77
v_exp_f32_e32 v78, v78
ds_read_b128 a[132:135], v9 offset:16384
v_add_f32_e32 v32, v32, v75
v_exp_f32_e32 v79, v79
v_mfma_f32_16x16x32_bf16 v[68:71], a[144:147], v[144:147], 0
s_waitcnt lgkmcnt(4)
v_exp_f32_e32 v64, v64
v_mfma_f32_16x16x32_bf16 v[68:71], a[148:151], v[148:151], v[68:71]
ds_read_b64_tr_b16 a[164:165], v0 offset:8192
v_cvt_pk_bf16_f32 v221, v78, v79
v_mfma_f32_16x16x32_bf16 v[68:71], a[152:155], v[152:155], v[68:71]
s_waitcnt vmcnt(12)
v_exp_f32_e32 v65, v65
v_mfma_f32_16x16x32_bf16 v[68:71], a[156:159], v[156:159], v[68:71]
ds_read_b64_tr_b16 a[166:167], v0 offset:12288
v_cvt_pk_bf16_f32 v198, v64, v65
s_barrier
v_exp_f32_e32 v66, v66
ds_read_b128 a[136:139], v10 offset:16384
v_add_f32_e32 v30, v30, v64
s_add_u32 m0, s68, 0x1c000
v_exp_f32_e32 v67, v67
s_nop 0
v_cvt_pk_bf16_f32 v199, v66, v67
v_add_f32_e32 v30, v30, v65
v_mfma_f32_16x16x32_bf16 v[38:41], v[34:37], v[200:203], v[38:41]
buffer_load_dwordx4 v24, s[40:43], s77 offen offset:0 nt lds
v_mfma_f32_16x16x32_bf16 v[72:75], a[144:147], v[160:163], 0
v_exp_f32_e32 v68, v68
v_mfma_f32_16x16x32_bf16 v[72:75], a[148:151], v[164:167], v[72:75]
ds_read_b64_tr_b16 a[168:169], v1 offset:0
v_add_f32_e32 v30, v30, v66
v_mfma_f32_16x16x32_bf16 v[72:75], a[152:155], v[168:171], v[72:75]
v_exp_f32_e32 v69, v69
v_mfma_f32_16x16x32_bf16 v[72:75], a[156:159], v[172:175], v[72:75]
ds_read_b64_tr_b16 a[170:171], v1 offset:4096
v_cvt_pk_bf16_f32 v206, v68, v69
v_exp_f32_e32 v70, v70
ds_read_b128 a[140:143], v11 offset:16384
v_add_f32_e32 v30, v30, v67
v_exp_f32_e32 v71, v71
buffer_load_dwordx4 v25, s[40:43], s77 offen offset:1024 nt lds
v_mfma_f32_16x16x32_bf16 v[76:79], a[144:147], v[176:179], 0
v_exp_f32_e32 v72, v72
v_mfma_f32_16x16x32_bf16 v[76:79], a[148:151], v[180:183], v[76:79]
ds_read_b64_tr_b16 a[172:173], v1 offset:8192
v_cvt_pk_bf16_f32 v207, v70, v71
v_mfma_f32_16x16x32_bf16 v[76:79], a[152:155], v[184:187], v[76:79]
s_waitcnt lgkmcnt(5)
v_exp_f32_e32 v73, v73
v_mfma_f32_16x16x32_bf16 v[76:79], a[156:159], v[188:191], v[76:79]
ds_read_b64_tr_b16 a[174:175], v1 offset:12288
v_cvt_pk_bf16_f32 v214, v72, v73
v_exp_f32_e32 v74, v74
ds_read_b128 a[144:147], v8 offset:20480
v_add_f32_e32 v32, v32, v72
s_waitcnt lgkmcnt(3)
v_exp_f32_e32 v75, v75
s_nop 0
v_cvt_pk_bf16_f32 v215, v74, v75
v_add_f32_e32 v32, v32, v73
v_mfma_f32_16x16x32_bf16 v[42:45], v[34:37], v[216:219], v[42:45]
buffer_load_dwordx4 v24, s[40:43], s77 offen offset:2048 nt lds
.p2align 6
; ---- loopA
.Lkf_top:
v_mfma_f32_16x16x32_bf16 v[64:67], a[128:131], v[128:131], 0
v_exp_f32_e32 v76, v76
v_mfma_f32_16x16x32_bf16 v[64:67], a[132:135], v[132:135], v[64:67]
ds_read_b64_tr_b16 a[176:177], v2 offset:0
v_add_f32_e32 v32, v32, v74
v_mfma_f32_16x16x32_bf16 v[64:67], a[136:139], v[136:139], v[64:67]
v_exp_f32_e32 v77, v77
v_mfma_f32_16x16x32_bf16 v[64:67], a[140:143], v[140:143], v[64:67]
ds_read_b64_tr_b16 a[178:179], v2 offset:4096
v_cvt_pk_bf16_f32 v222, v76, v77
v_mfma_f32_16x16x32_bf16 a[0:3], a[160:163], v[192:195], a[0:3]
v_exp_f32_e32 v78, v78
v_mfma_f32_16x16x32_bf16 a[0:3], a[164:167], v[196:199], a[0:3]
ds_read_b128 a[148:151], v9 offset:20480
v_add_f32_e32 v32, v32, v75
v_mfma_f32_16x16x32_bf16 a[4:7], a[160:163], v[200:203], a[4:7]
v_exp_f32_e32 v79, v79
v_mfma_f32_16x16x32_bf16 a[4:7], a[164:167], v[204:207], a[4:7]
buffer_load_dwordx4 v25, s[40:43], s77 offen offset:3072 nt lds
v_mfma_f32_16x16x32_bf16 v[68:71], a[128:131], v[144:147], 0
s_waitcnt lgkmcnt(4)
v_exp_f32_e32 v64, v64
v_mfma_f32_16x16x32_bf16 v[68:71], a[132:135], v[148:151], v[68:71]
ds_read_b64_tr_b16 a[180:181], v2 offset:8192
v_cvt_pk_bf16_f32 v223, v78, v79
v_mfma_f32_16x16x32_bf16 v[68:71], a[136:139], v[152:155], v[68:71]
s_mov_b32 m0, s68
v_exp_f32_e32 v65, v65
v_mfma_f32_16x16x32_bf16 v[68:71], a[140:143], v[156:159], v[68:71]
ds_read_b64_tr_b16 a[182:183], v2 offset:12288
v_cvt_pk_bf16_f32 v224, v64, v65
v_mfma_f32_16x16x32_bf16 a[8:11], a[160:163], v[208:211], a[8:11]
s_add_u32 s77, s77, 0x4000
v_exp_f32_e32 v66, v66
v_mfma_f32_16x16x32_bf16 a[8:11], a[164:167], v[212:215], a[8:11]
ds_read_b128 a[152:155], v10 offset:20480
v_add_f32_e32 v30, v30, v64
v_mfma_f32_16x16x32_bf16 a[12:15], a[160:163], v[216:219], a[12:15]
v_exp_f32_e32 v67, v67
v_mfma_f32_16x16x32_bf16 a[12:15], a[164:167], v[220:223], a[12:15]
v_cvt_pk_bf16_f32 v225, v66, v67
v_add_f32_e32 v30, v30, v65
v_mfma_f32_16x16x32_bf16 v[38:41], v[34:37], v[204:207], v[38:41]
buffer_load_dwordx4 v20, s[36:39], s76 offen offset:0 nt lds
v_mfma_f32_16x16x32_bf16 v[72:75], a[128:131], v[160:163], 0
v_exp_f32_e32 v68, v68
v_mfma_f32_16x16x32_bf16 v[72:75], a[132:135], v[164:167], v[72:75]
ds_read_b64_tr_b16 a[184:185], v3 offset:0
v_add_f32_e32 v30, v30, v66
v_mfma_f32_16x16x32_bf16 v[72:75], a[136:139], v[168:171], v[72:75]
v_exp_f32_e32 v69, v69
v_mfma_f32_16x16x32_bf16 v[72:75], a[140:143], v[172:175], v[72:75]
ds_read_b64_tr_b16 a[186:187], v3 offset:4096
v_cvt_pk_bf16_f32 v232, v68, v69
v_mfma_f32_16x16x32_bf16 a[16:19], a[168:171], v[192:195], a[16:19]
v_exp_f32_e32 v70, v70
v_mfma_f32_16x16x32_bf16 a[16:19], a[172:175], v[196:199], a[16:19]
ds_read_b128 a[156:159], v11 offset:20480
v_add_f32_e32 v30, v30, v67
v_mfma_f32_16x16x32_bf16 a[20:23], a[168:171], v[200:203], a[20:23]
v_exp_f32_e32 v71, v71
v_mfma_f32_16x16x32_bf16 a[20:23], a[172:175], v[204:207], a[20:23]
buffer_load_dwordx4 v21, s[36:39], s76 offen offset:1024 nt lds
v_mfma_f32_16x16x32_bf16 v[76:79], a[128:131], v[176:179], 0
s_waitcnt lgkmcnt(4)
v_exp_f32_e32 v72, v72
v_mfma_f32_16x16x32_bf16 v[76:79], a[132:135], v[180:183], v[76:79]
ds_read_b64_tr_b16 a[188:189], v3 offset:8192
v_cvt_pk_bf16_f32 v233, v70, v71
v_mfma_f32_16x16x32_bf16 v[76:79], a[136:139], v[184:187], v[76:79]
v_exp_f32_e32 v73, v73
v_mfma_f32_16x16x32_bf16 v[76:79], a[140:143], v[188:191], v[76:79]
ds_read_b64_tr_b16 a[190:191], v3 offset:12288
v_cvt_pk_bf16_f32 v240, v72, v73
v_mfma_f32_16x16x32_bf16 a[24:27], a[168:171], v[208:211], a[24:27]
v_exp_f32_e32 v74, v74
v_mfma_f32_16x16x32_bf16 a[24:27], a[172:175], v[212:215], a[24:27]
ds_read_b128 a[128:131], v8 offset:24576
v_add_f32_e32 v32, v32, v72
v_mfma_f32_16x16x32_bf16 a[28:31], a[168:171], v[216:219], a[28:31]
s_waitcnt lgkmcnt(3)
v_exp_f32_e32 v75, v75
v_mfma_f32_16x16x32_bf16 a[28:31], a[172:175], v[220:223], a[28:31]
v_cvt_pk_bf16_f32 v241, v74, v75
v_add_f32_e32 v32, v32, v73
v_mfma_f32_16x16x32_bf16 v[42:45], v[34:37], v[220:223], v[42:45]
buffer_load_dwordx4 v22, s[36:39], s76 offen offset:2048 nt lds
v_mfma_f32_16x16x32_bf16 v[64:67], a[144:147], v[128:131], 0
v_exp_f32_e32 v76, v76
v_mfma_f32_16x16x32_bf16 v[64:67], a[148:151], v[132:135], v[64:67]
ds_read_b64_tr_b16 a[160:161], v4 offset:0
v_add_f32_e32 v32, v32, v74
v_mfma_f32_16x16x32_bf16 v[64:67], a[152:155], v[136:139], v[64:67]
v_exp_f32_e32 v77, v77
v_mfma_f32_16x16x32_bf16 v[64:67], a[156:159], v[140:143], v[64:67]
ds_read_b64_tr_b16 a[162:163], v4 offset:4096
v_cvt_pk_bf16_f32 v248, v76, v77
v_mfma_f32_16x16x32_bf16 a[32:35], a[176:179], v[192:195], a[32:35]
v_exp_f32_e32 v78, v78
v_mfma_f32_16x16x32_bf16 a[32:35], a[180:183], v[196:199], a[32:35]
ds_read_b128 a[132:135], v9 offset:24576
v_add_f32_e32 v32, v32, v75
v_mfma_f32_16x16x32_bf16 a[36:39], a[176:179], v[200:203], a[36:39]
v_exp_f32_e32 v79, v79
v_mfma_f32_16x16x32_bf16 a[36:39], a[180:183], v[204:207], a[36:39]
buffer_load_dwordx4 v23, s[36:39], s76 offen offset:3072 nt lds
v_mfma_f32_16x16x32_bf16 v[68:71], a[144:147], v[144:147], 0
s_waitcnt lgkmcnt(4)
v_exp_f32_e32 v64, v64
v_mfma_f32_16x16x32_bf16 v[68:71], a[148:151], v[148:151], v[68:71]
ds_read_b64_tr_b16 a[164:165], v4 offset:8192
v_cvt_pk_bf16_f32 v249, v78, v79
v_mfma_f32_16x16x32_bf16 v[68:71], a[152:155], v[152:155], v[68:71]
s_add_u32 s76, s76, 0x4000
v_exp_f32_e32 v65, v65
v_mfma_f32_16x16x32_bf16 v[68:71], a[156:159], v[156:159], v[68:71]
ds_read_b64_tr_b16 a[166:167], v4 offset:12288
v_cvt_pk_bf16_f32 v226, v64, v65
v_mfma_f32_16x16x32_bf16 a[40:43], a[176:179], v[208:211], a[40:43]
v_exp_f32_e32 v66, v66
v_mfma_f32_16x16x32_bf16 a[40:43], a[180:183], v[212:215], a[40:43]
ds_read_b128 a[136:139], v10 offset:24576
v_add_f32_e32 v30, v30, v64
v_mfma_f32_16x16x32_bf16 a[44:47], a[176:179], v[216:219], a[44:47]
v_exp_f32_e32 v67, v67
v_mfma_f32_16x16x32_bf16 a[44:47], a[180:183], v[220:223], a[44:47]
v_cvt_pk_bf16_f32 v227, v66, v67
v_add_f32_e32 v30, v30, v65
v_mfma_f32_16x16x32_bf16 v[72:75], a[144:147], v[160:163], 0
v_exp_f32_e32 v68, v68
v_mfma_f32_16x16x32_bf16 v[72:75], a[148:151], v[164:167], v[72:75]
ds_read_b64_tr_b16 a[168:169], v5 offset:0
v_add_f32_e32 v30, v30, v66
v_mfma_f32_16x16x32_bf16 v[72:75], a[152:155], v[168:171], v[72:75]
v_exp_f32_e32 v69, v69
v_mfma_f32_16x16x32_bf16 v[72:75], a[156:159], v[172:175], v[72:75]
ds_read_b64_tr_b16 a[170:171], v5 offset:4096
v_cvt_pk_bf16_f32 v234, v68, v69
v_mfma_f32_16x16x32_bf16 a[48:51], a[184:187], v[192:195], a[48:51]
v_exp_f32_e32 v70, v70
v_mfma_f32_16x16x32_bf16 a[48:51], a[188:191], v[196:199], a[48:51]
ds_read_b128 a[140:143], v11 offset:24576
v_add_f32_e32 v30, v30, v67
v_mfma_f32_16x16x32_bf16 a[52:55], a[184:187], v[200:203], a[52:55]
v_exp_f32_e32 v71, v71
v_mfma_f32_16x16x32_bf16 a[52:55], a[188:191], v[204:207], a[52:55]
v_mfma_f32_16x16x32_bf16 v[76:79], a[144:147], v[176:179], 0
s_waitcnt lgkmcnt(4)
v_exp_f32_e32 v72, v72
v_mfma_f32_16x16x32_bf16 v[76:79], a[148:151], v[180:183], v[76:79]
ds_read_b64_tr_b16 a[172:173], v5 offset:8192
v_cvt_pk_bf16_f32 v235, v70, v71
v_mfma_f32_16x16x32_bf16 v[76:79], a[152:155], v[184:187], v[76:79]
v_exp_f32_e32 v73, v73
v_mfma_f32_16x16x32_bf16 v[76:79], a[156:159], v[188:191], v[76:79]
ds_read_b64_tr_b16 a[174:175], v5 offset:12288
v_cvt_pk_bf16_f32 v242, v72, v73
v_mfma_f32_16x16x32_bf16 a[56:59], a[184:187], v[208:211], a[56:59]
v_exp_f32_e32 v74, v74
v_mfma_f32_16x16x32_bf16 a[56:59], a[188:191], v[212:215], a[56:59]
ds_read_b128 a[144:147], v8 offset:28672
v_add_f32_e32 v32, v32, v72
v_mfma_f32_16x16x32_bf16 a[60:63], a[184:187], v[216:219], a[60:63]
s_waitcnt lgkmcnt(3)
v_exp_f32_e32 v75, v75
v_mfma_f32_16x16x32_bf16 a[60:63], a[188:191], v[220:223], a[60:63]
v_cvt_pk_bf16_f32 v243, v74, v75
v_add_f32_e32 v32, v32, v73
v_mfma_f32_16x16x32_bf16 v[64:67], a[128:131], v[128:131], 0
v_exp_f32_e32 v76, v76
v_mfma_f32_16x16x32_bf16 v[64:67], a[132:135], v[132:135], v[64:67]
ds_read_b64_tr_b16 a[176:177], v6 offset:0
v_add_f32_e32 v32, v32, v74
v_mfma_f32_16x16x32_bf16 v[64:67], a[136:139], v[136:139], v[64:67]
v_exp_f32_e32 v77, v77
v_mfma_f32_16x16x32_bf16 v[64:67], a[140:143], v[140:143], v[64:67]
ds_read_b64_tr_b16 a[178:179], v6 offset:4096
v_cvt_pk_bf16_f32 v250, v76, v77
v_mfma_f32_16x16x32_bf16 a[64:67], a[160:163], v[192:195], a[64:67]
v_exp_f32_e32 v78, v78
v_mfma_f32_16x16x32_bf16 a[64:67], a[164:167], v[196:199], a[64:67]
ds_read_b128 a[148:151], v9 offset:28672
v_add_f32_e32 v32, v32, v75
v_mfma_f32_16x16x32_bf16 a[68:71], a[160:163], v[200:203], a[68:71]
v_exp_f32_e32 v79, v79
v_mfma_f32_16x16x32_bf16 a[68:71], a[164:167], v[204:207], a[68:71]
buffer_load_dword v48, v29, s[8:11], s12 offen offset:0
v_mfma_f32_16x16x32_bf16 v[68:71], a[128:131], v[144:147], 0
s_waitcnt lgkmcnt(4)
v_exp_f32_e32 v64, v64
v_mfma_f32_16x16x32_bf16 v[68:71], a[132:135], v[148:151], v[68:71]
ds_read_b64_tr_b16 a[180:181], v6 offset:8192
v_cvt_pk_bf16_f32 v251, v78, v79
v_mfma_f32_16x16x32_bf16 v[68:71], a[136:139], v[152:155], v[68:71]
v_exp_f32_e32 v65, v65
v_mfma_f32_16x16x32_bf16 v[68:71], a[140:143], v[156:159], v[68:71]
ds_read_b64_tr_b16 a[182:183], v6 offset:12288
v_cvt_pk_bf16_f32 v228, v64, v65
v_mfma_f32_16x16x32_bf16 a[72:75], a[160:163], v[208:211], a[72:75]
v_exp_f32_e32 v66, v66
v_mfma_f32_16x16x32_bf16 a[72:75], a[164:167], v[212:215], a[72:75]
ds_read_b128 a[152:155], v10 offset:28672
v_add_f32_e32 v30, v30, v64
v_mfma_f32_16x16x32_bf16 a[76:79], a[160:163], v[216:219], a[76:79]
v_exp_f32_e32 v67, v67
v_mfma_f32_16x16x32_bf16 a[76:79], a[164:167], v[220:223], a[76:79]
v_cvt_pk_bf16_f32 v229, v66, v67
v_add_f32_e32 v30, v30, v65
v_mfma_f32_16x16x32_bf16 v[72:75], a[128:131], v[160:163], 0
v_exp_f32_e32 v68, v68
v_mfma_f32_16x16x32_bf16 v[72:75], a[132:135], v[164:167], v[72:75]
ds_read_b64_tr_b16 a[184:185], v7 offset:0
v_add_f32_e32 v30, v30, v66
v_mfma_f32_16x16x32_bf16 v[72:75], a[136:139], v[168:171], v[72:75]
s_add_u32 s12, s12, 0x400
v_exp_f32_e32 v69, v69
v_mfma_f32_16x16x32_bf16 v[72:75], a[140:143], v[172:175], v[72:75]
ds_read_b64_tr_b16 a[186:187], v7 offset:4096
v_cvt_pk_bf16_f32 v236, v68, v69
v_mfma_f32_16x16x32_bf16 a[80:83], a[168:171], v[192:195], a[80:83]
v_exp_f32_e32 v70, v70
v_mfma_f32_16x16x32_bf16 a[80:83], a[172:175], v[196:199], a[80:83]
ds_read_b128 a[156:159], v11 offset:28672
v_add_f32_e32 v30, v30, v67
v_mfma_f32_16x16x32_bf16 a[84:87], a[168:171], v[200:203], a[84:87]
v_exp_f32_e32 v71, v71
v_mfma_f32_16x16x32_bf16 a[84:87], a[172:175], v[204:207], a[84:87]
v_mfma_f32_16x16x32_bf16 v[76:79], a[128:131], v[176:179], 0
s_waitcnt lgkmcnt(4)
v_exp_f32_e32 v72, v72
v_mfma_f32_16x16x32_bf16 v[76:79], a[132:135], v[180:183], v[76:79]
ds_read_b64_tr_b16 a[188:189], v7 offset:8192
v_cvt_pk_bf16_f32 v237, v70, v71
v_mfma_f32_16x16x32_bf16 v[76:79], a[136:139], v[184:187], v[76:79]
v_exp_f32_e32 v73, v73
v_mfma_f32_16x16x32_bf16 v[76:79], a[140:143], v[188:191], v[76:79]
ds_read_b64_tr_b16 a[190:191], v7 offset:12288
v_cvt_pk_bf16_f32 v244, v72, v73
v_mfma_f32_16x16x32_bf16 a[88:91], a[168:171], v[208:211], a[88:91]
v_exp_f32_e32 v74, v74
v_mfma_f32_16x16x32_bf16 a[88:91], a[172:175], v[212:215], a[88:91]
ds_read_b128 a[128:131], v8 offset:32768
v_add_f32_e32 v32, v32, v72
v_mfma_f32_16x16x32_bf16 a[92:95], a[168:171], v[216:219], a[92:95]
s_waitcnt lgkmcnt(3)
v_exp_f32_e32 v75, v75
v_mfma_f32_16x16x32_bf16 a[92:95], a[172:175], v[220:223], a[92:95]
v_cvt_pk_bf16_f32 v245, v74, v75
v_add_f32_e32 v32, v32, v73
v_mfma_f32_16x16x32_bf16 v[64:67], a[144:147], v[128:131], 0
v_exp_f32_e32 v76, v76
v_mfma_f32_16x16x32_bf16 v[64:67], a[148:151], v[132:135], v[64:67]
ds_read_b64_tr_b16 a[160:161], v0 offset:16384
v_add_f32_e32 v32, v32, v74
v_mfma_f32_16x16x32_bf16 v[64:67], a[152:155], v[136:139], v[64:67]
v_exp_f32_e32 v77, v77
v_mfma_f32_16x16x32_bf16 v[64:67], a[156:159], v[140:143], v[64:67]
ds_read_b64_tr_b16 a[162:163], v0 offset:20480
v_cvt_pk_bf16_f32 v252, v76, v77
v_mfma_f32_16x16x32_bf16 a[96:99], a[176:179], v[192:195], a[96:99]
v_exp_f32_e32 v78, v78
v_mfma_f32_16x16x32_bf16 a[96:99], a[180:183], v[196:199], a[96:99]
ds_read_b128 a[132:135], v9 offset:32768
v_add_f32_e32 v32, v32, v75
v_mfma_f32_16x16x32_bf16 a[100:103], a[176:179], v[200:203], a[100:103]
v_exp_f32_e32 v79, v79
v_mfma_f32_16x16x32_bf16 a[100:103], a[180:183], v[204:207], a[100:103]
v_mfma_f32_16x16x32_bf16 v[68:71], a[144:147], v[144:147], 0
s_waitcnt lgkmcnt(4)
v_exp_f32_e32 v64, v64
v_mfma_f32_16x16x32_bf16 v[68:71], a[148:151], v[148:151], v[68:71]
ds_read_b64_tr_b16 a[164:165], v0 offset:24576
v_cvt_pk_bf16_f32 v253, v78, v79
v_mfma_f32_16x16x32_bf16 v[68:71], a[152:155], v[152:155], v[68:71]
s_waitcnt vmcnt(10)
v_exp_f32_e32 v65, v65
v_mfma_f32_16x16x32_bf16 v[68:71], a[156:159], v[156:159], v[68:71]
ds_read_b64_tr_b16 a[166:167], v0 offset:28672
v_cvt_pk_bf16_f32 v230, v64, v65
v_mfma_f32_16x16x32_bf16 a[104:107], a[176:179], v[208:211], a[104:107]
s_barrier
v_exp_f32_e32 v66, v66
v_mfma_f32_16x16x32_bf16 a[104:107], a[180:183], v[212:215], a[104:107]
ds_read_b128 a[136:139], v10 offset:32768
v_add_f32_e32 v30, v30, v64
v_mfma_f32_16x16x32_bf16 a[108:111], a[176:179], v[216:219], a[108:111]
s_add_u32 m0, s68, 0x10000
v_exp_f32_e32 v67, v67
v_mfma_f32_16x16x32_bf16 a[108:111], a[180:183], v[220:223], a[108:111]
v_cvt_pk_bf16_f32 v231, v66, v67
v_add_f32_e32 v30, v30, v65
v_mfma_f32_16x16x32_bf16 v[38:41], v[34:37], v[232:235], v[38:41]
buffer_load_dwordx4 v24, s[40:43], s77 offen offset:0 nt lds
v_mfma_f32_16x16x32_bf16 v[72:75], a[144:147], v[160:163], 0
v_exp_f32_e32 v68, v68
v_mfma_f32_16x16x32_bf16 v[72:75], a[148:151], v[164:167], v[72:75]
ds_read_b64_tr_b16 a[168:169], v1 offset:16384
v_add_f32_e32 v30, v30, v66
v_mfma_f32_16x16x32_bf16 v[72:75], a[152:155], v[168:171], v[72:75]
v_exp_f32_e32 v69, v69
v_mfma_f32_16x16x32_bf16 v[72:75], a[156:159], v[172:175], v[72:75]
ds_read_b64_tr_b16 a[170:171], v1 offset:20480
v_cvt_pk_bf16_f32 v238, v68, v69
v_mfma_f32_16x16x32_bf16 a[112:115], a[184:187], v[192:195], a[112:115]
v_exp_f32_e32 v70, v70
v_mfma_f32_16x16x32_bf16 a[112:115], a[188:191], v[196:199], a[112:115]
ds_read_b128 a[140:143], v11 offset:32768
v_add_f32_e32 v30, v30, v67
v_mfma_f32_16x16x32_bf16 a[116:119], a[184:187], v[200:203], a[116:119]
v_exp_f32_e32 v71, v71
v_mfma_f32_16x16x32_bf16 a[116:119], a[188:191], v[204:207], a[116:119]
buffer_load_dwordx4 v25, s[40:43], s77 offen offset:1024 nt lds
v_mfma_f32_16x16x32_bf16 v[76:79], a[144:147], v[176:179], 0
v_exp_f32_e32 v72, v72
v_mfma_f32_16x16x32_bf16 v[76:79], a[148:151], v[180:183], v[76:79]
ds_read_b64_tr_b16 a[172:173], v1 offset:24576
v_cvt_pk_bf16_f32 v239, v70, v71
v_mfma_f32_16x16x32_bf16 v[76:79], a[152:155], v[184:187], v[76:79]
s_waitcnt lgkmcnt(5)
v_exp_f32_e32 v73, v73
v_mfma_f32_16x16x32_bf16 v[76:79], a[156:159], v[188:191], v[76:79]
ds_read_b64_tr_b16 a[174:175], v1 offset:28672
v_cvt_pk_bf16_f32 v246, v72, v73
v_mfma_f32_16x16x32_bf16 a[120:123], a[184:187], v[208:211], a[120:123]
v_exp_f32_e32 v74, v74
v_mfma_f32_16x16x32_bf16 a[120:123], a[188:191], v[212:215], a[120:123]
ds_read_b128 a[144:147], v8 offset:36864
v_add_f32_e32 v32, v32, v72
v_mfma_f32_16x16x32_bf16 a[124:127], a[184:187], v[216:219], a[124:127]
s_waitcnt lgkmcnt(3)
v_exp_f32_e32 v75, v75
v_mfma_f32_16x16x32_bf16 a[124:127], a[188:191], v[220:223], a[124:127]
v_cvt_pk_bf16_f32 v247, v74, v75
v_add_f32_e32 v32, v32, v73
v_mfma_f32_16x16x32_bf16 v[42:45], v[34:37], v[248:251], v[42:45]
buffer_load_dwordx4 v24, s[40:43], s77 offen offset:2048 nt lds
v_mfma_f32_16x16x32_bf16 v[64:67], a[128:131], v[128:131], 0
v_exp_f32_e32 v76, v76
v_mfma_f32_16x16x32_bf16 v[64:67], a[132:135], v[132:135], v[64:67]
ds_read_b64_tr_b16 a[176:177], v2 offset:16384
v_add_f32_e32 v32, v32, v74
v_mfma_f32_16x16x32_bf16 v[64:67], a[136:139], v[136:139], v[64:67]
v_exp_f32_e32 v77, v77
v_mfma_f32_16x16x32_bf16 v[64:67], a[140:143], v[140:143], v[64:67]
ds_read_b64_tr_b16 a[178:179], v2 offset:20480
v_cvt_pk_bf16_f32 v254, v76, v77
v_mfma_f32_16x16x32_bf16 a[0:3], a[160:163], v[224:227], a[0:3]
v_exp_f32_e32 v78, v78
v_mfma_f32_16x16x32_bf16 a[0:3], a[164:167], v[228:231], a[0:3]
ds_read_b128 a[148:151], v9 offset:36864
v_add_f32_e32 v32, v32, v75
v_mfma_f32_16x16x32_bf16 a[4:7], a[160:163], v[232:235], a[4:7]
v_exp_f32_e32 v79, v79
v_mfma_f32_16x16x32_bf16 a[4:7], a[164:167], v[236:239], a[4:7]
buffer_load_dwordx4 v25, s[40:43], s77 offen offset:3072 nt lds
v_mfma_f32_16x16x32_bf16 v[68:71], a[128:131], v[144:147], 0
s_waitcnt lgkmcnt(4)
v_exp_f32_e32 v64, v64
v_mfma_f32_16x16x32_bf16 v[68:71], a[132:135], v[148:151], v[68:71]
ds_read_b64_tr_b16 a[180:181], v2 offset:24576
v_cvt_pk_bf16_f32 v255, v78, v79
v_mfma_f32_16x16x32_bf16 v[68:71], a[136:139], v[152:155], v[68:71]
s_add_u32 m0, s68, 0x4000
v_exp_f32_e32 v65, v65
v_mfma_f32_16x16x32_bf16 v[68:71], a[140:143], v[156:159], v[68:71]
ds_read_b64_tr_b16 a[182:183], v2 offset:28672
v_cvt_pk_bf16_f32 v192, v64, v65
v_mfma_f32_16x16x32_bf16 a[8:11], a[160:163], v[240:243], a[8:11]
s_add_u32 s77, s77, 0x4000
v_exp_f32_e32 v66, v66
v_mfma_f32_16x16x32_bf16 a[8:11], a[164:167], v[244:247], a[8:11]
ds_read_b128 a[152:155], v10 offset:36864
v_add_f32_e32 v30, v30, v64
v_mfma_f32_16x16x32_bf16 a[12:15], a[160:163], v[248:251], a[12:15]
v_exp_f32_e32 v67, v67
v_mfma_f32_16x16x32_bf16 a[12:15], a[164:167], v[252:255], a[12:15]
v_cvt_pk_bf16_f32 v193, v66, v67
v_add_f32_e32 v30, v30, v65
v_mfma_f32_16x16x32_bf16 v[38:41], v[34:37], v[236:239], v[38:41]
buffer_load_dwordx4 v20, s[36:39], s76 offen offset:0 nt lds
v_mfma_f32_16x16x32_bf16 v[72:75], a[128:131], v[160:163], 0
v_exp_f32_e32 v68, v68
v_mfma_f32_16x16x32_bf16 v[72:75], a[132:135], v[164:167], v[72:75]
ds_read_b64_tr_b16 a[184:185], v3 offset:16384
v_add_f32_e32 v30, v30, v66
v_mfma_f32_16x16x32_bf16 v[72:75], a[136:139], v[168:171], v[72:75]
v_exp_f32_e32 v69, v69
v_mfma_f32_16x16x32_bf16 v[72:75], a[140:143], v[172:175], v[72:75]
ds_read_b64_tr_b16 a[186:187], v3 offset:20480
v_cvt_pk_bf16_f32 v200, v68, v69
v_mfma_f32_16x16x32_bf16 a[16:19], a[168:171], v[224:227], a[16:19]
v_exp_f32_e32 v70, v70
v_mfma_f32_16x16x32_bf16 a[16:19], a[172:175], v[228:231], a[16:19]
ds_read_b128 a[156:159], v11 offset:36864
v_add_f32_e32 v30, v30, v67
v_mfma_f32_16x16x32_bf16 a[20:23], a[168:171], v[232:235], a[20:23]
v_exp_f32_e32 v71, v71
v_mfma_f32_16x16x32_bf16 a[20:23], a[172:175], v[236:239], a[20:23]
buffer_load_dwordx4 v21, s[36:39], s76 offen offset:1024 nt lds
v_mfma_f32_16x16x32_bf16 v[76:79], a[128:131], v[176:179], 0
s_waitcnt lgkmcnt(4)
v_exp_f32_e32 v72, v72
v_mfma_f32_16x16x32_bf16 v[76:79], a[132:135], v[180:183], v[76:79]
ds_read_b64_tr_b16 a[188:189], v3 offset:24576
v_cvt_pk_bf16_f32 v201, v70, v71
v_mfma_f32_16x16x32_bf16 v[76:79], a[136:139], v[184:187], v[76:79]
v_exp_f32_e32 v73, v73
v_mfma_f32_16x16x32_bf16 v[76:79], a[140:143], v[188:191], v[76:79]
ds_read_b64_tr_b16 a[190:191], v3 offset:28672
v_cvt_pk_bf16_f32 v208, v72, v73
v_mfma_f32_16x16x32_bf16 a[24:27], a[168:171], v[240:243], a[24:27]
v_exp_f32_e32 v74, v74
v_mfma_f32_16x16x32_bf16 a[24:27], a[172:175], v[244:247], a[24:27]
ds_read_b128 a[128:131], v8 offset:40960
v_add_f32_e32 v32, v32, v72
v_mfma_f32_16x16x32_bf16 a[28:31], a[168:171], v[248:251], a[28:31]
s_waitcnt lgkmcnt(3)
v_exp_f32_e32 v75, v75
v_mfma_f32_16x16x32_bf16 a[28:31], a[172:175], v[252:255], a[28:31]
v_cvt_pk_bf16_f32 v209, v74, v75
v_add_f32_e32 v32, v32, v73
v_mfma_f32_16x16x32_bf16 v[42:45], v[34:37], v[252:255], v[42:45]
buffer_load_dwordx4 v22, s[36:39], s76 offen offset:2048 nt lds
v_mfma_f32_16x16x32_bf16 v[64:67], a[144:147], v[128:131], 0
v_exp_f32_e32 v76, v76
v_mfma_f32_16x16x32_bf16 v[64:67], a[148:151], v[132:135], v[64:67]
ds_read_b64_tr_b16 a[160:161], v4 offset:16384
v_add_f32_e32 v32, v32, v74
v_mfma_f32_16x16x32_bf16 v[64:67], a[152:155], v[136:139], v[64:67]
v_exp_f32_e32 v77, v77
v_mfma_f32_16x16x32_bf16 v[64:67], a[156:159], v[140:143], v[64:67]
ds_read_b64_tr_b16 a[162:163], v4 offset:20480
v_cvt_pk_bf16_f32 v216, v76, v77
v_mfma_f32_16x16x32_bf16 a[32:35], a[176:179], v[224:227], a[32:35]
v_exp_f32_e32 v78, v78
v_mfma_f32_16x16x32_bf16 a[32:35], a[180:183], v[228:231], a[32:35]
ds_read_b128 a[132:135], v9 offset:40960
v_add_f32_e32 v32, v32, v75
v_mfma_f32_16x16x32_bf16 a[36:39], a[176:179], v[232:235], a[36:39]
v_exp_f32_e32 v79, v79
v_mfma_f32_16x16x32_bf16 a[36:39], a[180:183], v[236:239], a[36:39]
buffer_load_dwordx4 v23, s[36:39], s76 offen offset:3072 nt lds
v_mfma_f32_16x16x32_bf16 v[68:71], a[144:147], v[144:147], 0
s_waitcnt lgkmcnt(4)
v_exp_f32_e32 v64, v64
v_mfma_f32_16x16x32_bf16 v[68:71], a[148:151], v[148:151], v[68:71]
ds_read_b64_tr_b16 a[164:165], v4 offset:24576
v_cvt_pk_bf16_f32 v217, v78, v79
v_mfma_f32_16x16x32_bf16 v[68:71], a[152:155], v[152:155], v[68:71]
s_add_u32 s76, s76, 0x4000
v_exp_f32_e32 v65, v65
v_mfma_f32_16x16x32_bf16 v[68:71], a[156:159], v[156:159], v[68:71]
ds_read_b64_tr_b16 a[166:167], v4 offset:28672
v_cvt_pk_bf16_f32 v194, v64, v65
v_mfma_f32_16x16x32_bf16 a[40:43], a[176:179], v[240:243], a[40:43]
v_exp_f32_e32 v66, v66
v_mfma_f32_16x16x32_bf16 a[40:43], a[180:183], v[244:247], a[40:43]
ds_read_b128 a[136:139], v10 offset:40960
v_add_f32_e32 v30, v30, v64
v_mfma_f32_16x16x32_bf16 a[44:47], a[176:179], v[248:251], a[44:47]
v_exp_f32_e32 v67, v67
v_mfma_f32_16x16x32_bf16 a[44:47], a[180:183], v[252:255], a[44:47]
v_cvt_pk_bf16_f32 v195, v66, v67
v_add_f32_e32 v30, v30, v65
v_mfma_f32_16x16x32_bf16 v[72:75], a[144:147], v[160:163], 0
v_exp_f32_e32 v68, v68
v_mfma_f32_16x16x32_bf16 v[72:75], a[148:151], v[164:167], v[72:75]
ds_read_b64_tr_b16 a[168:169], v5 offset:16384
v_add_f32_e32 v30, v30, v66
v_mfma_f32_16x16x32_bf16 v[72:75], a[152:155], v[168:171], v[72:75]
v_exp_f32_e32 v69, v69
v_mfma_f32_16x16x32_bf16 v[72:75], a[156:159], v[172:175], v[72:75]
ds_read_b64_tr_b16 a[170:171], v5 offset:20480
v_cvt_pk_bf16_f32 v202, v68, v69
v_mfma_f32_16x16x32_bf16 a[48:51], a[184:187], v[224:227], a[48:51]
v_exp_f32_e32 v70, v70
v_mfma_f32_16x16x32_bf16 a[48:51], a[188:191], v[228:231], a[48:51]
ds_read_b128 a[140:143], v11 offset:40960
v_add_f32_e32 v30, v30, v67
v_mfma_f32_16x16x32_bf16 a[52:55], a[184:187], v[232:235], a[52:55]
v_exp_f32_e32 v71, v71
v_mfma_f32_16x16x32_bf16 a[52:55], a[188:191], v[236:239], a[52:55]
v_mfma_f32_16x16x32_bf16 v[76:79], a[144:147], v[176:179], 0
s_waitcnt lgkmcnt(4)
v_exp_f32_e32 v72, v72
v_mfma_f32_16x16x32_bf16 v[76:79], a[148:151], v[180:183], v[76:79]
ds_read_b64_tr_b16 a[172:173], v5 offset:24576
v_cvt_pk_bf16_f32 v203, v70, v71
v_mfma_f32_16x16x32_bf16 v[76:79], a[152:155], v[184:187], v[76:79]
v_exp_f32_e32 v73, v73
v_mfma_f32_16x16x32_bf16 v[76:79], a[156:159], v[188:191], v[76:79]
ds_read_b64_tr_b16 a[174:175], v5 offset:28672
v_cvt_pk_bf16_f32 v210, v72, v73
v_mfma_f32_16x16x32_bf16 a[56:59], a[184:187], v[240:243], a[56:59]
v_exp_f32_e32 v74, v74
v_mfma_f32_16x16x32_bf16 a[56:59], a[188:191], v[244:247], a[56:59]
ds_read_b128 a[144:147], v8 offset:45056
v_add_f32_e32 v32, v32, v72
v_mfma_f32_16x16x32_bf16 a[60:63], a[184:187], v[248:251], a[60:63]
s_waitcnt lgkmcnt(3)
v_exp_f32_e32 v75, v75
v_mfma_f32_16x16x32_bf16 a[60:63], a[188:191], v[252:255], a[60:63]
v_cvt_pk_bf16_f32 v211, v74, v75
v_add_f32_e32 v32, v32, v73
v_mfma_f32_16x16x32_bf16 v[64:67], a[128:131], v[128:131], 0
v_exp_f32_e32 v76, v76
v_mfma_f32_16x16x32_bf16 v[64:67], a[132:135], v[132:135], v[64:67]
ds_read_b64_tr_b16 a[176:177], v6 offset:16384
v_add_f32_e32 v32, v32, v74
v_mfma_f32_16x16x32_bf16 v[64:67], a[136:139], v[136:139], v[64:67]
v_exp_f32_e32 v77, v77
v_mfma_f32_16x16x32_bf16 v[64:67], a[140:143], v[140:143], v[64:67]
ds_read_b64_tr_b16 a[178:179], v6 offset:20480
v_cvt_pk_bf16_f32 v218, v76, v77
v_mfma_f32_16x16x32_bf16 a[64:67], a[160:163], v[224:227], a[64:67]
v_exp_f32_e32 v78, v78
v_mfma_f32_16x16x32_bf16 a[64:67], a[164:167], v[228:231], a[64:67]
ds_read_b128 a[148:151], v9 offset:45056
v_add_f32_e32 v32, v32, v75
v_mfma_f32_16x16x32_bf16 a[68:71], a[160:163], v[232:235], a[68:71]
v_exp_f32_e32 v79, v79
v_mfma_f32_16x16x32_bf16 a[68:71], a[164:167], v[236:239], a[68:71]
v_mfma_f32_16x16x32_bf16 v[68:71], a[128:131], v[144:147], 0
s_waitcnt lgkmcnt(4)
v_exp_f32_e32 v64, v64
v_mfma_f32_16x16x32_bf16 v[68:71], a[132:135], v[148:151], v[68:71]
ds_read_b64_tr_b16 a[180:181], v6 offset:24576
v_cvt_pk_bf16_f32 v219, v78, v79
v_mfma_f32_16x16x32_bf16 v[68:71], a[136:139], v[152:155], v[68:71]
v_exp_f32_e32 v65, v65
v_mfma_f32_16x16x32_bf16 v[68:71], a[140:143], v[156:159], v[68:71]
ds_read_b64_tr_b16 a[182:183], v6 offset:28672
v_cvt_pk_bf16_f32 v196, v64, v65
v_mfma_f32_16x16x32_bf16 a[72:75], a[160:163], v[240:243], a[72:75]
v_exp_f32_e32 v66, v66
v_mfma_f32_16x16x32_bf16 a[72:75], a[164:167], v[244:247], a[72:75]
ds_read_b128 a[152:155], v10 offset:45056
v_add_f32_e32 v30, v30, v64
v_mfma_f32_16x16x32_bf16 a[76:79], a[160:163], v[248:251], a[76:79]
v_exp_f32_e32 v67, v67
v_mfma_f32_16x16x32_bf16 a[76:79], a[164:167], v[252:255], a[76:79]
v_cvt_pk_bf16_f32 v197, v66, v67
v_add_f32_e32 v30, v30, v65
v_mfma_f32_16x16x32_bf16 v[72:75], a[128:131], v[160:163], 0
v_exp_f32_e32 v68, v68
v_mfma_f32_16x16x32_bf16 v[72:75], a[132:135], v[164:167], v[72:75]
ds_read_b64_tr_b16 a[184:185], v7 offset:16384
v_add_f32_e32 v30, v30, v66
v_mfma_f32_16x16x32_bf16 v[72:75], a[136:139], v[168:171], v[72:75]
v_exp_f32_e32 v69, v69
v_mfma_f32_16x16x32_bf16 v[72:75], a[140:143], v[172:175], v[72:75]
ds_read_b64_tr_b16 a[186:187], v7 offset:20480
v_cvt_pk_bf16_f32 v204, v68, v69
v_mfma_f32_16x16x32_bf16 a[80:83], a[168:171], v[224:227], a[80:83]
v_exp_f32_e32 v70, v70
v_mfma_f32_16x16x32_bf16 a[80:83], a[172:175], v[228:231], a[80:83]
ds_read_b128 a[156:159], v11 offset:45056
v_add_f32_e32 v30, v30, v67
v_mfma_f32_16x16x32_bf16 a[84:87], a[168:171], v[232:235], a[84:87]
v_exp_f32_e32 v71, v71
v_mfma_f32_16x16x32_bf16 a[84:87], a[172:175], v[236:239], a[84:87]
v_mfma_f32_16x16x32_bf16 v[76:79], a[128:131], v[176:179], 0
s_waitcnt lgkmcnt(4)
v_exp_f32_e32 v72, v72
v_mfma_f32_16x16x32_bf16 v[76:79], a[132:135], v[180:183], v[76:79]
ds_read_b64_tr_b16 a[188:189], v7 offset:24576
v_cvt_pk_bf16_f32 v205, v70, v71
v_mfma_f32_16x16x32_bf16 v[76:79], a[136:139], v[184:187], v[76:79]
v_exp_f32_e32 v73, v73
v_mfma_f32_16x16x32_bf16 v[76:79], a[140:143], v[188:191], v[76:79]
ds_read_b64_tr_b16 a[190:191], v7 offset:28672
v_cvt_pk_bf16_f32 v212, v72, v73
v_mfma_f32_16x16x32_bf16 a[88:91], a[168:171], v[240:243], a[88:91]
v_exp_f32_e32 v74, v74
v_mfma_f32_16x16x32_bf16 a[88:91], a[172:175], v[244:247], a[88:91]
ds_read_b128 a[128:131], v8 offset:49152
v_add_f32_e32 v32, v32, v72
v_mfma_f32_16x16x32_bf16 a[92:95], a[168:171], v[248:251], a[92:95]
s_waitcnt lgkmcnt(3)
v_exp_f32_e32 v75, v75
v_mfma_f32_16x16x32_bf16 a[92:95], a[172:175], v[252:255], a[92:95]
v_cvt_pk_bf16_f32 v213, v74, v75
v_add_f32_e32 v32, v32, v73
v_mfma_f32_16x16x32_bf16 v[64:67], a[144:147], v[128:131], 0
v_exp_f32_e32 v76, v76
v_mfma_f32_16x16x32_bf16 v[64:67], a[148:151], v[132:135], v[64:67]
ds_read_b64_tr_b16 a[160:161], v0 offset:32768
v_add_f32_e32 v32, v32, v74
v_mfma_f32_16x16x32_bf16 v[64:67], a[152:155], v[136:139], v[64:67]
v_exp_f32_e32 v77, v77
v_mfma_f32_16x16x32_bf16 v[64:67], a[156:159], v[140:143], v[64:67]
ds_read_b64_tr_b16 a[162:163], v0 offset:36864
v_cvt_pk_bf16_f32 v220, v76, v77
v_mfma_f32_16x16x32_bf16 a[96:99], a[176:179], v[224:227], a[96:99]
v_exp_f32_e32 v78, v78
v_mfma_f32_16x16x32_bf16 a[96:99], a[180:183], v[228:231], a[96:99]
ds_read_b128 a[132:135], v9 offset:49152
v_add_f32_e32 v32, v32, v75
v_mfma_f32_16x16x32_bf16 a[100:103], a[176:179], v[232:235], a[100:103]
v_exp_f32_e32 v79, v79
v_mfma_f32_16x16x32_bf16 a[100:103], a[180:183], v[236:239], a[100:103]
v_mfma_f32_16x16x32_bf16 v[68:71], a[144:147], v[144:147], 0
s_waitcnt lgkmcnt(4)
v_exp_f32_e32 v64, v64
v_mfma_f32_16x16x32_bf16 v[68:71], a[148:151], v[148:151], v[68:71]
ds_read_b64_tr_b16 a[164:165], v0 offset:40960
v_cvt_pk_bf16_f32 v221, v78, v79
v_mfma_f32_16x16x32_bf16 v[68:71], a[152:155], v[152:155], v[68:71]
s_waitcnt vmcnt(9)
v_exp_f32_e32 v65, v65
v_mfma_f32_16x16x32_bf16 v[68:71], a[156:159], v[156:159], v[68:71]
ds_read_b64_tr_b16 a[166:167], v0 offset:45056
v_cvt_pk_bf16_f32 v198, v64, v65
v_mfma_f32_16x16x32_bf16 a[104:107], a[176:179], v[240:243], a[104:107]
s_barrier
v_exp_f32_e32 v66, v66
v_mfma_f32_16x16x32_bf16 a[104:107], a[180:183], v[244:247], a[104:107]
ds_read_b128 a[136:139], v10 offset:49152
v_add_f32_e32 v30, v30, v64
v_mfma_f32_16x16x32_bf16 a[108:111], a[176:179], v[248:251], a[108:111]
s_add_u32 m0, s68, 0x14000
v_exp_f32_e32 v67, v67
v_mfma_f32_16x16x32_bf16 a[108:111], a[180:183], v[252:255], a[108:111]
v_cvt_pk_bf16_f32 v199, v66, v67
v_add_f32_e32 v30, v30, v65
v_mfma_f32_16x16x32_bf16 v[38:41], v[34:37], v[200:203], v[38:41]
buffer_load_dwordx4 v24, s[40:43], s77 offen offset:0 nt lds
v_mfma_f32_16x16x32_bf16 v[72:75], a[144:147], v[160:163], 0
v_exp_f32_e32 v68, v68
v_mfma_f32_16x16x32_bf16 v[72:75], a[148:151], v[164:167], v[72:75]
ds_read_b64_tr_b16 a[168:169], v1 offset:32768
v_add_f32_e32 v30, v30, v66
v_mfma_f32_16x16x32_bf16 v[72:75], a[152:155], v[168:171], v[72:75]
v_exp_f32_e32 v69, v69
v_mfma_f32_16x16x32_bf16 v[72:75], a[156:159], v[172:175], v[72:75]
ds_read_b64_tr_b16 a[170:171], v1 offset:36864
v_cvt_pk_bf16_f32 v206, v68, v69
v_mfma_f32_16x16x32_bf16 a[112:115], a[184:187], v[224:227], a[112:115]
v_exp_f32_e32 v70, v70
v_mfma_f32_16x16x32_bf16 a[112:115], a[188:191], v[228:231], a[112:115]
ds_read_b128 a[140:143], v11 offset:49152
v_add_f32_e32 v30, v30, v67
v_mfma_f32_16x16x32_bf16 a[116:119], a[184:187], v[232:235], a[116:119]
v_exp_f32_e32 v71, v71
v_mfma_f32_16x16x32_bf16 a[116:119], a[188:191], v[236:239], a[116:119]
buffer_load_dwordx4 v25, s[40:43], s77 offen offset:1024 nt lds
v_mfma_f32_16x16x32_bf16 v[76:79], a[144:147], v[176:179], 0
v_exp_f32_e32 v72, v72
v_mfma_f32_16x16x32_bf16 v[76:79], a[148:151], v[180:183], v[76:79]
ds_read_b64_tr_b16 a[172:173], v1 offset:40960
v_cvt_pk_bf16_f32 v207, v70, v71
v_mfma_f32_16x16x32_bf16 v[76:79], a[152:155], v[184:187], v[76:79]
s_waitcnt lgkmcnt(5)
v_exp_f32_e32 v73, v73
v_mfma_f32_16x16x32_bf16 v[76:79], a[156:159], v[188:191], v[76:79]
ds_read_b64_tr_b16 a[174:175], v1 offset:45056
v_cvt_pk_bf16_f32 v214, v72, v73
v_mfma_f32_16x16x32_bf16 a[120:123], a[184:187], v[240:243], a[120:123]
v_exp_f32_e32 v74, v74
v_mfma_f32_16x16x32_bf16 a[120:123], a[188:191], v[244:247], a[120:123]
ds_read_b128 a[144:147], v8 offset:53248
v_add_f32_e32 v32, v32, v72
v_mfma_f32_16x16x32_bf16 a[124:127], a[184:187], v[248:251], a[124:127]
s_waitcnt lgkmcnt(3)
v_exp_f32_e32 v75, v75
v_mfma_f32_16x16x32_bf16 a[124:127], a[188:191], v[252:255], a[124:127]
v_cvt_pk_bf16_f32 v215, v74, v75
v_add_f32_e32 v32, v32, v73
v_mfma_f32_16x16x32_bf16 v[42:45], v[34:37], v[216:219], v[42:45]
buffer_load_dwordx4 v24, s[40:43], s77 offen offset:2048 nt lds
v_mfma_f32_16x16x32_bf16 v[64:67], a[128:131], v[128:131], 0
v_exp_f32_e32 v76, v76
v_mfma_f32_16x16x32_bf16 v[64:67], a[132:135], v[132:135], v[64:67]
ds_read_b64_tr_b16 a[176:177], v2 offset:32768
v_add_f32_e32 v32, v32, v74
v_mfma_f32_16x16x32_bf16 v[64:67], a[136:139], v[136:139], v[64:67]
v_exp_f32_e32 v77, v77
v_mfma_f32_16x16x32_bf16 v[64:67], a[140:143], v[140:143], v[64:67]
ds_read_b64_tr_b16 a[178:179], v2 offset:36864
v_cvt_pk_bf16_f32 v222, v76, v77
v_mfma_f32_16x16x32_bf16 a[0:3], a[160:163], v[192:195], a[0:3]
v_exp_f32_e32 v78, v78
v_mfma_f32_16x16x32_bf16 a[0:3], a[164:167], v[196:199], a[0:3]
ds_read_b128 a[148:151], v9 offset:53248
v_add_f32_e32 v32, v32, v75
v_mfma_f32_16x16x32_bf16 a[4:7], a[160:163], v[200:203], a[4:7]
v_exp_f32_e32 v79, v79
v_mfma_f32_16x16x32_bf16 a[4:7], a[164:167], v[204:207], a[4:7]
buffer_load_dwordx4 v25, s[40:43], s77 offen offset:3072 nt lds
v_mfma_f32_16x16x32_bf16 v[68:71], a[128:131], v[144:147], 0
s_waitcnt lgkmcnt(4)
v_exp_f32_e32 v64, v64
v_mfma_f32_16x16x32_bf16 v[68:71], a[132:135], v[148:151], v[68:71]
ds_read_b64_tr_b16 a[180:181], v2 offset:40960
v_cvt_pk_bf16_f32 v223, v78, v79
v_mfma_f32_16x16x32_bf16 v[68:71], a[136:139], v[152:155], v[68:71]
s_add_u32 m0, s68, 0x8000
v_exp_f32_e32 v65, v65
v_mfma_f32_16x16x32_bf16 v[68:71], a[140:143], v[156:159], v[68:71]
ds_read_b64_tr_b16 a[182:183], v2 offset:45056
v_cvt_pk_bf16_f32 v224, v64, v65
v_mfma_f32_16x16x32_bf16 a[8:11], a[160:163], v[208:211], a[8:11]
s_add_u32 s77, s77, 0x4000
v_exp_f32_e32 v66, v66
v_mfma_f32_16x16x32_bf16 a[8:11], a[164:167], v[212:215], a[8:11]
ds_read_b128 a[152:155], v10 offset:53248
v_add_f32_e32 v30, v30, v64
v_mfma_f32_16x16x32_bf16 a[12:15], a[160:163], v[216:219], a[12:15]
v_exp_f32_e32 v67, v67
v_mfma_f32_16x16x32_bf16 a[12:15], a[164:167], v[220:223], a[12:15]
v_cvt_pk_bf16_f32 v225, v66, v67
v_add_f32_e32 v30, v30, v65
v_mfma_f32_16x16x32_bf16 v[38:41], v[34:37], v[204:207], v[38:41]
buffer_load_dwordx4 v20, s[36:39], s76 offen offset:0 nt lds
v_mfma_f32_16x16x32_bf16 v[72:75], a[128:131], v[160:163], 0
v_exp_f32_e32 v68, v68
v_mfma_f32_16x16x32_bf16 v[72:75], a[132:135], v[164:167], v[72:75]
ds_read_b64_tr_b16 a[184:185], v3 offset:32768
v_add_f32_e32 v30, v30, v66
v_mfma_f32_16x16x32_bf16 v[72:75], a[136:139], v[168:171], v[72:75]
v_exp_f32_e32 v69, v69
v_mfma_f32_16x16x32_bf16 v[72:75], a[140:143], v[172:175], v[72:75]
ds_read_b64_tr_b16 a[186:187], v3 offset:36864
v_cvt_pk_bf16_f32 v232, v68, v69
v_mfma_f32_16x16x32_bf16 a[16:19], a[168:171], v[192:195], a[16:19]
v_exp_f32_e32 v70, v70
v_mfma_f32_16x16x32_bf16 a[16:19], a[172:175], v[196:199], a[16:19]
ds_read_b128 a[156:159], v11 offset:53248
v_add_f32_e32 v30, v30, v67
v_mfma_f32_16x16x32_bf16 a[20:23], a[168:171], v[200:203], a[20:23]
v_exp_f32_e32 v71, v71
v_mfma_f32_16x16x32_bf16 a[20:23], a[172:175], v[204:207], a[20:23]
buffer_load_dwordx4 v21, s[36:39], s76 offen offset:1024 nt lds
v_mfma_f32_16x16x32_bf16 v[76:79], a[128:131], v[176:179], 0
s_waitcnt lgkmcnt(4)
v_exp_f32_e32 v72, v72
v_mfma_f32_16x16x32_bf16 v[76:79], a[132:135], v[180:183], v[76:79]
ds_read_b64_tr_b16 a[188:189], v3 offset:40960
v_cvt_pk_bf16_f32 v233, v70, v71
v_mfma_f32_16x16x32_bf16 v[76:79], a[136:139], v[184:187], v[76:79]
v_exp_f32_e32 v73, v73
v_mfma_f32_16x16x32_bf16 v[76:79], a[140:143], v[188:191], v[76:79]
ds_read_b64_tr_b16 a[190:191], v3 offset:45056
v_cvt_pk_bf16_f32 v240, v72, v73
v_mfma_f32_16x16x32_bf16 a[24:27], a[168:171], v[208:211], a[24:27]
v_exp_f32_e32 v74, v74
v_mfma_f32_16x16x32_bf16 a[24:27], a[172:175], v[212:215], a[24:27]
ds_read_b128 a[128:131], v8 offset:57344
v_add_f32_e32 v32, v32, v72
v_mfma_f32_16x16x32_bf16 a[28:31], a[168:171], v[216:219], a[28:31]
s_waitcnt lgkmcnt(3)
v_exp_f32_e32 v75, v75
v_mfma_f32_16x16x32_bf16 a[28:31], a[172:175], v[220:223], a[28:31]
v_cvt_pk_bf16_f32 v241, v74, v75
v_add_f32_e32 v32, v32, v73
v_mfma_f32_16x16x32_bf16 v[42:45], v[34:37], v[220:223], v[42:45]
buffer_load_dwordx4 v22, s[36:39], s76 offen offset:2048 nt lds
v_mfma_f32_16x16x32_bf16 v[64:67], a[144:147], v[128:131], 0
v_exp_f32_e32 v76, v76
v_mfma_f32_16x16x32_bf16 v[64:67], a[148:151], v[132:135], v[64:67]
ds_read_b64_tr_b16 a[160:161], v4 offset:32768
v_add_f32_e32 v32, v32, v74
v_mfma_f32_16x16x32_bf16 v[64:67], a[152:155], v[136:139], v[64:67]
v_exp_f32_e32 v77, v77
v_mfma_f32_16x16x32_bf16 v[64:67], a[156:159], v[140:143], v[64:67]
ds_read_b64_tr_b16 a[162:163], v4 offset:36864
v_cvt_pk_bf16_f32 v248, v76, v77
v_mfma_f32_16x16x32_bf16 a[32:35], a[176:179], v[192:195], a[32:35]
v_exp_f32_e32 v78, v78
v_mfma_f32_16x16x32_bf16 a[32:35], a[180:183], v[196:199], a[32:35]
ds_read_b128 a[132:135], v9 offset:57344
v_add_f32_e32 v32, v32, v75
v_mfma_f32_16x16x32_bf16 a[36:39], a[176:179], v[200:203], a[36:39]
v_exp_f32_e32 v79, v79
v_mfma_f32_16x16x32_bf16 a[36:39], a[180:183], v[204:207], a[36:39]
buffer_load_dwordx4 v23, s[36:39], s76 offen offset:3072 nt lds
v_mfma_f32_16x16x32_bf16 v[68:71], a[144:147], v[144:147], 0
s_waitcnt lgkmcnt(4)
v_exp_f32_e32 v64, v64
v_mfma_f32_16x16x32_bf16 v[68:71], a[148:151], v[148:151], v[68:71]
ds_read_b64_tr_b16 a[164:165], v4 offset:40960
v_cvt_pk_bf16_f32 v249, v78, v79
v_mfma_f32_16x16x32_bf16 v[68:71], a[152:155], v[152:155], v[68:71]
s_add_u32 s76, s76, 0x4000
v_exp_f32_e32 v65, v65
v_mfma_f32_16x16x32_bf16 v[68:71], a[156:159], v[156:159], v[68:71]
ds_read_b64_tr_b16 a[166:167], v4 offset:45056
v_cvt_pk_bf16_f32 v226, v64, v65
v_mfma_f32_16x16x32_bf16 a[40:43], a[176:179], v[208:211], a[40:43]
v_exp_f32_e32 v66, v66
v_mfma_f32_16x16x32_bf16 a[40:43], a[180:183], v[212:215], a[40:43]
ds_read_b128 a[136:139], v10 offset:57344
v_add_f32_e32 v30, v30, v64
v_mfma_f32_16x16x32_bf16 a[44:47], a[176:179], v[216:219], a[44:47]
v_exp_f32_e32 v67, v67
v_mfma_f32_16x16x32_bf16 a[44:47], a[180:183], v[220:223], a[44:47]
v_cvt_pk_bf16_f32 v227, v66, v67
v_add_f32_e32 v30, v30, v65
v_mfma_f32_16x16x32_bf16 v[72:75], a[144:147], v[160:163], 0
v_exp_f32_e32 v68, v68
v_mfma_f32_16x16x32_bf16 v[72:75], a[148:151], v[164:167], v[72:75]
ds_read_b64_tr_b16 a[168:169], v5 offset:32768
v_add_f32_e32 v30, v30, v66
v_mfma_f32_16x16x32_bf16 v[72:75], a[152:155], v[168:171], v[72:75]
v_exp_f32_e32 v69, v69
v_mfma_f32_16x16x32_bf16 v[72:75], a[156:159], v[172:175], v[72:75]
ds_read_b64_tr_b16 a[170:171], v5 offset:36864
v_cvt_pk_bf16_f32 v234, v68, v69
v_mfma_f32_16x16x32_bf16 a[48:51], a[184:187], v[192:195], a[48:51]
v_exp_f32_e32 v70, v70
v_mfma_f32_16x16x32_bf16 a[48:51], a[188:191], v[196:199], a[48:51]
ds_read_b128 a[140:143], v11 offset:57344
v_add_f32_e32 v30, v30, v67
v_mfma_f32_16x16x32_bf16 a[52:55], a[184:187], v[200:203], a[52:55]
v_exp_f32_e32 v71, v71
v_mfma_f32_16x16x32_bf16 a[52:55], a[188:191], v[204:207], a[52:55]
buffer_store_dwordx4 v[60:63], v57, s[24:27], s81 offen offset:0 sc1
v_mfma_f32_16x16x32_bf16 v[76:79], a[144:147], v[176:179], 0
s_waitcnt lgkmcnt(4)
v_exp_f32_e32 v72, v72
v_mfma_f32_16x16x32_bf16 v[76:79], a[148:151], v[180:183], v[76:79]
ds_read_b64_tr_b16 a[172:173], v5 offset:40960
v_cvt_pk_bf16_f32 v235, v70, v71
v_mfma_f32_16x16x32_bf16 v[76:79], a[152:155], v[184:187], v[76:79]
v_exp_f32_e32 v73, v73
v_mfma_f32_16x16x32_bf16 v[76:79], a[156:159], v[188:191], v[76:79]
ds_read_b64_tr_b16 a[174:175], v5 offset:45056
v_cvt_pk_bf16_f32 v242, v72, v73
v_mfma_f32_16x16x32_bf16 a[56:59], a[184:187], v[208:211], a[56:59]
v_exp_f32_e32 v74, v74
v_mfma_f32_16x16x32_bf16 a[56:59], a[188:191], v[212:215], a[56:59]
ds_read_b128 a[144:147], v8 offset:61440
v_add_f32_e32 v32, v32, v72
v_mfma_f32_16x16x32_bf16 a[60:63], a[184:187], v[216:219], a[60:63]
s_waitcnt lgkmcnt(3)
v_exp_f32_e32 v75, v75
v_mfma_f32_16x16x32_bf16 a[60:63], a[188:191], v[220:223], a[60:63]
v_cvt_pk_bf16_f32 v243, v74, v75
v_add_f32_e32 v32, v32, v73
v_mfma_f32_16x16x32_bf16 v[64:67], a[128:131], v[128:131], 0
v_exp_f32_e32 v76, v76
v_mfma_f32_16x16x32_bf16 v[64:67], a[132:135], v[132:135], v[64:67]
ds_read_b64_tr_b16 a[176:177], v6 offset:32768
v_add_f32_e32 v32, v32, v74
v_mfma_f32_16x16x32_bf16 v[64:67], a[136:139], v[136:139], v[64:67]
v_exp_f32_e32 v77, v77
v_mfma_f32_16x16x32_bf16 v[64:67], a[140:143], v[140:143], v[64:67]
ds_read_b64_tr_b16 a[178:179], v6 offset:36864
v_cvt_pk_bf16_f32 v250, v76, v77
v_mfma_f32_16x16x32_bf16 a[64:67], a[160:163], v[192:195], a[64:67]
v_exp_f32_e32 v78, v78
v_mfma_f32_16x16x32_bf16 a[64:67], a[164:167], v[196:199], a[64:67]
ds_read_b128 a[148:151], v9 offset:61440
v_add_f32_e32 v32, v32, v75
v_mfma_f32_16x16x32_bf16 a[68:71], a[160:163], v[200:203], a[68:71]
v_exp_f32_e32 v79, v79
v_mfma_f32_16x16x32_bf16 a[68:71], a[164:167], v[204:207], a[68:71]
v_mfma_f32_16x16x32_bf16 v[68:71], a[128:131], v[144:147], 0
s_waitcnt lgkmcnt(4)
v_exp_f32_e32 v64, v64
v_mfma_f32_16x16x32_bf16 v[68:71], a[132:135], v[148:151], v[68:71]
ds_read_b64_tr_b16 a[180:181], v6 offset:40960
v_cvt_pk_bf16_f32 v251, v78, v79
v_mfma_f32_16x16x32_bf16 v[68:71], a[136:139], v[152:155], v[68:71]
v_exp_f32_e32 v65, v65
v_mfma_f32_16x16x32_bf16 v[68:71], a[140:143], v[156:159], v[68:71]
ds_read_b64_tr_b16 a[182:183], v6 offset:45056
v_cvt_pk_bf16_f32 v228, v64, v65
v_mfma_f32_16x16x32_bf16 a[72:75], a[160:163], v[208:211], a[72:75]
v_exp_f32_e32 v66, v66
v_mfma_f32_16x16x32_bf16 a[72:75], a[164:167], v[212:215], a[72:75]
ds_read_b128 a[152:155], v10 offset:61440
v_add_f32_e32 v30, v30, v64
v_mfma_f32_16x16x32_bf16 a[76:79], a[160:163], v[216:219], a[76:79]
v_exp_f32_e32 v67, v67
v_mfma_f32_16x16x32_bf16 a[76:79], a[164:167], v[220:223], a[76:79]
v_cvt_pk_bf16_f32 v229, v66, v67
v_add_f32_e32 v30, v30, v65
v_mfma_f32_16x16x32_bf16 v[72:75], a[128:131], v[160:163], 0
v_exp_f32_e32 v68, v68
v_mfma_f32_16x16x32_bf16 v[72:75], a[132:135], v[164:167], v[72:75]
ds_read_b64_tr_b16 a[184:185], v7 offset:32768
v_add_f32_e32 v30, v30, v66
v_mfma_f32_16x16x32_bf16 v[72:75], a[136:139], v[168:171], v[72:75]
v_exp_f32_e32 v69, v69
v_mfma_f32_16x16x32_bf16 v[72:75], a[140:143], v[172:175], v[72:75]
ds_read_b64_tr_b16 a[186:187], v7 offset:36864
v_cvt_pk_bf16_f32 v236, v68, v69
v_mfma_f32_16x16x32_bf16 a[80:83], a[168:171], v[192:195], a[80:83]
v_exp_f32_e32 v70, v70
v_mfma_f32_16x16x32_bf16 a[80:83], a[172:175], v[196:199], a[80:83]
ds_read_b128 a[156:159], v11 offset:61440
v_add_f32_e32 v30, v30, v67
v_mfma_f32_16x16x32_bf16 a[84:87], a[168:171], v[200:203], a[84:87]
v_exp_f32_e32 v71, v71
v_mfma_f32_16x16x32_bf16 a[84:87], a[172:175], v[204:207], a[84:87]
v_mfma_f32_16x16x32_bf16 v[76:79], a[128:131], v[176:179], 0
s_waitcnt lgkmcnt(4)
v_exp_f32_e32 v72, v72
v_mfma_f32_16x16x32_bf16 v[76:79], a[132:135], v[180:183], v[76:79]
ds_read_b64_tr_b16 a[188:189], v7 offset:40960
v_cvt_pk_bf16_f32 v237, v70, v71
v_mfma_f32_16x16x32_bf16 v[76:79], a[136:139], v[184:187], v[76:79]
v_exp_f32_e32 v73, v73
v_mfma_f32_16x16x32_bf16 v[76:79], a[140:143], v[188:191], v[76:79]
ds_read_b64_tr_b16 a[190:191], v7 offset:45056
v_cvt_pk_bf16_f32 v244, v72, v73
v_mfma_f32_16x16x32_bf16 a[88:91], a[168:171], v[208:211], a[88:91]
v_exp_f32_e32 v74, v74
v_mfma_f32_16x16x32_bf16 a[88:91], a[172:175], v[212:215], a[88:91]
ds_read_b128 a[128:131], v8 offset:0
v_add_f32_e32 v32, v32, v72
v_mfma_f32_16x16x32_bf16 a[92:95], a[168:171], v[216:219], a[92:95]
s_waitcnt lgkmcnt(3)
v_exp_f32_e32 v75, v75
v_mfma_f32_16x16x32_bf16 a[92:95], a[172:175], v[220:223], a[92:95]
v_cvt_pk_bf16_f32 v245, v74, v75
v_add_f32_e32 v32, v32, v73
v_mfma_f32_16x16x32_bf16 v[64:67], a[144:147], v[128:131], 0
v_exp_f32_e32 v76, v76
v_mfma_f32_16x16x32_bf16 v[64:67], a[148:151], v[132:135], v[64:67]
ds_read_b64_tr_b16 a[160:161], v0 offset:49152
v_add_f32_e32 v32, v32, v74
v_mfma_f32_16x16x32_bf16 v[64:67], a[152:155], v[136:139], v[64:67]
v_exp_f32_e32 v77, v77
v_mfma_f32_16x16x32_bf16 v[64:67], a[156:159], v[140:143], v[64:67]
ds_read_b64_tr_b16 a[162:163], v0 offset:53248
v_cvt_pk_bf16_f32 v252, v76, v77
v_mfma_f32_16x16x32_bf16 a[96:99], a[176:179], v[192:195], a[96:99]
v_exp_f32_e32 v78, v78
v_mfma_f32_16x16x32_bf16 a[96:99], a[180:183], v[196:199], a[96:99]
ds_read_b128 a[132:135], v9 offset:0
v_add_f32_e32 v32, v32, v75
v_mfma_f32_16x16x32_bf16 a[100:103], a[176:179], v[200:203], a[100:103]
v_exp_f32_e32 v79, v79
v_mfma_f32_16x16x32_bf16 a[100:103], a[180:183], v[204:207], a[100:103]
v_mfma_f32_16x16x32_bf16 v[68:71], a[144:147], v[144:147], 0
s_waitcnt lgkmcnt(4)
v_exp_f32_e32 v64, v64
v_mfma_f32_16x16x32_bf16 v[68:71], a[148:151], v[148:151], v[68:71]
ds_read_b64_tr_b16 a[164:165], v0 offset:57344
v_cvt_pk_bf16_f32 v253, v78, v79
v_mfma_f32_16x16x32_bf16 v[68:71], a[152:155], v[152:155], v[68:71]
s_waitcnt vmcnt(9)
v_exp_f32_e32 v65, v65
v_mfma_f32_16x16x32_bf16 v[68:71], a[156:159], v[156:159], v[68:71]
ds_read_b64_tr_b16 a[166:167], v0 offset:61440
v_cvt_pk_bf16_f32 v230, v64, v65
v_mfma_f32_16x16x32_bf16 a[104:107], a[176:179], v[208:211], a[104:107]
s_barrier
v_exp_f32_e32 v66, v66
v_mfma_f32_16x16x32_bf16 a[104:107], a[180:183], v[212:215], a[104:107]
ds_read_b128 a[136:139], v10 offset:0
v_add_f32_e32 v30, v30, v64
v_mfma_f32_16x16x32_bf16 a[108:111], a[176:179], v[216:219], a[108:111]
s_add_u32 m0, s68, 0x18000
v_exp_f32_e32 v67, v67
v_mfma_f32_16x16x32_bf16 a[108:111], a[180:183], v[220:223], a[108:111]
v_cvt_pk_bf16_f32 v231, v66, v67
v_add_f32_e32 v30, v30, v65
v_mfma_f32_16x16x32_bf16 v[38:41], v[34:37], v[232:235], v[38:41]
buffer_load_dwordx4 v24, s[40:43], s77 offen offset:0 nt lds
v_mfma_f32_16x16x32_bf16 v[72:75], a[144:147], v[160:163], 0
v_exp_f32_e32 v68, v68
v_mfma_f32_16x16x32_bf16 v[72:75], a[148:151], v[164:167], v[72:75]
ds_read_b64_tr_b16 a[168:169], v1 offset:49152
v_add_f32_e32 v30, v30, v66
v_mfma_f32_16x16x32_bf16 v[72:75], a[152:155], v[168:171], v[72:75]
v_exp_f32_e32 v69, v69
v_mfma_f32_16x16x32_bf16 v[72:75], a[156:159], v[172:175], v[72:75]
ds_read_b64_tr_b16 a[170:171], v1 offset:53248
v_cvt_pk_bf16_f32 v238, v68, v69
v_mfma_f32_16x16x32_bf16 a[112:115], a[184:187], v[192:195], a[112:115]
v_exp_f32_e32 v70, v70
v_mfma_f32_16x16x32_bf16 a[112:115], a[188:191], v[196:199], a[112:115]
ds_read_b128 a[140:143], v11 offset:0
v_add_f32_e32 v30, v30, v67
v_mfma_f32_16x16x32_bf16 a[116:119], a[184:187], v[200:203], a[116:119]
v_exp_f32_e32 v71, v71
v_mfma_f32_16x16x32_bf16 a[116:119], a[188:191], v[204:207], a[116:119]
buffer_load_dwordx4 v25, s[40:43], s77 offen offset:1024 nt lds
v_mfma_f32_16x16x32_bf16 v[76:79], a[144:147], v[176:179], 0
v_exp_f32_e32 v72, v72
v_mfma_f32_16x16x32_bf16 v[76:79], a[148:151], v[180:183], v[76:79]
ds_read_b64_tr_b16 a[172:173], v1 offset:57344
v_cvt_pk_bf16_f32 v239, v70, v71
v_mfma_f32_16x16x32_bf16 v[76:79], a[152:155], v[184:187], v[76:79]
s_waitcnt lgkmcnt(5)
v_exp_f32_e32 v73, v73
v_mfma_f32_16x16x32_bf16 v[76:79], a[156:159], v[188:191], v[76:79]
ds_read_b64_tr_b16 a[174:175], v1 offset:61440
v_cvt_pk_bf16_f32 v246, v72, v73
v_mfma_f32_16x16x32_bf16 a[120:123], a[184:187], v[208:211], a[120:123]
v_exp_f32_e32 v74, v74
v_mfma_f32_16x16x32_bf16 a[120:123], a[188:191], v[212:215], a[120:123]
ds_read_b128 a[144:147], v8 offset:4096
v_add_f32_e32 v32, v32, v72
v_mfma_f32_16x16x32_bf16 a[124:127], a[184:187], v[216:219], a[124:127]
s_waitcnt lgkmcnt(3)
v_exp_f32_e32 v75, v75
v_mfma_f32_16x16x32_bf16 a[124:127], a[188:191], v[220:223], a[124:127]
v_cvt_pk_bf16_f32 v247, v74, v75
v_add_f32_e32 v32, v32, v73
v_mfma_f32_16x16x32_bf16 v[42:45], v[34:37], v[248:251], v[42:45]
buffer_load_dwordx4 v24, s[40:43], s77 offen offset:2048 nt lds
v_mfma_f32_16x16x32_bf16 v[64:67], a[128:131], v[128:131], 0
v_exp_f32_e32 v76, v76
v_mfma_f32_16x16x32_bf16 v[64:67], a[132:135], v[132:135], v[64:67]
ds_read_b64_tr_b16 a[176:177], v2 offset:49152
v_add_f32_e32 v32, v32, v74
v_mfma_f32_16x16x32_bf16 v[64:67], a[136:139], v[136:139], v[64:67]
v_exp_f32_e32 v77, v77
v_mfma_f32_16x16x32_bf16 v[64:67], a[140:143], v[140:143], v[64:67]
ds_read_b64_tr_b16 a[178:179], v2 offset:53248
v_cvt_pk_bf16_f32 v254, v76, v77
v_mfma_f32_16x16x32_bf16 a[0:3], a[160:163], v[224:227], a[0:3]
v_exp_f32_e32 v78, v78
v_mfma_f32_16x16x32_bf16 a[0:3], a[164:167], v[228:231], a[0:3]
ds_read_b128 a[148:151], v9 offset:4096
v_add_f32_e32 v32, v32, v75
v_mfma_f32_16x16x32_bf16 a[4:7], a[160:163], v[232:235], a[4:7]
v_exp_f32_e32 v79, v79
v_mfma_f32_16x16x32_bf16 a[4:7], a[164:167], v[236:239], a[4:7]
buffer_load_dwordx4 v25, s[40:43], s77 offen offset:3072 nt lds
v_mfma_f32_16x16x32_bf16 v[68:71], a[128:131], v[144:147], 0
s_waitcnt lgkmcnt(4)
v_exp_f32_e32 v64, v64
v_mfma_f32_16x16x32_bf16 v[68:71], a[132:135], v[148:151], v[68:71]
ds_read_b64_tr_b16 a[180:181], v2 offset:57344
v_cvt_pk_bf16_f32 v255, v78, v79
v_mfma_f32_16x16x32_bf16 v[68:71], a[136:139], v[152:155], v[68:71]
s_add_u32 m0, s68, 0xc000
v_exp_f32_e32 v65, v65
v_mfma_f32_16x16x32_bf16 v[68:71], a[140:143], v[156:159], v[68:71]
ds_read_b64_tr_b16 a[182:183], v2 offset:61440
v_cvt_pk_bf16_f32 v192, v64, v65
v_mfma_f32_16x16x32_bf16 a[8:11], a[160:163], v[240:243], a[8:11]
s_add_u32 s77, s77, 0x4000
v_exp_f32_e32 v66, v66
v_mfma_f32_16x16x32_bf16 a[8:11], a[164:167], v[244:247], a[8:11]
ds_read_b128 a[152:155], v10 offset:4096
v_add_f32_e32 v30, v30, v64
v_mfma_f32_16x16x32_bf16 a[12:15], a[160:163], v[248:251], a[12:15]
v_exp_f32_e32 v67, v67
v_mfma_f32_16x16x32_bf16 a[12:15], a[164:167], v[252:255], a[12:15]
v_cvt_pk_bf16_f32 v193, v66, v67
v_add_f32_e32 v30, v30, v65
v_mfma_f32_16x16x32_bf16 v[38:41], v[34:37], v[236:239], v[38:41]
buffer_load_dwordx4 v20, s[36:39], s76 offen offset:0 nt lds
v_mfma_f32_16x16x32_bf16 v[72:75], a[128:131], v[160:163], 0
s_sub_u32 s78, s78, 1
v_exp_f32_e32 v68, v68
v_mfma_f32_16x16x32_bf16 v[72:75], a[132:135], v[164:167], v[72:75]
ds_read_b64_tr_b16 a[184:185], v3 offset:49152
v_add_f32_e32 v30, v30, v66
v_mfma_f32_16x16x32_bf16 v[72:75], a[136:139], v[168:171], v[72:75]
v_exp_f32_e32 v69, v69
v_mfma_f32_16x16x32_bf16 v[72:75], a[140:143], v[172:175], v[72:75]
ds_read_b64_tr_b16 a[186:187], v3 offset:53248
v_cvt_pk_bf16_f32 v200, v68, v69
v_mfma_f32_16x16x32_bf16 a[16:19], a[168:171], v[224:227], a[16:19]
v_exp_f32_e32 v70, v70
v_mfma_f32_16x16x32_bf16 a[16:19], a[172:175], v[228:231], a[16:19]
ds_read_b128 a[156:159], v11 offset:4096
v_add_f32_e32 v30, v30, v67
v_mfma_f32_16x16x32_bf16 a[20:23], a[168:171], v[232:235], a[20:23]
v_exp_f32_e32 v71, v71
v_mfma_f32_16x16x32_bf16 a[20:23], a[172:175], v[236:239], a[20:23]
buffer_load_dwordx4 v21, s[36:39], s76 offen offset:1024 nt lds
v_mfma_f32_16x16x32_bf16 v[76:79], a[128:131], v[176:179], 0
s_waitcnt lgkmcnt(4)
v_exp_f32_e32 v72, v72
v_mfma_f32_16x16x32_bf16 v[76:79], a[132:135], v[180:183], v[76:79]
ds_read_b64_tr_b16 a[188:189], v3 offset:57344
v_cvt_pk_bf16_f32 v201, v70, v71
v_mfma_f32_16x16x32_bf16 v[76:79], a[136:139], v[184:187], v[76:79]
v_exp_f32_e32 v73, v73
v_mfma_f32_16x16x32_bf16 v[76:79], a[140:143], v[188:191], v[76:79]
ds_read_b64_tr_b16 a[190:191], v3 offset:61440
v_cvt_pk_bf16_f32 v208, v72, v73
v_mfma_f32_16x16x32_bf16 a[24:27], a[168:171], v[240:243], a[24:27]
v_exp_f32_e32 v74, v74
v_mfma_f32_16x16x32_bf16 a[24:27], a[172:175], v[244:247], a[24:27]
ds_read_b128 a[128:131], v8 offset:8192
v_add_f32_e32 v32, v32, v72
v_mfma_f32_16x16x32_bf16 a[28:31], a[168:171], v[248:251], a[28:31]
s_waitcnt lgkmcnt(3)
v_exp_f32_e32 v75, v75
v_mfma_f32_16x16x32_bf16 a[28:31], a[172:175], v[252:255], a[28:31]
v_cvt_pk_bf16_f32 v209, v74, v75
v_add_f32_e32 v32, v32, v73
v_mfma_f32_16x16x32_bf16 v[42:45], v[34:37], v[252:255], v[42:45]
buffer_load_dwordx4 v22, s[36:39], s76 offen offset:2048 nt lds
v_mfma_f32_16x16x32_bf16 v[64:67], a[144:147], v[128:131], 0
v_exp_f32_e32 v76, v76
v_mfma_f32_16x16x32_bf16 v[64:67], a[148:151], v[132:135], v[64:67]
ds_read_b64_tr_b16 a[160:161], v4 offset:49152
v_add_f32_e32 v32, v32, v74
v_mfma_f32_16x16x32_bf16 v[64:67], a[152:155], v[136:139], v[64:67]
v_exp_f32_e32 v77, v77
v_mfma_f32_16x16x32_bf16 v[64:67], a[156:159], v[140:143], v[64:67]
ds_read_b64_tr_b16 a[162:163], v4 offset:53248
v_cvt_pk_bf16_f32 v216, v76, v77
v_mfma_f32_16x16x32_bf16 a[32:35], a[176:179], v[224:227], a[32:35]
v_exp_f32_e32 v78, v78
v_mfma_f32_16x16x32_bf16 a[32:35], a[180:183], v[228:231], a[32:35]
ds_read_b128 a[132:135], v9 offset:8192
v_add_f32_e32 v32, v32, v75
v_mfma_f32_16x16x32_bf16 a[36:39], a[176:179], v[232:235], a[36:39]
v_exp_f32_e32 v79, v79
v_mfma_f32_16x16x32_bf16 a[36:39], a[180:183], v[236:239], a[36:39]
buffer_load_dwordx4 v23, s[36:39], s76 offen offset:3072 nt lds
v_mfma_f32_16x16x32_bf16 v[68:71], a[144:147], v[144:147], 0
s_waitcnt lgkmcnt(4)
v_exp_f32_e32 v64, v64
v_mfma_f32_16x16x32_bf16 v[68:71], a[148:151], v[148:151], v[68:71]
ds_read_b64_tr_b16 a[164:165], v4 offset:57344
v_cvt_pk_bf16_f32 v217, v78, v79
v_mfma_f32_16x16x32_bf16 v[68:71], a[152:155], v[152:155], v[68:71]
s_add_u32 s76, s76, 0x4000
v_exp_f32_e32 v65, v65
v_mfma_f32_16x16x32_bf16 v[68:71], a[156:159], v[156:159], v[68:71]
ds_read_b64_tr_b16 a[166:167], v4 offset:61440
v_cvt_pk_bf16_f32 v194, v64, v65
v_mfma_f32_16x16x32_bf16 a[40:43], a[176:179], v[240:243], a[40:43]
v_exp_f32_e32 v66, v66
v_mfma_f32_16x16x32_bf16 a[40:43], a[180:183], v[244:247], a[40:43]
ds_read_b128 a[136:139], v10 offset:8192
v_add_f32_e32 v30, v30, v64
v_mfma_f32_16x16x32_bf16 a[44:47], a[176:179], v[248:251], a[44:47]
v_exp_f32_e32 v67, v67
v_mfma_f32_16x16x32_bf16 a[44:47], a[180:183], v[252:255], a[44:47]
v_cvt_pk_bf16_f32 v195, v66, v67
v_add_f32_e32 v30, v30, v65
v_mfma_f32_16x16x32_bf16 v[72:75], a[144:147], v[160:163], 0
v_exp_f32_e32 v68, v68
v_mfma_f32_16x16x32_bf16 v[72:75], a[148:151], v[164:167], v[72:75]
ds_read_b64_tr_b16 a[168:169], v5 offset:49152
v_add_f32_e32 v30, v30, v66
v_mfma_f32_16x16x32_bf16 v[72:75], a[152:155], v[168:171], v[72:75]
v_exp_f32_e32 v69, v69
v_mfma_f32_16x16x32_bf16 v[72:75], a[156:159], v[172:175], v[72:75]
ds_read_b64_tr_b16 a[170:171], v5 offset:53248
v_cvt_pk_bf16_f32 v202, v68, v69
v_mfma_f32_16x16x32_bf16 a[48:51], a[184:187], v[224:227], a[48:51]
v_exp_f32_e32 v70, v70
v_mfma_f32_16x16x32_bf16 a[48:51], a[188:191], v[228:231], a[48:51]
ds_read_b128 a[140:143], v11 offset:8192
v_add_f32_e32 v30, v30, v67
v_mfma_f32_16x16x32_bf16 a[52:55], a[184:187], v[232:235], a[52:55]
v_exp_f32_e32 v71, v71
v_mfma_f32_16x16x32_bf16 a[52:55], a[188:191], v[236:239], a[52:55]
buffer_load_dwordx2 v[46:47], v58, s[24:27], s82 offen offset:0 sc1
v_mfma_f32_16x16x32_bf16 v[76:79], a[144:147], v[176:179], 0
s_waitcnt lgkmcnt(4)
v_exp_f32_e32 v72, v72
v_mfma_f32_16x16x32_bf16 v[76:79], a[148:151], v[180:183], v[76:79]
ds_read_b64_tr_b16 a[172:173], v5 offset:57344
v_cvt_pk_bf16_f32 v203, v70, v71
v_mfma_f32_16x16x32_bf16 v[76:79], a[152:155], v[184:187], v[76:79]
v_exp_f32_e32 v73, v73
v_mfma_f32_16x16x32_bf16 v[76:79], a[156:159], v[188:191], v[76:79]
ds_read_b64_tr_b16 a[174:175], v5 offset:61440
v_cvt_pk_bf16_f32 v210, v72, v73
v_mfma_f32_16x16x32_bf16 a[56:59], a[184:187], v[240:243], a[56:59]
v_exp_f32_e32 v74, v74
v_mfma_f32_16x16x32_bf16 a[56:59], a[188:191], v[244:247], a[56:59]
ds_read_b128 a[144:147], v8 offset:12288
v_add_f32_e32 v32, v32, v72
v_mfma_f32_16x16x32_bf16 a[60:63], a[184:187], v[248:251], a[60:63]
s_waitcnt lgkmcnt(3)
v_exp_f32_e32 v75, v75
v_mfma_f32_16x16x32_bf16 a[60:63], a[188:191], v[252:255], a[60:63]
v_cvt_pk_bf16_f32 v211, v74, v75
v_add_f32_e32 v32, v32, v73
v_mfma_f32_16x16x32_bf16 v[64:67], a[128:131], v[128:131], 0
v_exp_f32_e32 v76, v76
v_mfma_f32_16x16x32_bf16 v[64:67], a[132:135], v[132:135], v[64:67]
ds_read_b64_tr_b16 a[176:177], v6 offset:49152
v_add_f32_e32 v32, v32, v74
v_mfma_f32_16x16x32_bf16 v[64:67], a[136:139], v[136:139], v[64:67]
v_exp_f32_e32 v77, v77
v_mfma_f32_16x16x32_bf16 v[64:67], a[140:143], v[140:143], v[64:67]
ds_read_b64_tr_b16 a[178:179], v6 offset:53248
v_cvt_pk_bf16_f32 v218, v76, v77
v_mfma_f32_16x16x32_bf16 a[64:67], a[160:163], v[224:227], a[64:67]
v_exp_f32_e32 v78, v78
v_mfma_f32_16x16x32_bf16 a[64:67], a[164:167], v[228:231], a[64:67]
ds_read_b128 a[148:151], v9 offset:12288
v_add_f32_e32 v32, v32, v75
v_mfma_f32_16x16x32_bf16 a[68:71], a[160:163], v[232:235], a[68:71]
v_exp_f32_e32 v79, v79
v_mfma_f32_16x16x32_bf16 a[68:71], a[164:167], v[236:239], a[68:71]
v_mfma_f32_16x16x32_bf16 v[68:71], a[128:131], v[144:147], 0
s_waitcnt lgkmcnt(4)
v_exp_f32_e32 v64, v64
v_mfma_f32_16x16x32_bf16 v[68:71], a[132:135], v[148:151], v[68:71]
ds_read_b64_tr_b16 a[180:181], v6 offset:57344
v_cvt_pk_bf16_f32 v219, v78, v79
v_mfma_f32_16x16x32_bf16 v[68:71], a[136:139], v[152:155], v[68:71]
v_exp_f32_e32 v65, v65
v_mfma_f32_16x16x32_bf16 v[68:71], a[140:143], v[156:159], v[68:71]
ds_read_b64_tr_b16 a[182:183], v6 offset:61440
v_cvt_pk_bf16_f32 v196, v64, v65
v_mfma_f32_16x16x32_bf16 a[72:75], a[160:163], v[240:243], a[72:75]
v_exp_f32_e32 v66, v66
v_mfma_f32_16x16x32_bf16 a[72:75], a[164:167], v[244:247], a[72:75]
ds_read_b128 a[152:155], v10 offset:12288
v_add_f32_e32 v30, v30, v64
v_mfma_f32_16x16x32_bf16 a[76:79], a[160:163], v[248:251], a[76:79]
v_exp_f32_e32 v67, v67
v_mfma_f32_16x16x32_bf16 a[76:79], a[164:167], v[252:255], a[76:79]
v_cvt_pk_bf16_f32 v197, v66, v67
v_add_f32_e32 v30, v30, v65
v_mfma_f32_16x16x32_bf16 v[72:75], a[128:131], v[160:163], 0
v_exp_f32_e32 v68, v68
v_mfma_f32_16x16x32_bf16 v[72:75], a[132:135], v[164:167], v[72:75]
ds_read_b64_tr_b16 a[184:185], v7 offset:49152
v_add_f32_e32 v30, v30, v66
v_mfma_f32_16x16x32_bf16 v[72:75], a[136:139], v[168:171], v[72:75]
v_exp_f32_e32 v69, v69
v_mfma_f32_16x16x32_bf16 v[72:75], a[140:143], v[172:175], v[72:75]
ds_read_b64_tr_b16 a[186:187], v7 offset:53248
v_cvt_pk_bf16_f32 v204, v68, v69
v_mfma_f32_16x16x32_bf16 a[80:83], a[168:171], v[224:227], a[80:83]
v_exp_f32_e32 v70, v70
v_mfma_f32_16x16x32_bf16 a[80:83], a[172:175], v[228:231], a[80:83]
ds_read_b128 a[156:159], v11 offset:12288
v_add_f32_e32 v30, v30, v67
v_mfma_f32_16x16x32_bf16 a[84:87], a[168:171], v[232:235], a[84:87]
v_exp_f32_e32 v71, v71
v_mfma_f32_16x16x32_bf16 a[84:87], a[172:175], v[236:239], a[84:87]
v_mfma_f32_16x16x32_bf16 v[76:79], a[128:131], v[176:179], 0
s_waitcnt lgkmcnt(4)
v_exp_f32_e32 v72, v72
v_mfma_f32_16x16x32_bf16 v[76:79], a[132:135], v[180:183], v[76:79]
ds_read_b64_tr_b16 a[188:189], v7 offset:57344
v_cvt_pk_bf16_f32 v205, v70, v71
v_mfma_f32_16x16x32_bf16 v[76:79], a[136:139], v[184:187], v[76:79]
v_exp_f32_e32 v73, v73
v_mfma_f32_16x16x32_bf16 v[76:79], a[140:143], v[188:191], v[76:79]
ds_read_b64_tr_b16 a[190:191], v7 offset:61440
v_cvt_pk_bf16_f32 v212, v72, v73
v_mfma_f32_16x16x32_bf16 a[88:91], a[168:171], v[240:243], a[88:91]
v_exp_f32_e32 v74, v74
v_mfma_f32_16x16x32_bf16 a[88:91], a[172:175], v[244:247], a[88:91]
ds_read_b128 a[128:131], v8 offset:16384
v_add_f32_e32 v32, v32, v72
v_mfma_f32_16x16x32_bf16 a[92:95], a[168:171], v[248:251], a[92:95]
s_waitcnt lgkmcnt(3)
v_exp_f32_e32 v75, v75
v_mfma_f32_16x16x32_bf16 a[92:95], a[172:175], v[252:255], a[92:95]
v_cvt_pk_bf16_f32 v213, v74, v75
v_add_f32_e32 v32, v32, v73
v_mfma_f32_16x16x32_bf16 v[64:67], a[144:147], v[128:131], 0
v_exp_f32_e32 v76, v76
v_mfma_f32_16x16x32_bf16 v[64:67], a[148:151], v[132:135], v[64:67]
ds_read_b64_tr_b16 a[160:161], v0 offset:0
v_add_f32_e32 v32, v32, v74
v_mfma_f32_16x16x32_bf16 v[64:67], a[152:155], v[136:139], v[64:67]
v_exp_f32_e32 v77, v77
v_mfma_f32_16x16x32_bf16 v[64:67], a[156:159], v[140:143], v[64:67]
ds_read_b64_tr_b16 a[162:163], v0 offset:4096
v_cvt_pk_bf16_f32 v220, v76, v77
v_mfma_f32_16x16x32_bf16 a[96:99], a[176:179], v[224:227], a[96:99]
v_exp_f32_e32 v78, v78
v_mfma_f32_16x16x32_bf16 a[96:99], a[180:183], v[228:231], a[96:99]
ds_read_b128 a[132:135], v9 offset:16384
v_add_f32_e32 v32, v32, v75
v_mfma_f32_16x16x32_bf16 a[100:103], a[176:179], v[232:235], a[100:103]
v_exp_f32_e32 v79, v79
v_mfma_f32_16x16x32_bf16 a[100:103], a[180:183], v[236:239], a[100:103]
v_mfma_f32_16x16x32_bf16 v[68:71], a[144:147], v[144:147], 0
s_waitcnt lgkmcnt(4)
v_exp_f32_e32 v64, v64
v_mfma_f32_16x16x32_bf16 v[68:71], a[148:151], v[148:151], v[68:71]
ds_read_b64_tr_b16 a[164:165], v0 offset:8192
v_cvt_pk_bf16_f32 v221, v78, v79
v_mfma_f32_16x16x32_bf16 v[68:71], a[152:155], v[152:155], v[68:71]
s_waitcnt vmcnt(10)
v_exp_f32_e32 v65, v65
v_mfma_f32_16x16x32_bf16 v[68:71], a[156:159], v[156:159], v[68:71]
ds_read_b64_tr_b16 a[166:167], v0 offset:12288
v_cvt_pk_bf16_f32 v198, v64, v65
v_mfma_f32_16x16x32_bf16 a[104:107], a[176:179], v[240:243], a[104:107]
s_barrier
v_exp_f32_e32 v66, v66
v_mfma_f32_16x16x32_bf16 a[104:107], a[180:183], v[244:247], a[104:107]
ds_read_b128 a[136:139], v10 offset:16384
v_add_f32_e32 v30, v30, v64
v_mfma_f32_16x16x32_bf16 a[108:111], a[176:179], v[248:251], a[108:111]
s_add_u32 m0, s68, 0x1c000
v_exp_f32_e32 v67, v67
v_mfma_f32_16x16x32_bf16 a[108:111], a[180:183], v[252:255], a[108:111]
v_cvt_pk_bf16_f32 v199, v66, v67
v_add_f32_e32 v30, v30, v65
v_mfma_f32_16x16x32_bf16 v[38:41], v[34:37], v[200:203], v[38:41]
buffer_load_dwordx4 v24, s[40:43], s77 offen offset:0 nt lds
v_mfma_f32_16x16x32_bf16 v[72:75], a[144:147], v[160:163], 0
v_exp_f32_e32 v68, v68
v_mfma_f32_16x16x32_bf16 v[72:75], a[148:151], v[164:167], v[72:75]
ds_read_b64_tr_b16 a[168:169], v1 offset:0
v_add_f32_e32 v30, v30, v66
v_mfma_f32_16x16x32_bf16 v[72:75], a[152:155], v[168:171], v[72:75]
v_exp_f32_e32 v69, v69
v_mfma_f32_16x16x32_bf16 v[72:75], a[156:159], v[172:175], v[72:75]
ds_read_b64_tr_b16 a[170:171], v1 offset:4096
v_cvt_pk_bf16_f32 v206, v68, v69
v_mfma_f32_16x16x32_bf16 a[112:115], a[184:187], v[224:227], a[112:115]
v_exp_f32_e32 v70, v70
v_mfma_f32_16x16x32_bf16 a[112:115], a[188:191], v[228:231], a[112:115]
ds_read_b128 a[140:143], v11 offset:16384
v_add_f32_e32 v30, v30, v67
v_mfma_f32_16x16x32_bf16 a[116:119], a[184:187], v[232:235], a[116:119]
v_exp_f32_e32 v71, v71
v_mfma_f32_16x16x32_bf16 a[116:119], a[188:191], v[236:239], a[116:119]
buffer_load_dwordx4 v25, s[40:43], s77 offen offset:1024 nt lds
v_mfma_f32_16x16x32_bf16 v[76:79], a[144:147], v[176:179], 0
v_exp_f32_e32 v72, v72
v_mfma_f32_16x16x32_bf16 v[76:79], a[148:151], v[180:183], v[76:79]
ds_read_b64_tr_b16 a[172:173], v1 offset:8192
v_cvt_pk_bf16_f32 v207, v70, v71
v_mfma_f32_16x16x32_bf16 v[76:79], a[152:155], v[184:187], v[76:79]
s_waitcnt lgkmcnt(5)
v_exp_f32_e32 v73, v73
v_mfma_f32_16x16x32_bf16 v[76:79], a[156:159], v[188:191], v[76:79]
ds_read_b64_tr_b16 a[174:175], v1 offset:12288
v_cvt_pk_bf16_f32 v214, v72, v73
v_mfma_f32_16x16x32_bf16 a[120:123], a[184:187], v[240:243], a[120:123]
v_exp_f32_e32 v74, v74
v_mfma_f32_16x16x32_bf16 a[120:123], a[188:191], v[244:247], a[120:123]
ds_read_b128 a[144:147], v8 offset:20480
v_add_f32_e32 v32, v32, v72
v_mfma_f32_16x16x32_bf16 a[124:127], a[184:187], v[248:251], a[124:127]
s_waitcnt lgkmcnt(3)
v_exp_f32_e32 v75, v75
v_mfma_f32_16x16x32_bf16 a[124:127], a[188:191], v[252:255], a[124:127]
v_cvt_pk_bf16_f32 v215, v74, v75
v_add_f32_e32 v32, v32, v73
v_mfma_f32_16x16x32_bf16 v[42:45], v[34:37], v[216:219], v[42:45]
buffer_load_dwordx4 v24, s[40:43], s77 offen offset:2048 nt lds
s_cmp_eq_u32 s78, 0
s_cbranch_scc0 .Lkf_top
; ---- early0
s_waitcnt vmcnt(3)
v_cmp_eq_u64_e32 vcc, s[94:95], v[46:47]
s_nop 0
s_and_b32 s83, vcc_lo, 0xff
s_cmp_eq_u32 s83, 0xff
s_cbranch_scc0 .Lkf_sph
; ---- last
.Lkf_eb1:
buffer_load_dword v48, v29, s[8:11], s12 offen offset:0
buffer_load_dword v48, v58, s[8:11], 0 offen offset:0
v_mfma_f32_16x16x32_bf16 v[64:67], a[128:131], v[128:131], 0
v_exp_f32_e32 v76, v76
v_mfma_f32_16x16x32_bf16 v[64:67], a[132:135], v[132:135], v[64:67]
ds_read_b64_tr_b16 a[176:177], v2 offset:0
v_add_f32_e32 v32, v32, v74
v_mfma_f32_16x16x32_bf16 v[64:67], a[136:139], v[136:139], v[64:67]
v_exp_f32_e32 v77, v77
v_mfma_f32_16x16x32_bf16 v[64:67], a[140:143], v[140:143], v[64:67]
ds_read_b64_tr_b16 a[178:179], v2 offset:4096
v_cvt_pk_bf16_f32 v222, v76, v77
v_mfma_f32_16x16x32_bf16 a[0:3], a[160:163], v[192:195], a[0:3]
v_exp_f32_e32 v78, v78
v_mfma_f32_16x16x32_bf16 a[0:3], a[164:167], v[196:199], a[0:3]
ds_read_b128 a[148:151], v9 offset:20480
v_add_f32_e32 v32, v32, v75
v_mfma_f32_16x16x32_bf16 a[4:7], a[160:163], v[200:203], a[4:7]
v_exp_f32_e32 v79, v79
v_mfma_f32_16x16x32_bf16 a[4:7], a[164:167], v[204:207], a[4:7]
buffer_load_dwordx4 v25, s[40:43], s77 offen offset:3072 nt lds
v_mfma_f32_16x16x32_bf16 v[68:71], a[128:131], v[144:147], 0
s_waitcnt lgkmcnt(4)
v_exp_f32_e32 v64, v64
v_mfma_f32_16x16x32_bf16 v[68:71], a[132:135], v[148:151], v[68:71]
ds_read_b64_tr_b16 a[180:181], v2 offset:8192
v_cvt_pk_bf16_f32 v223, v78, v79
v_mfma_f32_16x16x32_bf16 v[68:71], a[136:139], v[152:155], v[68:71]
s_add_u32 m0, s68, 0x20000
v_exp_f32_e32 v65, v65
v_mfma_f32_16x16x32_bf16 v[68:71], a[140:143], v[156:159], v[68:71]
ds_read_b64_tr_b16 a[182:183], v2 offset:12288
v_cvt_pk_bf16_f32 v224, v64, v65
v_mfma_f32_16x16x32_bf16 a[8:11], a[160:163], v[208:211], a[8:11]
s_add_u32 s77, s77, 0x4000
v_exp_f32_e32 v66, v66
v_mfma_f32_16x16x32_bf16 a[8:11], a[164:167], v[212:215], a[8:11]
ds_read_b128 a[152:155], v10 offset:20480
v_add_f32_e32 v30, v30, v64
v_mfma_f32_16x16x32_bf16 a[12:15], a[160:163], v[216:219], a[12:15]
s_mov_b32 s20, s15
v_exp_f32_e32 v67, v67
v_mfma_f32_16x16x32_bf16 a[12:15], a[164:167], v[220:223], a[12:15]
v_cvt_pk_bf16_f32 v225, v66, v67
v_add_f32_e32 v30, v30, v65
v_mfma_f32_16x16x32_bf16 v[38:41], v[34:37], v[204:207], v[38:41]
buffer_load_dwordx4 v52, s[52:55], s20 offen offset:0 lds
v_mfma_f32_16x16x32_bf16 v[72:75], a[128:131], v[160:163], 0
v_exp_f32_e32 v68, v68
v_mfma_f32_16x16x32_bf16 v[72:75], a[132:135], v[164:167], v[72:75]
ds_read_b64_tr_b16 a[184:185], v3 offset:0
v_add_f32_e32 v30, v30, v66
v_mfma_f32_16x16x32_bf16 v[72:75], a[136:139], v[168:171], v[72:75]
v_exp_f32_e32 v69, v69
v_mfma_f32_16x16x32_bf16 v[72:75], a[140:143], v[172:175], v[72:75]
ds_read_b64_tr_b16 a[186:187], v3 offset:4096
v_cvt_pk_bf16_f32 v232, v68, v69
v_mfma_f32_16x16x32_bf16 a[16:19], a[168:171], v[192:195], a[16:19]
v_exp_f32_e32 v70, v70
v_mfma_f32_16x16x32_bf16 a[16:19], a[172:175], v[196:199], a[16:19]
ds_read_b128 a[156:159], v11 offset:20480
v_add_f32_e32 v30, v30, v67
v_mfma_f32_16x16x32_bf16 a[20:23], a[168:171], v[200:203], a[20:23]
v_exp_f32_e32 v71, v71
v_mfma_f32_16x16x32_bf16 a[20:23], a[172:175], v[204:207], a[20:23]
buffer_load_dwordx4 v53, s[52:55], s20 offen offset:1024 lds
v_mfma_f32_16x16x32_bf16 v[76:79], a[128:131], v[176:179], 0
s_waitcnt lgkmcnt(4)
v_exp_f32_e32 v72, v72
v_mfma_f32_16x16x32_bf16 v[76:79], a[132:135], v[180:183], v[76:79]
ds_read_b64_tr_b16 a[188:189], v3 offset:8192
v_cvt_pk_bf16_f32 v233, v70, v71
v_mfma_f32_16x16x32_bf16 v[76:79], a[136:139], v[184:187], v[76:79]
v_exp_f32_e32 v73, v73
v_mfma_f32_16x16x32_bf16 v[76:79], a[140:143], v[188:191], v[76:79]
ds_read_b64_tr_b16 a[190:191], v3 offset:12288
v_cvt_pk_bf16_f32 v240, v72, v73
v_mfma_f32_16x16x32_bf16 a[24:27], a[168:171], v[208:211], a[24:27]
v_exp_f32_e32 v74, v74
v_mfma_f32_16x16x32_bf16 a[24:27], a[172:175], v[212:215], a[24:27]
ds_read_b128 a[128:131], v8 offset:24576
v_add_f32_e32 v32, v32, v72
v_mfma_f32_16x16x32_bf16 a[28:31], a[168:171], v[216:219], a[28:31]
s_waitcnt lgkmcnt(3)
v_exp_f32_e32 v75, v75
v_mfma_f32_16x16x32_bf16 a[28:31], a[172:175], v[220:223], a[28:31]
v_cvt_pk_bf16_f32 v241, v74, v75
v_add_f32_e32 v32, v32, v73
v_mfma_f32_16x16x32_bf16 v[42:45], v[34:37], v[220:223], v[42:45]
buffer_load_dwordx4 v54, s[52:55], s20 offen offset:2048 lds
v_mfma_f32_16x16x32_bf16 v[64:67], a[144:147], v[128:131], 0
v_exp_f32_e32 v76, v76
v_mfma_f32_16x16x32_bf16 v[64:67], a[148:151], v[132:135], v[64:67]
ds_read_b64_tr_b16 a[160:161], v4 offset:0
v_add_f32_e32 v32, v32, v74
v_mfma_f32_16x16x32_bf16 v[64:67], a[152:155], v[136:139], v[64:67]
v_exp_f32_e32 v77, v77
v_mfma_f32_16x16x32_bf16 v[64:67], a[156:159], v[140:143], v[64:67]
ds_read_b64_tr_b16 a[162:163], v4 offset:4096
v_cvt_pk_bf16_f32 v248, v76, v77
v_mfma_f32_16x16x32_bf16 a[32:35], a[176:179], v[192:195], a[32:35]
v_exp_f32_e32 v78, v78
v_mfma_f32_16x16x32_bf16 a[32:35], a[180:183], v[196:199], a[32:35]
ds_read_b128 a[132:135], v9 offset:24576
v_add_f32_e32 v32, v32, v75
v_mfma_f32_16x16x32_bf16 a[36:39], a[176:179], v[200:203], a[36:39]
v_exp_f32_e32 v79, v79
v_mfma_f32_16x16x32_bf16 a[36:39], a[180:183], v[204:207], a[36:39]
buffer_load_dwordx4 v55, s[52:55], s20 offen offset:3072 lds
v_mfma_f32_16x16x32_bf16 v[68:71], a[144:147], v[144:147], 0
s_waitcnt lgkmcnt(4)
v_exp_f32_e32 v64, v64
v_mfma_f32_16x16x32_bf16 v[68:71], a[148:151], v[148:151], v[68:71]
ds_read_b64_tr_b16 a[164:165], v4 offset:8192
v_cvt_pk_bf16_f32 v249, v78, v79
v_mfma_f32_16x16x32_bf16 v[68:71], a[152:155], v[152:155], v[68:71]
s_add_u32 m0, s68, 0x24000
v_exp_f32_e32 v65, v65
v_mfma_f32_16x16x32_bf16 v[68:71], a[156:159], v[156:159], v[68:71]
ds_read_b64_tr_b16 a[166:167], v4 offset:12288
v_cvt_pk_bf16_f32 v226, v64, v65
v_mfma_f32_16x16x32_bf16 a[40:43], a[176:179], v[208:211], a[40:43]
s_add_u32 s20, s20, 0x10000
v_exp_f32_e32 v66, v66
v_mfma_f32_16x16x32_bf16 a[40:43], a[180:183], v[212:215], a[40:43]
ds_read_b128 a[136:139], v10 offset:24576
v_add_f32_e32 v30, v30, v64
v_mfma_f32_16x16x32_bf16 a[44:47], a[176:179], v[216:219], a[44:47]
v_exp_f32_e32 v67, v67
v_mfma_f32_16x16x32_bf16 a[44:47], a[180:183], v[220:223], a[44:47]
buffer_load_dwordx4 v52, s[52:55], s20 offen offset:0 lds
v_cvt_pk_bf16_f32 v227, v66, v67
v_add_f32_e32 v30, v30, v65
v_mfma_f32_16x16x32_bf16 v[72:75], a[144:147], v[160:163], 0
v_exp_f32_e32 v68, v68
v_mfma_f32_16x16x32_bf16 v[72:75], a[148:151], v[164:167], v[72:75]
ds_read_b64_tr_b16 a[168:169], v5 offset:0
v_add_f32_e32 v30, v30, v66
v_mfma_f32_16x16x32_bf16 v[72:75], a[152:155], v[168:171], v[72:75]
v_exp_f32_e32 v69, v69
v_mfma_f32_16x16x32_bf16 v[72:75], a[156:159], v[172:175], v[72:75]
ds_read_b64_tr_b16 a[170:171], v5 offset:4096
v_cvt_pk_bf16_f32 v234, v68, v69
v_mfma_f32_16x16x32_bf16 a[48:51], a[184:187], v[192:195], a[48:51]
v_exp_f32_e32 v70, v70
v_mfma_f32_16x16x32_bf16 a[48:51], a[188:191], v[196:199], a[48:51]
ds_read_b128 a[140:143], v11 offset:24576
v_add_f32_e32 v30, v30, v67
v_mfma_f32_16x16x32_bf16 a[52:55], a[184:187], v[200:203], a[52:55]
v_exp_f32_e32 v71, v71
v_mfma_f32_16x16x32_bf16 a[52:55], a[188:191], v[204:207], a[52:55]
buffer_load_dwordx4 v53, s[52:55], s20 offen offset:1024 lds
v_mfma_f32_16x16x32_bf16 v[76:79], a[144:147], v[176:179], 0
s_waitcnt lgkmcnt(4)
v_exp_f32_e32 v72, v72
v_mfma_f32_16x16x32_bf16 v[76:79], a[148:151], v[180:183], v[76:79]
ds_read_b64_tr_b16 a[172:173], v5 offset:8192
v_cvt_pk_bf16_f32 v235, v70, v71
v_mfma_f32_16x16x32_bf16 v[76:79], a[152:155], v[184:187], v[76:79]
v_exp_f32_e32 v73, v73
v_mfma_f32_16x16x32_bf16 v[76:79], a[156:159], v[188:191], v[76:79]
ds_read_b64_tr_b16 a[174:175], v5 offset:12288
v_cvt_pk_bf16_f32 v242, v72, v73
v_mfma_f32_16x16x32_bf16 a[56:59], a[184:187], v[208:211], a[56:59]
v_exp_f32_e32 v74, v74
v_mfma_f32_16x16x32_bf16 a[56:59], a[188:191], v[212:215], a[56:59]
ds_read_b128 a[144:147], v8 offset:28672
v_add_f32_e32 v32, v32, v72
v_mfma_f32_16x16x32_bf16 a[60:63], a[184:187], v[216:219], a[60:63]
s_waitcnt lgkmcnt(3)
v_exp_f32_e32 v75, v75
v_mfma_f32_16x16x32_bf16 a[60:63], a[188:191], v[220:223], a[60:63]
buffer_load_dwordx4 v54, s[52:55], s20 offen offset:2048 lds
v_cvt_pk_bf16_f32 v243, v74, v75
v_add_f32_e32 v32, v32, v73
v_mfma_f32_16x16x32_bf16 v[64:67], a[128:131], v[128:131], 0
v_exp_f32_e32 v76, v76
v_mfma_f32_16x16x32_bf16 v[64:67], a[132:135], v[132:135], v[64:67]
ds_read_b64_tr_b16 a[176:177], v6 offset:0
v_add_f32_e32 v32, v32, v74
v_mfma_f32_16x16x32_bf16 v[64:67], a[136:139], v[136:139], v[64:67]
v_exp_f32_e32 v77, v77
v_mfma_f32_16x16x32_bf16 v[64:67], a[140:143], v[140:143], v[64:67]
ds_read_b64_tr_b16 a[178:179], v6 offset:4096
v_cvt_pk_bf16_f32 v250, v76, v77
v_mfma_f32_16x16x32_bf16 a[64:67], a[160:163], v[192:195], a[64:67]
v_exp_f32_e32 v78, v78
v_mfma_f32_16x16x32_bf16 a[64:67], a[164:167], v[196:199], a[64:67]
ds_read_b128 a[148:151], v9 offset:28672
v_add_f32_e32 v32, v32, v75
v_mfma_f32_16x16x32_bf16 a[68:71], a[160:163], v[200:203], a[68:71]
v_exp_f32_e32 v79, v79
v_mfma_f32_16x16x32_bf16 a[68:71], a[164:167], v[204:207], a[68:71]
buffer_load_dwordx4 v55, s[52:55], s20 offen offset:3072 lds
v_mfma_f32_16x16x32_bf16 v[68:71], a[128:131], v[144:147], 0
s_waitcnt lgkmcnt(4)
v_exp_f32_e32 v64, v64
v_mfma_f32_16x16x32_bf16 v[68:71], a[132:135], v[148:151], v[68:71]
ds_read_b64_tr_b16 a[180:181], v6 offset:8192
v_cvt_pk_bf16_f32 v251, v78, v79
v_mfma_f32_16x16x32_bf16 v[68:71], a[136:139], v[152:155], v[68:71]
s_mov_b32 m0, s68
v_exp_f32_e32 v65, v65
v_mfma_f32_16x16x32_bf16 v[68:71], a[140:143], v[156:159], v[68:71]
ds_read_b64_tr_b16 a[182:183], v6 offset:12288
v_cvt_pk_bf16_f32 v228, v64, v65
v_mfma_f32_16x16x32_bf16 a[72:75], a[160:163], v[208:211], a[72:75]
s_add_u32 s20, s20, 0x10000
v_exp_f32_e32 v66, v66
v_mfma_f32_16x16x32_bf16 a[72:75], a[164:167], v[212:215], a[72:75]
ds_read_b128 a[152:155], v10 offset:28672
v_add_f32_e32 v30, v30, v64
v_mfma_f32_16x16x32_bf16 a[76:79], a[160:163], v[216:219], a[76:79]
v_exp_f32_e32 v67, v67
v_mfma_f32_16x16x32_bf16 a[76:79], a[164:167], v[220:223], a[76:79]
buffer_load_dwordx4 v52, s[52:55], s20 offen offset:0 lds
v_cvt_pk_bf16_f32 v229, v66, v67
v_add_f32_e32 v30, v30, v65
v_mfma_f32_16x16x32_bf16 v[72:75], a[128:131], v[160:163], 0
v_exp_f32_e32 v68, v68
v_mfma_f32_16x16x32_bf16 v[72:75], a[132:135], v[164:167], v[72:75]
ds_read_b64_tr_b16 a[184:185], v7 offset:0
v_add_f32_e32 v30, v30, v66
v_mfma_f32_16x16x32_bf16 v[72:75], a[136:139], v[168:171], v[72:75]
v_exp_f32_e32 v69, v69
v_mfma_f32_16x16x32_bf16 v[72:75], a[140:143], v[172:175], v[72:75]
ds_read_b64_tr_b16 a[186:187], v7 offset:4096
v_cvt_pk_bf16_f32 v236, v68, v69
v_mfma_f32_16x16x32_bf16 a[80:83], a[168:171], v[192:195], a[80:83]
v_exp_f32_e32 v70, v70
v_mfma_f32_16x16x32_bf16 a[80:83], a[172:175], v[196:199], a[80:83]
ds_read_b128 a[156:159], v11 offset:28672
v_add_f32_e32 v30, v30, v67
v_mfma_f32_16x16x32_bf16 a[84:87], a[168:171], v[200:203], a[84:87]
v_exp_f32_e32 v71, v71
v_mfma_f32_16x16x32_bf16 a[84:87], a[172:175], v[204:207], a[84:87]
buffer_load_dwordx4 v53, s[52:55], s20 offen offset:1024 lds
v_mfma_f32_16x16x32_bf16 v[76:79], a[128:131], v[176:179], 0
s_waitcnt lgkmcnt(4)
v_exp_f32_e32 v72, v72
v_mfma_f32_16x16x32_bf16 v[76:79], a[132:135], v[180:183], v[76:79]
ds_read_b64_tr_b16 a[188:189], v7 offset:8192
v_cvt_pk_bf16_f32 v237, v70, v71
v_mfma_f32_16x16x32_bf16 v[76:79], a[136:139], v[184:187], v[76:79]
v_exp_f32_e32 v73, v73
v_mfma_f32_16x16x32_bf16 v[76:79], a[140:143], v[188:191], v[76:79]
ds_read_b64_tr_b16 a[190:191], v7 offset:12288
v_cvt_pk_bf16_f32 v244, v72, v73
v_mfma_f32_16x16x32_bf16 a[88:91], a[168:171], v[208:211], a[88:91]
v_exp_f32_e32 v74, v74
v_mfma_f32_16x16x32_bf16 a[88:91], a[172:175], v[212:215], a[88:91]
ds_read_b128 a[128:131], v8 offset:32768
v_add_f32_e32 v32, v32, v72
v_mfma_f32_16x16x32_bf16 a[92:95], a[168:171], v[216:219], a[92:95]
s_waitcnt lgkmcnt(3)
v_exp_f32_e32 v75, v75
v_mfma_f32_16x16x32_bf16 a[92:95], a[172:175], v[220:223], a[92:95]
buffer_load_dwordx4 v54, s[52:55], s20 offen offset:2048 lds
v_cvt_pk_bf16_f32 v245, v74, v75
v_add_f32_e32 v32, v32, v73
v_mfma_f32_16x16x32_bf16 v[64:67], a[144:147], v[128:131], 0
v_exp_f32_e32 v76, v76
v_mfma_f32_16x16x32_bf16 v[64:67], a[148:151], v[132:135], v[64:67]
ds_read_b64_tr_b16 a[160:161], v0 offset:16384
v_add_f32_e32 v32, v32, v74
v_mfma_f32_16x16x32_bf16 v[64:67], a[152:155], v[136:139], v[64:67]
v_exp_f32_e32 v77, v77
v_mfma_f32_16x16x32_bf16 v[64:67], a[156:159], v[140:143], v[64:67]
ds_read_b64_tr_b16 a[162:163], v0 offset:20480
v_cvt_pk_bf16_f32 v252, v76, v77
v_mfma_f32_16x16x32_bf16 a[96:99], a[176:179], v[192:195], a[96:99]
v_exp_f32_e32 v78, v78
v_mfma_f32_16x16x32_bf16 a[96:99], a[180:183], v[196:199], a[96:99]
ds_read_b128 a[132:135], v9 offset:32768
v_add_f32_e32 v32, v32, v75
v_mfma_f32_16x16x32_bf16 a[100:103], a[176:179], v[200:203], a[100:103]
v_exp_f32_e32 v79, v79
v_mfma_f32_16x16x32_bf16 a[100:103], a[180:183], v[204:207], a[100:103]
buffer_load_dwordx4 v55, s[52:55], s20 offen offset:3072 lds
v_mfma_f32_16x16x32_bf16 v[68:71], a[144:147], v[144:147], 0
s_waitcnt lgkmcnt(4)
v_exp_f32_e32 v64, v64
v_mfma_f32_16x16x32_bf16 v[68:71], a[148:151], v[148:151], v[68:71]
ds_read_b64_tr_b16 a[164:165], v0 offset:24576
v_cvt_pk_bf16_f32 v253, v78, v79
v_mfma_f32_16x16x32_bf16 v[68:71], a[152:155], v[152:155], v[68:71]
s_waitcnt vmcnt(19)
v_exp_f32_e32 v65, v65
v_mfma_f32_16x16x32_bf16 v[68:71], a[156:159], v[156:159], v[68:71]
ds_read_b64_tr_b16 a[166:167], v0 offset:28672
v_cvt_pk_bf16_f32 v230, v64, v65
v_mfma_f32_16x16x32_bf16 a[104:107], a[176:179], v[208:211], a[104:107]
s_barrier
v_exp_f32_e32 v66, v66
v_mfma_f32_16x16x32_bf16 a[104:107], a[180:183], v[212:215], a[104:107]
ds_read_b128 a[136:139], v10 offset:32768
v_add_f32_e32 v30, v30, v64
v_mfma_f32_16x16x32_bf16 a[108:111], a[176:179], v[216:219], a[108:111]
s_mov_b32 s13, 0
s_mov_b32 s14, 0xe000
v_exp_f32_e32 v67, v67
v_mfma_f32_16x16x32_bf16 a[108:111], a[180:183], v[220:223], a[108:111]
v_cvt_pk_bf16_f32 v231, v66, v67
v_add_f32_e32 v30, v30, v65
v_mfma_f32_16x16x32_bf16 v[38:41], v[34:37], v[232:235], v[38:41]
buffer_load_dwordx4 a[192:195], v29, s[44:47], s13 offen offset:0 sc1
v_mfma_f32_16x16x32_bf16 v[72:75], a[144:147], v[160:163], 0
v_exp_f32_e32 v68, v68
v_mfma_f32_16x16x32_bf16 v[72:75], a[148:151], v[164:167], v[72:75]
ds_read_b64_tr_b16 a[168:169], v1 offset:16384
v_add_f32_e32 v30, v30, v66
v_mfma_f32_16x16x32_bf16 v[72:75], a[152:155], v[168:171], v[72:75]
s_add_u32 m0, s68, 0x4000
v_exp_f32_e32 v69, v69
v_mfma_f32_16x16x32_bf16 v[72:75], a[156:159], v[172:175], v[72:75]
ds_read_b64_tr_b16 a[170:171], v1 offset:20480
v_cvt_pk_bf16_f32 v238, v68, v69
v_mfma_f32_16x16x32_bf16 a[112:115], a[184:187], v[192:195], a[112:115]
s_add_u32 s20, s20, 0x10000
v_exp_f32_e32 v70, v70
v_mfma_f32_16x16x32_bf16 a[112:115], a[188:191], v[196:199], a[112:115]
ds_read_b128 a[140:143], v11 offset:32768
v_add_f32_e32 v30, v30, v67
v_mfma_f32_16x16x32_bf16 a[116:119], a[184:187], v[200:203], a[116:119]
v_exp_f32_e32 v71, v71
v_mfma_f32_16x16x32_bf16 a[116:119], a[188:191], v[204:207], a[116:119]
buffer_load_dwordx4 v52, s[52:55], s20 offen offset:0 lds
v_mfma_f32_16x16x32_bf16 v[76:79], a[144:147], v[176:179], 0
v_exp_f32_e32 v72, v72
v_mfma_f32_16x16x32_bf16 v[76:79], a[148:151], v[180:183], v[76:79]
ds_read_b64_tr_b16 a[172:173], v1 offset:24576
v_cvt_pk_bf16_f32 v239, v70, v71
v_mfma_f32_16x16x32_bf16 v[76:79], a[152:155], v[184:187], v[76:79]
s_waitcnt lgkmcnt(5)
v_exp_f32_e32 v73, v73
v_mfma_f32_16x16x32_bf16 v[76:79], a[156:159], v[188:191], v[76:79]
ds_read_b64_tr_b16 a[174:175], v1 offset:28672
v_cvt_pk_bf16_f32 v246, v72, v73
v_mfma_f32_16x16x32_bf16 a[120:123], a[184:187], v[208:211], a[120:123]
v_exp_f32_e32 v74, v74
v_mfma_f32_16x16x32_bf16 a[120:123], a[188:191], v[212:215], a[120:123]
ds_read_b128 a[144:147], v8 offset:36864
v_add_f32_e32 v32, v32, v72
v_mfma_f32_16x16x32_bf16 a[124:127], a[184:187], v[216:219], a[124:127]
s_waitcnt lgkmcnt(3)
v_exp_f32_e32 v75, v75
v_mfma_f32_16x16x32_bf16 a[124:127], a[188:191], v[220:223], a[124:127]
v_cvt_pk_bf16_f32 v247, v74, v75
v_add_f32_e32 v32, v32, v73
v_mfma_f32_16x16x32_bf16 v[42:45], v[34:37], v[248:251], v[42:45]
buffer_load_dwordx4 v53, s[52:55], s20 offen offset:1024 lds
v_mfma_f32_16x16x32_bf16 v[64:67], a[128:131], v[128:131], 0
v_exp_f32_e32 v76, v76
v_mfma_f32_16x16x32_bf16 v[64:67], a[132:135], v[132:135], v[64:67]
ds_read_b64_tr_b16 a[176:177], v2 offset:16384
v_add_f32_e32 v32, v32, v74
v_mfma_f32_16x16x32_bf16 v[64:67], a[136:139], v[136:139], v[64:67]
v_exp_f32_e32 v77, v77
v_mfma_f32_16x16x32_bf16 v[64:67], a[140:143], v[140:143], v[64:67]
ds_read_b64_tr_b16 a[178:179], v2 offset:20480
v_cvt_pk_bf16_f32 v254, v76, v77
v_mfma_f32_16x16x32_bf16 a[0:3], a[160:163], v[224:227], a[0:3]
v_exp_f32_e32 v78, v78
v_mfma_f32_16x16x32_bf16 a[0:3], a[164:167], v[228:231], a[0:3]
ds_read_b128 a[148:151], v9 offset:36864
v_add_f32_e32 v32, v32, v75
v_mfma_f32_16x16x32_bf16 a[4:7], a[160:163], v[232:235], a[4:7]
v_exp_f32_e32 v79, v79
v_mfma_f32_16x16x32_bf16 a[4:7], a[164:167], v[236:239], a[4:7]
buffer_load_dwordx4 v54, s[52:55], s20 offen offset:2048 lds
v_mfma_f32_16x16x32_bf16 v[68:71], a[128:131], v[144:147], 0
s_waitcnt lgkmcnt(4)
v_exp_f32_e32 v64, v64
v_mfma_f32_16x16x32_bf16 v[68:71], a[132:135], v[148:151], v[68:71]
ds_read_b64_tr_b16 a[180:181], v2 offset:24576
v_cvt_pk_bf16_f32 v255, v78, v79
v_mfma_f32_16x16x32_bf16 v[68:71], a[136:139], v[152:155], v[68:71]
v_exp_f32_e32 v65, v65
v_mfma_f32_16x16x32_bf16 v[68:71], a[140:143], v[156:159], v[68:71]
ds_read_b64_tr_b16 a[182:183], v2 offset:28672
v_cvt_pk_bf16_f32 v192, v64, v65
v_mfma_f32_16x16x32_bf16 a[8:11], a[160:163], v[240:243], a[8:11]
v_exp_f32_e32 v66, v66
v_mfma_f32_16x16x32_bf16 a[8:11], a[164:167], v[244:247], a[8:11]
ds_read_b128 a[152:155], v10 offset:36864
v_add_f32_e32 v30, v30, v64
v_mfma_f32_16x16x32_bf16 a[12:15], a[160:163], v[248:251], a[12:15]
v_exp_f32_e32 v67, v67
v_mfma_f32_16x16x32_bf16 a[12:15], a[164:167], v[252:255], a[12:15]
v_cvt_pk_bf16_f32 v193, v66, v67
v_add_f32_e32 v30, v30, v65
v_mfma_f32_16x16x32_bf16 v[38:41], v[34:37], v[236:239], v[38:41]
buffer_load_dwordx4 v55, s[52:55], s20 offen offset:3072 lds
v_mfma_f32_16x16x32_bf16 v[72:75], a[128:131], v[160:163], 0
v_exp_f32_e32 v68, v68
v_mfma_f32_16x16x32_bf16 v[72:75], a[132:135], v[164:167], v[72:75]
ds_read_b64_tr_b16 a[184:185], v3 offset:16384
v_add_f32_e32 v30, v30, v66
v_mfma_f32_16x16x32_bf16 v[72:75], a[136:139], v[168:171], v[72:75]
s_add_u32 m0, s68, 0x10000
v_exp_f32_e32 v69, v69
v_mfma_f32_16x16x32_bf16 v[72:75], a[140:143], v[172:175], v[72:75]
ds_read_b64_tr_b16 a[186:187], v3 offset:20480
v_cvt_pk_bf16_f32 v200, v68, v69
v_mfma_f32_16x16x32_bf16 a[16:19], a[168:171], v[224:227], a[16:19]
s_add_u32 s20, s20, 0x10000
v_exp_f32_e32 v70, v70
v_mfma_f32_16x16x32_bf16 a[16:19], a[172:175], v[228:231], a[16:19]
ds_read_b128 a[156:159], v11 offset:36864
v_add_f32_e32 v30, v30, v67
v_mfma_f32_16x16x32_bf16 a[20:23], a[168:171], v[232:235], a[20:23]
v_exp_f32_e32 v71, v71
v_mfma_f32_16x16x32_bf16 a[20:23], a[172:175], v[236:239], a[20:23]
buffer_load_dwordx4 v52, s[52:55], s20 offen offset:0 lds
v_mfma_f32_16x16x32_bf16 v[76:79], a[128:131], v[176:179], 0
s_waitcnt lgkmcnt(4)
v_exp_f32_e32 v72, v72
v_mfma_f32_16x16x32_bf16 v[76:79], a[132:135], v[180:183], v[76:79]
ds_read_b64_tr_b16 a[188:189], v3 offset:24576
v_cvt_pk_bf16_f32 v201, v70, v71
v_mfma_f32_16x16x32_bf16 v[76:79], a[136:139], v[184:187], v[76:79]
v_exp_f32_e32 v73, v73
v_mfma_f32_16x16x32_bf16 v[76:79], a[140:143], v[188:191], v[76:79]
ds_read_b64_tr_b16 a[190:191], v3 offset:28672
v_cvt_pk_bf16_f32 v208, v72, v73
v_mfma_f32_16x16x32_bf16 a[24:27], a[168:171], v[240:243], a[24:27]
v_exp_f32_e32 v74, v74
v_mfma_f32_16x16x32_bf16 a[24:27], a[172:175], v[244:247], a[24:27]
ds_read_b128 a[128:131], v8 offset:40960
v_add_f32_e32 v32, v32, v72
v_mfma_f32_16x16x32_bf16 a[28:31], a[168:171], v[248:251], a[28:31]
s_waitcnt lgkmcnt(3)
v_exp_f32_e32 v75, v75
v_mfma_f32_16x16x32_bf16 a[28:31], a[172:175], v[252:255], a[28:31]
v_cvt_pk_bf16_f32 v209, v74, v75
v_add_f32_e32 v32, v32, v73
v_mfma_f32_16x16x32_bf16 v[42:45], v[34:37], v[252:255], v[42:45]
buffer_load_dwordx4 v53, s[52:55], s20 offen offset:1024 lds
v_mfma_f32_16x16x32_bf16 v[64:67], a[144:147], v[128:131], 0
v_exp_f32_e32 v76, v76
v_mfma_f32_16x16x32_bf16 v[64:67], a[148:151], v[132:135], v[64:67]
ds_read_b64_tr_b16 a[160:161], v4 offset:16384
v_add_f32_e32 v32, v32, v74
v_mfma_f32_16x16x32_bf16 v[64:67], a[152:155], v[136:139], v[64:67]
v_exp_f32_e32 v77, v77
v_mfma_f32_16x16x32_bf16 v[64:67], a[156:159], v[140:143], v[64:67]
ds_read_b64_tr_b16 a[162:163], v4 offset:20480
v_cvt_pk_bf16_f32 v216, v76, v77
v_mfma_f32_16x16x32_bf16 a[32:35], a[176:179], v[224:227], a[32:35]
v_exp_f32_e32 v78, v78
v_mfma_f32_16x16x32_bf16 a[32:35], a[180:183], v[228:231], a[32:35]
ds_read_b128 a[132:135], v9 offset:40960
v_add_f32_e32 v32, v32, v75
v_mfma_f32_16x16x32_bf16 a[36:39], a[176:179], v[232:235], a[36:39]
v_exp_f32_e32 v79, v79
v_mfma_f32_16x16x32_bf16 a[36:39], a[180:183], v[236:239], a[36:39]
buffer_load_dwordx4 v54, s[52:55], s20 offen offset:2048 lds
v_mfma_f32_16x16x32_bf16 v[68:71], a[144:147], v[144:147], 0
s_waitcnt lgkmcnt(4)
v_exp_f32_e32 v64, v64
v_mfma_f32_16x16x32_bf16 v[68:71], a[148:151], v[148:151], v[68:71]
ds_read_b64_tr_b16 a[164:165], v4 offset:24576
v_cvt_pk_bf16_f32 v217, v78, v79
v_mfma_f32_16x16x32_bf16 v[68:71], a[152:155], v[152:155], v[68:71]
v_exp_f32_e32 v65, v65
v_mfma_f32_16x16x32_bf16 v[68:71], a[156:159], v[156:159], v[68:71]
ds_read_b64_tr_b16 a[166:167], v4 offset:28672
v_cvt_pk_bf16_f32 v194, v64, v65
v_mfma_f32_16x16x32_bf16 a[40:43], a[176:179], v[240:243], a[40:43]
v_exp_f32_e32 v66, v66
v_mfma_f32_16x16x32_bf16 a[40:43], a[180:183], v[244:247], a[40:43]
ds_read_b128 a[136:139], v10 offset:40960
v_add_f32_e32 v30, v30, v64
v_mfma_f32_16x16x32_bf16 a[44:47], a[176:179], v[248:251], a[44:47]
v_exp_f32_e32 v67, v67
v_mfma_f32_16x16x32_bf16 a[44:47], a[180:183], v[252:255], a[44:47]
buffer_load_dwordx4 v55, s[52:55], s20 offen offset:3072 lds
v_cvt_pk_bf16_f32 v195, v66, v67
v_add_f32_e32 v30, v30, v65
v_mfma_f32_16x16x32_bf16 v[72:75], a[144:147], v[160:163], 0
v_exp_f32_e32 v68, v68
v_mfma_f32_16x16x32_bf16 v[72:75], a[148:151], v[164:167], v[72:75]
ds_read_b64_tr_b16 a[168:169], v5 offset:16384
v_add_f32_e32 v30, v30, v66
v_mfma_f32_16x16x32_bf16 v[72:75], a[152:155], v[168:171], v[72:75]
v_exp_f32_e32 v69, v69
v_mfma_f32_16x16x32_bf16 v[72:75], a[156:159], v[172:175], v[72:75]
ds_read_b64_tr_b16 a[170:171], v5 offset:20480
v_cvt_pk_bf16_f32 v202, v68, v69
v_mfma_f32_16x16x32_bf16 a[48:51], a[184:187], v[224:227], a[48:51]
v_exp_f32_e32 v70, v70
v_mfma_f32_16x16x32_bf16 a[48:51], a[188:191], v[228:231], a[48:51]
ds_read_b128 a[140:143], v11 offset:40960
v_add_f32_e32 v30, v30, v67
v_mfma_f32_16x16x32_bf16 a[52:55], a[184:187], v[232:235], a[52:55]
v_exp_f32_e32 v71, v71
v_mfma_f32_16x16x32_bf16 a[52:55], a[188:191], v[236:239], a[52:55]
buffer_load_dwordx4 a[196:199], v29, s[44:47], s13 offen offset:1024 sc1
v_mfma_f32_16x16x32_bf16 v[76:79], a[144:147], v[176:179], 0
s_waitcnt lgkmcnt(4)
v_exp_f32_e32 v72, v72
v_mfma_f32_16x16x32_bf16 v[76:79], a[148:151], v[180:183], v[76:79]
ds_read_b64_tr_b16 a[172:173], v5 offset:24576
v_cvt_pk_bf16_f32 v203, v70, v71
v_mfma_f32_16x16x32_bf16 v[76:79], a[152:155], v[184:187], v[76:79]
v_exp_f32_e32 v73, v73
v_mfma_f32_16x16x32_bf16 v[76:79], a[156:159], v[188:191], v[76:79]
ds_read_b64_tr_b16 a[174:175], v5 offset:28672
v_cvt_pk_bf16_f32 v210, v72, v73
v_mfma_f32_16x16x32_bf16 a[56:59], a[184:187], v[240:243], a[56:59]
v_exp_f32_e32 v74, v74
v_mfma_f32_16x16x32_bf16 a[56:59], a[188:191], v[244:247], a[56:59]
ds_read_b128 a[144:147], v8 offset:45056
v_add_f32_e32 v32, v32, v72
v_mfma_f32_16x16x32_bf16 a[60:63], a[184:187], v[248:251], a[60:63]
s_waitcnt lgkmcnt(3)
v_exp_f32_e32 v75, v75
v_mfma_f32_16x16x32_bf16 a[60:63], a[188:191], v[252:255], a[60:63]
buffer_load_dwordx4 a[200:203], v29, s[44:47], s14 offen offset:0 sc1
v_cvt_pk_bf16_f32 v211, v74, v75
v_add_f32_e32 v32, v32, v73
v_mfma_f32_16x16x32_bf16 v[64:67], a[128:131], v[128:131], 0
v_exp_f32_e32 v76, v76
v_mfma_f32_16x16x32_bf16 v[64:67], a[132:135], v[132:135], v[64:67]
ds_read_b64_tr_b16 a[176:177], v6 offset:16384
v_add_f32_e32 v32, v32, v74
v_mfma_f32_16x16x32_bf16 v[64:67], a[136:139], v[136:139], v[64:67]
v_exp_f32_e32 v77, v77
v_mfma_f32_16x16x32_bf16 v[64:67], a[140:143], v[140:143], v[64:67]
ds_read_b64_tr_b16 a[178:179], v6 offset:20480
v_cvt_pk_bf16_f32 v218, v76, v77
v_mfma_f32_16x16x32_bf16 a[64:67], a[160:163], v[224:227], a[64:67]
v_exp_f32_e32 v78, v78
v_mfma_f32_16x16x32_bf16 a[64:67], a[164:167], v[228:231], a[64:67]
ds_read_b128 a[148:151], v9 offset:45056
v_add_f32_e32 v32, v32, v75
v_mfma_f32_16x16x32_bf16 a[68:71], a[160:163], v[232:235], a[68:71]
v_exp_f32_e32 v79, v79
v_mfma_f32_16x16x32_bf16 a[68:71], a[164:167], v[236:239], a[68:71]
buffer_load_dwordx4 a[204:207], v29, s[44:47], s14 offen offset:1024 sc1
v_mfma_f32_16x16x32_bf16 v[68:71], a[128:131], v[144:147], 0
s_waitcnt lgkmcnt(4)
v_exp_f32_e32 v64, v64
v_mfma_f32_16x16x32_bf16 v[68:71], a[132:135], v[148:151], v[68:71]
ds_read_b64_tr_b16 a[180:181], v6 offset:24576
v_cvt_pk_bf16_f32 v219, v78, v79
v_mfma_f32_16x16x32_bf16 v[68:71], a[136:139], v[152:155], v[68:71]
v_exp_f32_e32 v65, v65
v_mfma_f32_16x16x32_bf16 v[68:71], a[140:143], v[156:159], v[68:71]
ds_read_b64_tr_b16 a[182:183], v6 offset:28672
v_cvt_pk_bf16_f32 v196, v64, v65
v_mfma_f32_16x16x32_bf16 a[72:75], a[160:163], v[240:243], a[72:75]
v_exp_f32_e32 v66, v66
v_mfma_f32_16x16x32_bf16 a[72:75], a[164:167], v[244:247], a[72:75]
ds_read_b128 a[152:155], v10 offset:45056
v_add_f32_e32 v30, v30, v64
v_mfma_f32_16x16x32_bf16 a[76:79], a[160:163], v[248:251], a[76:79]
v_exp_f32_e32 v67, v67
v_mfma_f32_16x16x32_bf16 a[76:79], a[164:167], v[252:255], a[76:79]
buffer_load_dwordx4 a[208:211], v29, s[44:47], s13 offen offset:2048 sc1
v_cvt_pk_bf16_f32 v197, v66, v67
v_add_f32_e32 v30, v30, v65
v_mfma_f32_16x16x32_bf16 v[72:75], a[128:131], v[160:163], 0
v_exp_f32_e32 v68, v68
v_mfma_f32_16x16x32_bf16 v[72:75], a[132:135], v[164:167], v[72:75]
ds_read_b64_tr_b16 a[184:185], v7 offset:16384
v_add_f32_e32 v30, v30, v66
v_mfma_f32_16x16x32_bf16 v[72:75], a[136:139], v[168:171], v[72:75]
v_exp_f32_e32 v69, v69
v_mfma_f32_16x16x32_bf16 v[72:75], a[140:143], v[172:175], v[72:75]
ds_read_b64_tr_b16 a[186:187], v7 offset:20480
v_cvt_pk_bf16_f32 v204, v68, v69
v_mfma_f32_16x16x32_bf16 a[80:83], a[168:171], v[224:227], a[80:83]
v_exp_f32_e32 v70, v70
v_mfma_f32_16x16x32_bf16 a[80:83], a[172:175], v[228:231], a[80:83]
ds_read_b128 a[156:159], v11 offset:45056
v_add_f32_e32 v30, v30, v67
v_mfma_f32_16x16x32_bf16 a[84:87], a[168:171], v[232:235], a[84:87]
v_exp_f32_e32 v71, v71
v_mfma_f32_16x16x32_bf16 a[84:87], a[172:175], v[236:239], a[84:87]
buffer_load_dwordx4 a[212:215], v29, s[44:47], s13 offen offset:3072 sc1
s_add_u32 s13, s13, 0x1000
v_mfma_f32_16x16x32_bf16 v[76:79], a[128:131], v[176:179], 0
s_waitcnt lgkmcnt(4)
v_exp_f32_e32 v72, v72
v_mfma_f32_16x16x32_bf16 v[76:79], a[132:135], v[180:183], v[76:79]
ds_read_b64_tr_b16 a[188:189], v7 offset:24576
v_cvt_pk_bf16_f32 v205, v70, v71
v_mfma_f32_16x16x32_bf16 v[76:79], a[136:139], v[184:187], v[76:79]
v_exp_f32_e32 v73, v73
v_mfma_f32_16x16x32_bf16 v[76:79], a[140:143], v[188:191], v[76:79]
ds_read_b64_tr_b16 a[190:191], v7 offset:28672
v_cvt_pk_bf16_f32 v212, v72, v73
v_mfma_f32_16x16x32_bf16 a[88:91], a[168:171], v[240:243], a[88:91]
v_exp_f32_e32 v74, v74
v_mfma_f32_16x16x32_bf16 a[88:91], a[172:175], v[244:247], a[88:91]
ds_read_b128 a[128:131], v8 offset:49152
v_add_f32_e32 v32, v32, v72
v_mfma_f32_16x16x32_bf16 a[92:95], a[168:171], v[248:251], a[92:95]
s_waitcnt lgkmcnt(3)
v_exp_f32_e32 v75, v75
v_mfma_f32_16x16x32_bf16 a[92:95], a[172:175], v[252:255], a[92:95]
buffer_load_dwordx4 a[216:219], v29, s[44:47], s14 offen offset:2048 sc1
v_cvt_pk_bf16_f32 v213, v74, v75
v_add_f32_e32 v32, v32, v73
v_mfma_f32_16x16x32_bf16 v[64:67], a[144:147], v[128:131], 0
v_exp_f32_e32 v76, v76
v_mfma_f32_16x16x32_bf16 v[64:67], a[148:151], v[132:135], v[64:67]
ds_read_b64_tr_b16 a[160:161], v0 offset:32768
v_add_f32_e32 v32, v32, v74
v_mfma_f32_16x16x32_bf16 v[64:67], a[152:155], v[136:139], v[64:67]
v_exp_f32_e32 v77, v77
v_mfma_f32_16x16x32_bf16 v[64:67], a[156:159], v[140:143], v[64:67]
ds_read_b64_tr_b16 a[162:163], v0 offset:36864
v_cvt_pk_bf16_f32 v220, v76, v77
v_mfma_f32_16x16x32_bf16 a[96:99], a[176:179], v[224:227], a[96:99]
v_exp_f32_e32 v78, v78
v_mfma_f32_16x16x32_bf16 a[96:99], a[180:183], v[228:231], a[96:99]
ds_read_b128 a[132:135], v9 offset:49152
v_add_f32_e32 v32, v32, v75
v_mfma_f32_16x16x32_bf16 a[100:103], a[176:179], v[232:235], a[100:103]
v_exp_f32_e32 v79, v79
v_mfma_f32_16x16x32_bf16 a[100:103], a[180:183], v[236:239], a[100:103]
buffer_load_dwordx4 a[220:223], v29, s[44:47], s14 offen offset:3072 sc1
s_add_u32 s14, s14, 0x1000
v_mfma_f32_16x16x32_bf16 v[68:71], a[144:147], v[144:147], 0
s_waitcnt lgkmcnt(4)
v_exp_f32_e32 v64, v64
v_mfma_f32_16x16x32_bf16 v[68:71], a[148:151], v[148:151], v[68:71]
ds_read_b64_tr_b16 a[164:165], v0 offset:40960
v_cvt_pk_bf16_f32 v221, v78, v79
v_mfma_f32_16x16x32_bf16 v[68:71], a[152:155], v[152:155], v[68:71]
s_waitcnt vmcnt(28)
v_exp_f32_e32 v65, v65
v_mfma_f32_16x16x32_bf16 v[68:71], a[156:159], v[156:159], v[68:71]
ds_read_b64_tr_b16 a[166:167], v0 offset:45056
v_cvt_pk_bf16_f32 v198, v64, v65
v_mfma_f32_16x16x32_bf16 a[104:107], a[176:179], v[240:243], a[104:107]
s_barrier
v_exp_f32_e32 v66, v66
v_mfma_f32_16x16x32_bf16 a[104:107], a[180:183], v[244:247], a[104:107]
ds_read_b128 a[136:139], v10 offset:49152
v_add_f32_e32 v30, v30, v64
v_mfma_f32_16x16x32_bf16 a[108:111], a[176:179], v[248:251], a[108:111]
v_exp_f32_e32 v67, v67
v_mfma_f32_16x16x32_bf16 a[108:111], a[180:183], v[252:255], a[108:111]
v_cvt_pk_bf16_f32 v199, v66, v67
v_add_f32_e32 v30, v30, v65
v_mfma_f32_16x16x32_bf16 v[38:41], v[34:37], v[200:203], v[38:41]
buffer_load_dwordx4 a[224:227], v29, s[44:47], s13 offen offset:0 sc1
v_mfma_f32_16x16x32_bf16 v[72:75], a[144:147], v[160:163], 0
v_exp_f32_e32 v68, v68
v_mfma_f32_16x16x32_bf16 v[72:75], a[148:151], v[164:167], v[72:75]
ds_read_b64_tr_b16 a[168:169], v1 offset:32768
v_add_f32_e32 v30, v30, v66
v_mfma_f32_16x16x32_bf16 v[72:75], a[152:155], v[168:171], v[72:75]
s_add_u32 m0, s68, 0x8000
v_exp_f32_e32 v69, v69
v_mfma_f32_16x16x32_bf16 v[72:75], a[156:159], v[172:175], v[72:75]
ds_read_b64_tr_b16 a[170:171], v1 offset:36864
v_cvt_pk_bf16_f32 v206, v68, v69
v_mfma_f32_16x16x32_bf16 a[112:115], a[184:187], v[224:227], a[112:115]
s_add_u32 s20, s20, 0x10000
v_exp_f32_e32 v70, v70
v_mfma_f32_16x16x32_bf16 a[112:115], a[188:191], v[228:231], a[112:115]
ds_read_b128 a[140:143], v11 offset:49152
v_add_f32_e32 v30, v30, v67
v_mfma_f32_16x16x32_bf16 a[116:119], a[184:187], v[232:235], a[116:119]
v_exp_f32_e32 v71, v71
v_mfma_f32_16x16x32_bf16 a[116:119], a[188:191], v[236:239], a[116:119]
buffer_load_dwordx4 v52, s[52:55], s20 offen offset:0 lds
v_mfma_f32_16x16x32_bf16 v[76:79], a[144:147], v[176:179], 0
v_exp_f32_e32 v72, v72
v_mfma_f32_16x16x32_bf16 v[76:79], a[148:151], v[180:183], v[76:79]
ds_read_b64_tr_b16 a[172:173], v1 offset:40960
v_cvt_pk_bf16_f32 v207, v70, v71
v_mfma_f32_16x16x32_bf16 v[76:79], a[152:155], v[184:187], v[76:79]
s_waitcnt lgkmcnt(5)
v_exp_f32_e32 v73, v73
v_mfma_f32_16x16x32_bf16 v[76:79], a[156:159], v[188:191], v[76:79]
ds_read_b64_tr_b16 a[174:175], v1 offset:45056
v_cvt_pk_bf16_f32 v214, v72, v73
v_mfma_f32_16x16x32_bf16 a[120:123], a[184:187], v[240:243], a[120:123]
v_exp_f32_e32 v74, v74
v_mfma_f32_16x16x32_bf16 a[120:123], a[188:191], v[244:247], a[120:123]
ds_read_b128 a[144:147], v8 offset:53248
v_add_f32_e32 v32, v32, v72
v_mfma_f32_16x16x32_bf16 a[124:127], a[184:187], v[248:251], a[124:127]
s_waitcnt lgkmcnt(3)
v_exp_f32_e32 v75, v75
v_mfma_f32_16x16x32_bf16 a[124:127], a[188:191], v[252:255], a[124:127]
v_cvt_pk_bf16_f32 v215, v74, v75
v_add_f32_e32 v32, v32, v73
v_mfma_f32_16x16x32_bf16 v[42:45], v[34:37], v[216:219], v[42:45]
buffer_load_dwordx4 v53, s[52:55], s20 offen offset:1024 lds
v_mfma_f32_16x16x32_bf16 v[64:67], a[128:131], v[128:131], 0
v_exp_f32_e32 v76, v76
v_mfma_f32_16x16x32_bf16 v[64:67], a[132:135], v[132:135], v[64:67]
ds_read_b64_tr_b16 a[176:177], v2 offset:32768
v_add_f32_e32 v32, v32, v74
v_mfma_f32_16x16x32_bf16 v[64:67], a[136:139], v[136:139], v[64:67]
v_exp_f32_e32 v77, v77
v_mfma_f32_16x16x32_bf16 v[64:67], a[140:143], v[140:143], v[64:67]
ds_read_b64_tr_b16 a[178:179], v2 offset:36864
v_cvt_pk_bf16_f32 v222, v76, v77
v_mfma_f32_16x16x32_bf16 a[0:3], a[160:163], v[192:195], a[0:3]
v_exp_f32_e32 v78, v78
v_mfma_f32_16x16x32_bf16 a[0:3], a[164:167], v[196:199], a[0:3]
ds_read_b128 a[148:151], v9 offset:53248
v_add_f32_e32 v32, v32, v75
v_mfma_f32_16x16x32_bf16 a[4:7], a[160:163], v[200:203], a[4:7]
v_exp_f32_e32 v79, v79
v_mfma_f32_16x16x32_bf16 a[4:7], a[164:167], v[204:207], a[4:7]
buffer_load_dwordx4 v54, s[52:55], s20 offen offset:2048 lds
v_mfma_f32_16x16x32_bf16 v[68:71], a[128:131], v[144:147], 0
s_waitcnt lgkmcnt(4)
v_exp_f32_e32 v64, v64
v_mfma_f32_16x16x32_bf16 v[68:71], a[132:135], v[148:151], v[68:71]
ds_read_b64_tr_b16 a[180:181], v2 offset:40960
v_cvt_pk_bf16_f32 v223, v78, v79
v_mfma_f32_16x16x32_bf16 v[68:71], a[136:139], v[152:155], v[68:71]
v_exp_f32_e32 v65, v65
v_mfma_f32_16x16x32_bf16 v[68:71], a[140:143], v[156:159], v[68:71]
ds_read_b64_tr_b16 a[182:183], v2 offset:45056
v_cvt_pk_bf16_f32 v224, v64, v65
v_mfma_f32_16x16x32_bf16 a[8:11], a[160:163], v[208:211], a[8:11]
v_exp_f32_e32 v66, v66
v_mfma_f32_16x16x32_bf16 a[8:11], a[164:167], v[212:215], a[8:11]
ds_read_b128 a[152:155], v10 offset:53248
v_add_f32_e32 v30, v30, v64
v_mfma_f32_16x16x32_bf16 a[12:15], a[160:163], v[216:219], a[12:15]
v_exp_f32_e32 v67, v67
v_mfma_f32_16x16x32_bf16 a[12:15], a[164:167], v[220:223], a[12:15]
v_cvt_pk_bf16_f32 v225, v66, v67
v_add_f32_e32 v30, v30, v65
v_mfma_f32_16x16x32_bf16 v[38:41], v[34:37], v[204:207], v[38:41]
buffer_load_dwordx4 v55, s[52:55], s20 offen offset:3072 lds
v_mfma_f32_16x16x32_bf16 v[72:75], a[128:131], v[160:163], 0
v_exp_f32_e32 v68, v68
v_mfma_f32_16x16x32_bf16 v[72:75], a[132:135], v[164:167], v[72:75]
ds_read_b64_tr_b16 a[184:185], v3 offset:32768
v_add_f32_e32 v30, v30, v66
v_mfma_f32_16x16x32_bf16 v[72:75], a[136:139], v[168:171], v[72:75]
s_add_u32 m0, s68, 0x14000
v_exp_f32_e32 v69, v69
v_mfma_f32_16x16x32_bf16 v[72:75], a[140:143], v[172:175], v[72:75]
ds_read_b64_tr_b16 a[186:187], v3 offset:36864
v_cvt_pk_bf16_f32 v232, v68, v69
v_mfma_f32_16x16x32_bf16 a[16:19], a[168:171], v[192:195], a[16:19]
s_add_u32 s20, s20, 0x10000
v_exp_f32_e32 v70, v70
v_mfma_f32_16x16x32_bf16 a[16:19], a[172:175], v[196:199], a[16:19]
ds_read_b128 a[156:159], v11 offset:53248
v_add_f32_e32 v30, v30, v67
v_mfma_f32_16x16x32_bf16 a[20:23], a[168:171], v[200:203], a[20:23]
v_exp_f32_e32 v71, v71
v_mfma_f32_16x16x32_bf16 a[20:23], a[172:175], v[204:207], a[20:23]
buffer_load_dwordx4 v52, s[52:55], s20 offen offset:0 lds
v_mfma_f32_16x16x32_bf16 v[76:79], a[128:131], v[176:179], 0
s_waitcnt lgkmcnt(4)
v_exp_f32_e32 v72, v72
v_mfma_f32_16x16x32_bf16 v[76:79], a[132:135], v[180:183], v[76:79]
ds_read_b64_tr_b16 a[188:189], v3 offset:40960
v_cvt_pk_bf16_f32 v233, v70, v71
v_mfma_f32_16x16x32_bf16 v[76:79], a[136:139], v[184:187], v[76:79]
v_exp_f32_e32 v73, v73
v_mfma_f32_16x16x32_bf16 v[76:79], a[140:143], v[188:191], v[76:79]
ds_read_b64_tr_b16 a[190:191], v3 offset:45056
v_cvt_pk_bf16_f32 v240, v72, v73
v_mfma_f32_16x16x32_bf16 a[24:27], a[168:171], v[208:211], a[24:27]
v_exp_f32_e32 v74, v74
v_mfma_f32_16x16x32_bf16 a[24:27], a[172:175], v[212:215], a[24:27]
ds_read_b128 a[128:131], v8 offset:57344
v_add_f32_e32 v32, v32, v72
v_mfma_f32_16x16x32_bf16 a[28:31], a[168:171], v[216:219], a[28:31]
s_waitcnt lgkmcnt(3)
v_exp_f32_e32 v75, v75
v_mfma_f32_16x16x32_bf16 a[28:31], a[172:175], v[220:223], a[28:31]
v_cvt_pk_bf16_f32 v241, v74, v75
v_add_f32_e32 v32, v32, v73
v_mfma_f32_16x16x32_bf16 v[42:45], v[34:37], v[220:223], v[42:45]
buffer_load_dwordx4 v53, s[52:55], s20 offen offset:1024 lds
v_mfma_f32_16x16x32_bf16 v[64:67], a[144:147], v[128:131], 0
v_exp_f32_e32 v76, v76
v_mfma_f32_16x16x32_bf16 v[64:67], a[148:151], v[132:135], v[64:67]
ds_read_b64_tr_b16 a[160:161], v4 offset:32768
v_add_f32_e32 v32, v32, v74
v_mfma_f32_16x16x32_bf16 v[64:67], a[152:155], v[136:139], v[64:67]
v_exp_f32_e32 v77, v77
v_mfma_f32_16x16x32_bf16 v[64:67], a[156:159], v[140:143], v[64:67]
ds_read_b64_tr_b16 a[162:163], v4 offset:36864
v_cvt_pk_bf16_f32 v248, v76, v77
v_mfma_f32_16x16x32_bf16 a[32:35], a[176:179], v[192:195], a[32:35]
v_exp_f32_e32 v78, v78
v_mfma_f32_16x16x32_bf16 a[32:35], a[180:183], v[196:199], a[32:35]
ds_read_b128 a[132:135], v9 offset:57344
v_add_f32_e32 v32, v32, v75
v_mfma_f32_16x16x32_bf16 a[36:39], a[176:179], v[200:203], a[36:39]
v_exp_f32_e32 v79, v79
v_mfma_f32_16x16x32_bf16 a[36:39], a[180:183], v[204:207], a[36:39]
buffer_load_dwordx4 v54, s[52:55], s20 offen offset:2048 lds
v_mfma_f32_16x16x32_bf16 v[68:71], a[144:147], v[144:147], 0
s_waitcnt lgkmcnt(4)
v_exp_f32_e32 v64, v64
v_mfma_f32_16x16x32_bf16 v[68:71], a[148:151], v[148:151], v[68:71]
ds_read_b64_tr_b16 a[164:165], v4 offset:40960
v_cvt_pk_bf16_f32 v249, v78, v79
v_mfma_f32_16x16x32_bf16 v[68:71], a[152:155], v[152:155], v[68:71]
v_exp_f32_e32 v65, v65
v_mfma_f32_16x16x32_bf16 v[68:71], a[156:159], v[156:159], v[68:71]
ds_read_b64_tr_b16 a[166:167], v4 offset:45056
v_cvt_pk_bf16_f32 v226, v64, v65
v_mfma_f32_16x16x32_bf16 a[40:43], a[176:179], v[208:211], a[40:43]
v_exp_f32_e32 v66, v66
v_mfma_f32_16x16x32_bf16 a[40:43], a[180:183], v[212:215], a[40:43]
ds_read_b128 a[136:139], v10 offset:57344
v_add_f32_e32 v30, v30, v64
v_mfma_f32_16x16x32_bf16 a[44:47], a[176:179], v[216:219], a[44:47]
v_exp_f32_e32 v67, v67
v_mfma_f32_16x16x32_bf16 a[44:47], a[180:183], v[220:223], a[44:47]
buffer_load_dwordx4 v55, s[52:55], s20 offen offset:3072 lds
v_cvt_pk_bf16_f32 v227, v66, v67
v_add_f32_e32 v30, v30, v65
v_mfma_f32_16x16x32_bf16 v[72:75], a[144:147], v[160:163], 0
v_exp_f32_e32 v68, v68
v_mfma_f32_16x16x32_bf16 v[72:75], a[148:151], v[164:167], v[72:75]
ds_read_b64_tr_b16 a[168:169], v5 offset:32768
v_add_f32_e32 v30, v30, v66
v_mfma_f32_16x16x32_bf16 v[72:75], a[152:155], v[168:171], v[72:75]
v_exp_f32_e32 v69, v69
v_mfma_f32_16x16x32_bf16 v[72:75], a[156:159], v[172:175], v[72:75]
ds_read_b64_tr_b16 a[170:171], v5 offset:36864
v_cvt_pk_bf16_f32 v234, v68, v69
v_mfma_f32_16x16x32_bf16 a[48:51], a[184:187], v[192:195], a[48:51]
v_exp_f32_e32 v70, v70
v_mfma_f32_16x16x32_bf16 a[48:51], a[188:191], v[196:199], a[48:51]
ds_read_b128 a[140:143], v11 offset:57344
v_add_f32_e32 v30, v30, v67
v_mfma_f32_16x16x32_bf16 a[52:55], a[184:187], v[200:203], a[52:55]
v_exp_f32_e32 v71, v71
v_mfma_f32_16x16x32_bf16 a[52:55], a[188:191], v[204:207], a[52:55]
buffer_load_dwordx4 a[228:231], v29, s[44:47], s13 offen offset:1024 sc1
v_mfma_f32_16x16x32_bf16 v[76:79], a[144:147], v[176:179], 0
s_waitcnt lgkmcnt(4)
v_exp_f32_e32 v72, v72
v_mfma_f32_16x16x32_bf16 v[76:79], a[148:151], v[180:183], v[76:79]
ds_read_b64_tr_b16 a[172:173], v5 offset:40960
v_cvt_pk_bf16_f32 v235, v70, v71
v_mfma_f32_16x16x32_bf16 v[76:79], a[152:155], v[184:187], v[76:79]
v_exp_f32_e32 v73, v73
v_mfma_f32_16x16x32_bf16 v[76:79], a[156:159], v[188:191], v[76:79]
ds_read_b64_tr_b16 a[174:175], v5 offset:45056
v_cvt_pk_bf16_f32 v242, v72, v73
v_mfma_f32_16x16x32_bf16 a[56:59], a[184:187], v[208:211], a[56:59]
v_exp_f32_e32 v74, v74
v_mfma_f32_16x16x32_bf16 a[56:59], a[188:191], v[212:215], a[56:59]
ds_read_b128 a[144:147], v8 offset:61440
v_add_f32_e32 v32, v32, v72
v_mfma_f32_16x16x32_bf16 a[60:63], a[184:187], v[216:219], a[60:63]
s_waitcnt lgkmcnt(3)
v_exp_f32_e32 v75, v75
v_mfma_f32_16x16x32_bf16 a[60:63], a[188:191], v[220:223], a[60:63]
buffer_load_dwordx4 a[232:235], v29, s[44:47], s14 offen offset:0 sc1
v_cvt_pk_bf16_f32 v243, v74, v75
v_add_f32_e32 v32, v32, v73
v_mfma_f32_16x16x32_bf16 v[64:67], a[128:131], v[128:131], 0
v_exp_f32_e32 v76, v76
v_mfma_f32_16x16x32_bf16 v[64:67], a[132:135], v[132:135], v[64:67]
ds_read_b64_tr_b16 a[176:177], v6 offset:32768
v_add_f32_e32 v32, v32, v74
v_mfma_f32_16x16x32_bf16 v[64:67], a[136:139], v[136:139], v[64:67]
v_exp_f32_e32 v77, v77
v_mfma_f32_16x16x32_bf16 v[64:67], a[140:143], v[140:143], v[64:67]
ds_read_b64_tr_b16 a[178:179], v6 offset:36864
v_cvt_pk_bf16_f32 v250, v76, v77
v_mfma_f32_16x16x32_bf16 a[64:67], a[160:163], v[192:195], a[64:67]
v_exp_f32_e32 v78, v78
v_mfma_f32_16x16x32_bf16 a[64:67], a[164:167], v[196:199], a[64:67]
ds_read_b128 a[148:151], v9 offset:61440
v_add_f32_e32 v32, v32, v75
v_mfma_f32_16x16x32_bf16 a[68:71], a[160:163], v[200:203], a[68:71]
v_exp_f32_e32 v79, v79
v_mfma_f32_16x16x32_bf16 a[68:71], a[164:167], v[204:207], a[68:71]
buffer_load_dwordx4 a[236:239], v29, s[44:47], s14 offen offset:1024 sc1
v_mfma_f32_16x16x32_bf16 v[68:71], a[128:131], v[144:147], 0
s_waitcnt lgkmcnt(4)
v_exp_f32_e32 v64, v64
v_mfma_f32_16x16x32_bf16 v[68:71], a[132:135], v[148:151], v[68:71]
ds_read_b64_tr_b16 a[180:181], v6 offset:40960
v_cvt_pk_bf16_f32 v251, v78, v79
v_mfma_f32_16x16x32_bf16 v[68:71], a[136:139], v[152:155], v[68:71]
v_exp_f32_e32 v65, v65
v_mfma_f32_16x16x32_bf16 v[68:71], a[140:143], v[156:159], v[68:71]
ds_read_b64_tr_b16 a[182:183], v6 offset:45056
v_cvt_pk_bf16_f32 v228, v64, v65
v_mfma_f32_16x16x32_bf16 a[72:75], a[160:163], v[208:211], a[72:75]
v_exp_f32_e32 v66, v66
v_mfma_f32_16x16x32_bf16 a[72:75], a[164:167], v[212:215], a[72:75]
ds_read_b128 a[152:155], v10 offset:61440
v_add_f32_e32 v30, v30, v64
v_mfma_f32_16x16x32_bf16 a[76:79], a[160:163], v[216:219], a[76:79]
v_exp_f32_e32 v67, v67
v_mfma_f32_16x16x32_bf16 a[76:79], a[164:167], v[220:223], a[76:79]
buffer_load_dwordx4 a[240:243], v29, s[44:47], s13 offen offset:2048 sc1
v_cvt_pk_bf16_f32 v229, v66, v67
v_add_f32_e32 v30, v30, v65
v_mfma_f32_16x16x32_bf16 v[72:75], a[128:131], v[160:163], 0
v_exp_f32_e32 v68, v68
v_mfma_f32_16x16x32_bf16 v[72:75], a[132:135], v[164:167], v[72:75]
ds_read_b64_tr_b16 a[184:185], v7 offset:32768
v_add_f32_e32 v30, v30, v66
v_mfma_f32_16x16x32_bf16 v[72:75], a[136:139], v[168:171], v[72:75]
v_exp_f32_e32 v69, v69
v_mfma_f32_16x16x32_bf16 v[72:75], a[140:143], v[172:175], v[72:75]
ds_read_b64_tr_b16 a[186:187], v7 offset:36864
v_cvt_pk_bf16_f32 v236, v68, v69
v_mfma_f32_16x16x32_bf16 a[80:83], a[168:171], v[192:195], a[80:83]
v_exp_f32_e32 v70, v70
v_mfma_f32_16x16x32_bf16 a[80:83], a[172:175], v[196:199], a[80:83]
ds_read_b128 a[156:159], v11 offset:61440
v_add_f32_e32 v30, v30, v67
v_mfma_f32_16x16x32_bf16 a[84:87], a[168:171], v[200:203], a[84:87]
v_exp_f32_e32 v71, v71
v_mfma_f32_16x16x32_bf16 a[84:87], a[172:175], v[204:207], a[84:87]
buffer_load_dwordx4 a[244:247], v29, s[44:47], s13 offen offset:3072 sc1
s_add_u32 s13, s13, 0x1000
v_mfma_f32_16x16x32_bf16 v[76:79], a[128:131], v[176:179], 0
s_waitcnt lgkmcnt(4)
v_exp_f32_e32 v72, v72
v_mfma_f32_16x16x32_bf16 v[76:79], a[132:135], v[180:183], v[76:79]
ds_read_b64_tr_b16 a[188:189], v7 offset:40960
v_cvt_pk_bf16_f32 v237, v70, v71
v_mfma_f32_16x16x32_bf16 v[76:79], a[136:139], v[184:187], v[76:79]
v_exp_f32_e32 v73, v73
v_mfma_f32_16x16x32_bf16 v[76:79], a[140:143], v[188:191], v[76:79]
ds_read_b64_tr_b16 a[190:191], v7 offset:45056
v_cvt_pk_bf16_f32 v244, v72, v73
v_mfma_f32_16x16x32_bf16 a[88:91], a[168:171], v[208:211], a[88:91]
v_exp_f32_e32 v74, v74
v_mfma_f32_16x16x32_bf16 a[88:91], a[172:175], v[212:215], a[88:91]
v_add_f32_e32 v32, v32, v72
v_mfma_f32_16x16x32_bf16 a[92:95], a[168:171], v[216:219], a[92:95]
s_waitcnt lgkmcnt(2)
v_exp_f32_e32 v75, v75
v_mfma_f32_16x16x32_bf16 a[92:95], a[172:175], v[220:223], a[92:95]
buffer_load_dwordx4 a[248:251], v29, s[44:47], s14 offen offset:2048 sc1
v_cvt_pk_bf16_f32 v245, v74, v75
v_add_f32_e32 v32, v32, v73
v_mfma_f32_16x16x32_bf16 v[64:67], a[144:147], v[128:131], 0
v_exp_f32_e32 v76, v76
v_mfma_f32_16x16x32_bf16 v[64:67], a[148:151], v[132:135], v[64:67]
ds_read_b64_tr_b16 a[160:161], v0 offset:49152
v_add_f32_e32 v32, v32, v74
v_mfma_f32_16x16x32_bf16 v[64:67], a[152:155], v[136:139], v[64:67]
v_exp_f32_e32 v77, v77
v_mfma_f32_16x16x32_bf16 v[64:67], a[156:159], v[140:143], v[64:67]
ds_read_b64_tr_b16 a[162:163], v0 offset:53248
v_cvt_pk_bf16_f32 v252, v76, v77
v_mfma_f32_16x16x32_bf16 a[96:99], a[176:179], v[192:195], a[96:99]
v_exp_f32_e32 v78, v78
v_mfma_f32_16x16x32_bf16 a[96:99], a[180:183], v[196:199], a[96:99]
v_add_f32_e32 v32, v32, v75
v_mfma_f32_16x16x32_bf16 a[100:103], a[176:179], v[200:203], a[100:103]
v_exp_f32_e32 v79, v79
v_mfma_f32_16x16x32_bf16 a[100:103], a[180:183], v[204:207], a[100:103]
buffer_load_dwordx4 a[252:255], v29, s[44:47], s14 offen offset:3072 sc1
s_add_u32 s14, s14, 0x1000
v_mfma_f32_16x16x32_bf16 v[68:71], a[144:147], v[144:147], 0
s_waitcnt lgkmcnt(2)
v_exp_f32_e32 v64, v64
v_mfma_f32_16x16x32_bf16 v[68:71], a[148:151], v[148:151], v[68:71]
ds_read_b64_tr_b16 a[164:165], v0 offset:57344
v_cvt_pk_bf16_f32 v253, v78, v79
v_mfma_f32_16x16x32_bf16 v[68:71], a[152:155], v[152:155], v[68:71]
s_waitcnt vmcnt(23)
v_exp_f32_e32 v65, v65
v_mfma_f32_16x16x32_bf16 v[68:71], a[156:159], v[156:159], v[68:71]
ds_read_b64_tr_b16 a[166:167], v0 offset:61440
v_cvt_pk_bf16_f32 v230, v64, v65
v_mfma_f32_16x16x32_bf16 a[104:107], a[176:179], v[208:211], a[104:107]
s_barrier
v_exp_f32_e32 v66, v66
v_mfma_f32_16x16x32_bf16 a[104:107], a[180:183], v[212:215], a[104:107]
v_add_f32_e32 v30, v30, v64
v_mfma_f32_16x16x32_bf16 a[108:111], a[176:179], v[216:219], a[108:111]
v_exp_f32_e32 v67, v67
v_mfma_f32_16x16x32_bf16 a[108:111], a[180:183], v[220:223], a[108:111]
v_cvt_pk_bf16_f32 v231, v66, v67
v_add_f32_e32 v30, v30, v65
v_mfma_f32_16x16x32_bf16 v[38:41], v[34:37], v[232:235], v[38:41]
buffer_load_dwordx4 v[80:83], v29, s[44:47], s13 offen offset:0 sc1
v_mfma_f32_16x16x32_bf16 v[72:75], a[144:147], v[160:163], 0
v_exp_f32_e32 v68, v68
v_mfma_f32_16x16x32_bf16 v[72:75], a[148:151], v[164:167], v[72:75]
ds_read_b64_tr_b16 a[168:169], v1 offset:49152
v_add_f32_e32 v30, v30, v66
v_mfma_f32_16x16x32_bf16 v[72:75], a[152:155], v[168:171], v[72:75]
v_exp_f32_e32 v69, v69
v_mfma_f32_16x16x32_bf16 v[72:75], a[156:159], v[172:175], v[72:75]
ds_read_b64_tr_b16 a[170:171], v1 offset:53248
v_cvt_pk_bf16_f32 v238, v68, v69
v_mfma_f32_16x16x32_bf16 a[112:115], a[184:187], v[192:195], a[112:115]
v_exp_f32_e32 v70, v70
v_mfma_f32_16x16x32_bf16 a[112:115], a[188:191], v[196:199], a[112:115]
v_add_f32_e32 v30, v30, v67
v_mfma_f32_16x16x32_bf16 a[116:119], a[184:187], v[200:203], a[116:119]
v_exp_f32_e32 v71, v71
v_mfma_f32_16x16x32_bf16 a[116:119], a[188:191], v[204:207], a[116:119]
buffer_load_dwordx4 v[84:87], v29, s[44:47], s13 offen offset:1024 sc1
v_mfma_f32_16x16x32_bf16 v[76:79], a[144:147], v[176:179], 0
v_exp_f32_e32 v72, v72
v_mfma_f32_16x16x32_bf16 v[76:79], a[148:151], v[180:183], v[76:79]
ds_read_b64_tr_b16 a[172:173], v1 offset:57344
v_cvt_pk_bf16_f32 v239, v70, v71
v_mfma_f32_16x16x32_bf16 v[76:79], a[152:155], v[184:187], v[76:79]
s_waitcnt lgkmcnt(3)
v_exp_f32_e32 v73, v73
v_mfma_f32_16x16x32_bf16 v[76:79], a[156:159], v[188:191], v[76:79]
ds_read_b64_tr_b16 a[174:175], v1 offset:61440
v_cvt_pk_bf16_f32 v246, v72, v73
v_mfma_f32_16x16x32_bf16 a[120:123], a[184:187], v[208:211], a[120:123]
v_exp_f32_e32 v74, v74
v_mfma_f32_16x16x32_bf16 a[120:123], a[188:191], v[212:215], a[120:123]
v_add_f32_e32 v32, v32, v72
v_mfma_f32_16x16x32_bf16 a[124:127], a[184:187], v[216:219], a[124:127]
v_exp_f32_e32 v75, v75
v_mfma_f32_16x16x32_bf16 a[124:127], a[188:191], v[220:223], a[124:127]
v_cvt_pk_bf16_f32 v247, v74, v75
v_add_f32_e32 v32, v32, v73
v_mfma_f32_16x16x32_bf16 v[42:45], v[34:37], v[248:251], v[42:45]
buffer_load_dwordx4 v[88:91], v29, s[44:47], s14 offen offset:0 sc1
; ---- tailA
v_exp_f32_e32 v76, v76
ds_read_b64_tr_b16 a[176:177], v2 offset:49152
v_add_f32_e32 v32, v32, v74
v_exp_f32_e32 v77, v77
ds_read_b64_tr_b16 a[178:179], v2 offset:53248
v_cvt_pk_bf16_f32 v254, v76, v77
v_mfma_f32_16x16x32_bf16 a[0:3], a[160:163], v[224:227], a[0:3]
v_exp_f32_e32 v78, v78
v_mfma_f32_16x16x32_bf16 a[0:3], a[164:167], v[228:231], a[0:3]
v_add_f32_e32 v32, v32, v75
v_mfma_f32_16x16x32_bf16 a[4:7], a[160:163], v[232:235], a[4:7]
v_exp_f32_e32 v79, v79
v_mfma_f32_16x16x32_bf16 a[4:7], a[164:167], v[236:239], a[4:7]
s_waitcnt lgkmcnt(2)
ds_read_b64_tr_b16 a[180:181], v2 offset:57344
v_cvt_pk_bf16_f32 v255, v78, v79
ds_read_b64_tr_b16 a[182:183], v2 offset:61440
v_mfma_f32_16x16x32_bf16 a[8:11], a[160:163], v[240:243], a[8:11]
v_mfma_f32_16x16x32_bf16 a[8:11], a[164:167], v[244:247], a[8:11]
v_mfma_f32_16x16x32_bf16 a[12:15], a[160:163], v[248:251], a[12:15]
v_mfma_f32_16x16x32_bf16 a[12:15], a[164:167], v[252:255], a[12:15]
v_mfma_f32_16x16x32_bf16 v[38:41], v[34:37], v[236:239], v[38:41]
v_mfma_f32_16x16x32_bf16 v[42:45], v[34:37], v[252:255], v[42:45]
v_mfma_f32_16x16x32_bf16 a[16:19], a[168:171], v[224:227], a[16:19]
ds_read_b64_tr_b16 a[184:185], v3 offset:49152
s_nop 3
v_mov_b32_e32 v64, v30
v_mov_b32_e32 v65, v38
v_mov_b32_e32 v66, v32
v_mov_b32_e32 v67, v42
v_mov_b32_e32 v68, v64
v_mov_b32_e32 v69, v65
v_mov_b32_e32 v70, v66
v_mfma_f32_16x16x32_bf16 a[16:19], a[172:175], v[228:231], a[16:19]
buffer_load_dwordx4 v[92:95], v29, s[44:47], s14 offen offset:1024 sc1
v_mov_b32_e32 v71, v67
v_permlane16_swap_b32_e32 v64, v68
v_permlane16_swap_b32_e32 v65, v69
v_permlane16_swap_b32_e32 v66, v70
v_permlane16_swap_b32_e32 v67, v71
v_add_f32_e32 v64, v64, v68
v_add_f32_e32 v65, v65, v69
v_add_f32_e32 v66, v66, v70
v_mfma_f32_16x16x32_bf16 a[20:23], a[168:171], v[232:235], a[20:23]
ds_read_b64_tr_b16 a[186:187], v3 offset:53248
v_add_f32_e32 v67, v67, v71
v_mov_b32_e32 v68, v64
v_mov_b32_e32 v69, v65
v_mov_b32_e32 v70, v66
v_mov_b32_e32 v71, v67
v_permlane32_swap_b32_e32 v64, v68
v_permlane32_swap_b32_e32 v65, v69
v_permlane32_swap_b32_e32 v66, v70
v_mfma_f32_16x16x32_bf16 a[20:23], a[172:175], v[236:239], a[20:23]
buffer_load_dwordx4 v[96:99], v29, s[44:47], s13 offen offset:2048 sc1
v_permlane32_swap_b32_e32 v67, v71
v_add_f32_e32 v64, v64, v68
v_add_f32_e32 v65, v65, v69
v_add_f32_e32 v66, v66, v70
v_add_f32_e32 v67, v67, v71
s_waitcnt lgkmcnt(2)
v_mfma_f32_16x16x32_bf16 a[24:27], a[168:171], v[240:243], a[24:27]
ds_read_b64_tr_b16 a[188:189], v3 offset:57344
s_mov_b32 s79, 0x1f800000
s_mov_b32 s80, 0x67800000
v_min3_f32 v68, v64, v65, v66
v_max3_f32 v69, v64, v65, v66
v_mfma_f32_16x16x32_bf16 a[24:27], a[172:175], v[244:247], a[24:27]
v_add_f32_e32 v70, v64, v65
v_add_f32_e32 v71, v66, v67
v_min_f32_e32 v68, v68, v67
v_max_f32_e32 v69, v69, v67
v_mfma_f32_16x16x32_bf16 a[28:31], a[168:171], v[248:251], a[28:31]
ds_read_b64_tr_b16 a[190:191], v3 offset:61440
v_add_f32_e32 v70, v70, v71
v_cmp_ge_f32_e64 s[90:91], v68, s79
v_cmp_le_f32_e64 s[92:93], v69, s80
v_cmp_o_f32_e64 s[84:85], v70, v70
v_mfma_f32_16x16x32_bf16 a[28:31], a[172:175], v[252:255], a[28:31]
s_and_b64 s[90:91], s[90:91], s[92:93]
s_and_b64 s[90:91], s[90:91], s[84:85]
s_cmp_eq_u64 s[90:91], -1
s_cbranch_scc0 .Lkf_slow
; ---- tailB
v_mfma_f32_16x16x32_bf16 a[32:35], a[176:179], v[224:227], a[32:35]
ds_read_b64_tr_b16 a[160:161], v4 offset:49152
v_rcp_f32_e32 v34, v64
v_rcp_f32_e32 v36, v65
v_rcp_f32_e32 v38, v66
v_rcp_f32_e32 v40, v67
s_mov_b32 s90, 0
s_mov_b32 s91, 0x20000
s_mov_b32 s92, 0x40000
v_mfma_f32_16x16x32_bf16 a[32:35], a[180:183], v[228:231], a[32:35]
buffer_load_dwordx4 v[100:103], v29, s[44:47], s13 offen offset:3072 sc1
s_add_u32 s13, s13, 0x1000
s_mov_b32 s93, 0x60000
v_accvgpr_read_b32 v128, a0
v_accvgpr_read_b32 v129, a1
v_accvgpr_read_b32 v130, a2
v_accvgpr_read_b32 v131, a3
v_accvgpr_read_b32 v132, a16
v_accvgpr_read_b32 v133, a17
v_mfma_f32_16x16x32_bf16 a[36:39], a[176:179], v[232:235], a[36:39]
ds_read_b64_tr_b16 a[162:163], v4 offset:53248
v_accvgpr_read_b32 v134, a18
v_accvgpr_read_b32 v135, a19
v_mul_f32_e32 v128, v128, v34
v_mul_f32_e32 v129, v129, v34
v_mul_f32_e32 v130, v130, v34
v_mul_f32_e32 v131, v131, v34
v_mul_f32_e32 v132, v132, v34
v_mfma_f32_16x16x32_bf16 a[36:39], a[180:183], v[236:239], a[36:39]
buffer_load_dwordx4 v[104:107], v29, s[44:47], s14 offen offset:2048 sc1
v_mul_f32_e32 v133, v133, v34
v_mul_f32_e32 v134, v134, v34
v_mul_f32_e32 v135, v135, v34
v_cvt_pk_bf16_f32 v160, v128, v129
v_cvt_pk_bf16_f32 v161, v130, v131
v_cvt_pk_bf16_f32 v162, v132, v133
v_cvt_pk_bf16_f32 v163, v134, v135
s_waitcnt lgkmcnt(2)
v_mfma_f32_16x16x32_bf16 a[40:43], a[176:179], v[240:243], a[40:43]
ds_read_b64_tr_b16 a[164:165], v4 offset:57344
v_accvgpr_read_b32 v136, a4
v_accvgpr_read_b32 v137, a5
v_accvgpr_read_b32 v138, a6
v_accvgpr_read_b32 v139, a7
v_accvgpr_read_b32 v140, a20
v_mfma_f32_16x16x32_bf16 a[40:43], a[180:183], v[244:247], a[40:43]
buffer_load_dwordx4 v[108:111], v29, s[44:47], s14 offen offset:3072 sc1
s_add_u32 s14, s14, 0x1000
v_accvgpr_read_b32 v141, a21
v_accvgpr_read_b32 v142, a22
v_accvgpr_read_b32 v143, a23
v_mul_f32_e32 v136, v136, v36
v_mul_f32_e32 v137, v137, v36
v_mfma_f32_16x16x32_bf16 a[44:47], a[176:179], v[248:251], a[44:47]
ds_read_b64_tr_b16 a[166:167], v4 offset:61440
v_mul_f32_e32 v138, v138, v36
v_mul_f32_e32 v139, v139, v36
v_mul_f32_e32 v140, v140, v36
v_mul_f32_e32 v141, v141, v36
v_mul_f32_e32 v142, v142, v36
v_mfma_f32_16x16x32_bf16 a[44:47], a[180:183], v[252:255], a[44:47]
buffer_store_dwordx4 v[160:163], v29, s[60:63], s90 offen offset:0 sc1
v_mul_f32_e32 v143, v143, v36
v_cvt_pk_bf16_f32 v164, v136, v137
v_cvt_pk_bf16_f32 v165, v138, v139
v_cvt_pk_bf16_f32 v166, v140, v141
v_cvt_pk_bf16_f32 v167, v142, v143
v_mfma_f32_16x16x32_bf16 a[48:51], a[184:187], v[224:227], a[48:51]
ds_read_b64_tr_b16 a[168:169], v5 offset:49152
v_accvgpr_read_b32 v144, a8
v_accvgpr_read_b32 v145, a9
v_accvgpr_read_b32 v146, a10
v_accvgpr_read_b32 v147, a11
v_accvgpr_read_b32 v148, a24
v_mfma_f32_16x16x32_bf16 a[48:51], a[188:191], v[228:231], a[48:51]
s_add_u32 m0, s68, 0xc000
s_add_u32 s12, s15, s101
s_add_u32 s20, s12, s6
buffer_load_dwordx4 v52, s[56:59], s20 offen offset:0 lds
v_accvgpr_read_b32 v149, a25
v_accvgpr_read_b32 v150, a26
v_accvgpr_read_b32 v151, a27
v_mul_f32_e32 v144, v144, v38
v_mul_f32_e32 v145, v145, v38
v_mfma_f32_16x16x32_bf16 a[52:55], a[184:187], v[232:235], a[52:55]
ds_read_b64_tr_b16 a[170:171], v5 offset:53248
v_mul_f32_e32 v146, v146, v38
v_mul_f32_e32 v147, v147, v38
v_mul_f32_e32 v148, v148, v38
v_mul_f32_e32 v149, v149, v38
v_mul_f32_e32 v150, v150, v38
v_mfma_f32_16x16x32_bf16 a[52:55], a[188:191], v[236:239], a[52:55]
buffer_store_dwordx4 v[164:167], v29, s[60:63], s91 offen offset:0 sc1
v_mul_f32_e32 v151, v151, v38
v_cvt_pk_bf16_f32 v168, v144, v145
v_cvt_pk_bf16_f32 v169, v146, v147
v_cvt_pk_bf16_f32 v170, v148, v149
v_cvt_pk_bf16_f32 v171, v150, v151
s_waitcnt lgkmcnt(2)
v_mfma_f32_16x16x32_bf16 a[56:59], a[184:187], v[240:243], a[56:59]
ds_read_b64_tr_b16 a[172:173], v5 offset:57344
v_accvgpr_read_b32 v152, a12
v_accvgpr_read_b32 v153, a13
v_accvgpr_read_b32 v154, a14
v_accvgpr_read_b32 v155, a15
v_accvgpr_read_b32 v156, a28
v_mfma_f32_16x16x32_bf16 a[56:59], a[188:191], v[244:247], a[56:59]
buffer_load_dwordx4 v53, s[56:59], s20 offen offset:1024 lds
s_add_u32 s6, s6, 0x10000
s_and_b32 s6, s6, 0x7ffff
v_accvgpr_read_b32 v157, a29
v_accvgpr_read_b32 v158, a30
v_accvgpr_read_b32 v159, a31
v_mul_f32_e32 v152, v152, v40
v_mul_f32_e32 v153, v153, v40
v_mfma_f32_16x16x32_bf16 a[60:63], a[184:187], v[248:251], a[60:63]
ds_read_b64_tr_b16 a[174:175], v5 offset:61440
v_mul_f32_e32 v154, v154, v40
v_mul_f32_e32 v155, v155, v40
v_mul_f32_e32 v156, v156, v40
v_mul_f32_e32 v157, v157, v40
v_mul_f32_e32 v158, v158, v40
v_mfma_f32_16x16x32_bf16 a[60:63], a[188:191], v[252:255], a[60:63]
buffer_store_dwordx4 v[168:171], v29, s[60:63], s92 offen offset:0 sc1
v_mul_f32_e32 v159, v159, v40
v_cvt_pk_bf16_f32 v172, v152, v153
v_cvt_pk_bf16_f32 v173, v154, v155
v_cvt_pk_bf16_f32 v174, v156, v157
v_cvt_pk_bf16_f32 v175, v158, v159
v_mfma_f32_16x16x32_bf16 a[64:67], a[160:163], v[224:227], a[64:67]
ds_read_b64_tr_b16 a[176:177], v6 offset:49152
v_accvgpr_read_b32 v128, a32
v_accvgpr_read_b32 v129, a33
v_accvgpr_read_b32 v130, a34
v_accvgpr_read_b32 v131, a35
v_accvgpr_read_b32 v132, a48
v_mfma_f32_16x16x32_bf16 a[64:67], a[164:167], v[228:231], a[64:67]
buffer_load_dwordx4 v54, s[56:59], s20 offen offset:2048 lds
v_accvgpr_read_b32 v133, a49
v_accvgpr_read_b32 v134, a50
v_accvgpr_read_b32 v135, a51
v_mul_f32_e32 v128, v128, v34
v_mul_f32_e32 v129, v129, v34
v_mfma_f32_16x16x32_bf16 a[68:71], a[160:163], v[232:235], a[68:71]
ds_read_b64_tr_b16 a[178:179], v6 offset:53248
v_mul_f32_e32 v130, v130, v34
v_mul_f32_e32 v131, v131, v34
v_mul_f32_e32 v132, v132, v34
v_mul_f32_e32 v133, v133, v34
v_mul_f32_e32 v134, v134, v34
v_mfma_f32_16x16x32_bf16 a[68:71], a[164:167], v[236:239], a[68:71]
buffer_store_dwordx4 v[172:175], v29, s[60:63], s93 offen offset:0 sc1
v_mul_f32_e32 v135, v135, v34
v_cvt_pk_bf16_f32 v176, v128, v129
v_cvt_pk_bf16_f32 v177, v130, v131
v_cvt_pk_bf16_f32 v178, v132, v133
v_cvt_pk_bf16_f32 v179, v134, v135
s_waitcnt lgkmcnt(2)
v_mfma_f32_16x16x32_bf16 a[72:75], a[160:163], v[240:243], a[72:75]
ds_read_b64_tr_b16 a[180:181], v6 offset:57344
v_accvgpr_read_b32 v136, a36
v_accvgpr_read_b32 v137, a37
v_accvgpr_read_b32 v138, a38
v_accvgpr_read_b32 v139, a39
v_accvgpr_read_b32 v140, a52
v_mfma_f32_16x16x32_bf16 a[72:75], a[164:167], v[244:247], a[72:75]
buffer_load_dwordx4 v55, s[56:59], s20 offen offset:3072 lds
v_accvgpr_read_b32 v141, a53
v_accvgpr_read_b32 v142, a54
v_accvgpr_read_b32 v143, a55
v_mul_f32_e32 v136, v136, v36
v_mul_f32_e32 v137, v137, v36
v_mfma_f32_16x16x32_bf16 a[76:79], a[160:163], v[248:251], a[76:79]
ds_read_b64_tr_b16 a[182:183], v6 offset:61440
v_mul_f32_e32 v138, v138, v36
v_mul_f32_e32 v139, v139, v36
v_mul_f32_e32 v140, v140, v36
v_mul_f32_e32 v141, v141, v36
v_mul_f32_e32 v142, v142, v36
v_mfma_f32_16x16x32_bf16 a[76:79], a[164:167], v[252:255], a[76:79]
buffer_store_dwordx4 v[176:179], v29, s[60:63], s90 offen offset:1024 sc1
v_mul_f32_e32 v143, v143, v36
v_cvt_pk_bf16_f32 v180, v136, v137
v_cvt_pk_bf16_f32 v181, v138, v139
v_cvt_pk_bf16_f32 v182, v140, v141
v_cvt_pk_bf16_f32 v183, v142, v143
v_mfma_f32_16x16x32_bf16 a[80:83], a[168:171], v[224:227], a[80:83]
ds_read_b64_tr_b16 a[184:185], v7 offset:49152
v_accvgpr_read_b32 v144, a40
v_accvgpr_read_b32 v145, a41
v_accvgpr_read_b32 v146, a42
v_accvgpr_read_b32 v147, a43
v_accvgpr_read_b32 v148, a56
v_mfma_f32_16x16x32_bf16 a[80:83], a[172:175], v[228:231], a[80:83]
s_add_u32 m0, s68, 0x18000
s_add_u32 s20, s12, s6
buffer_load_dwordx4 v52, s[56:59], s20 offen offset:0 lds
v_accvgpr_read_b32 v149, a57
v_accvgpr_read_b32 v150, a58
v_accvgpr_read_b32 v151, a59
v_mul_f32_e32 v144, v144, v38
v_mul_f32_e32 v145, v145, v38
v_mfma_f32_16x16x32_bf16 a[84:87], a[168:171], v[232:235], a[84:87]
ds_read_b64_tr_b16 a[186:187], v7 offset:53248
v_mul_f32_e32 v146, v146, v38
v_mul_f32_e32 v147, v147, v38
v_mul_f32_e32 v148, v148, v38
v_mul_f32_e32 v149, v149, v38
v_mul_f32_e32 v150, v150, v38
v_mfma_f32_16x16x32_bf16 a[84:87], a[172:175], v[236:239], a[84:87]
buffer_store_dwordx4 v[180:183], v29, s[60:63], s91 offen offset:1024 sc1
v_mul_f32_e32 v151, v151, v38
v_cvt_pk_bf16_f32 v184, v144, v145
v_cvt_pk_bf16_f32 v185, v146, v147
v_cvt_pk_bf16_f32 v186, v148, v149
v_cvt_pk_bf16_f32 v187, v150, v151
s_waitcnt lgkmcnt(2)
v_mfma_f32_16x16x32_bf16 a[88:91], a[168:171], v[240:243], a[88:91]
ds_read_b64_tr_b16 a[188:189], v7 offset:57344
v_accvgpr_read_b32 v152, a44
v_accvgpr_read_b32 v153, a45
v_accvgpr_read_b32 v154, a46
v_accvgpr_read_b32 v155, a47
v_accvgpr_read_b32 v156, a60
v_mfma_f32_16x16x32_bf16 a[88:91], a[172:175], v[244:247], a[88:91]
buffer_load_dwordx4 v53, s[56:59], s20 offen offset:1024 lds
s_add_u32 s6, s6, 0x10000
s_and_b32 s6, s6, 0x7ffff
v_accvgpr_read_b32 v157, a61
v_accvgpr_read_b32 v158, a62
v_accvgpr_read_b32 v159, a63
v_mul_f32_e32 v152, v152, v40
v_mul_f32_e32 v153, v153, v40
v_mfma_f32_16x16x32_bf16 a[92:95], a[168:171], v[248:251], a[92:95]
ds_read_b64_tr_b16 a[190:191], v7 offset:61440
v_mul_f32_e32 v154, v154, v40
v_mul_f32_e32 v155, v155, v40
v_mul_f32_e32 v156, v156, v40
v_mul_f32_e32 v157, v157, v40
v_mul_f32_e32 v158, v158, v40
v_mfma_f32_16x16x32_bf16 a[92:95], a[172:175], v[252:255], a[92:95]
buffer_store_dwordx4 v[184:187], v29, s[60:63], s92 offen offset:1024 sc1
v_mul_f32_e32 v159, v159, v40
v_cvt_pk_bf16_f32 v188, v152, v153
v_cvt_pk_bf16_f32 v189, v154, v155
v_cvt_pk_bf16_f32 v190, v156, v157
v_cvt_pk_bf16_f32 v191, v158, v159
v_mfma_f32_16x16x32_bf16 a[96:99], a[176:179], v[224:227], a[96:99]
v_accvgpr_read_b32 v128, a64
v_accvgpr_read_b32 v129, a65
v_accvgpr_read_b32 v130, a66
v_accvgpr_read_b32 v131, a67
v_accvgpr_read_b32 v132, a80
v_mfma_f32_16x16x32_bf16 a[96:99], a[180:183], v[228:231], a[96:99]
buffer_load_dwordx4 v54, s[56:59], s20 offen offset:2048 lds
v_accvgpr_read_b32 v133, a81
v_accvgpr_read_b32 v134, a82
v_accvgpr_read_b32 v135, a83
v_mul_f32_e32 v128, v128, v34
v_mul_f32_e32 v129, v129, v34
v_mfma_f32_16x16x32_bf16 a[100:103], a[176:179], v[232:235], a[100:103]
v_mul_f32_e32 v130, v130, v34
v_mul_f32_e32 v131, v131, v34
v_mul_f32_e32 v132, v132, v34
v_mul_f32_e32 v133, v133, v34
v_mul_f32_e32 v134, v134, v34
v_mfma_f32_16x16x32_bf16 a[100:103], a[180:183], v[236:239], a[100:103]
buffer_store_dwordx4 v[188:191], v29, s[60:63], s93 offen offset:1024 sc1
v_mul_f32_e32 v135, v135, v34
v_cvt_pk_bf16_f32 v208, v128, v129
v_cvt_pk_bf16_f32 v209, v130, v131
v_cvt_pk_bf16_f32 v210, v132, v133
v_cvt_pk_bf16_f32 v211, v134, v135
s_waitcnt lgkmcnt(0)
v_mfma_f32_16x16x32_bf16 a[104:107], a[176:179], v[240:243], a[104:107]
v_accvgpr_read_b32 v136, a68
v_accvgpr_read_b32 v137, a69
v_accvgpr_read_b32 v138, a70
v_accvgpr_read_b32 v139, a71
v_accvgpr_read_b32 v140, a84
v_mfma_f32_16x16x32_bf16 a[104:107], a[180:183], v[244:247], a[104:107]
buffer_load_dwordx4 v55, s[56:59], s20 offen offset:3072 lds
v_accvgpr_read_b32 v141, a85
v_accvgpr_read_b32 v142, a86
v_accvgpr_read_b32 v143, a87
v_mul_f32_e32 v136, v136, v36
v_mul_f32_e32 v137, v137, v36
v_mfma_f32_16x16x32_bf16 a[108:111], a[176:179], v[248:251], a[108:111]
v_mul_f32_e32 v138, v138, v36
v_mul_f32_e32 v139, v139, v36
v_mul_f32_e32 v140, v140, v36
v_mul_f32_e32 v141, v141, v36
v_mul_f32_e32 v142, v142, v36
v_mfma_f32_16x16x32_bf16 a[108:111], a[180:183], v[252:255], a[108:111]
buffer_load_dwordx4 a[128:131], v29, s[44:47], s13 offen offset:0 sc1
v_mul_f32_e32 v143, v143, v36
v_cvt_pk_bf16_f32 v212, v136, v137
v_cvt_pk_bf16_f32 v213, v138, v139
v_cvt_pk_bf16_f32 v214, v140, v141
v_cvt_pk_bf16_f32 v215, v142, v143
v_mfma_f32_16x16x32_bf16 a[112:115], a[184:187], v[224:227], a[112:115]
v_accvgpr_read_b32 v144, a72
v_accvgpr_read_b32 v145, a73
v_accvgpr_read_b32 v146, a74
v_accvgpr_read_b32 v147, a75
v_accvgpr_read_b32 v148, a88
v_mfma_f32_16x16x32_bf16 a[112:115], a[188:191], v[228:231], a[112:115]
buffer_load_dwordx4 a[132:135], v29, s[44:47], s13 offen offset:1024 sc1
v_accvgpr_read_b32 v149, a89
v_accvgpr_read_b32 v150, a90
v_accvgpr_read_b32 v151, a91
v_mul_f32_e32 v144, v144, v38
v_mul_f32_e32 v145, v145, v38
v_mfma_f32_16x16x32_bf16 a[116:119], a[184:187], v[232:235], a[116:119]
v_mul_f32_e32 v146, v146, v38
v_mul_f32_e32 v147, v147, v38
v_mul_f32_e32 v148, v148, v38
v_mul_f32_e32 v149, v149, v38
v_mul_f32_e32 v150, v150, v38
v_mfma_f32_16x16x32_bf16 a[116:119], a[188:191], v[236:239], a[116:119]
buffer_load_dwordx4 a[136:139], v29, s[44:47], s14 offen offset:0 sc1
v_mul_f32_e32 v151, v151, v38
v_cvt_pk_bf16_f32 v216, v144, v145
v_cvt_pk_bf16_f32 v217, v146, v147
v_cvt_pk_bf16_f32 v218, v148, v149
v_cvt_pk_bf16_f32 v219, v150, v151
v_mfma_f32_16x16x32_bf16 a[120:123], a[184:187], v[240:243], a[120:123]
v_accvgpr_read_b32 v152, a76
v_accvgpr_read_b32 v153, a77
v_accvgpr_read_b32 v154, a78
v_accvgpr_read_b32 v155, a79
v_accvgpr_read_b32 v156, a92
v_mfma_f32_16x16x32_bf16 a[120:123], a[188:191], v[244:247], a[120:123]
buffer_load_dwordx4 a[140:143], v29, s[44:47], s14 offen offset:1024 sc1
v_accvgpr_read_b32 v157, a93
v_accvgpr_read_b32 v158, a94
v_accvgpr_read_b32 v159, a95
v_mul_f32_e32 v152, v152, v40
v_mul_f32_e32 v153, v153, v40
v_mfma_f32_16x16x32_bf16 a[124:127], a[184:187], v[248:251], a[124:127]
v_mul_f32_e32 v154, v154, v40
v_mul_f32_e32 v155, v155, v40
v_mul_f32_e32 v156, v156, v40
v_mul_f32_e32 v157, v157, v40
v_mul_f32_e32 v158, v158, v40
v_mfma_f32_16x16x32_bf16 a[124:127], a[188:191], v[252:255], a[124:127]
v_mul_f32_e32 v159, v159, v40
v_cvt_pk_bf16_f32 v220, v152, v153
v_cvt_pk_bf16_f32 v221, v154, v155
v_cvt_pk_bf16_f32 v222, v156, v157
v_cvt_pk_bf16_f32 v223, v158, v159
v_accvgpr_read_b32 v128, a96
v_accvgpr_read_b32 v129, a97
v_accvgpr_read_b32 v130, a98
v_accvgpr_read_b32 v131, a99
v_accvgpr_read_b32 v132, a112
v_accvgpr_read_b32 v133, a113
v_accvgpr_read_b32 v134, a114
v_accvgpr_read_b32 v135, a115
v_pk_mul_f32 v[128:129], v[128:129], v[34:35] op_sel_hi:[1,0]
v_pk_mul_f32 v[130:131], v[130:131], v[34:35] op_sel_hi:[1,0]
v_pk_mul_f32 v[132:133], v[132:133], v[34:35] op_sel_hi:[1,0]
v_pk_mul_f32 v[134:135], v[134:135], v[34:35] op_sel_hi:[1,0]
v_cvt_pk_bf16_f32 v224, v128, v129
v_cvt_pk_bf16_f32 v225, v130, v131
v_cvt_pk_bf16_f32 v226, v132, v133
v_cvt_pk_bf16_f32 v227, v134, v135
v_accvgpr_read_b32 v136, a100
v_accvgpr_read_b32 v137, a101
v_accvgpr_read_b32 v138, a102
v_accvgpr_read_b32 v139, a103
v_accvgpr_read_b32 v140, a116
v_accvgpr_read_b32 v141, a117
v_accvgpr_read_b32 v142, a118
v_accvgpr_read_b32 v143, a119
v_pk_mul_f32 v[136:137], v[136:137], v[36:37] op_sel_hi:[1,0]
v_pk_mul_f32 v[138:139], v[138:139], v[36:37] op_sel_hi:[1,0]
v_pk_mul_f32 v[140:141], v[140:141], v[36:37] op_sel_hi:[1,0]
v_pk_mul_f32 v[142:143], v[142:143], v[36:37] op_sel_hi:[1,0]
v_cvt_pk_bf16_f32 v228, v136, v137
v_cvt_pk_bf16_f32 v229, v138, v139
v_cvt_pk_bf16_f32 v230, v140, v141
v_cvt_pk_bf16_f32 v231, v142, v143
v_accvgpr_read_b32 v144, a104
v_accvgpr_read_b32 v145, a105
v_accvgpr_read_b32 v146, a106
v_accvgpr_read_b32 v147, a107
v_accvgpr_read_b32 v148, a120
v_accvgpr_read_b32 v149, a121
v_accvgpr_read_b32 v150, a122
v_accvgpr_read_b32 v151, a123
v_pk_mul_f32 v[144:145], v[144:145], v[38:39] op_sel_hi:[1,0]
v_pk_mul_f32 v[146:147], v[146:147], v[38:39] op_sel_hi:[1,0]
v_pk_mul_f32 v[148:149], v[148:149], v[38:39] op_sel_hi:[1,0]
v_pk_mul_f32 v[150:151], v[150:151], v[38:39] op_sel_hi:[1,0]
v_cvt_pk_bf16_f32 v232, v144, v145
v_cvt_pk_bf16_f32 v233, v146, v147
v_cvt_pk_bf16_f32 v234, v148, v149
v_cvt_pk_bf16_f32 v235, v150, v151
v_accvgpr_read_b32 v152, a108
v_accvgpr_read_b32 v153, a109
v_accvgpr_read_b32 v154, a110
v_accvgpr_read_b32 v155, a111
v_accvgpr_read_b32 v156, a124
v_accvgpr_read_b32 v157, a125
v_accvgpr_read_b32 v158, a126
v_accvgpr_read_b32 v159, a127
v_pk_mul_f32 v[152:153], v[152:153], v[40:41] op_sel_hi:[1,0]
v_pk_mul_f32 v[154:155], v[154:155], v[40:41] op_sel_hi:[1,0]
v_pk_mul_f32 v[156:157], v[156:157], v[40:41] op_sel_hi:[1,0]
v_pk_mul_f32 v[158:159], v[158:159], v[40:41] op_sel_hi:[1,0]
v_cvt_pk_bf16_f32 v236, v152, v153
v_cvt_pk_bf16_f32 v237, v154, v155
v_cvt_pk_bf16_f32 v238, v156, v157
v_cvt_pk_bf16_f32 v239, v158, v159
s_branch .Lkf_o0
; ---- epi
.Lkf_epi:
buffer_load_dwordx4 v[100:103], v29, s[44:47], s13 offen offset:3072 sc1
s_add_u32 s13, s13, 0x1000
buffer_load_dwordx4 v[104:107], v29, s[44:47], s14 offen offset:2048 sc1
buffer_load_dwordx4 v[108:111], v29, s[44:47], s14 offen offset:3072 sc1
s_add_u32 s14, s14, 0x1000
s_add_u32 m0, s68, 0xc000
s_add_u32 s12, s15, s101
s_add_u32 s20, s12, s6
buffer_load_dwordx4 v52, s[56:59], s20 offen offset:0 lds
buffer_load_dwordx4 v53, s[56:59], s20 offen offset:1024 lds
s_add_u32 s6, s6, 0x10000
s_and_b32 s6, s6, 0x7ffff
buffer_load_dwordx4 v54, s[56:59], s20 offen offset:2048 lds
buffer_load_dwordx4 v55, s[56:59], s20 offen offset:3072 lds
s_add_u32 m0, s68, 0x18000
s_add_u32 s20, s12, s6
buffer_load_dwordx4 v52, s[56:59], s20 offen offset:0 lds
buffer_load_dwordx4 v53, s[56:59], s20 offen offset:1024 lds
s_add_u32 s6, s6, 0x10000
s_and_b32 s6, s6, 0x7ffff
buffer_load_dwordx4 v54, s[56:59], s20 offen offset:2048 lds
buffer_load_dwordx4 v55, s[56:59], s20 offen offset:3072 lds
buffer_load_dwordx4 a[128:131], v29, s[44:47], s13 offen offset:0 sc1
buffer_load_dwordx4 a[132:135], v29, s[44:47], s13 offen offset:1024 sc1
buffer_load_dwordx4 a[136:139], v29, s[44:47], s14 offen offset:0 sc1
buffer_load_dwordx4 a[140:143], v29, s[44:47], s14 offen offset:1024 sc1
v_rcp_f32_e32 v34, v64
v_rcp_f32_e32 v36, v65
v_rcp_f32_e32 v38, v66
v_rcp_f32_e32 v40, v67
s_mov_b32 s90, 0
s_mov_b32 s91, 0x20000
s_mov_b32 s92, 0x40000
s_mov_b32 s93, 0x60000
v_accvgpr_read_b32 v128, a0
v_accvgpr_read_b32 v129, a1
v_accvgpr_read_b32 v130, a2
v_accvgpr_read_b32 v131, a3
v_accvgpr_read_b32 v132, a16
v_accvgpr_read_b32 v133, a17
v_accvgpr_read_b32 v134, a18
v_accvgpr_read_b32 v135, a19
v_pk_mul_f32 v[128:129], v[128:129], v[34:35] op_sel_hi:[1,0]
v_pk_mul_f32 v[130:131], v[130:131], v[34:35] op_sel_hi:[1,0]
v_pk_mul_f32 v[132:133], v[132:133], v[34:35] op_sel_hi:[1,0]
v_pk_mul_f32 v[134:135], v[134:135], v[34:35] op_sel_hi:[1,0]
v_cvt_pk_bf16_f32 v160, v128, v129
v_cvt_pk_bf16_f32 v161, v130, v131
v_cvt_pk_bf16_f32 v162, v132, v133
v_cvt_pk_bf16_f32 v163, v134, v135
buffer_store_dwordx4 v[160:163], v29, s[60:63], s90 offen offset:0 sc1
v_accvgpr_read_b32 v136, a4
v_accvgpr_read_b32 v137, a5
v_accvgpr_read_b32 v138, a6
v_accvgpr_read_b32 v139, a7
v_accvgpr_read_b32 v140, a20
v_accvgpr_read_b32 v141, a21
v_accvgpr_read_b32 v142, a22
v_accvgpr_read_b32 v143, a23
v_pk_mul_f32 v[136:137], v[136:137], v[36:37] op_sel_hi:[1,0]
v_pk_mul_f32 v[138:139], v[138:139], v[36:37] op_sel_hi:[1,0]
v_pk_mul_f32 v[140:141], v[140:141], v[36:37] op_sel_hi:[1,0]
v_pk_mul_f32 v[142:143], v[142:143], v[36:37] op_sel_hi:[1,0]
v_cvt_pk_bf16_f32 v164, v136, v137
v_cvt_pk_bf16_f32 v165, v138, v139
v_cvt_pk_bf16_f32 v166, v140, v141
v_cvt_pk_bf16_f32 v167, v142, v143
buffer_store_dwordx4 v[164:167], v29, s[60:63], s91 offen offset:0 sc1
v_accvgpr_read_b32 v144, a8
v_accvgpr_read_b32 v145, a9
v_accvgpr_read_b32 v146, a10
v_accvgpr_read_b32 v147, a11
v_accvgpr_read_b32 v148, a24
v_accvgpr_read_b32 v149, a25
v_accvgpr_read_b32 v150, a26
v_accvgpr_read_b32 v151, a27
v_pk_mul_f32 v[144:145], v[144:145], v[38:39] op_sel_hi:[1,0]
v_pk_mul_f32 v[146:147], v[146:147], v[38:39] op_sel_hi:[1,0]
v_pk_mul_f32 v[148:149], v[148:149], v[38:39] op_sel_hi:[1,0]
v_pk_mul_f32 v[150:151], v[150:151], v[38:39] op_sel_hi:[1,0]
v_cvt_pk_bf16_f32 v168, v144, v145
v_cvt_pk_bf16_f32 v169, v146, v147
v_cvt_pk_bf16_f32 v170, v148, v149
v_cvt_pk_bf16_f32 v171, v150, v151
buffer_store_dwordx4 v[168:171], v29, s[60:63], s92 offen offset:0 sc1
v_accvgpr_read_b32 v152, a12
v_accvgpr_read_b32 v153, a13
v_accvgpr_read_b32 v154, a14
v_accvgpr_read_b32 v155, a15
v_accvgpr_read_b32 v156, a28
v_accvgpr_read_b32 v157, a29
v_accvgpr_read_b32 v158, a30
v_accvgpr_read_b32 v159, a31
v_pk_mul_f32 v[152:153], v[152:153], v[40:41] op_sel_hi:[1,0]
v_pk_mul_f32 v[154:155], v[154:155], v[40:41] op_sel_hi:[1,0]
v_pk_mul_f32 v[156:157], v[156:157], v[40:41] op_sel_hi:[1,0]
v_pk_mul_f32 v[158:159], v[158:159], v[40:41] op_sel_hi:[1,0]
v_cvt_pk_bf16_f32 v172, v152, v153
v_cvt_pk_bf16_f32 v173, v154, v155
v_cvt_pk_bf16_f32 v174, v156, v157
v_cvt_pk_bf16_f32 v175, v158, v159
buffer_store_dwordx4 v[172:175], v29, s[60:63], s93 offen offset:0 sc1
v_accvgpr_read_b32 v128, a32
v_accvgpr_read_b32 v129, a33
v_accvgpr_read_b32 v130, a34
v_accvgpr_read_b32 v131, a35
v_accvgpr_read_b32 v132, a48
v_accvgpr_read_b32 v133, a49
v_accvgpr_read_b32 v134, a50
v_accvgpr_read_b32 v135, a51
v_pk_mul_f32 v[128:129], v[128:129], v[34:35] op_sel_hi:[1,0]
v_pk_mul_f32 v[130:131], v[130:131], v[34:35] op_sel_hi:[1,0]
v_pk_mul_f32 v[132:133], v[132:133], v[34:35] op_sel_hi:[1,0]
v_pk_mul_f32 v[134:135], v[134:135], v[34:35] op_sel_hi:[1,0]
v_cvt_pk_bf16_f32 v176, v128, v129
v_cvt_pk_bf16_f32 v177, v130, v131
v_cvt_pk_bf16_f32 v178, v132, v133
v_cvt_pk_bf16_f32 v179, v134, v135
buffer_store_dwordx4 v[176:179], v29, s[60:63], s90 offen offset:1024 sc1
v_accvgpr_read_b32 v136, a36
v_accvgpr_read_b32 v137, a37
v_accvgpr_read_b32 v138, a38
v_accvgpr_read_b32 v139, a39
v_accvgpr_read_b32 v140, a52
v_accvgpr_read_b32 v141, a53
v_accvgpr_read_b32 v142, a54
v_accvgpr_read_b32 v143, a55
v_pk_mul_f32 v[136:137], v[136:137], v[36:37] op_sel_hi:[1,0]
v_pk_mul_f32 v[138:139], v[138:139], v[36:37] op_sel_hi:[1,0]
v_pk_mul_f32 v[140:141], v[140:141], v[36:37] op_sel_hi:[1,0]
v_pk_mul_f32 v[142:143], v[142:143], v[36:37] op_sel_hi:[1,0]
v_cvt_pk_bf16_f32 v180, v136, v137
v_cvt_pk_bf16_f32 v181, v138, v139
v_cvt_pk_bf16_f32 v182, v140, v141
v_cvt_pk_bf16_f32 v183, v142, v143
buffer_store_dwordx4 v[180:183], v29, s[60:63], s91 offen offset:1024 sc1
v_accvgpr_read_b32 v144, a40
v_accvgpr_read_b32 v145, a41
v_accvgpr_read_b32 v146, a42
v_accvgpr_read_b32 v147, a43
v_accvgpr_read_b32 v148, a56
v_accvgpr_read_b32 v149, a57
v_accvgpr_read_b32 v150, a58
v_accvgpr_read_b32 v151, a59
v_pk_mul_f32 v[144:145], v[144:145], v[38:39] op_sel_hi:[1,0]
v_pk_mul_f32 v[146:147], v[146:147], v[38:39] op_sel_hi:[1,0]
v_pk_mul_f32 v[148:149], v[148:149], v[38:39] op_sel_hi:[1,0]
v_pk_mul_f32 v[150:151], v[150:151], v[38:39] op_sel_hi:[1,0]
v_cvt_pk_bf16_f32 v184, v144, v145
v_cvt_pk_bf16_f32 v185, v146, v147
v_cvt_pk_bf16_f32 v186, v148, v149
v_cvt_pk_bf16_f32 v187, v150, v151
buffer_store_dwordx4 v[184:187], v29, s[60:63], s92 offen offset:1024 sc1
v_accvgpr_read_b32 v152, a44
v_accvgpr_read_b32 v153, a45
v_accvgpr_read_b32 v154, a46
v_accvgpr_read_b32 v155, a47
v_accvgpr_read_b32 v156, a60
v_accvgpr_read_b32 v157, a61
v_accvgpr_read_b32 v158, a62
v_accvgpr_read_b32 v159, a63
v_pk_mul_f32 v[152:153], v[152:153], v[40:41] op_sel_hi:[1,0]
v_pk_mul_f32 v[154:155], v[154:155], v[40:41] op_sel_hi:[1,0]
v_pk_mul_f32 v[156:157], v[156:157], v[40:41] op_sel_hi:[1,0]
v_pk_mul_f32 v[158:159], v[158:159], v[40:41] op_sel_hi:[1,0]
v_cvt_pk_bf16_f32 v188, v152, v153
v_cvt_pk_bf16_f32 v189, v154, v155
v_cvt_pk_bf16_f32 v190, v156, v157
v_cvt_pk_bf16_f32 v191, v158, v159
buffer_store_dwordx4 v[188:191], v29, s[60:63], s93 offen offset:1024 sc1
v_accvgpr_read_b32 v128, a64
v_accvgpr_read_b32 v129, a65
v_accvgpr_read_b32 v130, a66
v_accvgpr_read_b32 v131, a67
v_accvgpr_read_b32 v132, a80
v_accvgpr_read_b32 v133, a81
v_accvgpr_read_b32 v134, a82
v_accvgpr_read_b32 v135, a83
v_pk_mul_f32 v[128:129], v[128:129], v[34:35] op_sel_hi:[1,0]
v_pk_mul_f32 v[130:131], v[130:131], v[34:35] op_sel_hi:[1,0]
v_pk_mul_f32 v[132:133], v[132:133], v[34:35] op_sel_hi:[1,0]
v_pk_mul_f32 v[134:135], v[134:135], v[34:35] op_sel_hi:[1,0]
v_cvt_pk_bf16_f32 v208, v128, v129
v_cvt_pk_bf16_f32 v209, v130, v131
v_cvt_pk_bf16_f32 v210, v132, v133
v_cvt_pk_bf16_f32 v211, v134, v135
v_accvgpr_read_b32 v136, a68
v_accvgpr_read_b32 v137, a69
v_accvgpr_read_b32 v138, a70
v_accvgpr_read_b32 v139, a71
v_accvgpr_read_b32 v140, a84
v_accvgpr_read_b32 v141, a85
v_accvgpr_read_b32 v142, a86
v_accvgpr_read_b32 v143, a87
v_pk_mul_f32 v[136:137], v[136:137], v[36:37] op_sel_hi:[1,0]
v_pk_mul_f32 v[138:139], v[138:139], v[36:37] op_sel_hi:[1,0]
v_pk_mul_f32 v[140:141], v[140:141], v[36:37] op_sel_hi:[1,0]
v_pk_mul_f32 v[142:143], v[142:143], v[36:37] op_sel_hi:[1,0]
v_cvt_pk_bf16_f32 v212, v136, v137
v_cvt_pk_bf16_f32 v213, v138, v139
v_cvt_pk_bf16_f32 v214, v140, v141
v_cvt_pk_bf16_f32 v215, v142, v143
v_accvgpr_read_b32 v144, a72
v_accvgpr_read_b32 v145, a73
v_accvgpr_read_b32 v146, a74
v_accvgpr_read_b32 v147, a75
v_accvgpr_read_b32 v148, a88
v_accvgpr_read_b32 v149, a89
v_accvgpr_read_b32 v150, a90
v_accvgpr_read_b32 v151, a91
v_pk_mul_f32 v[144:145], v[144:145], v[38:39] op_sel_hi:[1,0]
v_pk_mul_f32 v[146:147], v[146:147], v[38:39] op_sel_hi:[1,0]
v_pk_mul_f32 v[148:149], v[148:149], v[38:39] op_sel_hi:[1,0]
v_pk_mul_f32 v[150:151], v[150:151], v[38:39] op_sel_hi:[1,0]
v_cvt_pk_bf16_f32 v216, v144, v145
v_cvt_pk_bf16_f32 v217, v146, v147
v_cvt_pk_bf16_f32 v218, v148, v149
v_cvt_pk_bf16_f32 v219, v150, v151
v_accvgpr_read_b32 v152, a76
v_accvgpr_read_b32 v153, a77
v_accvgpr_read_b32 v154, a78
v_accvgpr_read_b32 v155, a79
v_accvgpr_read_b32 v156, a92
v_accvgpr_read_b32 v157, a93
v_accvgpr_read_b32 v158, a94
v_accvgpr_read_b32 v159, a95
v_pk_mul_f32 v[152:153], v[152:153], v[40:41] op_sel_hi:[1,0]
v_pk_mul_f32 v[154:155], v[154:155], v[40:41] op_sel_hi:[1,0]
v_pk_mul_f32 v[156:157], v[156:157], v[40:41] op_sel_hi:[1,0]
v_pk_mul_f32 v[158:159], v[158:159], v[40:41] op_sel_hi:[1,0]
v_cvt_pk_bf16_f32 v220, v152, v153
v_cvt_pk_bf16_f32 v221, v154, v155
v_cvt_pk_bf16_f32 v222, v156, v157
v_cvt_pk_bf16_f32 v223, v158, v159
v_accvgpr_read_b32 v128, a96
v_accvgpr_read_b32 v129, a97
v_accvgpr_read_b32 v130, a98
v_accvgpr_read_b32 v131, a99
v_accvgpr_read_b32 v132, a112
v_accvgpr_read_b32 v133, a113
v_accvgpr_read_b32 v134, a114
v_accvgpr_read_b32 v135, a115
v_pk_mul_f32 v[128:129], v[128:129], v[34:35] op_sel_hi:[1,0]
v_pk_mul_f32 v[130:131], v[130:131], v[34:35] op_sel_hi:[1,0]
v_pk_mul_f32 v[132:133], v[132:133], v[34:35] op_sel_hi:[1,0]
v_pk_mul_f32 v[134:135], v[134:135], v[34:35] op_sel_hi:[1,0]
v_cvt_pk_bf16_f32 v224, v128, v129
v_cvt_pk_bf16_f32 v225, v130, v131
v_cvt_pk_bf16_f32 v226, v132, v133
v_cvt_pk_bf16_f32 v227, v134, v135
v_accvgpr_read_b32 v136, a100
v_accvgpr_read_b32 v137, a101
v_accvgpr_read_b32 v138, a102
v_accvgpr_read_b32 v139, a103
v_accvgpr_read_b32 v140, a116
v_accvgpr_read_b32 v141, a117
v_accvgpr_read_b32 v142, a118
v_accvgpr_read_b32 v143, a119
v_pk_mul_f32 v[136:137], v[136:137], v[36:37] op_sel_hi:[1,0]
v_pk_mul_f32 v[138:139], v[138:139], v[36:37] op_sel_hi:[1,0]
v_pk_mul_f32 v[140:141], v[140:141], v[36:37] op_sel_hi:[1,0]
v_pk_mul_f32 v[142:143], v[142:143], v[36:37] op_sel_hi:[1,0]
v_cvt_pk_bf16_f32 v228, v136, v137
v_cvt_pk_bf16_f32 v229, v138, v139
v_cvt_pk_bf16_f32 v230, v140, v141
v_cvt_pk_bf16_f32 v231, v142, v143
v_accvgpr_read_b32 v144, a104
v_accvgpr_read_b32 v145, a105
v_accvgpr_read_b32 v146, a106
v_accvgpr_read_b32 v147, a107
v_accvgpr_read_b32 v148, a120
v_accvgpr_read_b32 v149, a121
v_accvgpr_read_b32 v150, a122
v_accvgpr_read_b32 v151, a123
v_pk_mul_f32 v[144:145], v[144:145], v[38:39] op_sel_hi:[1,0]
v_pk_mul_f32 v[146:147], v[146:147], v[38:39] op_sel_hi:[1,0]
v_pk_mul_f32 v[148:149], v[148:149], v[38:39] op_sel_hi:[1,0]
v_pk_mul_f32 v[150:151], v[150:151], v[38:39] op_sel_hi:[1,0]
v_cvt_pk_bf16_f32 v232, v144, v145
v_cvt_pk_bf16_f32 v233, v146, v147
v_cvt_pk_bf16_f32 v234, v148, v149
v_cvt_pk_bf16_f32 v235, v150, v151
v_accvgpr_read_b32 v152, a108
v_accvgpr_read_b32 v153, a109
v_accvgpr_read_b32 v154, a110
v_accvgpr_read_b32 v155, a111
v_accvgpr_read_b32 v156, a124
v_accvgpr_read_b32 v157, a125
v_accvgpr_read_b32 v158, a126
v_accvgpr_read_b32 v159, a127
v_pk_mul_f32 v[152:153], v[152:153], v[40:41] op_sel_hi:[1,0]
v_pk_mul_f32 v[154:155], v[154:155], v[40:41] op_sel_hi:[1,0]
v_pk_mul_f32 v[156:157], v[156:157], v[40:41] op_sel_hi:[1,0]
v_pk_mul_f32 v[158:159], v[158:159], v[40:41] op_sel_hi:[1,0]
v_cvt_pk_bf16_f32 v236, v152, v153
v_cvt_pk_bf16_f32 v237, v154, v155
v_cvt_pk_bf16_f32 v238, v156, v157
v_cvt_pk_bf16_f32 v239, v158, v159
; ---- o0
.Lkf_o0:
s_lshl_b32 s82, s73, 9
s_lshr_b32 s80, s101, 10
s_add_u32 s82, s82, 0x3000
s_xor_b32 s100, s80, 0x800
s_add_u32 s100, s100, s82
s_add_u32 s82, s82, s80
s_add_u32 s81, s72, 0x3000
s_mov_b32 s79, 0x4000
s_lshl_b32 s21, s73, 7
s_add_u32 s21, s21, s75
s_add_u32 s22, s75, 0x1000
s_add_u32 s23, s21, 0x1000
s_bfe_u32 s76, s75, 0x20009
s_lshl_b32 s76, s76, 12
v_add_u32_e32 v8, 0x10000, v112
v_add_u32_e32 v9, 0x10000, v113
v_add_u32_e32 v10, 0x10000, v114
v_add_u32_e32 v11, 0x10000, v115
v_add_u32_e32 v12, 0x10000, v116
v_add_u32_e32 v13, 0x10000, v117
v_add_u32_e32 v14, 0x10000, v118
v_add_u32_e32 v15, 0x10000, v119
s_lshr_b32 s80, s5, 1
s_lshr_b32 s83, s101, 21
s_and_b32 s78, s5, 1
s_lshl_b32 s78, s78, 13
s_add_u32 s78, s78, 0x1c000
v_add_u32_e32 v72, s78, v29
v_add_u32_e32 v73, 0x1c000, v51
s_add_u32 s76, s76, 0x1000
s_and_b32 s76, s76, 0x3fff
s_lshr_b32 s78, s101, 5
s_waitcnt vmcnt(20)
s_barrier
ds_read_b64_tr_b16 a[0:1], v120 offset:0
ds_read_b64_tr_b16 a[2:3], v120 offset:4096
ds_read_b64_tr_b16 a[4:5], v120 offset:8192
ds_read_b64_tr_b16 a[6:7], v120 offset:12288
ds_read_b64_tr_b16 a[8:9], v121 offset:0
ds_read_b64_tr_b16 a[10:11], v121 offset:4096
ds_read_b64_tr_b16 a[12:13], v121 offset:8192
ds_read_b64_tr_b16 a[14:15], v121 offset:12288
s_cmp_eq_u32 s80, s83
s_cbranch_scc0 .Lkf_pt0
ds_write_b128 v72, v[160:163] offset:0
ds_write_b128 v72, v[164:167] offset:2048
ds_write_b128 v72, v[168:171] offset:4096
ds_write_b128 v72, v[172:175] offset:6144
ds_write_b128 v72, v[176:179] offset:1024
ds_write_b128 v72, v[180:183] offset:3072
ds_write_b128 v72, v[184:187] offset:5120
ds_write_b128 v72, v[188:191] offset:7168
.Lkf_pt0:
s_waitcnt lgkmcnt(4)
v_mfma_f32_16x16x32_bf16 v[128:131], a[0:3], a[192:195], 0
ds_read_b64_tr_b16 a[16:17], v122 offset:0
ds_read_b64_tr_b16 a[18:19], v122 offset:4096
v_mfma_f32_16x16x32_bf16 v[128:131], a[4:7], a[196:199], v[128:131]
buffer_store_dwordx4 v[208:211], v29, s[60:63], s90 offen offset:2048 sc1
v_mfma_f32_16x16x32_bf16 v[132:135], a[0:3], a[200:203], 0
ds_read_b64_tr_b16 a[20:21], v122 offset:8192
v_mfma_f32_16x16x32_bf16 v[132:135], a[4:7], a[204:207], v[132:135]
ds_read_b64_tr_b16 a[22:23], v122 offset:12288
s_waitcnt lgkmcnt(4)
v_mfma_f32_16x16x32_bf16 v[136:139], a[8:11], a[192:195], 0
ds_read_b64_tr_b16 a[24:25], v123 offset:0
v_mfma_f32_16x16x32_bf16 v[136:139], a[12:15], a[196:199], v[136:139]
ds_read_b64_tr_b16 a[26:27], v123 offset:4096
v_mfma_f32_16x16x32_bf16 v[140:143], a[8:11], a[200:203], 0
ds_read_b64_tr_b16 a[28:29], v123 offset:8192
v_mfma_f32_16x16x32_bf16 v[140:143], a[12:15], a[204:207], v[140:143]
ds_read_b64_tr_b16 a[30:31], v123 offset:12288
s_waitcnt lgkmcnt(4)
v_mfma_f32_16x16x32_bf16 v[144:147], a[16:19], a[192:195], 0
ds_read_b64_tr_b16 a[0:1], v124 offset:0
ds_read_b64_tr_b16 a[2:3], v124 offset:4096
v_mfma_f32_16x16x32_bf16 v[144:147], a[20:23], a[196:199], v[144:147]
buffer_store_dwordx4 v[212:215], v29, s[60:63], s91 offen offset:2048 sc1
v_mfma_f32_16x16x32_bf16 v[148:151], a[16:19], a[200:203], 0
ds_read_b64_tr_b16 a[4:5], v124 offset:8192
v_mfma_f32_16x16x32_bf16 v[148:151], a[20:23], a[204:207], v[148:151]
ds_read_b64_tr_b16 a[6:7], v124 offset:12288
s_waitcnt lgkmcnt(4)
v_mfma_f32_16x16x32_bf16 v[152:155], a[24:27], a[192:195], 0
ds_read_b64_tr_b16 a[8:9], v125 offset:0
v_mfma_f32_16x16x32_bf16 v[152:155], a[28:31], a[196:199], v[152:155]
ds_read_b64_tr_b16 a[10:11], v125 offset:4096
v_mfma_f32_16x16x32_bf16 v[156:159], a[24:27], a[200:203], 0
ds_read_b64_tr_b16 a[12:13], v125 offset:8192
v_mfma_f32_16x16x32_bf16 v[156:159], a[28:31], a[204:207], v[156:159]
ds_read_b64_tr_b16 a[14:15], v125 offset:12288
s_waitcnt lgkmcnt(4)
v_mfma_f32_16x16x32_bf16 v[160:163], a[0:3], a[192:195], 0
ds_read_b64_tr_b16 a[16:17], v126 offset:0
ds_read_b64_tr_b16 a[18:19], v126 offset:4096
v_mfma_f32_16x16x32_bf16 v[160:163], a[4:7], a[196:199], v[160:163]
buffer_store_dwordx4 v[216:219], v29, s[60:63], s92 offen offset:2048 sc1
v_mfma_f32_16x16x32_bf16 v[164:167], a[0:3], a[200:203], 0
ds_read_b64_tr_b16 a[20:21], v126 offset:8192
v_mfma_f32_16x16x32_bf16 v[164:167], a[4:7], a[204:207], v[164:167]
ds_read_b64_tr_b16 a[22:23], v126 offset:12288
s_waitcnt lgkmcnt(4)
v_mfma_f32_16x16x32_bf16 v[168:171], a[8:11], a[192:195], 0
ds_read_b64_tr_b16 a[24:25], v127 offset:0
v_mfma_f32_16x16x32_bf16 v[168:171], a[12:15], a[196:199], v[168:171]
ds_read_b64_tr_b16 a[26:27], v127 offset:4096
v_mfma_f32_16x16x32_bf16 v[172:175], a[8:11], a[200:203], 0
ds_read_b64_tr_b16 a[28:29], v127 offset:8192
v_mfma_f32_16x16x32_bf16 v[172:175], a[12:15], a[204:207], v[172:175]
ds_read_b64_tr_b16 a[30:31], v127 offset:12288
s_waitcnt lgkmcnt(4)
v_mfma_f32_16x16x32_bf16 v[176:179], a[16:19], a[192:195], 0
ds_read_b64_tr_b16 a[0:1], v120 offset:16384
ds_read_b64_tr_b16 a[2:3], v120 offset:20480
v_mfma_f32_16x16x32_bf16 v[176:179], a[20:23], a[196:199], v[176:179]
buffer_store_dwordx4 v[220:223], v29, s[60:63], s93 offen offset:2048 sc1
v_mfma_f32_16x16x32_bf16 v[180:183], a[16:19], a[200:203], 0
ds_read_b64_tr_b16 a[4:5], v120 offset:24576
v_mfma_f32_16x16x32_bf16 v[180:183], a[20:23], a[204:207], v[180:183]
ds_read_b64_tr_b16 a[6:7], v120 offset:28672
s_waitcnt lgkmcnt(4)
v_mfma_f32_16x16x32_bf16 v[184:187], a[24:27], a[192:195], 0
ds_read_b64_tr_b16 a[8:9], v121 offset:16384
v_mfma_f32_16x16x32_bf16 v[184:187], a[28:31], a[196:199], v[184:187]
ds_read_b64_tr_b16 a[10:11], v121 offset:20480
v_mfma_f32_16x16x32_bf16 v[188:191], a[24:27], a[200:203], 0
ds_read_b64_tr_b16 a[12:13], v121 offset:24576
v_mfma_f32_16x16x32_bf16 v[188:191], a[28:31], a[204:207], v[188:191]
ds_read_b64_tr_b16 a[14:15], v121 offset:28672
s_waitcnt lgkmcnt(4)
v_mfma_f32_16x16x32_bf16 v[128:131], a[0:3], a[208:211], v[128:131]
ds_read_b64_tr_b16 a[16:17], v122 offset:16384
ds_read_b64_tr_b16 a[18:19], v122 offset:20480
v_mfma_f32_16x16x32_bf16 v[128:131], a[4:7], a[212:215], v[128:131]
buffer_store_dwordx4 v[224:227], v29, s[60:63], s90 offen offset:3072 sc1
v_mfma_f32_16x16x32_bf16 v[132:135], a[0:3], a[216:219], v[132:135]
ds_read_b64_tr_b16 a[20:21], v122 offset:24576
v_mfma_f32_16x16x32_bf16 v[132:135], a[4:7], a[220:223], v[132:135]
ds_read_b64_tr_b16 a[22:23], v122 offset:28672
s_waitcnt lgkmcnt(4)
v_mfma_f32_16x16x32_bf16 v[136:139], a[8:11], a[208:211], v[136:139]
ds_read_b64_tr_b16 a[24:25], v123 offset:16384
v_mfma_f32_16x16x32_bf16 v[136:139], a[12:15], a[212:215], v[136:139]
ds_read_b64_tr_b16 a[26:27], v123 offset:20480
v_mfma_f32_16x16x32_bf16 v[140:143], a[8:11], a[216:219], v[140:143]
ds_read_b64_tr_b16 a[28:29], v123 offset:24576
v_mfma_f32_16x16x32_bf16 v[140:143], a[12:15], a[220:223], v[140:143]
ds_read_b64_tr_b16 a[30:31], v123 offset:28672
s_waitcnt lgkmcnt(4)
v_mfma_f32_16x16x32_bf16 v[144:147], a[16:19], a[208:211], v[144:147]
ds_read_b64_tr_b16 a[0:1], v124 offset:16384
ds_read_b64_tr_b16 a[2:3], v124 offset:20480
v_mfma_f32_16x16x32_bf16 v[144:147], a[20:23], a[212:215], v[144:147]
buffer_store_dwordx4 v[228:231], v29, s[60:63], s91 offen offset:3072 sc1
v_mfma_f32_16x16x32_bf16 v[148:151], a[16:19], a[216:219], v[148:151]
ds_read_b64_tr_b16 a[4:5], v124 offset:24576
v_mfma_f32_16x16x32_bf16 v[148:151], a[20:23], a[220:223], v[148:151]
ds_read_b64_tr_b16 a[6:7], v124 offset:28672
s_waitcnt lgkmcnt(4)
v_mfma_f32_16x16x32_bf16 v[152:155], a[24:27], a[208:211], v[152:155]
ds_read_b64_tr_b16 a[8:9], v125 offset:16384
v_mfma_f32_16x16x32_bf16 v[152:155], a[28:31], a[212:215], v[152:155]
ds_read_b64_tr_b16 a[10:11], v125 offset:20480
v_mfma_f32_16x16x32_bf16 v[156:159], a[24:27], a[216:219], v[156:159]
ds_read_b64_tr_b16 a[12:13], v125 offset:24576
v_mfma_f32_16x16x32_bf16 v[156:159], a[28:31], a[220:223], v[156:159]
ds_read_b64_tr_b16 a[14:15], v125 offset:28672
s_waitcnt lgkmcnt(4)
v_mfma_f32_16x16x32_bf16 v[160:163], a[0:3], a[208:211], v[160:163]
ds_read_b64_tr_b16 a[16:17], v126 offset:16384
ds_read_b64_tr_b16 a[18:19], v126 offset:20480
v_mfma_f32_16x16x32_bf16 v[160:163], a[4:7], a[212:215], v[160:163]
buffer_store_dwordx4 v[232:235], v29, s[60:63], s92 offen offset:3072 sc1
v_mfma_f32_16x16x32_bf16 v[164:167], a[0:3], a[216:219], v[164:167]
ds_read_b64_tr_b16 a[20:21], v126 offset:24576
v_mfma_f32_16x16x32_bf16 v[164:167], a[4:7], a[220:223], v[164:167]
ds_read_b64_tr_b16 a[22:23], v126 offset:28672
s_waitcnt lgkmcnt(4)
v_mfma_f32_16x16x32_bf16 v[168:171], a[8:11], a[208:211], v[168:171]
ds_read_b64_tr_b16 a[24:25], v127 offset:16384
v_mfma_f32_16x16x32_bf16 v[168:171], a[12:15], a[212:215], v[168:171]
ds_read_b64_tr_b16 a[26:27], v127 offset:20480
v_mfma_f32_16x16x32_bf16 v[172:175], a[8:11], a[216:219], v[172:175]
ds_read_b64_tr_b16 a[28:29], v127 offset:24576
v_mfma_f32_16x16x32_bf16 v[172:175], a[12:15], a[220:223], v[172:175]
ds_read_b64_tr_b16 a[30:31], v127 offset:28672
s_waitcnt lgkmcnt(4)
v_mfma_f32_16x16x32_bf16 v[176:179], a[16:19], a[208:211], v[176:179]
ds_read_b64_tr_b16 a[0:1], v112 offset:0
ds_read_b64_tr_b16 a[2:3], v112 offset:4096
v_mfma_f32_16x16x32_bf16 v[176:179], a[20:23], a[212:215], v[176:179]
buffer_store_dwordx4 v[236:239], v29, s[60:63], s93 offen offset:3072 sc1
v_mfma_f32_16x16x32_bf16 v[180:183], a[16:19], a[216:219], v[180:183]
ds_read_b64_tr_b16 a[4:5], v112 offset:8192
v_mfma_f32_16x16x32_bf16 v[180:183], a[20:23], a[220:223], v[180:183]
ds_read_b64_tr_b16 a[6:7], v112 offset:12288
s_waitcnt lgkmcnt(4)
v_mfma_f32_16x16x32_bf16 v[184:187], a[24:27], a[208:211], v[184:187]
ds_read_b64_tr_b16 a[8:9], v113 offset:0
s_barrier
v_mfma_f32_16x16x32_bf16 v[184:187], a[28:31], a[212:215], v[184:187]
ds_read_b64_tr_b16 a[10:11], v113 offset:4096
v_mfma_f32_16x16x32_bf16 v[188:191], a[24:27], a[216:219], v[188:191]
ds_read_b64_tr_b16 a[12:13], v113 offset:8192
v_mfma_f32_16x16x32_bf16 v[188:191], a[28:31], a[220:223], v[188:191]
ds_read_b64_tr_b16 a[14:15], v113 offset:12288
s_waitcnt lgkmcnt(4)
s_lshr_b32 s80, s5, 1
s_lshr_b32 s83, s101, 21
s_cmp_eq_u32 s80, s83
s_cbranch_scc0 .Lkf_pt1
ds_write_b128 v72, v[208:211] offset:16384
ds_write_b128 v72, v[212:215] offset:18432
ds_write_b128 v72, v[216:219] offset:20480
ds_write_b128 v72, v[220:223] offset:22528
ds_write_b128 v72, v[224:227] offset:17408
ds_write_b128 v72, v[228:231] offset:19456
ds_write_b128 v72, v[232:235] offset:21504
ds_write_b128 v72, v[236:239] offset:23552
.Lkf_pt1:
v_mfma_f32_16x16x32_bf16 v[128:131], a[0:3], a[224:227], v[128:131]
ds_read_b64_tr_b16 a[16:17], v114 offset:0
ds_read_b64_tr_b16 a[18:19], v114 offset:4096
v_mfma_f32_16x16x32_bf16 v[128:131], a[4:7], a[228:231], v[128:131]
s_add_u32 m0, s68, 0x24000
s_add_u32 s20, s12, s6
buffer_load_dwordx4 v52, s[56:59], s20 offen offset:0 lds
v_mfma_f32_16x16x32_bf16 v[132:135], a[0:3], a[232:235], v[132:135]
ds_read_b64_tr_b16 a[20:21], v114 offset:8192
ds_read_b128 a[32:35], v73 offset:0
v_mfma_f32_16x16x32_bf16 v[132:135], a[4:7], a[236:239], v[132:135]
ds_read_b64_tr_b16 a[22:23], v114 offset:12288
s_waitcnt lgkmcnt(5)
v_mfma_f32_16x16x32_bf16 v[136:139], a[8:11], a[224:227], v[136:139]
ds_read_b64_tr_b16 a[24:25], v115 offset:0
ds_read_b64_tr_b16 a[26:27], v115 offset:4096
v_mfma_f32_16x16x32_bf16 v[136:139], a[12:15], a[228:231], v[136:139]
buffer_load_dwordx4 v53, s[56:59], s20 offen offset:1024 lds
s_add_u32 s6, s6, 0x10000
s_and_b32 s6, s6, 0x7ffff
v_mfma_f32_16x16x32_bf16 v[140:143], a[8:11], a[232:235], v[140:143]
ds_read_b64_tr_b16 a[28:29], v115 offset:8192
ds_read_b128 a[36:39], v73 offset:1024
v_mfma_f32_16x16x32_bf16 v[140:143], a[12:15], a[236:239], v[140:143]
ds_read_b64_tr_b16 a[30:31], v115 offset:12288
s_waitcnt lgkmcnt(5)
v_mfma_f32_16x16x32_bf16 v[144:147], a[16:19], a[224:227], v[144:147]
ds_read_b64_tr_b16 a[0:1], v116 offset:0
ds_read_b64_tr_b16 a[2:3], v116 offset:4096
v_mfma_f32_16x16x32_bf16 v[144:147], a[20:23], a[228:231], v[144:147]
buffer_load_dwordx4 v54, s[56:59], s20 offen offset:2048 lds
v_mfma_f32_16x16x32_bf16 v[148:151], a[16:19], a[232:235], v[148:151]
ds_read_b64_tr_b16 a[4:5], v116 offset:8192
ds_read_b128 a[40:43], v73 offset:2048
v_mfma_f32_16x16x32_bf16 v[148:151], a[20:23], a[236:239], v[148:151]
ds_read_b64_tr_b16 a[6:7], v116 offset:12288
s_waitcnt lgkmcnt(5)
v_mfma_f32_16x16x32_bf16 v[152:155], a[24:27], a[224:227], v[152:155]
ds_read_b64_tr_b16 a[8:9], v117 offset:0
ds_read_b64_tr_b16 a[10:11], v117 offset:4096
v_mfma_f32_16x16x32_bf16 v[152:155], a[28:31], a[228:231], v[152:155]
buffer_load_dwordx4 v55, s[56:59], s20 offen offset:3072 lds
v_mfma_f32_16x16x32_bf16 v[156:159], a[24:27], a[232:235], v[156:159]
ds_read_b64_tr_b16 a[12:13], v117 offset:8192
ds_read_b128 a[44:47], v73 offset:3072
v_mfma_f32_16x16x32_bf16 v[156:159], a[28:31], a[236:239], v[156:159]
ds_read_b64_tr_b16 a[14:15], v117 offset:12288
s_waitcnt lgkmcnt(5)
v_mfma_f32_16x16x32_bf16 v[160:163], a[0:3], a[224:227], v[160:163]
ds_read_b64_tr_b16 a[16:17], v118 offset:0
v_mfma_f32_16x16x32_bf16 v[160:163], a[4:7], a[228:231], v[160:163]
ds_read_b64_tr_b16 a[18:19], v118 offset:4096
v_mfma_f32_16x16x32_bf16 v[164:167], a[0:3], a[232:235], v[164:167]
ds_read_b64_tr_b16 a[20:21], v118 offset:8192
v_mfma_f32_16x16x32_bf16 v[164:167], a[4:7], a[236:239], v[164:167]
ds_read_b64_tr_b16 a[22:23], v118 offset:12288
s_waitcnt lgkmcnt(4)
v_mfma_f32_16x16x32_bf16 v[168:171], a[8:11], a[224:227], v[168:171]
ds_read_b64_tr_b16 a[24:25], v119 offset:0
v_mfma_f32_16x16x32_bf16 v[168:171], a[12:15], a[228:231], v[168:171]
ds_read_b64_tr_b16 a[26:27], v119 offset:4096
v_mfma_f32_16x16x32_bf16 v[172:175], a[8:11], a[232:235], v[172:175]
ds_read_b64_tr_b16 a[28:29], v119 offset:8192
v_mfma_f32_16x16x32_bf16 v[172:175], a[12:15], a[236:239], v[172:175]
ds_read_b64_tr_b16 a[30:31], v119 offset:12288
s_waitcnt lgkmcnt(4)
v_mfma_f32_16x16x32_bf16 v[176:179], a[16:19], a[224:227], v[176:179]
ds_read_b64_tr_b16 a[0:1], v112 offset:16384
v_mfma_f32_16x16x32_bf16 v[176:179], a[20:23], a[228:231], v[176:179]
ds_read_b64_tr_b16 a[2:3], v112 offset:20480
v_mfma_f32_16x16x32_bf16 v[180:183], a[16:19], a[232:235], v[180:183]
ds_read_b64_tr_b16 a[4:5], v112 offset:24576
v_mfma_f32_16x16x32_bf16 v[180:183], a[20:23], a[236:239], v[180:183]
ds_read_b64_tr_b16 a[6:7], v112 offset:28672
s_waitcnt lgkmcnt(4)
v_mfma_f32_16x16x32_bf16 v[184:187], a[24:27], a[224:227], v[184:187]
ds_read_b64_tr_b16 a[8:9], v113 offset:16384
v_mfma_f32_16x16x32_bf16 v[184:187], a[28:31], a[228:231], v[184:187]
ds_read_b64_tr_b16 a[10:11], v113 offset:20480
v_mfma_f32_16x16x32_bf16 v[188:191], a[24:27], a[232:235], v[188:191]
ds_read_b64_tr_b16 a[12:13], v113 offset:24576
v_mfma_f32_16x16x32_bf16 v[188:191], a[28:31], a[236:239], v[188:191]
ds_read_b64_tr_b16 a[14:15], v113 offset:28672
s_waitcnt lgkmcnt(4)
v_mfma_f32_16x16x32_bf16 v[128:131], a[0:3], a[240:243], v[128:131]
ds_read_b64_tr_b16 a[16:17], v114 offset:16384
v_mfma_f32_16x16x32_bf16 v[128:131], a[4:7], a[244:247], v[128:131]
ds_read_b64_tr_b16 a[18:19], v114 offset:20480
v_mfma_f32_16x16x32_bf16 v[132:135], a[0:3], a[248:251], v[132:135]
ds_read_b64_tr_b16 a[20:21], v114 offset:24576
v_mfma_f32_16x16x32_bf16 v[132:135], a[4:7], a[252:255], v[132:135]
ds_read_b64_tr_b16 a[22:23], v114 offset:28672
s_waitcnt lgkmcnt(4)
v_mfma_f32_16x16x32_bf16 v[136:139], a[8:11], a[240:243], v[136:139]
ds_read_b64_tr_b16 a[24:25], v115 offset:16384
v_mfma_f32_16x16x32_bf16 v[136:139], a[12:15], a[244:247], v[136:139]
ds_read_b64_tr_b16 a[26:27], v115 offset:20480
v_mfma_f32_16x16x32_bf16 v[140:143], a[8:11], a[248:251], v[140:143]
ds_read_b64_tr_b16 a[28:29], v115 offset:24576
v_mfma_f32_16x16x32_bf16 v[140:143], a[12:15], a[252:255], v[140:143]
ds_read_b64_tr_b16 a[30:31], v115 offset:28672
s_waitcnt lgkmcnt(4)
v_mfma_f32_16x16x32_bf16 v[144:147], a[16:19], a[240:243], v[144:147]
ds_read_b64_tr_b16 a[0:1], v116 offset:16384
v_mfma_f32_16x16x32_bf16 v[144:147], a[20:23], a[244:247], v[144:147]
ds_read_b64_tr_b16 a[2:3], v116 offset:20480
v_mfma_f32_16x16x32_bf16 v[148:151], a[16:19], a[248:251], v[148:151]
ds_read_b64_tr_b16 a[4:5], v116 offset:24576
v_mfma_f32_16x16x32_bf16 v[148:151], a[20:23], a[252:255], v[148:151]
ds_read_b64_tr_b16 a[6:7], v116 offset:28672
s_waitcnt lgkmcnt(4)
v_mfma_f32_16x16x32_bf16 v[152:155], a[24:27], a[240:243], v[152:155]
ds_read_b64_tr_b16 a[8:9], v117 offset:16384
v_mfma_f32_16x16x32_bf16 v[152:155], a[28:31], a[244:247], v[152:155]
ds_read_b64_tr_b16 a[10:11], v117 offset:20480
v_mfma_f32_16x16x32_bf16 v[156:159], a[24:27], a[248:251], v[156:159]
ds_read_b64_tr_b16 a[12:13], v117 offset:24576
v_mfma_f32_16x16x32_bf16 v[156:159], a[28:31], a[252:255], v[156:159]
ds_read_b64_tr_b16 a[14:15], v117 offset:28672
s_waitcnt lgkmcnt(4)
v_mfma_f32_16x16x32_bf16 v[160:163], a[0:3], a[240:243], v[160:163]
ds_read_b64_tr_b16 a[16:17], v118 offset:16384
v_mfma_f32_16x16x32_bf16 v[160:163], a[4:7], a[244:247], v[160:163]
ds_read_b64_tr_b16 a[18:19], v118 offset:20480
v_mfma_f32_16x16x32_bf16 v[164:167], a[0:3], a[248:251], v[164:167]
ds_read_b64_tr_b16 a[20:21], v118 offset:24576
v_mfma_f32_16x16x32_bf16 v[164:167], a[4:7], a[252:255], v[164:167]
ds_read_b64_tr_b16 a[22:23], v118 offset:28672
s_waitcnt lgkmcnt(4)
v_mfma_f32_16x16x32_bf16 v[168:171], a[8:11], a[240:243], v[168:171]
ds_read_b64_tr_b16 a[24:25], v119 offset:16384
v_mfma_f32_16x16x32_bf16 v[168:171], a[12:15], a[244:247], v[168:171]
ds_read_b64_tr_b16 a[26:27], v119 offset:20480
v_mfma_f32_16x16x32_bf16 v[172:175], a[8:11], a[248:251], v[172:175]
ds_read_b64_tr_b16 a[28:29], v119 offset:24576
v_mfma_f32_16x16x32_bf16 v[172:175], a[12:15], a[252:255], v[172:175]
ds_read_b64_tr_b16 a[30:31], v119 offset:28672
s_waitcnt lgkmcnt(4)
v_mfma_f32_16x16x32_bf16 v[176:179], a[16:19], a[240:243], v[176:179]
ds_read_b64_tr_b16 a[0:1], v8 offset:0
v_mfma_f32_16x16x32_bf16 v[176:179], a[20:23], a[244:247], v[176:179]
ds_read_b64_tr_b16 a[2:3], v8 offset:4096
v_mfma_f32_16x16x32_bf16 v[180:183], a[16:19], a[248:251], v[180:183]
ds_read_b64_tr_b16 a[4:5], v8 offset:8192
v_mfma_f32_16x16x32_bf16 v[180:183], a[20:23], a[252:255], v[180:183]
ds_read_b64_tr_b16 a[6:7], v8 offset:12288
s_waitcnt lgkmcnt(4)
v_mfma_f32_16x16x32_bf16 v[184:187], a[24:27], a[240:243], v[184:187]
ds_read_b64_tr_b16 a[8:9], v9 offset:0
ds_read_b64_tr_b16 a[10:11], v9 offset:4096
s_waitcnt vmcnt(4)
s_barrier
v_mfma_f32_16x16x32_bf16 v[184:187], a[28:31], a[244:247], v[184:187]
buffer_store_dwordx4 v[60:63], v57, s[24:27], s81 offen offset:0 sc1
v_mfma_f32_16x16x32_bf16 v[188:191], a[24:27], a[248:251], v[188:191]
ds_read_b64_tr_b16 a[12:13], v9 offset:8192
v_mfma_f32_16x16x32_bf16 v[188:191], a[28:31], a[252:255], v[188:191]
ds_read_b64_tr_b16 a[14:15], v9 offset:12288
s_waitcnt lgkmcnt(4)
v_mfma_f32_16x16x32_bf16 v[128:131], a[0:3], v[80:83], v[128:131]
ds_read_b64_tr_b16 a[16:17], v10 offset:0
ds_read_b64_tr_b16 a[18:19], v10 offset:4096
v_mfma_f32_16x16x32_bf16 v[128:131], a[4:7], v[84:87], v[128:131]
s_mov_b32 m0, s68
s_add_u32 s20, s12, s6
buffer_load_dwordx4 v52, s[56:59], s20 offen offset:0 lds
v_mfma_f32_16x16x32_bf16 v[132:135], a[0:3], v[88:91], v[132:135]
ds_read_b64_tr_b16 a[20:21], v10 offset:8192
ds_read_b128 a[48:51], v73 offset:16384
v_mfma_f32_16x16x32_bf16 v[132:135], a[4:7], v[92:95], v[132:135]
ds_read_b64_tr_b16 a[22:23], v10 offset:12288
s_waitcnt lgkmcnt(5)
v_mfma_f32_16x16x32_bf16 v[136:139], a[8:11], v[80:83], v[136:139]
ds_read_b64_tr_b16 a[24:25], v11 offset:0
ds_read_b64_tr_b16 a[26:27], v11 offset:4096
v_mfma_f32_16x16x32_bf16 v[136:139], a[12:15], v[84:87], v[136:139]
buffer_load_dwordx4 v53, s[56:59], s20 offen offset:1024 lds
s_add_u32 s6, s6, 0x10000
s_and_b32 s6, s6, 0x7ffff
v_mfma_f32_16x16x32_bf16 v[140:143], a[8:11], v[88:91], v[140:143]
ds_read_b64_tr_b16 a[28:29], v11 offset:8192
ds_read_b128 a[52:55], v73 offset:17408
v_mfma_f32_16x16x32_bf16 v[140:143], a[12:15], v[92:95], v[140:143]
ds_read_b64_tr_b16 a[30:31], v11 offset:12288
s_waitcnt lgkmcnt(5)
v_mfma_f32_16x16x32_bf16 v[144:147], a[16:19], v[80:83], v[144:147]
ds_read_b64_tr_b16 a[0:1], v12 offset:0
ds_read_b64_tr_b16 a[2:3], v12 offset:4096
v_mfma_f32_16x16x32_bf16 v[144:147], a[20:23], v[84:87], v[144:147]
buffer_load_dwordx4 v54, s[56:59], s20 offen offset:2048 lds
v_mfma_f32_16x16x32_bf16 v[148:151], a[16:19], v[88:91], v[148:151]
ds_read_b64_tr_b16 a[4:5], v12 offset:8192
ds_read_b128 a[56:59], v73 offset:18432
v_mfma_f32_16x16x32_bf16 v[148:151], a[20:23], v[92:95], v[148:151]
ds_read_b64_tr_b16 a[6:7], v12 offset:12288
s_waitcnt lgkmcnt(5)
v_mfma_f32_16x16x32_bf16 v[152:155], a[24:27], v[80:83], v[152:155]
ds_read_b64_tr_b16 a[8:9], v13 offset:0
ds_read_b64_tr_b16 a[10:11], v13 offset:4096
v_mfma_f32_16x16x32_bf16 v[152:155], a[28:31], v[84:87], v[152:155]
buffer_load_dwordx4 v55, s[56:59], s20 offen offset:3072 lds
v_mfma_f32_16x16x32_bf16 v[156:159], a[24:27], v[88:91], v[156:159]
ds_read_b64_tr_b16 a[12:13], v13 offset:8192
ds_read_b128 a[60:63], v73 offset:19456
v_mfma_f32_16x16x32_bf16 v[156:159], a[28:31], v[92:95], v[156:159]
ds_read_b64_tr_b16 a[14:15], v13 offset:12288
s_waitcnt lgkmcnt(5)
v_mfma_f32_16x16x32_bf16 v[160:163], a[0:3], v[80:83], v[160:163]
ds_read_b64_tr_b16 a[16:17], v14 offset:0
ds_read_b64_tr_b16 a[18:19], v14 offset:4096
v_mfma_f32_16x16x32_bf16 v[160:163], a[4:7], v[84:87], v[160:163]
s_add_u32 m0, s68, 0x4000
s_add_u32 s20, s12, s6
buffer_load_dwordx4 v52, s[56:59], s20 offen offset:0 lds
v_mfma_f32_16x16x32_bf16 v[164:167], a[0:3], v[88:91], v[164:167]
ds_read_b64_tr_b16 a[20:21], v14 offset:8192
v_mfma_f32_16x16x32_bf16 v[164:167], a[4:7], v[92:95], v[164:167]
ds_read_b64_tr_b16 a[22:23], v14 offset:12288
s_waitcnt lgkmcnt(4)
v_mfma_f32_16x16x32_bf16 v[168:171], a[8:11], v[80:83], v[168:171]
ds_read_b64_tr_b16 a[24:25], v15 offset:0
ds_read_b64_tr_b16 a[26:27], v15 offset:4096
v_mfma_f32_16x16x32_bf16 v[168:171], a[12:15], v[84:87], v[168:171]
buffer_load_dwordx4 v53, s[56:59], s20 offen offset:1024 lds
s_add_u32 s6, s6, 0x10000
s_and_b32 s6, s6, 0x7ffff
v_mfma_f32_16x16x32_bf16 v[172:175], a[8:11], v[88:91], v[172:175]
ds_read_b64_tr_b16 a[28:29], v15 offset:8192
v_mfma_f32_16x16x32_bf16 v[172:175], a[12:15], v[92:95], v[172:175]
ds_read_b64_tr_b16 a[30:31], v15 offset:12288
s_waitcnt lgkmcnt(4)
v_mfma_f32_16x16x32_bf16 v[176:179], a[16:19], v[80:83], v[176:179]
ds_read_b64_tr_b16 a[0:1], v112 offset:32768
ds_read_b64_tr_b16 a[2:3], v112 offset:36864
v_mfma_f32_16x16x32_bf16 v[176:179], a[20:23], v[84:87], v[176:179]
buffer_load_dwordx4 v54, s[56:59], s20 offen offset:2048 lds
v_mfma_f32_16x16x32_bf16 v[180:183], a[16:19], v[88:91], v[180:183]
ds_read_b64_tr_b16 a[4:5], v112 offset:40960
v_mfma_f32_16x16x32_bf16 v[180:183], a[20:23], v[92:95], v[180:183]
ds_read_b64_tr_b16 a[6:7], v112 offset:45056
s_waitcnt lgkmcnt(4)
v_mfma_f32_16x16x32_bf16 v[184:187], a[24:27], v[80:83], v[184:187]
ds_read_b64_tr_b16 a[8:9], v113 offset:32768
ds_read_b64_tr_b16 a[10:11], v113 offset:36864
v_mfma_f32_16x16x32_bf16 v[184:187], a[28:31], v[84:87], v[184:187]
buffer_load_dwordx2 v[46:47], v58, s[24:27], s82 offen offset:0 sc1
v_mfma_f32_16x16x32_bf16 v[188:191], a[24:27], v[88:91], v[188:191]
ds_read_b64_tr_b16 a[12:13], v113 offset:40960
v_mfma_f32_16x16x32_bf16 v[188:191], a[28:31], v[92:95], v[188:191]
ds_read_b64_tr_b16 a[14:15], v113 offset:45056
s_waitcnt lgkmcnt(4)
v_mfma_f32_16x16x32_bf16 v[128:131], a[0:3], v[96:99], v[128:131]
ds_read_b64_tr_b16 a[16:17], v114 offset:32768
ds_read_b64_tr_b16 a[18:19], v114 offset:36864
v_mfma_f32_16x16x32_bf16 v[128:131], a[4:7], v[100:103], v[128:131]
buffer_load_dwordx4 v55, s[56:59], s20 offen offset:3072 lds
v_mfma_f32_16x16x32_bf16 v[132:135], a[0:3], v[104:107], v[132:135]
ds_read_b64_tr_b16 a[20:21], v114 offset:40960
v_mfma_f32_16x16x32_bf16 v[132:135], a[4:7], v[108:111], v[132:135]
ds_read_b64_tr_b16 a[22:23], v114 offset:45056
s_waitcnt lgkmcnt(4)
v_mfma_f32_16x16x32_bf16 v[136:139], a[8:11], v[96:99], v[136:139]
ds_read_b64_tr_b16 a[24:25], v115 offset:32768
ds_read_b64_tr_b16 a[26:27], v115 offset:36864
v_mfma_f32_16x16x32_bf16 v[136:139], a[12:15], v[100:103], v[136:139]
s_add_u32 m0, s68, 0x1c000
s_add_u32 s20, s12, s6
buffer_load_dwordx4 v52, s[56:59], s20 offen offset:0 lds
v_mfma_f32_16x16x32_bf16 v[140:143], a[8:11], v[104:107], v[140:143]
ds_read_b64_tr_b16 a[28:29], v115 offset:40960
v_mfma_f32_16x16x32_bf16 v[140:143], a[12:15], v[108:111], v[140:143]
ds_read_b64_tr_b16 a[30:31], v115 offset:45056
s_waitcnt lgkmcnt(4)
v_mfma_f32_16x16x32_bf16 v[144:147], a[16:19], v[96:99], v[144:147]
ds_read_b64_tr_b16 a[0:1], v116 offset:32768
ds_read_b64_tr_b16 a[2:3], v116 offset:36864
v_mfma_f32_16x16x32_bf16 v[144:147], a[20:23], v[100:103], v[144:147]
buffer_load_dwordx4 v53, s[56:59], s20 offen offset:1024 lds
s_add_u32 s6, s6, 0x10000
s_and_b32 s6, s6, 0x7ffff
v_mfma_f32_16x16x32_bf16 v[148:151], a[16:19], v[104:107], v[148:151]
ds_read_b64_tr_b16 a[4:5], v116 offset:40960
v_mfma_f32_16x16x32_bf16 v[148:151], a[20:23], v[108:111], v[148:151]
ds_read_b64_tr_b16 a[6:7], v116 offset:45056
s_waitcnt lgkmcnt(4)
v_mfma_f32_16x16x32_bf16 v[152:155], a[24:27], v[96:99], v[152:155]
ds_read_b64_tr_b16 a[8:9], v117 offset:32768
ds_read_b64_tr_b16 a[10:11], v117 offset:36864
v_mfma_f32_16x16x32_bf16 v[152:155], a[28:31], v[100:103], v[152:155]
buffer_load_dwordx4 v54, s[56:59], s20 offen offset:2048 lds
v_mfma_f32_16x16x32_bf16 v[156:159], a[24:27], v[104:107], v[156:159]
ds_read_b64_tr_b16 a[12:13], v117 offset:40960
v_mfma_f32_16x16x32_bf16 v[156:159], a[28:31], v[108:111], v[156:159]
ds_read_b64_tr_b16 a[14:15], v117 offset:45056
s_waitcnt lgkmcnt(4)
v_mfma_f32_16x16x32_bf16 v[160:163], a[0:3], v[96:99], v[160:163]
ds_read_b64_tr_b16 a[16:17], v118 offset:32768
ds_read_b64_tr_b16 a[18:19], v118 offset:36864
v_mfma_f32_16x16x32_bf16 v[160:163], a[4:7], v[100:103], v[160:163]
buffer_load_dwordx4 v55, s[56:59], s20 offen offset:3072 lds
v_mfma_f32_16x16x32_bf16 v[164:167], a[0:3], v[104:107], v[164:167]
ds_read_b64_tr_b16 a[20:21], v118 offset:40960
v_mfma_f32_16x16x32_bf16 v[164:167], a[4:7], v[108:111], v[164:167]
ds_read_b64_tr_b16 a[22:23], v118 offset:45056
s_waitcnt lgkmcnt(4)
v_mfma_f32_16x16x32_bf16 v[168:171], a[8:11], v[96:99], v[168:171]
ds_read_b64_tr_b16 a[24:25], v119 offset:32768
v_mfma_f32_16x16x32_bf16 v[168:171], a[12:15], v[100:103], v[168:171]
ds_read_b64_tr_b16 a[26:27], v119 offset:36864
v_mfma_f32_16x16x32_bf16 v[172:175], a[8:11], v[104:107], v[172:175]
ds_read_b64_tr_b16 a[28:29], v119 offset:40960
v_mfma_f32_16x16x32_bf16 v[172:175], a[12:15], v[108:111], v[172:175]
ds_read_b64_tr_b16 a[30:31], v119 offset:45056
s_waitcnt lgkmcnt(4)
v_mfma_f32_16x16x32_bf16 v[176:179], a[16:19], v[96:99], v[176:179]
ds_read_b64_tr_b16 a[0:1], v8 offset:16384
v_mfma_f32_16x16x32_bf16 v[176:179], a[20:23], v[100:103], v[176:179]
ds_read_b64_tr_b16 a[2:3], v8 offset:20480
v_mfma_f32_16x16x32_bf16 v[180:183], a[16:19], v[104:107], v[180:183]
ds_read_b64_tr_b16 a[4:5], v8 offset:24576
v_mfma_f32_16x16x32_bf16 v[180:183], a[20:23], v[108:111], v[180:183]
ds_read_b64_tr_b16 a[6:7], v8 offset:28672
s_waitcnt lgkmcnt(4)
v_mfma_f32_16x16x32_bf16 v[184:187], a[24:27], v[96:99], v[184:187]
ds_read_b64_tr_b16 a[8:9], v9 offset:16384
s_waitcnt vmcnt(26)
s_waitcnt vmcnt(30)
s_barrier
v_mfma_f32_16x16x32_bf16 v[184:187], a[28:31], v[100:103], v[184:187]
ds_read_b64_tr_b16 a[10:11], v9 offset:20480
v_mfma_f32_16x16x32_bf16 v[188:191], a[24:27], v[104:107], v[188:191]
ds_read_b64_tr_b16 a[12:13], v9 offset:24576
v_mfma_f32_16x16x32_bf16 v[188:191], a[28:31], v[108:111], v[188:191]
ds_read_b64_tr_b16 a[14:15], v9 offset:28672
s_waitcnt lgkmcnt(4)
s_waitcnt vmcnt(5)
v_cmp_eq_u64_e32 vcc, s[94:95], v[46:47]
s_nop 0
s_and_b32 s83, vcc_lo, 15
s_cmp_eq_u32 s83, 15
s_cbranch_scc0 .Lkf_spo
; ---- o1
.Lkf_o1:
v_mfma_f32_16x16x32_bf16 v[128:131], a[0:3], a[128:131], v[128:131]
ds_read_b64_tr_b16 a[16:17], v10 offset:16384
ds_read_b64_tr_b16 a[18:19], v10 offset:20480
v_mfma_f32_16x16x32_bf16 v[128:131], a[4:7], a[132:135], v[128:131]
s_add_u32 s13, s78, s76
buffer_load_dwordx4 a[64:67], v29, s[48:51], s13 offen offset:0 sc1
v_mfma_f32_16x16x32_bf16 v[132:135], a[0:3], a[136:139], v[132:135]
ds_read_b64_tr_b16 a[20:21], v10 offset:24576
buffer_load_dwordx4 a[68:71], v29, s[48:51], s13 offen offset:1024 sc1
s_add_u32 s14, s13, 0x20000
v_mfma_f32_16x16x32_bf16 v[132:135], a[4:7], a[140:143], v[132:135]
ds_read_b64_tr_b16 a[22:23], v10 offset:28672
s_waitcnt lgkmcnt(4)
v_mfma_f32_16x16x32_bf16 v[136:139], a[8:11], a[128:131], v[136:139]
ds_read_b64_tr_b16 a[24:25], v11 offset:16384
ds_read_b64_tr_b16 a[26:27], v11 offset:20480
v_mfma_f32_16x16x32_bf16 v[136:139], a[12:15], a[132:135], v[136:139]
s_add_u32 m0, s68, 0x8000
s_add_u32 s20, s12, s6
buffer_load_dwordx4 v52, s[56:59], s20 offen offset:0 lds
v_mfma_f32_16x16x32_bf16 v[140:143], a[8:11], a[136:139], v[140:143]
ds_read_b64_tr_b16 a[28:29], v11 offset:24576
buffer_load_dwordx4 a[72:75], v29, s[48:51], s14 offen offset:0 sc1
s_add_u32 s76, s76, 0x1000
s_and_b32 s76, s76, 0x3fff
v_mfma_f32_16x16x32_bf16 v[140:143], a[12:15], a[140:143], v[140:143]
ds_read_b64_tr_b16 a[30:31], v11 offset:28672
s_waitcnt lgkmcnt(4)
v_mfma_f32_16x16x32_bf16 v[144:147], a[16:19], a[128:131], v[144:147]
ds_read_b64_tr_b16 a[0:1], v12 offset:16384
ds_read_b64_tr_b16 a[2:3], v12 offset:20480
v_mfma_f32_16x16x32_bf16 v[144:147], a[20:23], a[132:135], v[144:147]
buffer_load_dwordx4 a[76:79], v29, s[48:51], s14 offen offset:1024 sc1
v_mfma_f32_16x16x32_bf16 v[148:151], a[16:19], a[136:139], v[148:151]
ds_read_b64_tr_b16 a[4:5], v12 offset:24576
v_mfma_f32_16x16x32_bf16 v[148:151], a[20:23], a[140:143], v[148:151]
ds_read_b64_tr_b16 a[6:7], v12 offset:28672
s_waitcnt lgkmcnt(4)
v_mfma_f32_16x16x32_bf16 v[152:155], a[24:27], a[128:131], v[152:155]
ds_read_b64_tr_b16 a[8:9], v13 offset:16384
ds_read_b64_tr_b16 a[10:11], v13 offset:20480
v_mfma_f32_16x16x32_bf16 v[152:155], a[28:31], a[132:135], v[152:155]
buffer_load_dwordx4 v53, s[56:59], s20 offen offset:1024 lds
s_add_u32 s6, s6, 0x10000
s_and_b32 s6, s6, 0x7ffff
v_mfma_f32_16x16x32_bf16 v[156:159], a[24:27], a[136:139], v[156:159]
ds_read_b64_tr_b16 a[12:13], v13 offset:24576
v_mfma_f32_16x16x32_bf16 v[156:159], a[28:31], a[140:143], v[156:159]
ds_read_b64_tr_b16 a[14:15], v13 offset:28672
s_waitcnt lgkmcnt(4)
v_mfma_f32_16x16x32_bf16 v[160:163], a[0:3], a[128:131], v[160:163]
ds_read_b64_tr_b16 a[16:17], v14 offset:16384
ds_read_b64_tr_b16 a[18:19], v14 offset:20480
v_mfma_f32_16x16x32_bf16 v[160:163], a[4:7], a[132:135], v[160:163]
buffer_load_dwordx4 a[80:83], v29, s[48:51], s13 offen offset:2048 sc1
v_mfma_f32_16x16x32_bf16 v[164:167], a[0:3], a[136:139], v[164:167]
ds_read_b64_tr_b16 a[20:21], v14 offset:24576
v_mfma_f32_16x16x32_bf16 v[164:167], a[4:7], a[140:143], v[164:167]
ds_read_b64_tr_b16 a[22:23], v14 offset:28672
s_waitcnt lgkmcnt(4)
v_mfma_f32_16x16x32_bf16 v[168:171], a[8:11], a[128:131], v[168:171]
ds_read_b64_tr_b16 a[24:25], v15 offset:16384
ds_read_b64_tr_b16 a[26:27], v15 offset:20480
v_mfma_f32_16x16x32_bf16 v[168:171], a[12:15], a[132:135], v[168:171]
buffer_load_dwordx4 v54, s[56:59], s20 offen offset:2048 lds
v_mfma_f32_16x16x32_bf16 v[172:175], a[8:11], a[136:139], v[172:175]
ds_read_b64_tr_b16 a[28:29], v15 offset:24576
v_mfma_f32_16x16x32_bf16 v[172:175], a[12:15], a[140:143], v[172:175]
ds_read_b64_tr_b16 a[30:31], v15 offset:28672
s_waitcnt lgkmcnt(4)
v_mfma_f32_16x16x32_bf16 v[176:179], a[16:19], a[128:131], v[176:179]
ds_read_b64_tr_b16 a[0:1], v112 offset:49152
ds_read_b64_tr_b16 a[2:3], v112 offset:53248
v_mfma_f32_16x16x32_bf16 v[176:179], a[20:23], a[132:135], v[176:179]
buffer_load_dwordx4 v55, s[56:59], s20 offen offset:3072 lds
v_mfma_f32_16x16x32_bf16 v[180:183], a[16:19], a[136:139], v[180:183]
ds_read_b64_tr_b16 a[4:5], v112 offset:57344
v_mfma_f32_16x16x32_bf16 v[180:183], a[20:23], a[140:143], v[180:183]
ds_read_b64_tr_b16 a[6:7], v112 offset:61440
s_waitcnt lgkmcnt(4)
v_mfma_f32_16x16x32_bf16 v[184:187], a[24:27], a[128:131], v[184:187]
ds_read_b64_tr_b16 a[8:9], v113 offset:49152
ds_read_b64_tr_b16 a[10:11], v113 offset:53248
v_mfma_f32_16x16x32_bf16 v[184:187], a[28:31], a[132:135], v[184:187]
buffer_load_dwordx2 v[46:47], v58, s[24:27], s100 offen offset:0 sc1
v_mfma_f32_16x16x32_bf16 v[188:191], a[24:27], a[136:139], v[188:191]
ds_read_b64_tr_b16 a[12:13], v113 offset:57344
v_mfma_f32_16x16x32_bf16 v[188:191], a[28:31], a[140:143], v[188:191]
ds_read_b64_tr_b16 a[14:15], v113 offset:61440
s_waitcnt lgkmcnt(4)
v_mfma_f32_16x16x32_bf16 v[192:195], a[0:3], a[32:35], 0
ds_read_b64_tr_b16 a[16:17], v114 offset:49152
ds_read_b64_tr_b16 a[18:19], v114 offset:53248
v_mfma_f32_16x16x32_bf16 v[192:195], a[4:7], a[36:39], v[192:195]
buffer_load_dwordx4 a[84:87], v29, s[48:51], s13 offen offset:3072 sc1
v_mfma_f32_16x16x32_bf16 v[196:199], a[0:3], a[40:43], 0
ds_read_b64_tr_b16 a[20:21], v114 offset:57344
buffer_load_dwordx4 a[88:91], v29, s[48:51], s14 offen offset:2048 sc1
v_mfma_f32_16x16x32_bf16 v[196:199], a[4:7], a[44:47], v[196:199]
ds_read_b64_tr_b16 a[22:23], v114 offset:61440
s_waitcnt lgkmcnt(4)
v_mfma_f32_16x16x32_bf16 v[200:203], a[8:11], a[32:35], 0
ds_read_b64_tr_b16 a[24:25], v115 offset:49152
ds_read_b64_tr_b16 a[26:27], v115 offset:53248
v_mfma_f32_16x16x32_bf16 v[200:203], a[12:15], a[36:39], v[200:203]
s_add_u32 m0, s68, 0x10000
s_add_u32 s20, s12, s6
buffer_load_dwordx4 v52, s[56:59], s20 offen offset:0 lds
v_mfma_f32_16x16x32_bf16 v[204:207], a[8:11], a[40:43], 0
ds_read_b64_tr_b16 a[28:29], v115 offset:57344
v_cvt_pk_bf16_f32 v64, v128, v129
v_mfma_f32_16x16x32_bf16 v[204:207], a[12:15], a[44:47], v[204:207]
ds_read_b64_tr_b16 a[30:31], v115 offset:61440
s_waitcnt lgkmcnt(4)
v_mfma_f32_16x16x32_bf16 v[208:211], a[16:19], a[32:35], 0
ds_read_b64_tr_b16 a[0:1], v116 offset:49152
ds_read_b64_tr_b16 a[2:3], v116 offset:53248
v_mfma_f32_16x16x32_bf16 v[208:211], a[20:23], a[36:39], v[208:211]
buffer_load_dwordx4 a[92:95], v29, s[48:51], s14 offen offset:3072 sc1
v_mfma_f32_16x16x32_bf16 v[212:215], a[16:19], a[40:43], 0
ds_read_b64_tr_b16 a[4:5], v116 offset:57344
v_cvt_pk_bf16_f32 v65, v130, v131
v_mfma_f32_16x16x32_bf16 v[212:215], a[20:23], a[44:47], v[212:215]
ds_read_b64_tr_b16 a[6:7], v116 offset:61440
s_waitcnt lgkmcnt(4)
v_mfma_f32_16x16x32_bf16 v[216:219], a[24:27], a[32:35], 0
ds_read_b64_tr_b16 a[8:9], v117 offset:49152
ds_read_b64_tr_b16 a[10:11], v117 offset:53248
v_mfma_f32_16x16x32_bf16 v[216:219], a[28:31], a[36:39], v[216:219]
buffer_load_dwordx4 v53, s[56:59], s20 offen offset:1024 lds
s_add_u32 s6, s6, 0x10000
s_and_b32 s6, s6, 0x7ffff
v_mfma_f32_16x16x32_bf16 v[220:223], a[24:27], a[40:43], 0
ds_read_b64_tr_b16 a[12:13], v117 offset:57344
v_cvt_pk_bf16_f32 v66, v132, v133
v_mfma_f32_16x16x32_bf16 v[220:223], a[28:31], a[44:47], v[220:223]
ds_read_b64_tr_b16 a[14:15], v117 offset:61440
s_waitcnt lgkmcnt(4)
v_mfma_f32_16x16x32_bf16 v[224:227], a[0:3], a[32:35], 0
ds_read_b64_tr_b16 a[16:17], v118 offset:49152
ds_read_b64_tr_b16 a[18:19], v118 offset:53248
v_mfma_f32_16x16x32_bf16 v[224:227], a[4:7], a[36:39], v[224:227]
s_add_u32 s13, s78, s76
buffer_load_dwordx4 a[96:99], v29, s[48:51], s13 offen offset:0 sc1
v_mfma_f32_16x16x32_bf16 v[228:231], a[0:3], a[40:43], 0
ds_read_b64_tr_b16 a[20:21], v118 offset:57344
v_cvt_pk_bf16_f32 v67, v134, v135
v_mfma_f32_16x16x32_bf16 v[228:231], a[4:7], a[44:47], v[228:231]
ds_read_b64_tr_b16 a[22:23], v118 offset:61440
s_waitcnt lgkmcnt(4)
v_mfma_f32_16x16x32_bf16 v[232:235], a[8:11], a[32:35], 0
ds_read_b64_tr_b16 a[24:25], v119 offset:49152
ds_read_b64_tr_b16 a[26:27], v119 offset:53248
v_mfma_f32_16x16x32_bf16 v[232:235], a[12:15], a[36:39], v[232:235]
s_mov_b32 s80, 0
buffer_store_dwordx4 v[64:67], v29, s[64:67], s80 offen offset:0 sc1
v_mfma_f32_16x16x32_bf16 v[236:239], a[8:11], a[40:43], 0
ds_read_b64_tr_b16 a[28:29], v119 offset:57344
v_mfma_f32_16x16x32_bf16 v[236:239], a[12:15], a[44:47], v[236:239]
ds_read_b64_tr_b16 a[30:31], v119 offset:61440
s_waitcnt lgkmcnt(4)
v_mfma_f32_16x16x32_bf16 v[240:243], a[16:19], a[32:35], 0
ds_read_b64_tr_b16 a[0:1], v8 offset:32768
ds_read_b64_tr_b16 a[2:3], v8 offset:36864
v_mfma_f32_16x16x32_bf16 v[240:243], a[20:23], a[36:39], v[240:243]
buffer_load_dwordx4 v54, s[56:59], s20 offen offset:2048 lds
v_mfma_f32_16x16x32_bf16 v[244:247], a[16:19], a[40:43], 0
ds_read_b64_tr_b16 a[4:5], v8 offset:40960
v_mfma_f32_16x16x32_bf16 v[244:247], a[20:23], a[44:47], v[244:247]
ds_read_b64_tr_b16 a[6:7], v8 offset:45056
s_waitcnt lgkmcnt(4)
v_mfma_f32_16x16x32_bf16 v[248:251], a[24:27], a[32:35], 0
ds_read_b64_tr_b16 a[8:9], v9 offset:32768
ds_read_b64_tr_b16 a[10:11], v9 offset:36864
s_waitcnt vmcnt(27)
s_barrier
v_mfma_f32_16x16x32_bf16 v[248:251], a[28:31], a[36:39], v[248:251]
buffer_load_dwordx4 a[100:103], v29, s[48:51], s13 offen offset:1024 sc1
s_add_u32 s14, s13, 0x20000
v_mfma_f32_16x16x32_bf16 v[252:255], a[24:27], a[40:43], 0
ds_read_b64_tr_b16 a[12:13], v9 offset:40960
v_mfma_f32_16x16x32_bf16 v[252:255], a[28:31], a[44:47], v[252:255]
ds_read_b64_tr_b16 a[14:15], v9 offset:45056
s_waitcnt lgkmcnt(4)
v_mfma_f32_16x16x32_bf16 v[192:195], a[0:3], a[48:51], v[192:195]
ds_read_b64_tr_b16 a[16:17], v10 offset:32768
ds_read_b64_tr_b16 a[18:19], v10 offset:36864
v_mfma_f32_16x16x32_bf16 v[192:195], a[4:7], a[52:55], v[192:195]
buffer_load_dwordx4 a[104:107], v29, s[48:51], s14 offen offset:0 sc1
s_add_u32 s76, s76, 0x1000
s_and_b32 s76, s76, 0x3fff
v_mfma_f32_16x16x32_bf16 v[196:199], a[0:3], a[56:59], v[196:199]
ds_read_b64_tr_b16 a[20:21], v10 offset:40960
v_cvt_pk_bf16_f32 v68, v136, v137
v_mfma_f32_16x16x32_bf16 v[196:199], a[4:7], a[60:63], v[196:199]
ds_read_b64_tr_b16 a[22:23], v10 offset:45056
s_waitcnt lgkmcnt(4)
v_mfma_f32_16x16x32_bf16 v[200:203], a[8:11], a[48:51], v[200:203]
ds_read_b64_tr_b16 a[24:25], v11 offset:32768
ds_read_b64_tr_b16 a[26:27], v11 offset:36864
v_mfma_f32_16x16x32_bf16 v[200:203], a[12:15], a[52:55], v[200:203]
buffer_load_dwordx4 v55, s[56:59], s20 offen offset:3072 lds
v_mfma_f32_16x16x32_bf16 v[204:207], a[8:11], a[56:59], v[204:207]
ds_read_b64_tr_b16 a[28:29], v11 offset:40960
v_cvt_pk_bf16_f32 v69, v138, v139
v_mfma_f32_16x16x32_bf16 v[204:207], a[12:15], a[60:63], v[204:207]
ds_read_b64_tr_b16 a[30:31], v11 offset:45056
s_waitcnt lgkmcnt(4)
v_mfma_f32_16x16x32_bf16 v[208:211], a[16:19], a[48:51], v[208:211]
ds_read_b64_tr_b16 a[0:1], v12 offset:32768
ds_read_b64_tr_b16 a[2:3], v12 offset:36864
v_mfma_f32_16x16x32_bf16 v[208:211], a[20:23], a[52:55], v[208:211]
buffer_load_dwordx4 a[108:111], v29, s[48:51], s14 offen offset:1024 sc1
v_mfma_f32_16x16x32_bf16 v[212:215], a[16:19], a[56:59], v[212:215]
ds_read_b64_tr_b16 a[4:5], v12 offset:40960
v_cvt_pk_bf16_f32 v70, v140, v141
v_mfma_f32_16x16x32_bf16 v[212:215], a[20:23], a[60:63], v[212:215]
ds_read_b64_tr_b16 a[6:7], v12 offset:45056
s_waitcnt lgkmcnt(4)
v_mfma_f32_16x16x32_bf16 v[216:219], a[24:27], a[48:51], v[216:219]
ds_read_b64_tr_b16 a[8:9], v13 offset:32768
ds_read_b64_tr_b16 a[10:11], v13 offset:36864
v_mfma_f32_16x16x32_bf16 v[216:219], a[28:31], a[52:55], v[216:219]
s_add_u32 m0, s68, 0x20000
s_add_u32 s12, s15, s7
s_add_u32 s20, s12, s6
buffer_load_dwordx4 v52, s[56:59], s20 offen offset:0 lds
v_mfma_f32_16x16x32_bf16 v[220:223], a[24:27], a[56:59], v[220:223]
ds_read_b64_tr_b16 a[12:13], v13 offset:40960
v_cvt_pk_bf16_f32 v71, v142, v143
v_mfma_f32_16x16x32_bf16 v[220:223], a[28:31], a[60:63], v[220:223]
ds_read_b64_tr_b16 a[14:15], v13 offset:45056
s_waitcnt lgkmcnt(4)
v_mfma_f32_16x16x32_bf16 v[224:227], a[0:3], a[48:51], v[224:227]
ds_read_b64_tr_b16 a[16:17], v14 offset:32768
ds_read_b64_tr_b16 a[18:19], v14 offset:36864
v_mfma_f32_16x16x32_bf16 v[224:227], a[4:7], a[52:55], v[224:227]
s_mov_b32 s80, 0
buffer_store_dwordx4 v[68:71], v29, s[64:67], s80 offen offset:1024 sc1
v_mfma_f32_16x16x32_bf16 v[228:231], a[0:3], a[56:59], v[228:231]
ds_read_b64_tr_b16 a[20:21], v14 offset:40960
v_mfma_f32_16x16x32_bf16 v[228:231], a[4:7], a[60:63], v[228:231]
ds_read_b64_tr_b16 a[22:23], v14 offset:45056
s_waitcnt lgkmcnt(4)
v_mfma_f32_16x16x32_bf16 v[232:235], a[8:11], a[48:51], v[232:235]
ds_read_b64_tr_b16 a[24:25], v15 offset:32768
ds_read_b64_tr_b16 a[26:27], v15 offset:36864
v_mfma_f32_16x16x32_bf16 v[232:235], a[12:15], a[52:55], v[232:235]
buffer_load_dwordx4 v53, s[56:59], s20 offen offset:1024 lds
s_add_u32 s6, s6, 0x10000
s_and_b32 s6, s6, 0x7ffff
v_mfma_f32_16x16x32_bf16 v[236:239], a[8:11], a[56:59], v[236:239]
ds_read_b64_tr_b16 a[28:29], v15 offset:40960
v_mfma_f32_16x16x32_bf16 v[236:239], a[12:15], a[60:63], v[236:239]
ds_read_b64_tr_b16 a[30:31], v15 offset:45056
s_waitcnt lgkmcnt(4)
v_mfma_f32_16x16x32_bf16 v[240:243], a[16:19], a[48:51], v[240:243]
ds_read_b64_tr_b16 a[0:1], v120 offset:16384
ds_read_b64_tr_b16 a[2:3], v120 offset:20480
v_mfma_f32_16x16x32_bf16 v[240:243], a[20:23], a[52:55], v[240:243]
buffer_load_dwordx4 v54, s[56:59], s20 offen offset:2048 lds
v_mfma_f32_16x16x32_bf16 v[244:247], a[16:19], a[56:59], v[244:247]
ds_read_b64_tr_b16 a[4:5], v120 offset:24576
v_mfma_f32_16x16x32_bf16 v[244:247], a[20:23], a[60:63], v[244:247]
ds_read_b64_tr_b16 a[6:7], v120 offset:28672
s_waitcnt lgkmcnt(4)
v_mfma_f32_16x16x32_bf16 v[248:251], a[24:27], a[48:51], v[248:251]
ds_read_b64_tr_b16 a[8:9], v121 offset:16384
ds_read_b64_tr_b16 a[10:11], v121 offset:20480
s_waitcnt vmcnt(21)
v_mfma_f32_16x16x32_bf16 v[248:251], a[28:31], a[52:55], v[248:251]
buffer_load_dwordx4 a[112:115], v29, s[48:51], s13 offen offset:2048 sc1
v_mfma_f32_16x16x32_bf16 v[252:255], a[24:27], a[56:59], v[252:255]
ds_read_b64_tr_b16 a[12:13], v121 offset:24576
v_mfma_f32_16x16x32_bf16 v[252:255], a[28:31], a[60:63], v[252:255]
ds_read_b64_tr_b16 a[14:15], v121 offset:28672
s_waitcnt lgkmcnt(4)
v_mfma_f32_16x16x32_bf16 v[192:195], a[0:3], a[64:67], v[192:195]
ds_read_b64_tr_b16 a[16:17], v122 offset:16384
ds_read_b64_tr_b16 a[18:19], v122 offset:20480
v_mfma_f32_16x16x32_bf16 v[192:195], a[4:7], a[68:71], v[192:195]
buffer_load_dwordx4 a[116:119], v29, s[48:51], s13 offen offset:3072 sc1
v_mfma_f32_16x16x32_bf16 v[196:199], a[0:3], a[72:75], v[196:199]
ds_read_b64_tr_b16 a[20:21], v122 offset:24576
buffer_load_dwordx4 a[120:123], v29, s[48:51], s14 offen offset:2048 sc1
v_mfma_f32_16x16x32_bf16 v[196:199], a[4:7], a[76:79], v[196:199]
ds_read_b64_tr_b16 a[22:23], v122 offset:28672
s_waitcnt lgkmcnt(4)
v_mfma_f32_16x16x32_bf16 v[200:203], a[8:11], a[64:67], v[200:203]
ds_read_b64_tr_b16 a[24:25], v123 offset:16384
ds_read_b64_tr_b16 a[26:27], v123 offset:20480
v_mfma_f32_16x16x32_bf16 v[200:203], a[12:15], a[68:71], v[200:203]
buffer_load_dwordx4 v55, s[56:59], s20 offen offset:3072 lds
v_mfma_f32_16x16x32_bf16 v[204:207], a[8:11], a[72:75], v[204:207]
ds_read_b64_tr_b16 a[28:29], v123 offset:24576
v_cvt_pk_bf16_f32 v64, v144, v145
v_mfma_f32_16x16x32_bf16 v[204:207], a[12:15], a[76:79], v[204:207]
ds_read_b64_tr_b16 a[30:31], v123 offset:28672
s_waitcnt lgkmcnt(4)
v_mfma_f32_16x16x32_bf16 v[208:211], a[16:19], a[64:67], v[208:211]
ds_read_b64_tr_b16 a[0:1], v124 offset:16384
ds_read_b64_tr_b16 a[2:3], v124 offset:20480
v_mfma_f32_16x16x32_bf16 v[208:211], a[20:23], a[68:71], v[208:211]
buffer_load_dwordx4 a[124:127], v29, s[48:51], s14 offen offset:3072 sc1
v_mfma_f32_16x16x32_bf16 v[212:215], a[16:19], a[72:75], v[212:215]
ds_read_b64_tr_b16 a[4:5], v124 offset:24576
v_cvt_pk_bf16_f32 v65, v146, v147
v_mfma_f32_16x16x32_bf16 v[212:215], a[20:23], a[76:79], v[212:215]
ds_read_b64_tr_b16 a[6:7], v124 offset:28672
s_waitcnt lgkmcnt(4)
v_mfma_f32_16x16x32_bf16 v[216:219], a[24:27], a[64:67], v[216:219]
ds_read_b64_tr_b16 a[8:9], v125 offset:16384
ds_read_b64_tr_b16 a[10:11], v125 offset:20480
v_mfma_f32_16x16x32_bf16 v[216:219], a[28:31], a[68:71], v[216:219]
s_add_u32 m0, s68, 0x14000
s_add_u32 s20, s12, s6
buffer_load_dwordx4 v52, s[56:59], s20 offen offset:0 lds
v_mfma_f32_16x16x32_bf16 v[220:223], a[24:27], a[72:75], v[220:223]
ds_read_b64_tr_b16 a[12:13], v125 offset:24576
v_cvt_pk_bf16_f32 v66, v148, v149
v_mfma_f32_16x16x32_bf16 v[220:223], a[28:31], a[76:79], v[220:223]
ds_read_b64_tr_b16 a[14:15], v125 offset:28672
s_waitcnt lgkmcnt(4)
v_mfma_f32_16x16x32_bf16 v[224:227], a[0:3], a[64:67], v[224:227]
ds_read_b64_tr_b16 a[16:17], v126 offset:16384
ds_read_b64_tr_b16 a[18:19], v126 offset:20480
v_mfma_f32_16x16x32_bf16 v[224:227], a[4:7], a[68:71], v[224:227]
s_add_u32 s13, s78, s76
buffer_load_dwordx4 a[144:147], v29, s[48:51], s13 offen offset:0 sc1
v_mfma_f32_16x16x32_bf16 v[228:231], a[0:3], a[72:75], v[228:231]
ds_read_b64_tr_b16 a[20:21], v126 offset:24576
v_cvt_pk_bf16_f32 v67, v150, v151
v_mfma_f32_16x16x32_bf16 v[228:231], a[4:7], a[76:79], v[228:231]
ds_read_b64_tr_b16 a[22:23], v126 offset:28672
s_waitcnt lgkmcnt(4)
v_mfma_f32_16x16x32_bf16 v[232:235], a[8:11], a[64:67], v[232:235]
ds_read_b64_tr_b16 a[24:25], v127 offset:16384
ds_read_b64_tr_b16 a[26:27], v127 offset:20480
v_mfma_f32_16x16x32_bf16 v[232:235], a[12:15], a[68:71], v[232:235]
s_mov_b32 s80, 0
buffer_store_dwordx4 v[64:67], v29, s[64:67], s80 offen offset:2048 sc1
v_mfma_f32_16x16x32_bf16 v[236:239], a[8:11], a[72:75], v[236:239]
ds_read_b64_tr_b16 a[28:29], v127 offset:24576
v_mfma_f32_16x16x32_bf16 v[236:239], a[12:15], a[76:79], v[236:239]
ds_read_b64_tr_b16 a[30:31], v127 offset:28672
s_waitcnt lgkmcnt(4)
v_mfma_f32_16x16x32_bf16 v[240:243], a[16:19], a[64:67], v[240:243]
ds_read_b64_tr_b16 a[0:1], v112 offset:0
ds_read_b64_tr_b16 a[2:3], v112 offset:4096
v_mfma_f32_16x16x32_bf16 v[240:243], a[20:23], a[68:71], v[240:243]
buffer_load_dwordx4 v53, s[56:59], s20 offen offset:1024 lds
s_add_u32 s6, s6, 0x10000
s_and_b32 s6, s6, 0x7ffff
v_mfma_f32_16x16x32_bf16 v[244:247], a[16:19], a[72:75], v[244:247]
ds_read_b64_tr_b16 a[4:5], v112 offset:8192
v_mfma_f32_16x16x32_bf16 v[244:247], a[20:23], a[76:79], v[244:247]
ds_read_b64_tr_b16 a[6:7], v112 offset:12288
s_waitcnt lgkmcnt(4)
v_mfma_f32_16x16x32_bf16 v[248:251], a[24:27], a[64:67], v[248:251]
ds_read_b64_tr_b16 a[8:9], v113 offset:0
ds_read_b64_tr_b16 a[10:11], v113 offset:4096
s_waitcnt vmcnt(21)
s_waitcnt vmcnt(35)
s_barrier
v_mfma_f32_16x16x32_bf16 v[248:251], a[28:31], a[68:71], v[248:251]
buffer_load_dwordx4 a[148:151], v29, s[48:51], s13 offen offset:1024 sc1
s_add_u32 s14, s13, 0x20000
v_mfma_f32_16x16x32_bf16 v[252:255], a[24:27], a[72:75], v[252:255]
ds_read_b64_tr_b16 a[12:13], v113 offset:8192
v_mfma_f32_16x16x32_bf16 v[252:255], a[28:31], a[76:79], v[252:255]
ds_read_b64_tr_b16 a[14:15], v113 offset:12288
s_waitcnt lgkmcnt(4)
s_waitcnt vmcnt(26)
v_cmp_eq_u64_e32 vcc, s[94:95], v[46:47]
s_nop 0
s_and_b32 s83, vcc_lo, 15
s_cmp_eq_u32 s83, 15
s_cbranch_scc0 .Lkf_spq
; ---- o1b
.Lkf_o1b:
v_mfma_f32_16x16x32_bf16 v[192:195], a[0:3], a[80:83], v[192:195]
ds_read_b64_tr_b16 a[16:17], v114 offset:0
ds_read_b64_tr_b16 a[18:19], v114 offset:4096
v_mfma_f32_16x16x32_bf16 v[192:195], a[4:7], a[84:87], v[192:195]
buffer_load_dwordx4 a[152:155], v29, s[48:51], s14 offen offset:0 sc1
s_add_u32 s76, s76, 0x1000
s_and_b32 s76, s76, 0x3fff
v_mfma_f32_16x16x32_bf16 v[196:199], a[0:3], a[88:91], v[196:199]
ds_read_b64_tr_b16 a[20:21], v114 offset:8192
v_cvt_pk_bf16_f32 v68, v152, v153
v_mfma_f32_16x16x32_bf16 v[196:199], a[4:7], a[92:95], v[196:199]
ds_read_b64_tr_b16 a[22:23], v114 offset:12288
s_waitcnt lgkmcnt(4)
v_mfma_f32_16x16x32_bf16 v[200:203], a[8:11], a[80:83], v[200:203]
ds_read_b64_tr_b16 a[24:25], v115 offset:0
ds_read_b64_tr_b16 a[26:27], v115 offset:4096
v_mfma_f32_16x16x32_bf16 v[200:203], a[12:15], a[84:87], v[200:203]
buffer_load_dwordx4 v54, s[56:59], s20 offen offset:2048 lds
v_mfma_f32_16x16x32_bf16 v[204:207], a[8:11], a[88:91], v[204:207]
ds_read_b64_tr_b16 a[28:29], v115 offset:8192
v_cvt_pk_bf16_f32 v69, v154, v155
v_mfma_f32_16x16x32_bf16 v[204:207], a[12:15], a[92:95], v[204:207]
ds_read_b64_tr_b16 a[30:31], v115 offset:12288
s_waitcnt lgkmcnt(4)
v_mfma_f32_16x16x32_bf16 v[208:211], a[16:19], a[80:83], v[208:211]
ds_read_b64_tr_b16 a[0:1], v116 offset:0
ds_read_b64_tr_b16 a[2:3], v116 offset:4096
v_mfma_f32_16x16x32_bf16 v[208:211], a[20:23], a[84:87], v[208:211]
buffer_load_dwordx4 a[156:159], v29, s[48:51], s14 offen offset:1024 sc1
v_mfma_f32_16x16x32_bf16 v[212:215], a[16:19], a[88:91], v[212:215]
ds_read_b64_tr_b16 a[4:5], v116 offset:8192
v_cvt_pk_bf16_f32 v70, v156, v157
v_mfma_f32_16x16x32_bf16 v[212:215], a[20:23], a[92:95], v[212:215]
ds_read_b64_tr_b16 a[6:7], v116 offset:12288
s_waitcnt lgkmcnt(4)
v_mfma_f32_16x16x32_bf16 v[216:219], a[24:27], a[80:83], v[216:219]
ds_read_b64_tr_b16 a[8:9], v117 offset:0
ds_read_b64_tr_b16 a[10:11], v117 offset:4096
v_mfma_f32_16x16x32_bf16 v[216:219], a[28:31], a[84:87], v[216:219]
buffer_load_dwordx4 v55, s[56:59], s20 offen offset:3072 lds
v_mfma_f32_16x16x32_bf16 v[220:223], a[24:27], a[88:91], v[220:223]
ds_read_b64_tr_b16 a[12:13], v117 offset:8192
v_cvt_pk_bf16_f32 v71, v158, v159
v_mfma_f32_16x16x32_bf16 v[220:223], a[28:31], a[92:95], v[220:223]
ds_read_b64_tr_b16 a[14:15], v117 offset:12288
s_waitcnt lgkmcnt(4)
v_mfma_f32_16x16x32_bf16 v[224:227], a[0:3], a[80:83], v[224:227]
ds_read_b64_tr_b16 a[16:17], v118 offset:0
ds_read_b64_tr_b16 a[18:19], v118 offset:4096
v_mfma_f32_16x16x32_bf16 v[224:227], a[4:7], a[84:87], v[224:227]
s_mov_b32 s80, 0
buffer_store_dwordx4 v[68:71], v29, s[64:67], s80 offen offset:3072 sc1
v_mfma_f32_16x16x32_bf16 v[228:231], a[0:3], a[88:91], v[228:231]
ds_read_b64_tr_b16 a[20:21], v118 offset:8192
v_mfma_f32_16x16x32_bf16 v[228:231], a[4:7], a[92:95], v[228:231]
ds_read_b64_tr_b16 a[22:23], v118 offset:12288
s_waitcnt lgkmcnt(4)
v_mfma_f32_16x16x32_bf16 v[232:235], a[8:11], a[80:83], v[232:235]
ds_read_b64_tr_b16 a[24:25], v119 offset:0
ds_read_b64_tr_b16 a[26:27], v119 offset:4096
v_mfma_f32_16x16x32_bf16 v[232:235], a[12:15], a[84:87], v[232:235]
s_add_u32 m0, s68, 0xc000
s_add_u32 s20, s12, s6
buffer_load_dwordx4 v52, s[56:59], s20 offen offset:0 lds
v_mfma_f32_16x16x32_bf16 v[236:239], a[8:11], a[88:91], v[236:239]
ds_read_b64_tr_b16 a[28:29], v119 offset:8192
v_mfma_f32_16x16x32_bf16 v[236:239], a[12:15], a[92:95], v[236:239]
ds_read_b64_tr_b16 a[30:31], v119 offset:12288
s_waitcnt lgkmcnt(4)
v_mfma_f32_16x16x32_bf16 v[240:243], a[16:19], a[80:83], v[240:243]
ds_read_b64_tr_b16 a[0:1], v112 offset:16384
ds_read_b64_tr_b16 a[2:3], v112 offset:20480
v_mfma_f32_16x16x32_bf16 v[240:243], a[20:23], a[84:87], v[240:243]
buffer_load_dwordx4 v53, s[56:59], s20 offen offset:1024 lds
s_add_u32 s6, s6, 0x10000
s_and_b32 s6, s6, 0x7ffff
v_mfma_f32_16x16x32_bf16 v[244:247], a[16:19], a[88:91], v[244:247]
ds_read_b64_tr_b16 a[4:5], v112 offset:24576
v_mfma_f32_16x16x32_bf16 v[244:247], a[20:23], a[92:95], v[244:247]
ds_read_b64_tr_b16 a[6:7], v112 offset:28672
s_waitcnt lgkmcnt(4)
v_mfma_f32_16x16x32_bf16 v[248:251], a[24:27], a[80:83], v[248:251]
ds_read_b64_tr_b16 a[8:9], v113 offset:16384
ds_read_b64_tr_b16 a[10:11], v113 offset:20480
s_waitcnt vmcnt(21)
v_mfma_f32_16x16x32_bf16 v[248:251], a[28:31], a[84:87], v[248:251]
buffer_load_dwordx4 a[160:163], v29, s[48:51], s13 offen offset:2048 sc1
v_mfma_f32_16x16x32_bf16 v[252:255], a[24:27], a[88:91], v[252:255]
ds_read_b64_tr_b16 a[12:13], v113 offset:24576
v_mfma_f32_16x16x32_bf16 v[252:255], a[28:31], a[92:95], v[252:255]
ds_read_b64_tr_b16 a[14:15], v113 offset:28672
s_waitcnt lgkmcnt(4)
v_mfma_f32_16x16x32_bf16 v[192:195], a[0:3], a[96:99], v[192:195]
ds_read_b64_tr_b16 a[16:17], v114 offset:16384
ds_read_b64_tr_b16 a[18:19], v114 offset:20480
v_mfma_f32_16x16x32_bf16 v[192:195], a[4:7], a[100:103], v[192:195]
buffer_load_dwordx4 a[164:167], v29, s[48:51], s13 offen offset:3072 sc1
v_mfma_f32_16x16x32_bf16 v[196:199], a[0:3], a[104:107], v[196:199]
ds_read_b64_tr_b16 a[20:21], v114 offset:24576
buffer_load_dwordx4 a[168:171], v29, s[48:51], s14 offen offset:2048 sc1
v_mfma_f32_16x16x32_bf16 v[196:199], a[4:7], a[108:111], v[196:199]
ds_read_b64_tr_b16 a[22:23], v114 offset:28672
s_waitcnt lgkmcnt(4)
v_mfma_f32_16x16x32_bf16 v[200:203], a[8:11], a[96:99], v[200:203]
ds_read_b64_tr_b16 a[24:25], v115 offset:16384
ds_read_b64_tr_b16 a[26:27], v115 offset:20480
v_mfma_f32_16x16x32_bf16 v[200:203], a[12:15], a[100:103], v[200:203]
buffer_load_dwordx4 v54, s[56:59], s20 offen offset:2048 lds
v_mfma_f32_16x16x32_bf16 v[204:207], a[8:11], a[104:107], v[204:207]
ds_read_b64_tr_b16 a[28:29], v115 offset:24576
v_cvt_pk_bf16_f32 v64, v160, v161
v_mfma_f32_16x16x32_bf16 v[204:207], a[12:15], a[108:111], v[204:207]
ds_read_b64_tr_b16 a[30:31], v115 offset:28672
s_waitcnt lgkmcnt(4)
v_mfma_f32_16x16x32_bf16 v[208:211], a[16:19], a[96:99], v[208:211]
ds_read_b64_tr_b16 a[0:1], v116 offset:16384
ds_read_b64_tr_b16 a[2:3], v116 offset:20480
v_mfma_f32_16x16x32_bf16 v[208:211], a[20:23], a[100:103], v[208:211]
buffer_load_dwordx4 a[172:175], v29, s[48:51], s14 offen offset:3072 sc1
v_mfma_f32_16x16x32_bf16 v[212:215], a[16:19], a[104:107], v[212:215]
ds_read_b64_tr_b16 a[4:5], v116 offset:24576
v_cvt_pk_bf16_f32 v65, v162, v163
v_mfma_f32_16x16x32_bf16 v[212:215], a[20:23], a[108:111], v[212:215]
ds_read_b64_tr_b16 a[6:7], v116 offset:28672
s_waitcnt lgkmcnt(4)
v_mfma_f32_16x16x32_bf16 v[216:219], a[24:27], a[96:99], v[216:219]
ds_read_b64_tr_b16 a[8:9], v117 offset:16384
ds_read_b64_tr_b16 a[10:11], v117 offset:20480
v_mfma_f32_16x16x32_bf16 v[216:219], a[28:31], a[100:103], v[216:219]
buffer_load_dwordx4 v55, s[56:59], s20 offen offset:3072 lds
v_mfma_f32_16x16x32_bf16 v[220:223], a[24:27], a[104:107], v[220:223]
ds_read_b64_tr_b16 a[12:13], v117 offset:24576
v_cvt_pk_bf16_f32 v66, v164, v165
v_mfma_f32_16x16x32_bf16 v[220:223], a[28:31], a[108:111], v[220:223]
ds_read_b64_tr_b16 a[14:15], v117 offset:28672
s_waitcnt lgkmcnt(4)
v_mfma_f32_16x16x32_bf16 v[224:227], a[0:3], a[96:99], v[224:227]
ds_read_b64_tr_b16 a[16:17], v118 offset:16384
ds_read_b64_tr_b16 a[18:19], v118 offset:20480
v_mfma_f32_16x16x32_bf16 v[224:227], a[4:7], a[100:103], v[224:227]
s_lshr_b32 s78, s7, 5
s_add_u32 s13, s78, s76
buffer_load_dwordx4 a[176:179], v29, s[48:51], s13 offen offset:0 sc1
v_mfma_f32_16x16x32_bf16 v[228:231], a[0:3], a[104:107], v[228:231]
ds_read_b64_tr_b16 a[20:21], v118 offset:24576
v_cvt_pk_bf16_f32 v67, v166, v167
v_mfma_f32_16x16x32_bf16 v[228:231], a[4:7], a[108:111], v[228:231]
ds_read_b64_tr_b16 a[22:23], v118 offset:28672
s_waitcnt lgkmcnt(4)
v_mfma_f32_16x16x32_bf16 v[232:235], a[8:11], a[96:99], v[232:235]
ds_read_b64_tr_b16 a[24:25], v119 offset:16384
ds_read_b64_tr_b16 a[26:27], v119 offset:20480
v_mfma_f32_16x16x32_bf16 v[232:235], a[12:15], a[100:103], v[232:235]
s_mov_b32 s80, 0x1000
buffer_store_dwordx4 v[64:67], v29, s[64:67], s80 offen offset:0 sc1
v_mfma_f32_16x16x32_bf16 v[236:239], a[8:11], a[104:107], v[236:239]
ds_read_b64_tr_b16 a[28:29], v119 offset:24576
v_mfma_f32_16x16x32_bf16 v[236:239], a[12:15], a[108:111], v[236:239]
ds_read_b64_tr_b16 a[30:31], v119 offset:28672
s_waitcnt lgkmcnt(4)
v_mfma_f32_16x16x32_bf16 v[240:243], a[16:19], a[96:99], v[240:243]
ds_read_b64_tr_b16 a[0:1], v8 offset:49152
ds_read_b64_tr_b16 a[2:3], v8 offset:53248
v_mfma_f32_16x16x32_bf16 v[240:243], a[20:23], a[100:103], v[240:243]
s_add_u32 m0, s68, 0x18000
s_add_u32 s20, s12, s6
buffer_load_dwordx4 v52, s[56:59], s20 offen offset:0 lds
v_mfma_f32_16x16x32_bf16 v[244:247], a[16:19], a[104:107], v[244:247]
ds_read_b64_tr_b16 a[4:5], v8 offset:57344
v_mfma_f32_16x16x32_bf16 v[244:247], a[20:23], a[108:111], v[244:247]
ds_read_b64_tr_b16 a[6:7], v8 offset:61440
s_waitcnt lgkmcnt(4)
v_mfma_f32_16x16x32_bf16 v[248:251], a[24:27], a[96:99], v[248:251]
ds_read_b64_tr_b16 a[8:9], v9 offset:49152
ds_read_b64_tr_b16 a[10:11], v9 offset:53248
s_waitcnt vmcnt(21)
s_waitcnt vmcnt(31)
s_barrier
v_mfma_f32_16x16x32_bf16 v[248:251], a[28:31], a[100:103], v[248:251]
buffer_load_dwordx4 a[180:183], v29, s[48:51], s13 offen offset:1024 sc1
s_add_u32 s14, s13, 0x20000
v_mfma_f32_16x16x32_bf16 v[252:255], a[24:27], a[104:107], v[252:255]
ds_read_b64_tr_b16 a[12:13], v9 offset:57344
v_mfma_f32_16x16x32_bf16 v[252:255], a[28:31], a[108:111], v[252:255]
ds_read_b64_tr_b16 a[14:15], v9 offset:61440
s_waitcnt lgkmcnt(4)
v_mfma_f32_16x16x32_bf16 v[192:195], a[0:3], a[112:115], v[192:195]
ds_read_b64_tr_b16 a[16:17], v10 offset:49152
ds_read_b64_tr_b16 a[18:19], v10 offset:53248
v_mfma_f32_16x16x32_bf16 v[192:195], a[4:7], a[116:119], v[192:195]
buffer_load_dwordx4 a[184:187], v29, s[48:51], s14 offen offset:0 sc1
s_add_u32 s76, s76, 0x1000
s_and_b32 s76, s76, 0x3fff
v_mfma_f32_16x16x32_bf16 v[196:199], a[0:3], a[120:123], v[196:199]
ds_read_b64_tr_b16 a[20:21], v10 offset:57344
v_cvt_pk_bf16_f32 v68, v168, v169
v_mfma_f32_16x16x32_bf16 v[196:199], a[4:7], a[124:127], v[196:199]
ds_read_b64_tr_b16 a[22:23], v10 offset:61440
s_waitcnt lgkmcnt(4)
v_mfma_f32_16x16x32_bf16 v[200:203], a[8:11], a[112:115], v[200:203]
ds_read_b64_tr_b16 a[24:25], v11 offset:49152
ds_read_b64_tr_b16 a[26:27], v11 offset:53248
v_mfma_f32_16x16x32_bf16 v[200:203], a[12:15], a[116:119], v[200:203]
buffer_load_dwordx4 v53, s[56:59], s20 offen offset:1024 lds
s_add_u32 s6, s6, 0x10000
s_and_b32 s6, s6, 0x7ffff
v_mfma_f32_16x16x32_bf16 v[204:207], a[8:11], a[120:123], v[204:207]
ds_read_b64_tr_b16 a[28:29], v11 offset:57344
v_cvt_pk_bf16_f32 v69, v170, v171
v_mfma_f32_16x16x32_bf16 v[204:207], a[12:15], a[124:127], v[204:207]
ds_read_b64_tr_b16 a[30:31], v11 offset:61440
s_waitcnt lgkmcnt(4)
v_mfma_f32_16x16x32_bf16 v[208:211], a[16:19], a[112:115], v[208:211]
ds_read_b64_tr_b16 a[0:1], v12 offset:49152
ds_read_b64_tr_b16 a[2:3], v12 offset:53248
v_mfma_f32_16x16x32_bf16 v[208:211], a[20:23], a[116:119], v[208:211]
buffer_load_dwordx4 a[188:191], v29, s[48:51], s14 offen offset:1024 sc1
v_mfma_f32_16x16x32_bf16 v[212:215], a[16:19], a[120:123], v[212:215]
ds_read_b64_tr_b16 a[4:5], v12 offset:57344
v_cvt_pk_bf16_f32 v70, v172, v173
v_mfma_f32_16x16x32_bf16 v[212:215], a[20:23], a[124:127], v[212:215]
ds_read_b64_tr_b16 a[6:7], v12 offset:61440
s_waitcnt lgkmcnt(4)
v_mfma_f32_16x16x32_bf16 v[216:219], a[24:27], a[112:115], v[216:219]
ds_read_b64_tr_b16 a[8:9], v13 offset:49152
ds_read_b64_tr_b16 a[10:11], v13 offset:53248
v_mfma_f32_16x16x32_bf16 v[216:219], a[28:31], a[116:119], v[216:219]
buffer_load_dwordx4 v54, s[56:59], s20 offen offset:2048 lds
v_mfma_f32_16x16x32_bf16 v[220:223], a[24:27], a[120:123], v[220:223]
ds_read_b64_tr_b16 a[12:13], v13 offset:57344
v_cvt_pk_bf16_f32 v71, v174, v175
v_mfma_f32_16x16x32_bf16 v[220:223], a[28:31], a[124:127], v[220:223]
ds_read_b64_tr_b16 a[14:15], v13 offset:61440
s_waitcnt lgkmcnt(4)
v_mfma_f32_16x16x32_bf16 v[224:227], a[0:3], a[112:115], v[224:227]
ds_read_b64_tr_b16 a[16:17], v14 offset:49152
ds_read_b64_tr_b16 a[18:19], v14 offset:53248
v_mfma_f32_16x16x32_bf16 v[224:227], a[4:7], a[116:119], v[224:227]
s_mov_b32 s80, 0x1000
buffer_store_dwordx4 v[68:71], v29, s[64:67], s80 offen offset:1024 sc1
v_mfma_f32_16x16x32_bf16 v[228:231], a[0:3], a[120:123], v[228:231]
ds_read_b64_tr_b16 a[20:21], v14 offset:57344
v_mfma_f32_16x16x32_bf16 v[228:231], a[4:7], a[124:127], v[228:231]
ds_read_b64_tr_b16 a[22:23], v14 offset:61440
s_waitcnt lgkmcnt(4)
v_mfma_f32_16x16x32_bf16 v[232:235], a[8:11], a[112:115], v[232:235]
ds_read_b64_tr_b16 a[24:25], v15 offset:49152
ds_read_b64_tr_b16 a[26:27], v15 offset:53248
v_mfma_f32_16x16x32_bf16 v[232:235], a[12:15], a[116:119], v[232:235]
buffer_load_dwordx4 v55, s[56:59], s20 offen offset:3072 lds
v_mfma_f32_16x16x32_bf16 v[236:239], a[8:11], a[120:123], v[236:239]
ds_read_b64_tr_b16 a[28:29], v15 offset:57344
v_mfma_f32_16x16x32_bf16 v[236:239], a[12:15], a[124:127], v[236:239]
ds_read_b64_tr_b16 a[30:31], v15 offset:61440
s_waitcnt lgkmcnt(4)
v_mfma_f32_16x16x32_bf16 v[240:243], a[16:19], a[112:115], v[240:243]
ds_read_b64_tr_b16 a[0:1], v112 offset:32768
ds_read_b64_tr_b16 a[2:3], v112 offset:36864
v_mfma_f32_16x16x32_bf16 v[240:243], a[20:23], a[116:119], v[240:243]
s_add_u32 m0, s68, 0x24000
s_add_u32 s20, s12, s6
buffer_load_dwordx4 v52, s[56:59], s20 offen offset:0 lds
v_mfma_f32_16x16x32_bf16 v[244:247], a[16:19], a[120:123], v[244:247]
ds_read_b64_tr_b16 a[4:5], v112 offset:40960
v_mfma_f32_16x16x32_bf16 v[244:247], a[20:23], a[124:127], v[244:247]
ds_read_b64_tr_b16 a[6:7], v112 offset:45056
s_waitcnt lgkmcnt(4)
v_mfma_f32_16x16x32_bf16 v[248:251], a[24:27], a[112:115], v[248:251]
ds_read_b64_tr_b16 a[8:9], v113 offset:32768
ds_read_b64_tr_b16 a[10:11], v113 offset:36864
s_waitcnt vmcnt(21)
v_mfma_f32_16x16x32_bf16 v[248:251], a[28:31], a[116:119], v[248:251]
buffer_load_dwordx4 a[32:35], v29, s[48:51], s13 offen offset:2048 sc1
v_mfma_f32_16x16x32_bf16 v[252:255], a[24:27], a[120:123], v[252:255]
ds_read_b64_tr_b16 a[12:13], v113 offset:40960
v_mfma_f32_16x16x32_bf16 v[252:255], a[28:31], a[124:127], v[252:255]
ds_read_b64_tr_b16 a[14:15], v113 offset:45056
s_waitcnt lgkmcnt(4)
v_mfma_f32_16x16x32_bf16 v[192:195], a[0:3], a[144:147], v[192:195]
ds_read_b64_tr_b16 a[16:17], v114 offset:32768
ds_read_b64_tr_b16 a[18:19], v114 offset:36864
v_mfma_f32_16x16x32_bf16 v[192:195], a[4:7], a[148:151], v[192:195]
buffer_load_dwordx4 a[36:39], v29, s[48:51], s13 offen offset:3072 sc1
v_mfma_f32_16x16x32_bf16 v[196:199], a[0:3], a[152:155], v[196:199]
ds_read_b64_tr_b16 a[20:21], v114 offset:40960
buffer_load_dwordx4 a[40:43], v29, s[48:51], s14 offen offset:2048 sc1
v_mfma_f32_16x16x32_bf16 v[196:199], a[4:7], a[156:159], v[196:199]
ds_read_b64_tr_b16 a[22:23], v114 offset:45056
s_waitcnt lgkmcnt(4)
v_mfma_f32_16x16x32_bf16 v[200:203], a[8:11], a[144:147], v[200:203]
ds_read_b64_tr_b16 a[24:25], v115 offset:32768
ds_read_b64_tr_b16 a[26:27], v115 offset:36864
v_mfma_f32_16x16x32_bf16 v[200:203], a[12:15], a[148:151], v[200:203]
buffer_load_dwordx4 v53, s[56:59], s20 offen offset:1024 lds
s_add_u32 s6, s6, 0x10000
s_and_b32 s6, s6, 0x7ffff
v_mfma_f32_16x16x32_bf16 v[204:207], a[8:11], a[152:155], v[204:207]
ds_read_b64_tr_b16 a[28:29], v115 offset:40960
v_cvt_pk_bf16_f32 v64, v176, v177
v_mfma_f32_16x16x32_bf16 v[204:207], a[12:15], a[156:159], v[204:207]
ds_read_b64_tr_b16 a[30:31], v115 offset:45056
s_waitcnt lgkmcnt(4)
v_mfma_f32_16x16x32_bf16 v[208:211], a[16:19], a[144:147], v[208:211]
ds_read_b64_tr_b16 a[0:1], v116 offset:32768
ds_read_b64_tr_b16 a[2:3], v116 offset:36864
v_mfma_f32_16x16x32_bf16 v[208:211], a[20:23], a[148:151], v[208:211]
buffer_load_dwordx4 a[44:47], v29, s[48:51], s14 offen offset:3072 sc1
v_mfma_f32_16x16x32_bf16 v[212:215], a[16:19], a[152:155], v[212:215]
ds_read_b64_tr_b16 a[4:5], v116 offset:40960
v_cvt_pk_bf16_f32 v65, v178, v179
v_mfma_f32_16x16x32_bf16 v[212:215], a[20:23], a[156:159], v[212:215]
ds_read_b64_tr_b16 a[6:7], v116 offset:45056
s_waitcnt lgkmcnt(4)
v_mfma_f32_16x16x32_bf16 v[216:219], a[24:27], a[144:147], v[216:219]
ds_read_b64_tr_b16 a[8:9], v117 offset:32768
ds_read_b64_tr_b16 a[10:11], v117 offset:36864
v_mfma_f32_16x16x32_bf16 v[216:219], a[28:31], a[148:151], v[216:219]
buffer_load_dwordx4 v54, s[56:59], s20 offen offset:2048 lds
v_mfma_f32_16x16x32_bf16 v[220:223], a[24:27], a[152:155], v[220:223]
ds_read_b64_tr_b16 a[12:13], v117 offset:40960
v_cvt_pk_bf16_f32 v66, v180, v181
v_mfma_f32_16x16x32_bf16 v[220:223], a[28:31], a[156:159], v[220:223]
ds_read_b64_tr_b16 a[14:15], v117 offset:45056
s_waitcnt lgkmcnt(4)
v_mfma_f32_16x16x32_bf16 v[224:227], a[0:3], a[144:147], v[224:227]
ds_read_b64_tr_b16 a[16:17], v118 offset:32768
ds_read_b64_tr_b16 a[18:19], v118 offset:36864
v_mfma_f32_16x16x32_bf16 v[224:227], a[4:7], a[148:151], v[224:227]
s_add_u32 s13, s78, s76
buffer_load_dwordx4 a[48:51], v29, s[48:51], s13 offen offset:0 sc1
v_mfma_f32_16x16x32_bf16 v[228:231], a[0:3], a[152:155], v[228:231]
ds_read_b64_tr_b16 a[20:21], v118 offset:40960
v_cvt_pk_bf16_f32 v67, v182, v183
v_mfma_f32_16x16x32_bf16 v[228:231], a[4:7], a[156:159], v[228:231]
ds_read_b64_tr_b16 a[22:23], v118 offset:45056
s_waitcnt lgkmcnt(4)
v_mfma_f32_16x16x32_bf16 v[232:235], a[8:11], a[144:147], v[232:235]
ds_read_b64_tr_b16 a[24:25], v119 offset:32768
ds_read_b64_tr_b16 a[26:27], v119 offset:36864
v_mfma_f32_16x16x32_bf16 v[232:235], a[12:15], a[148:151], v[232:235]
s_mov_b32 s80, 0x1000
buffer_store_dwordx4 v[64:67], v29, s[64:67], s80 offen offset:2048 sc1
v_mfma_f32_16x16x32_bf16 v[236:239], a[8:11], a[152:155], v[236:239]
ds_read_b64_tr_b16 a[28:29], v119 offset:40960
v_mfma_f32_16x16x32_bf16 v[236:239], a[12:15], a[156:159], v[236:239]
ds_read_b64_tr_b16 a[30:31], v119 offset:45056
s_waitcnt lgkmcnt(4)
v_mfma_f32_16x16x32_bf16 v[240:243], a[16:19], a[144:147], v[240:243]
ds_read_b64_tr_b16 a[0:1], v8 offset:0
ds_read_b64_tr_b16 a[2:3], v8 offset:4096
v_mfma_f32_16x16x32_bf16 v[240:243], a[20:23], a[148:151], v[240:243]
buffer_load_dwordx4 v55, s[56:59], s20 offen offset:3072 lds
v_mfma_f32_16x16x32_bf16 v[244:247], a[16:19], a[152:155], v[244:247]
ds_read_b64_tr_b16 a[4:5], v8 offset:8192
v_mfma_f32_16x16x32_bf16 v[244:247], a[20:23], a[156:159], v[244:247]
ds_read_b64_tr_b16 a[6:7], v8 offset:12288
s_waitcnt lgkmcnt(4)
v_mfma_f32_16x16x32_bf16 v[248:251], a[24:27], a[144:147], v[248:251]
ds_read_b64_tr_b16 a[8:9], v9 offset:0
ds_read_b64_tr_b16 a[10:11], v9 offset:4096
s_waitcnt vmcnt(21)
s_waitcnt vmcnt(29)
s_barrier
v_mfma_f32_16x16x32_bf16 v[248:251], a[28:31], a[148:151], v[248:251]
buffer_load_dwordx4 a[52:55], v29, s[48:51], s13 offen offset:1024 sc1
s_add_u32 s14, s13, 0x20000
v_mfma_f32_16x16x32_bf16 v[252:255], a[24:27], a[152:155], v[252:255]
ds_read_b64_tr_b16 a[12:13], v9 offset:8192
v_mfma_f32_16x16x32_bf16 v[252:255], a[28:31], a[156:159], v[252:255]
ds_read_b64_tr_b16 a[14:15], v9 offset:12288
s_waitcnt lgkmcnt(4)
v_mfma_f32_16x16x32_bf16 v[192:195], a[0:3], a[160:163], v[192:195]
ds_read_b64_tr_b16 a[16:17], v10 offset:0
ds_read_b64_tr_b16 a[18:19], v10 offset:4096
v_mfma_f32_16x16x32_bf16 v[192:195], a[4:7], a[164:167], v[192:195]
buffer_load_dwordx4 a[56:59], v29, s[48:51], s14 offen offset:0 sc1
s_add_u32 s76, s76, 0x1000
s_and_b32 s76, s76, 0x3fff
v_mfma_f32_16x16x32_bf16 v[196:199], a[0:3], a[168:171], v[196:199]
ds_read_b64_tr_b16 a[20:21], v10 offset:8192
buffer_load_dwordx4 a[60:63], v29, s[48:51], s14 offen offset:1024 sc1
v_mfma_f32_16x16x32_bf16 v[196:199], a[4:7], a[172:175], v[196:199]
ds_read_b64_tr_b16 a[22:23], v10 offset:12288
s_waitcnt lgkmcnt(4)
v_mfma_f32_16x16x32_bf16 v[200:203], a[8:11], a[160:163], v[200:203]
ds_read_b64_tr_b16 a[24:25], v11 offset:0
ds_read_b64_tr_b16 a[26:27], v11 offset:4096
v_mfma_f32_16x16x32_bf16 v[200:203], a[12:15], a[164:167], v[200:203]
s_mov_b32 m0, s68
s_add_u32 s20, s12, s6
buffer_load_dwordx4 v52, s[56:59], s20 offen offset:0 lds
v_mfma_f32_16x16x32_bf16 v[204:207], a[8:11], a[168:171], v[204:207]
ds_read_b64_tr_b16 a[28:29], v11 offset:8192
buffer_load_dwordx4 a[64:67], v29, s[48:51], s13 offen offset:2048 sc1
v_mfma_f32_16x16x32_bf16 v[204:207], a[12:15], a[172:175], v[204:207]
ds_read_b64_tr_b16 a[30:31], v11 offset:12288
s_waitcnt lgkmcnt(4)
v_mfma_f32_16x16x32_bf16 v[208:211], a[16:19], a[160:163], v[208:211]
ds_read_b64_tr_b16 a[0:1], v12 offset:0
ds_read_b64_tr_b16 a[2:3], v12 offset:4096
v_mfma_f32_16x16x32_bf16 v[208:211], a[20:23], a[164:167], v[208:211]
buffer_load_dwordx4 a[68:71], v29, s[48:51], s13 offen offset:3072 sc1
v_mfma_f32_16x16x32_bf16 v[212:215], a[16:19], a[168:171], v[212:215]
ds_read_b64_tr_b16 a[4:5], v12 offset:8192
buffer_load_dwordx4 a[72:75], v29, s[48:51], s14 offen offset:2048 sc1
v_mfma_f32_16x16x32_bf16 v[212:215], a[20:23], a[172:175], v[212:215]
ds_read_b64_tr_b16 a[6:7], v12 offset:12288
s_waitcnt lgkmcnt(4)
v_mfma_f32_16x16x32_bf16 v[216:219], a[24:27], a[160:163], v[216:219]
ds_read_b64_tr_b16 a[8:9], v13 offset:0
ds_read_b64_tr_b16 a[10:11], v13 offset:4096
v_mfma_f32_16x16x32_bf16 v[216:219], a[28:31], a[164:167], v[216:219]
buffer_load_dwordx4 v53, s[56:59], s20 offen offset:1024 lds
s_add_u32 s6, s6, 0x10000
s_and_b32 s6, s6, 0x7ffff
v_mfma_f32_16x16x32_bf16 v[220:223], a[24:27], a[168:171], v[220:223]
ds_read_b64_tr_b16 a[12:13], v13 offset:8192
buffer_load_dwordx4 a[76:79], v29, s[48:51], s14 offen offset:3072 sc1
v_mfma_f32_16x16x32_bf16 v[220:223], a[28:31], a[172:175], v[220:223]
ds_read_b64_tr_b16 a[14:15], v13 offset:12288
s_waitcnt lgkmcnt(4)
v_mfma_f32_16x16x32_bf16 v[224:227], a[0:3], a[160:163], v[224:227]
ds_read_b64_tr_b16 a[16:17], v14 offset:0
ds_read_b64_tr_b16 a[18:19], v14 offset:4096
v_mfma_f32_16x16x32_bf16 v[224:227], a[4:7], a[164:167], v[224:227]
s_add_u32 s13, s78, s76
buffer_load_dwordx4 a[80:83], v29, s[48:51], s13 offen offset:0 sc1
v_mfma_f32_16x16x32_bf16 v[228:231], a[0:3], a[168:171], v[228:231]
ds_read_b64_tr_b16 a[20:21], v14 offset:8192
buffer_load_dwordx4 a[84:87], v29, s[48:51], s13 offen offset:1024 sc1
s_add_u32 s14, s13, 0x20000
v_mfma_f32_16x16x32_bf16 v[228:231], a[4:7], a[172:175], v[228:231]
ds_read_b64_tr_b16 a[22:23], v14 offset:12288
s_waitcnt lgkmcnt(4)
v_mfma_f32_16x16x32_bf16 v[232:235], a[8:11], a[160:163], v[232:235]
ds_read_b64_tr_b16 a[24:25], v15 offset:0
ds_read_b64_tr_b16 a[26:27], v15 offset:4096
v_mfma_f32_16x16x32_bf16 v[232:235], a[12:15], a[164:167], v[232:235]
buffer_load_dwordx4 v54, s[56:59], s20 offen offset:2048 lds
v_mfma_f32_16x16x32_bf16 v[236:239], a[8:11], a[168:171], v[236:239]
ds_read_b64_tr_b16 a[28:29], v15 offset:8192
buffer_load_dwordx4 a[88:91], v29, s[48:51], s14 offen offset:0 sc1
s_add_u32 s76, s76, 0x1000
s_and_b32 s76, s76, 0x3fff
v_mfma_f32_16x16x32_bf16 v[236:239], a[12:15], a[172:175], v[236:239]
ds_read_b64_tr_b16 a[30:31], v15 offset:12288
s_waitcnt lgkmcnt(4)
v_mfma_f32_16x16x32_bf16 v[240:243], a[16:19], a[160:163], v[240:243]
ds_read_b64_tr_b16 a[0:1], v120 offset:0
ds_read_b64_tr_b16 a[2:3], v120 offset:4096
v_mfma_f32_16x16x32_bf16 v[240:243], a[20:23], a[164:167], v[240:243]
buffer_load_dwordx4 v55, s[56:59], s20 offen offset:3072 lds
v_mfma_f32_16x16x32_bf16 v[244:247], a[16:19], a[168:171], v[244:247]
ds_read_b64_tr_b16 a[4:5], v120 offset:8192
buffer_load_dwordx4 a[92:95], v29, s[48:51], s14 offen offset:1024 sc1
v_mfma_f32_16x16x32_bf16 v[244:247], a[20:23], a[172:175], v[244:247]
ds_read_b64_tr_b16 a[6:7], v120 offset:12288
s_waitcnt lgkmcnt(4)
v_mfma_f32_16x16x32_bf16 v[248:251], a[24:27], a[160:163], v[248:251]
ds_read_b64_tr_b16 a[8:9], v121 offset:0
ds_read_b64_tr_b16 a[10:11], v121 offset:4096
s_waitcnt vmcnt(28)
v_mfma_f32_16x16x32_bf16 v[248:251], a[28:31], a[164:167], v[248:251]
buffer_load_dwordx4 a[96:99], v29, s[48:51], s13 offen offset:2048 sc1
v_mfma_f32_16x16x32_bf16 v[252:255], a[24:27], a[168:171], v[252:255]
ds_read_b64_tr_b16 a[12:13], v121 offset:8192
buffer_load_dwordx4 a[100:103], v29, s[48:51], s13 offen offset:3072 sc1
v_mfma_f32_16x16x32_bf16 v[252:255], a[28:31], a[172:175], v[252:255]
ds_read_b64_tr_b16 a[14:15], v121 offset:12288
s_waitcnt lgkmcnt(4)
v_mfma_f32_16x16x32_bf16 v[192:195], a[0:3], a[176:179], v[192:195]
ds_read_b64_tr_b16 a[16:17], v122 offset:0
ds_read_b64_tr_b16 a[18:19], v122 offset:4096
v_mfma_f32_16x16x32_bf16 v[192:195], a[4:7], a[180:183], v[192:195]
buffer_load_dwordx4 a[104:107], v29, s[48:51], s14 offen offset:2048 sc1
v_mfma_f32_16x16x32_bf16 v[196:199], a[0:3], a[184:187], v[196:199]
ds_read_b64_tr_b16 a[20:21], v122 offset:8192
buffer_load_dwordx4 a[108:111], v29, s[48:51], s14 offen offset:3072 sc1
v_mfma_f32_16x16x32_bf16 v[196:199], a[4:7], a[188:191], v[196:199]
ds_read_b64_tr_b16 a[22:23], v122 offset:12288
s_waitcnt lgkmcnt(4)
v_mfma_f32_16x16x32_bf16 v[200:203], a[8:11], a[176:179], v[200:203]
ds_read_b64_tr_b16 a[24:25], v123 offset:0
ds_read_b64_tr_b16 a[26:27], v123 offset:4096
v_mfma_f32_16x16x32_bf16 v[200:203], a[12:15], a[180:183], v[200:203]
s_add_u32 m0, s68, 0x4000
s_add_u32 s20, s12, s6
buffer_load_dwordx4 v52, s[56:59], s20 offen offset:0 lds
v_mfma_f32_16x16x32_bf16 v[204:207], a[8:11], a[184:187], v[204:207]
ds_read_b64_tr_b16 a[28:29], v123 offset:8192
s_add_u32 s13, s78, s76
buffer_load_dwordx4 a[112:115], v29, s[48:51], s13 offen offset:0 sc1
v_mfma_f32_16x16x32_bf16 v[204:207], a[12:15], a[188:191], v[204:207]
ds_read_b64_tr_b16 a[30:31], v123 offset:12288
s_waitcnt lgkmcnt(4)
v_mfma_f32_16x16x32_bf16 v[208:211], a[16:19], a[176:179], v[208:211]
ds_read_b64_tr_b16 a[0:1], v124 offset:0
ds_read_b64_tr_b16 a[2:3], v124 offset:4096
v_mfma_f32_16x16x32_bf16 v[208:211], a[20:23], a[180:183], v[208:211]
buffer_load_dwordx4 a[116:119], v29, s[48:51], s13 offen offset:1024 sc1
s_add_u32 s14, s13, 0x20000
v_mfma_f32_16x16x32_bf16 v[212:215], a[16:19], a[184:187], v[212:215]
ds_read_b64_tr_b16 a[4:5], v124 offset:8192
buffer_load_dwordx4 a[120:123], v29, s[48:51], s14 offen offset:0 sc1
s_add_u32 s76, s76, 0x1000
s_and_b32 s76, s76, 0x3fff
v_mfma_f32_16x16x32_bf16 v[212:215], a[20:23], a[188:191], v[212:215]
ds_read_b64_tr_b16 a[6:7], v124 offset:12288
s_waitcnt lgkmcnt(4)
v_mfma_f32_16x16x32_bf16 v[216:219], a[24:27], a[176:179], v[216:219]
ds_read_b64_tr_b16 a[8:9], v125 offset:0
ds_read_b64_tr_b16 a[10:11], v125 offset:4096
v_mfma_f32_16x16x32_bf16 v[216:219], a[28:31], a[180:183], v[216:219]
buffer_load_dwordx4 v53, s[56:59], s20 offen offset:1024 lds
s_add_u32 s6, s6, 0x10000
s_and_b32 s6, s6, 0x7ffff
v_mfma_f32_16x16x32_bf16 v[220:223], a[24:27], a[184:187], v[220:223]
ds_read_b64_tr_b16 a[12:13], v125 offset:8192
v_cvt_pk_bf16_f32 v68, v184, v185
v_mfma_f32_16x16x32_bf16 v[220:223], a[28:31], a[188:191], v[220:223]
ds_read_b64_tr_b16 a[14:15], v125 offset:12288
s_waitcnt lgkmcnt(4)
v_mfma_f32_16x16x32_bf16 v[224:227], a[0:3], a[176:179], v[224:227]
ds_read_b64_tr_b16 a[16:17], v126 offset:0
ds_read_b64_tr_b16 a[18:19], v126 offset:4096
v_mfma_f32_16x16x32_bf16 v[224:227], a[4:7], a[180:183], v[224:227]
buffer_load_dwordx4 a[124:127], v29, s[48:51], s14 offen offset:1024 sc1
v_mfma_f32_16x16x32_bf16 v[228:231], a[0:3], a[184:187], v[228:231]
ds_read_b64_tr_b16 a[20:21], v126 offset:8192
v_cvt_pk_bf16_f32 v69, v186, v187
v_mfma_f32_16x16x32_bf16 v[228:231], a[4:7], a[188:191], v[228:231]
ds_read_b64_tr_b16 a[22:23], v126 offset:12288
s_waitcnt lgkmcnt(4)
v_mfma_f32_16x16x32_bf16 v[232:235], a[8:11], a[176:179], v[232:235]
ds_read_b64_tr_b16 a[24:25], v127 offset:0
ds_read_b64_tr_b16 a[26:27], v127 offset:4096
v_mfma_f32_16x16x32_bf16 v[232:235], a[12:15], a[180:183], v[232:235]
buffer_load_dwordx4 v54, s[56:59], s20 offen offset:2048 lds
v_mfma_f32_16x16x32_bf16 v[236:239], a[8:11], a[184:187], v[236:239]
ds_read_b64_tr_b16 a[28:29], v127 offset:8192
v_cvt_pk_bf16_f32 v70, v188, v189
v_mfma_f32_16x16x32_bf16 v[236:239], a[12:15], a[188:191], v[236:239]
ds_read_b64_tr_b16 a[30:31], v127 offset:12288
s_waitcnt lgkmcnt(4)
v_mfma_f32_16x16x32_bf16 v[240:243], a[16:19], a[176:179], v[240:243]
ds_read_b64_tr_b16 a[0:1], v8 offset:16384
ds_read_b64_tr_b16 a[2:3], v8 offset:20480
v_mfma_f32_16x16x32_bf16 v[240:243], a[20:23], a[180:183], v[240:243]
buffer_load_dwordx4 v55, s[56:59], s20 offen offset:3072 lds
v_mfma_f32_16x16x32_bf16 v[244:247], a[16:19], a[184:187], v[244:247]
ds_read_b64_tr_b16 a[4:5], v8 offset:24576
v_cvt_pk_bf16_f32 v71, v190, v191
v_mfma_f32_16x16x32_bf16 v[244:247], a[20:23], a[188:191], v[244:247]
ds_read_b64_tr_b16 a[6:7], v8 offset:28672
s_waitcnt lgkmcnt(4)
v_mfma_f32_16x16x32_bf16 v[248:251], a[24:27], a[176:179], v[248:251]
ds_read_b64_tr_b16 a[8:9], v9 offset:16384
ds_read_b64_tr_b16 a[10:11], v9 offset:20480
s_waitcnt vmcnt(31)
s_waitcnt vmcnt(37)
s_barrier
v_mfma_f32_16x16x32_bf16 v[248:251], a[28:31], a[180:183], v[248:251]
s_mov_b32 s80, 0x1000
buffer_store_dwordx4 v[68:71], v29, s[64:67], s80 offen offset:3072 sc1
v_mfma_f32_16x16x32_bf16 v[252:255], a[24:27], a[184:187], v[252:255]
ds_read_b64_tr_b16 a[12:13], v9 offset:24576
v_mfma_f32_16x16x32_bf16 v[252:255], a[28:31], a[188:191], v[252:255]
ds_read_b64_tr_b16 a[14:15], v9 offset:28672
s_waitcnt lgkmcnt(4)
v_mfma_f32_16x16x32_bf16 v[192:195], a[0:3], a[32:35], v[192:195]
ds_read_b64_tr_b16 a[16:17], v10 offset:16384
ds_read_b64_tr_b16 a[18:19], v10 offset:20480
v_mfma_f32_16x16x32_bf16 v[192:195], a[4:7], a[36:39], v[192:195]
buffer_load_dwordx4 a[144:147], v29, s[48:51], s13 offen offset:2048 sc1
v_mfma_f32_16x16x32_bf16 v[196:199], a[0:3], a[40:43], v[196:199]
ds_read_b64_tr_b16 a[20:21], v10 offset:24576
v_mfma_f32_16x16x32_bf16 v[196:199], a[4:7], a[44:47], v[196:199]
ds_read_b64_tr_b16 a[22:23], v10 offset:28672
s_waitcnt lgkmcnt(4)
v_mfma_f32_16x16x32_bf16 v[200:203], a[8:11], a[32:35], v[200:203]
ds_read_b64_tr_b16 a[24:25], v11 offset:16384
ds_read_b64_tr_b16 a[26:27], v11 offset:20480
v_mfma_f32_16x16x32_bf16 v[200:203], a[12:15], a[36:39], v[200:203]
s_add_u32 m0, s68, 0x1c000
s_add_u32 s20, s12, s6
buffer_load_dwordx4 v52, s[56:59], s20 offen offset:0 lds
v_mfma_f32_16x16x32_bf16 v[204:207], a[8:11], a[40:43], v[204:207]
ds_read_b64_tr_b16 a[28:29], v11 offset:24576
v_mfma_f32_16x16x32_bf16 v[204:207], a[12:15], a[44:47], v[204:207]
ds_read_b64_tr_b16 a[30:31], v11 offset:28672
s_waitcnt lgkmcnt(4)
v_mfma_f32_16x16x32_bf16 v[208:211], a[16:19], a[32:35], v[208:211]
ds_read_b64_tr_b16 a[0:1], v12 offset:16384
ds_read_b64_tr_b16 a[2:3], v12 offset:20480
v_mfma_f32_16x16x32_bf16 v[208:211], a[20:23], a[36:39], v[208:211]
buffer_load_dwordx4 a[148:151], v29, s[48:51], s13 offen offset:3072 sc1
v_mfma_f32_16x16x32_bf16 v[212:215], a[16:19], a[40:43], v[212:215]
ds_read_b64_tr_b16 a[4:5], v12 offset:24576
v_mfma_f32_16x16x32_bf16 v[212:215], a[20:23], a[44:47], v[212:215]
ds_read_b64_tr_b16 a[6:7], v12 offset:28672
s_waitcnt lgkmcnt(4)
v_mfma_f32_16x16x32_bf16 v[216:219], a[24:27], a[32:35], v[216:219]
ds_read_b64_tr_b16 a[8:9], v13 offset:16384
ds_read_b64_tr_b16 a[10:11], v13 offset:20480
v_mfma_f32_16x16x32_bf16 v[216:219], a[28:31], a[36:39], v[216:219]
buffer_load_dwordx4 v53, s[56:59], s20 offen offset:1024 lds
s_add_u32 s6, s6, 0x10000
s_and_b32 s6, s6, 0x7ffff
v_mfma_f32_16x16x32_bf16 v[220:223], a[24:27], a[40:43], v[220:223]
ds_read_b64_tr_b16 a[12:13], v13 offset:24576
v_mfma_f32_16x16x32_bf16 v[220:223], a[28:31], a[44:47], v[220:223]
ds_read_b64_tr_b16 a[14:15], v13 offset:28672
s_waitcnt lgkmcnt(4)
v_mfma_f32_16x16x32_bf16 v[224:227], a[0:3], a[32:35], v[224:227]
ds_read_b64_tr_b16 a[16:17], v14 offset:16384
ds_read_b64_tr_b16 a[18:19], v14 offset:20480
v_mfma_f32_16x16x32_bf16 v[224:227], a[4:7], a[36:39], v[224:227]
buffer_load_dwordx4 a[152:155], v29, s[48:51], s14 offen offset:2048 sc1
v_mfma_f32_16x16x32_bf16 v[228:231], a[0:3], a[40:43], v[228:231]
ds_read_b64_tr_b16 a[20:21], v14 offset:24576
v_mfma_f32_16x16x32_bf16 v[228:231], a[4:7], a[44:47], v[228:231]
ds_read_b64_tr_b16 a[22:23], v14 offset:28672
s_waitcnt lgkmcnt(4)
v_mfma_f32_16x16x32_bf16 v[232:235], a[8:11], a[32:35], v[232:235]
ds_read_b64_tr_b16 a[24:25], v15 offset:16384
ds_read_b64_tr_b16 a[26:27], v15 offset:20480
v_mfma_f32_16x16x32_bf16 v[232:235], a[12:15], a[36:39], v[232:235]
buffer_load_dwordx4 v54, s[56:59], s20 offen offset:2048 lds
v_mfma_f32_16x16x32_bf16 v[236:239], a[8:11], a[40:43], v[236:239]
ds_read_b64_tr_b16 a[28:29], v15 offset:24576
v_mfma_f32_16x16x32_bf16 v[236:239], a[12:15], a[44:47], v[236:239]
ds_read_b64_tr_b16 a[30:31], v15 offset:28672
s_waitcnt lgkmcnt(4)
v_mfma_f32_16x16x32_bf16 v[240:243], a[16:19], a[32:35], v[240:243]
ds_read_b64_tr_b16 a[0:1], v112 offset:49152
ds_read_b64_tr_b16 a[2:3], v112 offset:53248
v_mfma_f32_16x16x32_bf16 v[240:243], a[20:23], a[36:39], v[240:243]
buffer_load_dwordx4 v55, s[56:59], s20 offen offset:3072 lds
v_mfma_f32_16x16x32_bf16 v[244:247], a[16:19], a[40:43], v[244:247]
ds_read_b64_tr_b16 a[4:5], v112 offset:57344
v_mfma_f32_16x16x32_bf16 v[244:247], a[20:23], a[44:47], v[244:247]
ds_read_b64_tr_b16 a[6:7], v112 offset:61440
s_waitcnt lgkmcnt(4)
v_mfma_f32_16x16x32_bf16 v[248:251], a[24:27], a[32:35], v[248:251]
ds_read_b64_tr_b16 a[8:9], v113 offset:49152
ds_read_b64_tr_b16 a[10:11], v113 offset:53248
s_waitcnt vmcnt(32)
v_mfma_f32_16x16x32_bf16 v[248:251], a[28:31], a[36:39], v[248:251]
buffer_load_dwordx4 a[156:159], v29, s[48:51], s14 offen offset:3072 sc1
v_mfma_f32_16x16x32_bf16 v[252:255], a[24:27], a[40:43], v[252:255]
ds_read_b64_tr_b16 a[12:13], v113 offset:57344
v_mfma_f32_16x16x32_bf16 v[252:255], a[28:31], a[44:47], v[252:255]
ds_read_b64_tr_b16 a[14:15], v113 offset:61440
s_waitcnt lgkmcnt(4)
v_mfma_f32_16x16x32_bf16 v[192:195], a[0:3], a[48:51], v[192:195]
ds_read_b64_tr_b16 a[16:17], v114 offset:49152
v_mfma_f32_16x16x32_bf16 v[192:195], a[4:7], a[52:55], v[192:195]
ds_read_b64_tr_b16 a[18:19], v114 offset:53248
v_mfma_f32_16x16x32_bf16 v[196:199], a[0:3], a[56:59], v[196:199]
ds_read_b64_tr_b16 a[20:21], v114 offset:57344
v_mfma_f32_16x16x32_bf16 v[196:199], a[4:7], a[60:63], v[196:199]
ds_read_b64_tr_b16 a[22:23], v114 offset:61440
s_waitcnt lgkmcnt(4)
v_mfma_f32_16x16x32_bf16 v[200:203], a[8:11], a[48:51], v[200:203]
ds_read_b64_tr_b16 a[24:25], v115 offset:49152
v_mfma_f32_16x16x32_bf16 v[200:203], a[12:15], a[52:55], v[200:203]
ds_read_b64_tr_b16 a[26:27], v115 offset:53248
v_mfma_f32_16x16x32_bf16 v[204:207], a[8:11], a[56:59], v[204:207]
ds_read_b64_tr_b16 a[28:29], v115 offset:57344
v_mfma_f32_16x16x32_bf16 v[204:207], a[12:15], a[60:63], v[204:207]
ds_read_b64_tr_b16 a[30:31], v115 offset:61440
s_waitcnt lgkmcnt(4)
v_mfma_f32_16x16x32_bf16 v[208:211], a[16:19], a[48:51], v[208:211]
ds_read_b64_tr_b16 a[0:1], v116 offset:49152
v_mfma_f32_16x16x32_bf16 v[208:211], a[20:23], a[52:55], v[208:211]
ds_read_b64_tr_b16 a[2:3], v116 offset:53248
v_mfma_f32_16x16x32_bf16 v[212:215], a[16:19], a[56:59], v[212:215]
ds_read_b64_tr_b16 a[4:5], v116 offset:57344
v_mfma_f32_16x16x32_bf16 v[212:215], a[20:23], a[60:63], v[212:215]
ds_read_b64_tr_b16 a[6:7], v116 offset:61440
s_waitcnt lgkmcnt(4)
v_mfma_f32_16x16x32_bf16 v[216:219], a[24:27], a[48:51], v[216:219]
ds_read_b64_tr_b16 a[8:9], v117 offset:49152
v_mfma_f32_16x16x32_bf16 v[216:219], a[28:31], a[52:55], v[216:219]
ds_read_b64_tr_b16 a[10:11], v117 offset:53248
v_mfma_f32_16x16x32_bf16 v[220:223], a[24:27], a[56:59], v[220:223]
ds_read_b64_tr_b16 a[12:13], v117 offset:57344
v_mfma_f32_16x16x32_bf16 v[220:223], a[28:31], a[60:63], v[220:223]
ds_read_b64_tr_b16 a[14:15], v117 offset:61440
s_waitcnt lgkmcnt(4)
v_mfma_f32_16x16x32_bf16 v[224:227], a[0:3], a[48:51], v[224:227]
ds_read_b64_tr_b16 a[16:17], v118 offset:49152
v_mfma_f32_16x16x32_bf16 v[224:227], a[4:7], a[52:55], v[224:227]
ds_read_b64_tr_b16 a[18:19], v118 offset:53248
v_mfma_f32_16x16x32_bf16 v[228:231], a[0:3], a[56:59], v[228:231]
ds_read_b64_tr_b16 a[20:21], v118 offset:57344
v_mfma_f32_16x16x32_bf16 v[228:231], a[4:7], a[60:63], v[228:231]
ds_read_b64_tr_b16 a[22:23], v118 offset:61440
s_waitcnt lgkmcnt(4)
v_mfma_f32_16x16x32_bf16 v[232:235], a[8:11], a[48:51], v[232:235]
ds_read_b64_tr_b16 a[24:25], v119 offset:49152
v_mfma_f32_16x16x32_bf16 v[232:235], a[12:15], a[52:55], v[232:235]
ds_read_b64_tr_b16 a[26:27], v119 offset:53248
v_mfma_f32_16x16x32_bf16 v[236:239], a[8:11], a[56:59], v[236:239]
ds_read_b64_tr_b16 a[28:29], v119 offset:57344
v_mfma_f32_16x16x32_bf16 v[236:239], a[12:15], a[60:63], v[236:239]
ds_read_b64_tr_b16 a[30:31], v119 offset:61440
s_waitcnt lgkmcnt(4)
v_mfma_f32_16x16x32_bf16 v[240:243], a[16:19], a[48:51], v[240:243]
ds_read_b64_tr_b16 a[0:1], v8 offset:32768
v_mfma_f32_16x16x32_bf16 v[240:243], a[20:23], a[52:55], v[240:243]
ds_read_b64_tr_b16 a[2:3], v8 offset:36864
v_mfma_f32_16x16x32_bf16 v[244:247], a[16:19], a[56:59], v[244:247]
ds_read_b64_tr_b16 a[4:5], v8 offset:40960
v_mfma_f32_16x16x32_bf16 v[244:247], a[20:23], a[60:63], v[244:247]
ds_read_b64_tr_b16 a[6:7], v8 offset:45056
s_waitcnt lgkmcnt(4)
v_mfma_f32_16x16x32_bf16 v[248:251], a[24:27], a[48:51], v[248:251]
ds_read_b64_tr_b16 a[8:9], v9 offset:32768
ds_read_b64_tr_b16 a[10:11], v9 offset:36864
s_waitcnt vmcnt(8)
s_waitcnt vmcnt(27)
s_waitcnt vmcnt(22)
s_barrier
v_mfma_f32_16x16x32_bf16 v[248:251], a[28:31], a[52:55], v[248:251]
buffer_store_dwordx4 v[60:63], v57, s[24:27], s23 offen offset:0 sc1
v_mfma_f32_16x16x32_bf16 v[252:255], a[24:27], a[56:59], v[252:255]
ds_read_b64_tr_b16 a[12:13], v9 offset:40960
v_mfma_f32_16x16x32_bf16 v[252:255], a[28:31], a[60:63], v[252:255]
ds_read_b64_tr_b16 a[14:15], v9 offset:45056
s_waitcnt lgkmcnt(4)
v_mfma_f32_16x16x32_bf16 v[192:195], a[0:3], a[64:67], v[192:195]
ds_read_b64_tr_b16 a[16:17], v10 offset:32768
v_mfma_f32_16x16x32_bf16 v[192:195], a[4:7], a[68:71], v[192:195]
ds_read_b64_tr_b16 a[18:19], v10 offset:36864
v_mfma_f32_16x16x32_bf16 v[196:199], a[0:3], a[72:75], v[196:199]
ds_read_b64_tr_b16 a[20:21], v10 offset:40960
v_mfma_f32_16x16x32_bf16 v[196:199], a[4:7], a[76:79], v[196:199]
ds_read_b64_tr_b16 a[22:23], v10 offset:45056
s_waitcnt lgkmcnt(4)
v_mfma_f32_16x16x32_bf16 v[200:203], a[8:11], a[64:67], v[200:203]
ds_read_b64_tr_b16 a[24:25], v11 offset:32768
v_mfma_f32_16x16x32_bf16 v[200:203], a[12:15], a[68:71], v[200:203]
ds_read_b64_tr_b16 a[26:27], v11 offset:36864
v_mfma_f32_16x16x32_bf16 v[204:207], a[8:11], a[72:75], v[204:207]
ds_read_b64_tr_b16 a[28:29], v11 offset:40960
v_mfma_f32_16x16x32_bf16 v[204:207], a[12:15], a[76:79], v[204:207]
ds_read_b64_tr_b16 a[30:31], v11 offset:45056
s_waitcnt lgkmcnt(4)
v_mfma_f32_16x16x32_bf16 v[208:211], a[16:19], a[64:67], v[208:211]
ds_read_b64_tr_b16 a[0:1], v12 offset:32768
v_mfma_f32_16x16x32_bf16 v[208:211], a[20:23], a[68:71], v[208:211]
ds_read_b64_tr_b16 a[2:3], v12 offset:36864
v_mfma_f32_16x16x32_bf16 v[212:215], a[16:19], a[72:75], v[212:215]
ds_read_b64_tr_b16 a[4:5], v12 offset:40960
v_mfma_f32_16x16x32_bf16 v[212:215], a[20:23], a[76:79], v[212:215]
ds_read_b64_tr_b16 a[6:7], v12 offset:45056
s_waitcnt lgkmcnt(4)
v_mfma_f32_16x16x32_bf16 v[216:219], a[24:27], a[64:67], v[216:219]
ds_read_b64_tr_b16 a[8:9], v13 offset:32768
v_mfma_f32_16x16x32_bf16 v[216:219], a[28:31], a[68:71], v[216:219]
ds_read_b64_tr_b16 a[10:11], v13 offset:36864
v_mfma_f32_16x16x32_bf16 v[220:223], a[24:27], a[72:75], v[220:223]
ds_read_b64_tr_b16 a[12:13], v13 offset:40960
v_mfma_f32_16x16x32_bf16 v[220:223], a[28:31], a[76:79], v[220:223]
ds_read_b64_tr_b16 a[14:15], v13 offset:45056
s_waitcnt lgkmcnt(4)
v_mfma_f32_16x16x32_bf16 v[224:227], a[0:3], a[64:67], v[224:227]
ds_read_b64_tr_b16 a[16:17], v14 offset:32768
v_mfma_f32_16x16x32_bf16 v[224:227], a[4:7], a[68:71], v[224:227]
ds_read_b64_tr_b16 a[18:19], v14 offset:36864
v_mfma_f32_16x16x32_bf16 v[228:231], a[0:3], a[72:75], v[228:231]
ds_read_b64_tr_b16 a[20:21], v14 offset:40960
v_mfma_f32_16x16x32_bf16 v[228:231], a[4:7], a[76:79], v[228:231]
ds_read_b64_tr_b16 a[22:23], v14 offset:45056
s_waitcnt lgkmcnt(4)
v_mfma_f32_16x16x32_bf16 v[232:235], a[8:11], a[64:67], v[232:235]
ds_read_b64_tr_b16 a[24:25], v15 offset:32768
v_mfma_f32_16x16x32_bf16 v[232:235], a[12:15], a[68:71], v[232:235]
ds_read_b64_tr_b16 a[26:27], v15 offset:36864
v_mfma_f32_16x16x32_bf16 v[236:239], a[8:11], a[72:75], v[236:239]
ds_read_b64_tr_b16 a[28:29], v15 offset:40960
v_mfma_f32_16x16x32_bf16 v[236:239], a[12:15], a[76:79], v[236:239]
ds_read_b64_tr_b16 a[30:31], v15 offset:45056
s_waitcnt lgkmcnt(4)
v_mfma_f32_16x16x32_bf16 v[240:243], a[16:19], a[64:67], v[240:243]
ds_read_b64_tr_b16 a[0:1], v120 offset:16384
v_mfma_f32_16x16x32_bf16 v[240:243], a[20:23], a[68:71], v[240:243]
ds_read_b64_tr_b16 a[2:3], v120 offset:20480
v_mfma_f32_16x16x32_bf16 v[244:247], a[16:19], a[72:75], v[244:247]
ds_read_b64_tr_b16 a[4:5], v120 offset:24576
v_mfma_f32_16x16x32_bf16 v[244:247], a[20:23], a[76:79], v[244:247]
ds_read_b64_tr_b16 a[6:7], v120 offset:28672
s_waitcnt lgkmcnt(4)
v_mfma_f32_16x16x32_bf16 v[248:251], a[24:27], a[64:67], v[248:251]
ds_read_b64_tr_b16 a[8:9], v121 offset:16384
s_waitcnt vmcnt(22)
v_mfma_f32_16x16x32_bf16 v[248:251], a[28:31], a[68:71], v[248:251]
ds_read_b64_tr_b16 a[10:11], v121 offset:20480
v_mfma_f32_16x16x32_bf16 v[252:255], a[24:27], a[72:75], v[252:255]
ds_read_b64_tr_b16 a[12:13], v121 offset:24576
v_mfma_f32_16x16x32_bf16 v[252:255], a[28:31], a[76:79], v[252:255]
ds_read_b64_tr_b16 a[14:15], v121 offset:28672
s_waitcnt lgkmcnt(4)
v_mfma_f32_16x16x32_bf16 v[192:195], a[0:3], a[80:83], v[192:195]
ds_read_b64_tr_b16 a[16:17], v122 offset:16384
v_mfma_f32_16x16x32_bf16 v[192:195], a[4:7], a[84:87], v[192:195]
ds_read_b64_tr_b16 a[18:19], v122 offset:20480
v_mfma_f32_16x16x32_bf16 v[196:199], a[0:3], a[88:91], v[196:199]
ds_read_b64_tr_b16 a[20:21], v122 offset:24576
v_mfma_f32_16x16x32_bf16 v[196:199], a[4:7], a[92:95], v[196:199]
ds_read_b64_tr_b16 a[22:23], v122 offset:28672
s_waitcnt lgkmcnt(4)
v_mfma_f32_16x16x32_bf16 v[200:203], a[8:11], a[80:83], v[200:203]
ds_read_b64_tr_b16 a[24:25], v123 offset:16384
v_mfma_f32_16x16x32_bf16 v[200:203], a[12:15], a[84:87], v[200:203]
ds_read_b64_tr_b16 a[26:27], v123 offset:20480
v_mfma_f32_16x16x32_bf16 v[204:207], a[8:11], a[88:91], v[204:207]
ds_read_b64_tr_b16 a[28:29], v123 offset:24576
v_mfma_f32_16x16x32_bf16 v[204:207], a[12:15], a[92:95], v[204:207]
ds_read_b64_tr_b16 a[30:31], v123 offset:28672
s_waitcnt lgkmcnt(4)
v_mfma_f32_16x16x32_bf16 v[208:211], a[16:19], a[80:83], v[208:211]
ds_read_b64_tr_b16 a[0:1], v124 offset:16384
v_mfma_f32_16x16x32_bf16 v[208:211], a[20:23], a[84:87], v[208:211]
ds_read_b64_tr_b16 a[2:3], v124 offset:20480
v_mfma_f32_16x16x32_bf16 v[212:215], a[16:19], a[88:91], v[212:215]
ds_read_b64_tr_b16 a[4:5], v124 offset:24576
v_mfma_f32_16x16x32_bf16 v[212:215], a[20:23], a[92:95], v[212:215]
ds_read_b64_tr_b16 a[6:7], v124 offset:28672
s_waitcnt lgkmcnt(4)
v_mfma_f32_16x16x32_bf16 v[216:219], a[24:27], a[80:83], v[216:219]
ds_read_b64_tr_b16 a[8:9], v125 offset:16384
v_mfma_f32_16x16x32_bf16 v[216:219], a[28:31], a[84:87], v[216:219]
ds_read_b64_tr_b16 a[10:11], v125 offset:20480
v_mfma_f32_16x16x32_bf16 v[220:223], a[24:27], a[88:91], v[220:223]
ds_read_b64_tr_b16 a[12:13], v125 offset:24576
v_mfma_f32_16x16x32_bf16 v[220:223], a[28:31], a[92:95], v[220:223]
ds_read_b64_tr_b16 a[14:15], v125 offset:28672
s_waitcnt lgkmcnt(4)
v_mfma_f32_16x16x32_bf16 v[224:227], a[0:3], a[80:83], v[224:227]
ds_read_b64_tr_b16 a[16:17], v126 offset:16384
v_mfma_f32_16x16x32_bf16 v[224:227], a[4:7], a[84:87], v[224:227]
ds_read_b64_tr_b16 a[18:19], v126 offset:20480
v_mfma_f32_16x16x32_bf16 v[228:231], a[0:3], a[88:91], v[228:231]
ds_read_b64_tr_b16 a[20:21], v126 offset:24576
v_mfma_f32_16x16x32_bf16 v[228:231], a[4:7], a[92:95], v[228:231]
ds_read_b64_tr_b16 a[22:23], v126 offset:28672
s_waitcnt lgkmcnt(4)
v_mfma_f32_16x16x32_bf16 v[232:235], a[8:11], a[80:83], v[232:235]
ds_read_b64_tr_b16 a[24:25], v127 offset:16384
s_waitcnt vmcnt(2)
s_barrier
v_mfma_f32_16x16x32_bf16 v[232:235], a[12:15], a[84:87], v[232:235]
ds_read_b64_tr_b16 a[26:27], v127 offset:20480
v_mfma_f32_16x16x32_bf16 v[236:239], a[8:11], a[88:91], v[236:239]
ds_read_b64_tr_b16 a[28:29], v127 offset:24576
v_mfma_f32_16x16x32_bf16 v[236:239], a[12:15], a[92:95], v[236:239]
ds_read_b64_tr_b16 a[30:31], v127 offset:28672
s_waitcnt lgkmcnt(4)
v_mfma_f32_16x16x32_bf16 v[240:243], a[16:19], a[80:83], v[240:243]
ds_read_b64_tr_b16 a[0:1], v112 offset:0
v_mfma_f32_16x16x32_bf16 v[240:243], a[20:23], a[84:87], v[240:243]
ds_read_b64_tr_b16 a[2:3], v112 offset:4096
v_mfma_f32_16x16x32_bf16 v[244:247], a[16:19], a[88:91], v[244:247]
ds_read_b64_tr_b16 a[4:5], v112 offset:8192
v_mfma_f32_16x16x32_bf16 v[244:247], a[20:23], a[92:95], v[244:247]
ds_read_b64_tr_b16 a[6:7], v112 offset:12288
s_waitcnt lgkmcnt(4)
v_mfma_f32_16x16x32_bf16 v[248:251], a[24:27], a[80:83], v[248:251]
ds_read_b64_tr_b16 a[8:9], v112 offset:16384
ds_read_b64_tr_b16 a[10:11], v112 offset:20480
s_waitcnt vmcnt(1)
v_mfma_f32_16x16x32_bf16 v[248:251], a[28:31], a[84:87], v[248:251]
buffer_load_dwordx2 v[46:47], v58, s[24:27], s22 offen offset:0 sc1
v_mfma_f32_16x16x32_bf16 v[252:255], a[24:27], a[88:91], v[252:255]
ds_read_b64_tr_b16 a[12:13], v112 offset:24576
v_mfma_f32_16x16x32_bf16 v[252:255], a[28:31], a[92:95], v[252:255]
ds_read_b64_tr_b16 a[14:15], v112 offset:28672
s_waitcnt lgkmcnt(4)
v_mfma_f32_16x16x32_bf16 v[192:195], a[0:3], a[96:99], v[192:195]
ds_read_b64_tr_b16 a[16:17], v8 offset:49152
v_mfma_f32_16x16x32_bf16 v[192:195], a[4:7], a[100:103], v[192:195]
ds_read_b64_tr_b16 a[18:19], v8 offset:53248
v_mfma_f32_16x16x32_bf16 v[196:199], a[0:3], a[104:107], v[196:199]
ds_read_b64_tr_b16 a[20:21], v8 offset:57344
v_mfma_f32_16x16x32_bf16 v[196:199], a[4:7], a[108:111], v[196:199]
ds_read_b64_tr_b16 a[22:23], v8 offset:61440
s_waitcnt lgkmcnt(4)
v_mfma_f32_16x16x32_bf16 v[192:195], a[8:11], a[112:115], v[192:195]
ds_read_b64_tr_b16 a[24:25], v113 offset:0
v_mfma_f32_16x16x32_bf16 v[192:195], a[12:15], a[116:119], v[192:195]
ds_read_b64_tr_b16 a[26:27], v113 offset:4096
v_mfma_f32_16x16x32_bf16 v[196:199], a[8:11], a[120:123], v[196:199]
ds_read_b64_tr_b16 a[28:29], v113 offset:8192
v_mfma_f32_16x16x32_bf16 v[196:199], a[12:15], a[124:127], v[196:199]
ds_read_b64_tr_b16 a[30:31], v113 offset:12288
s_waitcnt lgkmcnt(4)
v_mfma_f32_16x16x32_bf16 v[192:195], a[16:19], a[144:147], v[192:195]
ds_read_b64_tr_b16 a[0:1], v113 offset:16384
v_mfma_f32_16x16x32_bf16 v[192:195], a[20:23], a[148:151], v[192:195]
ds_read_b64_tr_b16 a[2:3], v113 offset:20480
v_mfma_f32_16x16x32_bf16 v[196:199], a[16:19], a[152:155], v[196:199]
ds_read_b64_tr_b16 a[4:5], v113 offset:24576
v_mfma_f32_16x16x32_bf16 v[196:199], a[20:23], a[156:159], v[196:199]
ds_read_b64_tr_b16 a[6:7], v113 offset:28672
s_waitcnt lgkmcnt(4)
v_mfma_f32_16x16x32_bf16 v[200:203], a[24:27], a[96:99], v[200:203]
ds_read_b64_tr_b16 a[8:9], v9 offset:49152
v_mfma_f32_16x16x32_bf16 v[200:203], a[28:31], a[100:103], v[200:203]
ds_read_b64_tr_b16 a[10:11], v9 offset:53248
v_mfma_f32_16x16x32_bf16 v[204:207], a[24:27], a[104:107], v[204:207]
ds_read_b64_tr_b16 a[12:13], v9 offset:57344
v_mfma_f32_16x16x32_bf16 v[204:207], a[28:31], a[108:111], v[204:207]
ds_read_b64_tr_b16 a[14:15], v9 offset:61440
s_waitcnt lgkmcnt(4)
v_mfma_f32_16x16x32_bf16 v[200:203], a[0:3], a[112:115], v[200:203]
ds_read_b64_tr_b16 a[16:17], v114 offset:0
v_cvt_pk_bf16_f32 v72, v192, v193
v_mfma_f32_16x16x32_bf16 v[200:203], a[4:7], a[116:119], v[200:203]
ds_read_b64_tr_b16 a[18:19], v114 offset:4096
v_cvt_pk_bf16_f32 v73, v194, v195
v_mfma_f32_16x16x32_bf16 v[204:207], a[0:3], a[120:123], v[204:207]
ds_read_b64_tr_b16 a[20:21], v114 offset:8192
v_cvt_pk_bf16_f32 v74, v196, v197
v_mfma_f32_16x16x32_bf16 v[204:207], a[4:7], a[124:127], v[204:207]
ds_read_b64_tr_b16 a[22:23], v114 offset:12288
s_waitcnt lgkmcnt(4)
v_mfma_f32_16x16x32_bf16 v[200:203], a[8:11], a[144:147], v[200:203]
ds_read_b64_tr_b16 a[24:25], v114 offset:16384
v_cvt_pk_bf16_f32 v75, v198, v199
v_mfma_f32_16x16x32_bf16 v[200:203], a[12:15], a[148:151], v[200:203]
ds_read_b64_tr_b16 a[26:27], v114 offset:20480
v_mfma_f32_16x16x32_bf16 v[204:207], a[8:11], a[152:155], v[204:207]
ds_read_b64_tr_b16 a[28:29], v114 offset:24576
v_mfma_f32_16x16x32_bf16 v[204:207], a[12:15], a[156:159], v[204:207]
ds_read_b64_tr_b16 a[30:31], v114 offset:28672
s_waitcnt lgkmcnt(4)
v_mfma_f32_16x16x32_bf16 v[208:211], a[16:19], a[96:99], v[208:211]
ds_read_b64_tr_b16 a[0:1], v10 offset:49152
ds_read_b64_tr_b16 a[2:3], v10 offset:53248
v_mfma_f32_16x16x32_bf16 v[208:211], a[20:23], a[100:103], v[208:211]
s_mov_b32 s80, 0x2000
buffer_store_dwordx4 v[72:75], v29, s[64:67], s80 offen offset:0 sc1
v_mfma_f32_16x16x32_bf16 v[212:215], a[16:19], a[104:107], v[212:215]
ds_read_b64_tr_b16 a[4:5], v10 offset:57344
v_mfma_f32_16x16x32_bf16 v[212:215], a[20:23], a[108:111], v[212:215]
ds_read_b64_tr_b16 a[6:7], v10 offset:61440
s_waitcnt lgkmcnt(4)
v_mfma_f32_16x16x32_bf16 v[208:211], a[24:27], a[112:115], v[208:211]
ds_read_b64_tr_b16 a[8:9], v115 offset:0
v_cvt_pk_bf16_f32 v76, v200, v201
v_mfma_f32_16x16x32_bf16 v[208:211], a[28:31], a[116:119], v[208:211]
ds_read_b64_tr_b16 a[10:11], v115 offset:4096
v_cvt_pk_bf16_f32 v77, v202, v203
v_mfma_f32_16x16x32_bf16 v[212:215], a[24:27], a[120:123], v[212:215]
ds_read_b64_tr_b16 a[12:13], v115 offset:8192
v_cvt_pk_bf16_f32 v78, v204, v205
v_mfma_f32_16x16x32_bf16 v[212:215], a[28:31], a[124:127], v[212:215]
ds_read_b64_tr_b16 a[14:15], v115 offset:12288
s_waitcnt lgkmcnt(4)
v_mfma_f32_16x16x32_bf16 v[208:211], a[0:3], a[144:147], v[208:211]
ds_read_b64_tr_b16 a[16:17], v115 offset:16384
ds_read_b64_tr_b16 a[18:19], v115 offset:20480
v_mfma_f32_16x16x32_bf16 v[208:211], a[4:7], a[148:151], v[208:211]
buffer_load_dwordx2 v[80:81], v50, s[16:19], 0 offen offset:0
v_mfma_f32_16x16x32_bf16 v[212:215], a[0:3], a[152:155], v[212:215]
ds_read_b64_tr_b16 a[20:21], v115 offset:24576
v_cvt_pk_bf16_f32 v79, v206, v207
v_mfma_f32_16x16x32_bf16 v[212:215], a[4:7], a[156:159], v[212:215]
ds_read_b64_tr_b16 a[22:23], v115 offset:28672
s_waitcnt lgkmcnt(4)
v_mfma_f32_16x16x32_bf16 v[216:219], a[8:11], a[96:99], v[216:219]
ds_read_b64_tr_b16 a[24:25], v11 offset:49152
ds_read_b64_tr_b16 a[26:27], v11 offset:53248
v_mfma_f32_16x16x32_bf16 v[216:219], a[12:15], a[100:103], v[216:219]
s_mov_b32 s80, 0x2000
buffer_store_dwordx4 v[76:79], v29, s[64:67], s80 offen offset:1024 sc1
v_mfma_f32_16x16x32_bf16 v[220:223], a[8:11], a[104:107], v[220:223]
ds_read_b64_tr_b16 a[28:29], v11 offset:57344
v_mfma_f32_16x16x32_bf16 v[220:223], a[12:15], a[108:111], v[220:223]
ds_read_b64_tr_b16 a[30:31], v11 offset:61440
s_waitcnt lgkmcnt(4)
v_mfma_f32_16x16x32_bf16 v[216:219], a[16:19], a[112:115], v[216:219]
ds_read_b64_tr_b16 a[0:1], v116 offset:0
ds_read_b64_tr_b16 a[2:3], v116 offset:4096
v_mfma_f32_16x16x32_bf16 v[216:219], a[20:23], a[116:119], v[216:219]
buffer_load_dwordx2 v[82:83], v50, s[16:19], s79 offen offset:0
v_mfma_f32_16x16x32_bf16 v[220:223], a[16:19], a[120:123], v[220:223]
ds_read_b64_tr_b16 a[4:5], v116 offset:8192
v_cvt_pk_bf16_f32 v72, v208, v209
v_mfma_f32_16x16x32_bf16 v[220:223], a[20:23], a[124:127], v[220:223]
ds_read_b64_tr_b16 a[6:7], v116 offset:12288
s_waitcnt lgkmcnt(4)
v_mfma_f32_16x16x32_bf16 v[216:219], a[24:27], a[144:147], v[216:219]
ds_read_b64_tr_b16 a[8:9], v116 offset:16384
ds_read_b64_tr_b16 a[10:11], v116 offset:20480
v_mfma_f32_16x16x32_bf16 v[216:219], a[28:31], a[148:151], v[216:219]
buffer_load_dwordx2 v[84:85], v50, s[16:19], 0 offen offset:32
v_mfma_f32_16x16x32_bf16 v[220:223], a[24:27], a[152:155], v[220:223]
ds_read_b64_tr_b16 a[12:13], v116 offset:24576
v_cvt_pk_bf16_f32 v73, v210, v211
v_mfma_f32_16x16x32_bf16 v[220:223], a[28:31], a[156:159], v[220:223]
ds_read_b64_tr_b16 a[14:15], v116 offset:28672
s_waitcnt lgkmcnt(4)
v_mfma_f32_16x16x32_bf16 v[224:227], a[0:3], a[96:99], v[224:227]
ds_read_b64_tr_b16 a[16:17], v12 offset:49152
ds_read_b64_tr_b16 a[18:19], v12 offset:53248
v_mfma_f32_16x16x32_bf16 v[224:227], a[4:7], a[100:103], v[224:227]
buffer_load_dwordx2 v[86:87], v50, s[16:19], s79 offen offset:32
v_mfma_f32_16x16x32_bf16 v[228:231], a[0:3], a[104:107], v[228:231]
ds_read_b64_tr_b16 a[20:21], v12 offset:57344
v_cvt_pk_bf16_f32 v74, v212, v213
v_mfma_f32_16x16x32_bf16 v[228:231], a[4:7], a[108:111], v[228:231]
ds_read_b64_tr_b16 a[22:23], v12 offset:61440
s_waitcnt lgkmcnt(4)
v_mfma_f32_16x16x32_bf16 v[224:227], a[8:11], a[112:115], v[224:227]
ds_read_b64_tr_b16 a[24:25], v117 offset:0
v_cvt_pk_bf16_f32 v75, v214, v215
v_mfma_f32_16x16x32_bf16 v[224:227], a[12:15], a[116:119], v[224:227]
ds_read_b64_tr_b16 a[26:27], v117 offset:4096
v_mfma_f32_16x16x32_bf16 v[228:231], a[8:11], a[120:123], v[228:231]
ds_read_b64_tr_b16 a[28:29], v117 offset:8192
v_mfma_f32_16x16x32_bf16 v[228:231], a[12:15], a[124:127], v[228:231]
ds_read_b64_tr_b16 a[30:31], v117 offset:12288
s_waitcnt lgkmcnt(4)
v_mfma_f32_16x16x32_bf16 v[224:227], a[16:19], a[144:147], v[224:227]
ds_read_b64_tr_b16 a[0:1], v117 offset:16384
ds_read_b64_tr_b16 a[2:3], v117 offset:20480
v_mfma_f32_16x16x32_bf16 v[224:227], a[20:23], a[148:151], v[224:227]
s_mov_b32 s80, 0x2000
buffer_store_dwordx4 v[72:75], v29, s[64:67], s80 offen offset:2048 sc1
v_mfma_f32_16x16x32_bf16 v[228:231], a[16:19], a[152:155], v[228:231]
ds_read_b64_tr_b16 a[4:5], v117 offset:24576
v_cvt_pk_bf16_f32 v76, v216, v217
v_mfma_f32_16x16x32_bf16 v[228:231], a[20:23], a[156:159], v[228:231]
ds_read_b64_tr_b16 a[6:7], v117 offset:28672
s_waitcnt lgkmcnt(4)
v_mfma_f32_16x16x32_bf16 v[232:235], a[24:27], a[96:99], v[232:235]
ds_read_b64_tr_b16 a[8:9], v13 offset:49152
v_cvt_pk_bf16_f32 v77, v218, v219
v_mfma_f32_16x16x32_bf16 v[232:235], a[28:31], a[100:103], v[232:235]
ds_read_b64_tr_b16 a[10:11], v13 offset:53248
v_cvt_pk_bf16_f32 v78, v220, v221
v_mfma_f32_16x16x32_bf16 v[236:239], a[24:27], a[104:107], v[236:239]
ds_read_b64_tr_b16 a[12:13], v13 offset:57344
v_cvt_pk_bf16_f32 v79, v222, v223
v_mfma_f32_16x16x32_bf16 v[236:239], a[28:31], a[108:111], v[236:239]
ds_read_b64_tr_b16 a[14:15], v13 offset:61440
s_waitcnt lgkmcnt(4)
v_mfma_f32_16x16x32_bf16 v[232:235], a[0:3], a[112:115], v[232:235]
ds_read_b64_tr_b16 a[16:17], v118 offset:0
ds_read_b64_tr_b16 a[18:19], v118 offset:4096
v_mfma_f32_16x16x32_bf16 v[232:235], a[4:7], a[116:119], v[232:235]
s_mov_b32 s80, 0x2000
buffer_store_dwordx4 v[76:79], v29, s[64:67], s80 offen offset:3072 sc1
v_mfma_f32_16x16x32_bf16 v[236:239], a[0:3], a[120:123], v[236:239]
ds_read_b64_tr_b16 a[20:21], v118 offset:8192
v_cvt_pk_bf16_f32 v72, v224, v225
v_mfma_f32_16x16x32_bf16 v[236:239], a[4:7], a[124:127], v[236:239]
ds_read_b64_tr_b16 a[22:23], v118 offset:12288
s_waitcnt lgkmcnt(4)
v_mfma_f32_16x16x32_bf16 v[232:235], a[8:11], a[144:147], v[232:235]
ds_read_b64_tr_b16 a[24:25], v118 offset:16384
v_cvt_pk_bf16_f32 v73, v226, v227
v_mfma_f32_16x16x32_bf16 v[232:235], a[12:15], a[148:151], v[232:235]
ds_read_b64_tr_b16 a[26:27], v118 offset:20480
v_cvt_pk_bf16_f32 v74, v228, v229
v_mfma_f32_16x16x32_bf16 v[236:239], a[8:11], a[152:155], v[236:239]
ds_read_b64_tr_b16 a[28:29], v118 offset:24576
v_cvt_pk_bf16_f32 v75, v230, v231
v_mfma_f32_16x16x32_bf16 v[236:239], a[12:15], a[156:159], v[236:239]
ds_read_b64_tr_b16 a[30:31], v118 offset:28672
s_waitcnt lgkmcnt(4)
v_mfma_f32_16x16x32_bf16 v[240:243], a[16:19], a[96:99], v[240:243]
ds_read_b64_tr_b16 a[0:1], v14 offset:49152
ds_read_b64_tr_b16 a[2:3], v14 offset:53248
v_mfma_f32_16x16x32_bf16 v[240:243], a[20:23], a[100:103], v[240:243]
s_mov_b32 s80, 0x3000
buffer_store_dwordx4 v[72:75], v29, s[64:67], s80 offen offset:0 sc1
v_mfma_f32_16x16x32_bf16 v[244:247], a[16:19], a[104:107], v[244:247]
ds_read_b64_tr_b16 a[4:5], v14 offset:57344
v_mfma_f32_16x16x32_bf16 v[244:247], a[20:23], a[108:111], v[244:247]
ds_read_b64_tr_b16 a[6:7], v14 offset:61440
s_waitcnt lgkmcnt(4)
v_mfma_f32_16x16x32_bf16 v[240:243], a[24:27], a[112:115], v[240:243]
ds_read_b64_tr_b16 a[8:9], v119 offset:0
v_cvt_pk_bf16_f32 v76, v232, v233
v_mfma_f32_16x16x32_bf16 v[240:243], a[28:31], a[116:119], v[240:243]
ds_read_b64_tr_b16 a[10:11], v119 offset:4096
v_cvt_pk_bf16_f32 v77, v234, v235
v_mfma_f32_16x16x32_bf16 v[244:247], a[24:27], a[120:123], v[244:247]
ds_read_b64_tr_b16 a[12:13], v119 offset:8192
v_cvt_pk_bf16_f32 v78, v236, v237
v_mfma_f32_16x16x32_bf16 v[244:247], a[28:31], a[124:127], v[244:247]
ds_read_b64_tr_b16 a[14:15], v119 offset:12288
s_waitcnt lgkmcnt(4)
v_mfma_f32_16x16x32_bf16 v[240:243], a[0:3], a[144:147], v[240:243]
ds_read_b64_tr_b16 a[16:17], v119 offset:16384
v_cvt_pk_bf16_f32 v79, v238, v239
v_mfma_f32_16x16x32_bf16 v[240:243], a[4:7], a[148:151], v[240:243]
ds_read_b64_tr_b16 a[18:19], v119 offset:20480
v_mfma_f32_16x16x32_bf16 v[244:247], a[0:3], a[152:155], v[244:247]
ds_read_b64_tr_b16 a[20:21], v119 offset:24576
v_mfma_f32_16x16x32_bf16 v[244:247], a[4:7], a[156:159], v[244:247]
ds_read_b64_tr_b16 a[22:23], v119 offset:28672
s_waitcnt lgkmcnt(4)
v_mfma_f32_16x16x32_bf16 v[248:251], a[8:11], a[96:99], v[248:251]
ds_read_b64_tr_b16 a[24:25], v15 offset:49152
ds_read_b64_tr_b16 a[26:27], v15 offset:53248
v_mfma_f32_16x16x32_bf16 v[248:251], a[12:15], a[100:103], v[248:251]
s_mov_b32 s80, 0x3000
buffer_store_dwordx4 v[76:79], v29, s[64:67], s80 offen offset:1024 sc1
v_mfma_f32_16x16x32_bf16 v[252:255], a[8:11], a[104:107], v[252:255]
ds_read_b64_tr_b16 a[28:29], v15 offset:57344
v_mfma_f32_16x16x32_bf16 v[252:255], a[12:15], a[108:111], v[252:255]
ds_read_b64_tr_b16 a[30:31], v15 offset:61440
s_waitcnt lgkmcnt(4)
v_mfma_f32_16x16x32_bf16 v[248:251], a[16:19], a[112:115], v[248:251]
v_cvt_pk_bf16_f32 v72, v240, v241
v_mfma_f32_16x16x32_bf16 v[248:251], a[20:23], a[116:119], v[248:251]
v_cvt_pk_bf16_f32 v73, v242, v243
v_mfma_f32_16x16x32_bf16 v[252:255], a[16:19], a[120:123], v[252:255]
v_cvt_pk_bf16_f32 v74, v244, v245
v_mfma_f32_16x16x32_bf16 v[252:255], a[20:23], a[124:127], v[252:255]
s_waitcnt lgkmcnt(0)
v_mfma_f32_16x16x32_bf16 v[248:251], a[24:27], a[144:147], v[248:251]
v_cvt_pk_bf16_f32 v75, v246, v247
v_mfma_f32_16x16x32_bf16 v[248:251], a[28:31], a[148:151], v[248:251]
v_mfma_f32_16x16x32_bf16 v[252:255], a[24:27], a[152:155], v[252:255]
v_mfma_f32_16x16x32_bf16 v[252:255], a[28:31], a[156:159], v[252:255]
s_mov_b32 s80, 0x3000
buffer_store_dwordx4 v[72:75], v29, s[64:67], s80 offen offset:2048 sc1
s_nop 3
v_cvt_pk_bf16_f32 v76, v248, v249
v_cvt_pk_bf16_f32 v77, v250, v251
v_cvt_pk_bf16_f32 v78, v252, v253
v_cvt_pk_bf16_f32 v79, v254, v255
s_mov_b32 s80, 0x3000
buffer_store_dwordx4 v[76:79], v29, s[64:67], s80 offen offset:3072 sc1
s_cmp_eq_u32 s5, s73
s_cbranch_scc0 .Lkf_pub
; ---- publ
v_add_u32_e32 v64, 0x10000, v29
ds_write_b128 v29, v[192:195] offset:32768
ds_write_b128 v29, v[196:199] offset:33792
ds_write_b128 v29, v[200:203] offset:34816
ds_write_b128 v29, v[204:207] offset:35840
ds_write_b128 v29, v[208:211] offset:36864
ds_write_b128 v29, v[212:215] offset:37888
ds_write_b128 v29, v[216:219] offset:38912
ds_write_b128 v29, v[220:223] offset:39936
ds_write_b128 v29, v[224:227] offset:40960
ds_write_b128 v29, v[228:231] offset:41984
ds_write_b128 v29, v[232:235] offset:43008
ds_write_b128 v29, v[236:239] offset:44032
ds_write_b128 v29, v[240:243] offset:45056
ds_write_b128 v29, v[244:247] offset:46080
ds_write_b128 v29, v[248:251] offset:47104
ds_write_b128 v29, v[252:255] offset:48128
ds_write_b128 v64, v[128:131] offset:0
ds_write_b128 v64, v[132:135] offset:1024
ds_write_b128 v64, v[136:139] offset:2048
ds_write_b128 v64, v[140:143] offset:3072
ds_write_b128 v64, v[144:147] offset:4096
ds_write_b128 v64, v[148:151] offset:5120
ds_write_b128 v64, v[152:155] offset:6144
ds_write_b128 v64, v[156:159] offset:7168
ds_write_b128 v64, v[160:163] offset:8192
ds_write_b128 v64, v[164:167] offset:9216
ds_write_b128 v64, v[168:171] offset:10240
ds_write_b128 v64, v[172:175] offset:11264
ds_write_b128 v64, v[176:179] offset:12288
ds_write_b128 v64, v[180:183] offset:13312
ds_write_b128 v64, v[184:187] offset:14336
ds_write_b128 v64, v[188:191] offset:15360
; ---- pub
.Lkf_pub:
s_waitcnt vmcnt(12)
v_cmp_eq_u64_e32 vcc, s[94:95], v[46:47]
s_nop 0
s_and_b32 s83, vcc_lo, 15
s_cmp_eq_u32 s83, 15
s_cbranch_scc0 .Lkf_spf
; ---- o2
.Lkf_o2:
s_add_u32 s80, s73, 1
s_and_b32 s80, s80, 3
s_lshl_b32 s80, s80, 14
buffer_load_dwordx4 v[224:227], v29, s[28:31], s80 offen offset:0 sc1
buffer_load_dwordx4 v[228:231], v29, s[28:31], s80 offen offset:1024 sc1
s_add_u32 s80, s73, 2
s_and_b32 s80, s80, 3
s_lshl_b32 s80, s80, 14
buffer_load_dwordx4 v[232:235], v29, s[28:31], s80 offen offset:0 sc1
buffer_load_dwordx4 v[236:239], v29, s[28:31], s80 offen offset:1024 sc1
s_add_u32 s80, s73, 3
s_and_b32 s80, s80, 3
s_lshl_b32 s80, s80, 14
buffer_load_dwordx4 v[240:243], v29, s[28:31], s80 offen offset:0 sc1
buffer_load_dwordx4 v[244:247], v29, s[28:31], s80 offen offset:1024 sc1
; ---- pubd
s_waitcnt lgkmcnt(0)
s_waitcnt vmcnt(6)
s_barrier
buffer_store_dwordx4 v[60:63], v57, s[24:27], s21 offen offset:0 sc1
v_add_u32_e32 v64, 0x10000, v51
ds_read_b128 v[96:99], v64 offset:0
ds_read_b128 v[100:103], v64 offset:1024
ds_read_b128 v[104:107], v64 offset:2048
ds_read_b128 v[108:111], v64 offset:3072
s_waitcnt vmcnt(1)
v_lshlrev_b32_e32 v112, 16, v224
v_and_b32_e32 v113, 0xffff0000, v224
v_lshlrev_b32_e32 v114, 16, v225
v_and_b32_e32 v115, 0xffff0000, v225
v_lshlrev_b32_e32 v116, 16, v226
v_and_b32_e32 v117, 0xffff0000, v226
v_lshlrev_b32_e32 v118, 16, v227
v_and_b32_e32 v119, 0xffff0000, v227
v_lshlrev_b32_e32 v120, 16, v228
v_and_b32_e32 v121, 0xffff0000, v228
v_lshlrev_b32_e32 v122, 16, v229
v_and_b32_e32 v123, 0xffff0000, v229
v_lshlrev_b32_e32 v124, 16, v230
v_and_b32_e32 v125, 0xffff0000, v230
v_lshlrev_b32_e32 v126, 16, v231
v_and_b32_e32 v127, 0xffff0000, v231
s_waitcnt lgkmcnt(0)
v_pk_add_f32 v[96:97], v[96:97], v[112:113]
v_pk_add_f32 v[98:99], v[98:99], v[114:115]
v_pk_add_f32 v[100:101], v[100:101], v[116:117]
v_pk_add_f32 v[102:103], v[102:103], v[118:119]
v_pk_add_f32 v[104:105], v[104:105], v[120:121]
v_pk_add_f32 v[106:107], v[106:107], v[122:123]
v_pk_add_f32 v[108:109], v[108:109], v[124:125]
v_pk_add_f32 v[110:111], v[110:111], v[126:127]
v_lshlrev_b32_e32 v112, 16, v232
v_and_b32_e32 v113, 0xffff0000, v232
v_lshlrev_b32_e32 v114, 16, v233
v_and_b32_e32 v115, 0xffff0000, v233
v_lshlrev_b32_e32 v116, 16, v234
v_and_b32_e32 v117, 0xffff0000, v234
v_lshlrev_b32_e32 v118, 16, v235
v_and_b32_e32 v119, 0xffff0000, v235
v_lshlrev_b32_e32 v120, 16, v236
v_and_b32_e32 v121, 0xffff0000, v236
v_lshlrev_b32_e32 v122, 16, v237
v_and_b32_e32 v123, 0xffff0000, v237
v_lshlrev_b32_e32 v124, 16, v238
v_and_b32_e32 v125, 0xffff0000, v238
v_lshlrev_b32_e32 v126, 16, v239
v_and_b32_e32 v127, 0xffff0000, v239
v_pk_add_f32 v[96:97], v[96:97], v[112:113]
v_pk_add_f32 v[98:99], v[98:99], v[114:115]
v_pk_add_f32 v[100:101], v[100:101], v[116:117]
v_pk_add_f32 v[102:103], v[102:103], v[118:119]
v_pk_add_f32 v[104:105], v[104:105], v[120:121]
v_pk_add_f32 v[106:107], v[106:107], v[122:123]
v_pk_add_f32 v[108:109], v[108:109], v[124:125]
v_pk_add_f32 v[110:111], v[110:111], v[126:127]
v_lshlrev_b32_e32 v112, 16, v240
v_and_b32_e32 v113, 0xffff0000, v240
v_lshlrev_b32_e32 v114, 16, v241
v_and_b32_e32 v115, 0xffff0000, v241
v_lshlrev_b32_e32 v116, 16, v242
v_and_b32_e32 v117, 0xffff0000, v242
v_lshlrev_b32_e32 v118, 16, v243
v_and_b32_e32 v119, 0xffff0000, v243
v_lshlrev_b32_e32 v120, 16, v244
v_and_b32_e32 v121, 0xffff0000, v244
v_lshlrev_b32_e32 v122, 16, v245
v_and_b32_e32 v123, 0xffff0000, v245
v_lshlrev_b32_e32 v124, 16, v246
v_and_b32_e32 v125, 0xffff0000, v246
v_lshlrev_b32_e32 v126, 16, v247
v_and_b32_e32 v127, 0xffff0000, v247
v_pk_add_f32 v[96:97], v[96:97], v[112:113]
v_pk_add_f32 v[98:99], v[98:99], v[114:115]
v_pk_add_f32 v[100:101], v[100:101], v[116:117]
v_pk_add_f32 v[102:103], v[102:103], v[118:119]
v_pk_add_f32 v[104:105], v[104:105], v[120:121]
v_pk_add_f32 v[106:107], v[106:107], v[122:123]
v_pk_add_f32 v[108:109], v[108:109], v[124:125]
v_pk_add_f32 v[110:111], v[110:111], v[126:127]
v_pk_mul_f32 v[160:161], v[96:97], s[86:87]
v_pk_mul_f32 v[162:163], v[98:99], s[86:87]
v_pk_mul_f32 v[164:165], v[100:101], s[86:87]
v_pk_mul_f32 v[166:167], v[102:103], s[86:87]
v_pk_mul_f32 v[168:169], v[104:105], s[86:87]
v_pk_mul_f32 v[170:171], v[106:107], s[86:87]
v_pk_mul_f32 v[172:173], v[108:109], s[86:87]
v_pk_mul_f32 v[174:175], v[110:111], s[86:87]
v_exp_f32_e32 v160, v160
v_exp_f32_e32 v161, v161
v_exp_f32_e32 v162, v162
v_exp_f32_e32 v163, v163
v_exp_f32_e32 v164, v164
v_exp_f32_e32 v165, v165
v_exp_f32_e32 v166, v166
v_exp_f32_e32 v167, v167
v_exp_f32_e32 v168, v168
v_exp_f32_e32 v169, v169
v_exp_f32_e32 v170, v170
v_exp_f32_e32 v171, v171
v_exp_f32_e32 v172, v172
v_exp_f32_e32 v173, v173
v_exp_f32_e32 v174, v174
v_exp_f32_e32 v175, v175
v_pk_add_f32 v[160:161], v[160:161], s[88:89]
v_pk_add_f32 v[162:163], v[162:163], s[88:89]
v_pk_add_f32 v[164:165], v[164:165], s[88:89]
v_pk_add_f32 v[166:167], v[166:167], s[88:89]
v_pk_add_f32 v[168:169], v[168:169], s[88:89]
v_pk_add_f32 v[170:171], v[170:171], s[88:89]
v_pk_add_f32 v[172:173], v[172:173], s[88:89]
v_pk_add_f32 v[174:175], v[174:175], s[88:89]
v_rcp_f32_e32 v160, v160
v_rcp_f32_e32 v161, v161
v_rcp_f32_e32 v162, v162
v_rcp_f32_e32 v163, v163
v_rcp_f32_e32 v164, v164
v_rcp_f32_e32 v165, v165
v_rcp_f32_e32 v166, v166
v_rcp_f32_e32 v167, v167
v_rcp_f32_e32 v168, v168
v_rcp_f32_e32 v169, v169
v_rcp_f32_e32 v170, v170
v_rcp_f32_e32 v171, v171
v_rcp_f32_e32 v172, v172
v_rcp_f32_e32 v173, v173
v_rcp_f32_e32 v174, v174
v_rcp_f32_e32 v175, v175
v_pk_mul_f32 v[96:97], v[96:97], v[160:161]
v_pk_mul_f32 v[98:99], v[98:99], v[162:163]
v_pk_mul_f32 v[100:101], v[100:101], v[164:165]
v_pk_mul_f32 v[102:103], v[102:103], v[166:167]
v_pk_mul_f32 v[104:105], v[104:105], v[168:169]
v_pk_mul_f32 v[106:107], v[106:107], v[170:171]
v_pk_mul_f32 v[108:109], v[108:109], v[172:173]
v_pk_mul_f32 v[110:111], v[110:111], v[174:175]
v_lshlrev_b32_e32 v176, 16, v80
v_and_b32_e32 v177, 0xffff0000, v80
v_lshlrev_b32_e32 v178, 16, v81
v_and_b32_e32 v179, 0xffff0000, v81
v_lshlrev_b32_e32 v180, 16, v82
v_and_b32_e32 v181, 0xffff0000, v82
v_lshlrev_b32_e32 v182, 16, v83
v_and_b32_e32 v183, 0xffff0000, v83
v_lshlrev_b32_e32 v184, 16, v84
v_and_b32_e32 v185, 0xffff0000, v84
v_lshlrev_b32_e32 v186, 16, v85
v_and_b32_e32 v187, 0xffff0000, v85
v_lshlrev_b32_e32 v188, 16, v86
v_and_b32_e32 v189, 0xffff0000, v86
v_lshlrev_b32_e32 v190, 16, v87
v_and_b32_e32 v191, 0xffff0000, v87
; ---- spp
.Lkf_spp:
s_mov_b32 s4, 0x20000
buffer_load_dwordx2 v[46:47], v58, s[24:27], s75 offen offset:0 sc1
s_sleep 4
; ---- sppl
.Lkf_sppl:
buffer_load_dwordx2 v[44:45], v58, s[24:27], s75 offen offset:0 sc1
s_waitcnt vmcnt(1)
v_cmp_eq_u64_e32 vcc, s[94:95], v[46:47]
s_nop 0
s_and_b32 s83, vcc_lo, 15
s_cmp_eq_u32 s83, 15
s_cbranch_scc1 .Lkf_fin
buffer_load_dwordx2 v[46:47], v58, s[24:27], s75 offen offset:0 sc1
s_waitcnt vmcnt(1)
v_cmp_eq_u64_e32 vcc, s[94:95], v[44:45]
s_nop 0
s_and_b32 s83, vcc_lo, 15
s_cmp_eq_u32 s83, 15
s_cbranch_scc1 .Lkf_fin
s_sub_u32 s4, s4, 1
s_cbranch_scc0 .Lkf_sppl
s_branch .Lkf_fin
; ---- fin
.Lkf_fin:
s_add_u32 s80, s73, 1
s_and_b32 s80, s80, 3
s_lshl_b32 s80, s80, 14
s_add_u32 s80, s80, 0x2000
buffer_load_dwordx4 v[128:131], v29, s[28:31], s80 offen offset:0 sc1
buffer_load_dwordx4 v[132:135], v29, s[28:31], s80 offen offset:1024 sc1
s_add_u32 s80, s73, 2
s_and_b32 s80, s80, 3
s_lshl_b32 s80, s80, 14
s_add_u32 s80, s80, 0x2000
buffer_load_dwordx4 v[136:139], v29, s[28:31], s80 offen offset:0 sc1
buffer_load_dwordx4 v[140:143], v29, s[28:31], s80 offen offset:1024 sc1
s_add_u32 s80, s73, 3
s_and_b32 s80, s80, 3
s_lshl_b32 s80, s80, 14
s_add_u32 s80, s80, 0x2000
buffer_load_dwordx4 v[144:147], v29, s[28:31], s80 offen offset:0 sc1
buffer_load_dwordx4 v[148:151], v29, s[28:31], s80 offen offset:1024 sc1
ds_read_b128 v[208:211], v51 offset:32768
ds_read_b128 v[212:215], v51 offset:33792
ds_read_b128 v[216:219], v51 offset:34816
ds_read_b128 v[220:223], v51 offset:35840
s_waitcnt lgkmcnt(0)
s_waitcnt vmcnt(4)
v_lshlrev_b32_e32 v224, 16, v128
v_and_b32_e32 v225, 0xffff0000, v128
v_lshlrev_b32_e32 v226, 16, v129
v_and_b32_e32 v227, 0xffff0000, v129
v_lshlrev_b32_e32 v228, 16, v130
v_and_b32_e32 v229, 0xffff0000, v130
v_lshlrev_b32_e32 v230, 16, v131
v_and_b32_e32 v231, 0xffff0000, v131
v_lshlrev_b32_e32 v232, 16, v132
v_and_b32_e32 v233, 0xffff0000, v132
v_lshlrev_b32_e32 v234, 16, v133
v_and_b32_e32 v235, 0xffff0000, v133
v_lshlrev_b32_e32 v236, 16, v134
v_and_b32_e32 v237, 0xffff0000, v134
v_lshlrev_b32_e32 v238, 16, v135
v_and_b32_e32 v239, 0xffff0000, v135
v_pk_add_f32 v[208:209], v[208:209], v[224:225]
v_pk_add_f32 v[210:211], v[210:211], v[226:227]
v_pk_add_f32 v[212:213], v[212:213], v[228:229]
v_pk_add_f32 v[214:215], v[214:215], v[230:231]
v_pk_add_f32 v[216:217], v[216:217], v[232:233]
v_pk_add_f32 v[218:219], v[218:219], v[234:235]
v_pk_add_f32 v[220:221], v[220:221], v[236:237]
v_pk_add_f32 v[222:223], v[222:223], v[238:239]
s_waitcnt vmcnt(2)
v_lshlrev_b32_e32 v224, 16, v136
v_and_b32_e32 v225, 0xffff0000, v136
v_lshlrev_b32_e32 v226, 16, v137
v_and_b32_e32 v227, 0xffff0000, v137
v_lshlrev_b32_e32 v228, 16, v138
v_and_b32_e32 v229, 0xffff0000, v138
v_lshlrev_b32_e32 v230, 16, v139
v_and_b32_e32 v231, 0xffff0000, v139
v_lshlrev_b32_e32 v232, 16, v140
v_and_b32_e32 v233, 0xffff0000, v140
v_lshlrev_b32_e32 v234, 16, v141
v_and_b32_e32 v235, 0xffff0000, v141
v_lshlrev_b32_e32 v236, 16, v142
v_and_b32_e32 v237, 0xffff0000, v142
v_lshlrev_b32_e32 v238, 16, v143
v_and_b32_e32 v239, 0xffff0000, v143
v_pk_add_f32 v[208:209], v[208:209], v[224:225]
v_pk_add_f32 v[210:211], v[210:211], v[226:227]
v_pk_add_f32 v[212:213], v[212:213], v[228:229]
v_pk_add_f32 v[214:215], v[214:215], v[230:231]
v_pk_add_f32 v[216:217], v[216:217], v[232:233]
v_pk_add_f32 v[218:219], v[218:219], v[234:235]
v_pk_add_f32 v[220:221], v[220:221], v[236:237]
v_pk_add_f32 v[222:223], v[222:223], v[238:239]
s_waitcnt vmcnt(0)
v_lshlrev_b32_e32 v224, 16, v144
v_and_b32_e32 v225, 0xffff0000, v144
v_lshlrev_b32_e32 v226, 16, v145
v_and_b32_e32 v227, 0xffff0000, v145
v_lshlrev_b32_e32 v228, 16, v146
v_and_b32_e32 v229, 0xffff0000, v146
v_lshlrev_b32_e32 v230, 16, v147
v_and_b32_e32 v231, 0xffff0000, v147
v_lshlrev_b32_e32 v232, 16, v148
v_and_b32_e32 v233, 0xffff0000, v148
v_lshlrev_b32_e32 v234, 16, v149
v_and_b32_e32 v235, 0xffff0000, v149
v_lshlrev_b32_e32 v236, 16, v150
v_and_b32_e32 v237, 0xffff0000, v150
v_lshlrev_b32_e32 v238, 16, v151
v_and_b32_e32 v239, 0xffff0000, v151
v_pk_add_f32 v[208:209], v[208:209], v[224:225]
v_pk_add_f32 v[210:211], v[210:211], v[226:227]
v_pk_add_f32 v[212:213], v[212:213], v[228:229]
v_pk_add_f32 v[214:215], v[214:215], v[230:231]
v_pk_add_f32 v[216:217], v[216:217], v[232:233]
v_pk_add_f32 v[218:219], v[218:219], v[234:235]
v_pk_add_f32 v[220:221], v[220:221], v[236:237]
v_pk_add_f32 v[222:223], v[222:223], v[238:239]
v_pk_fma_f32 v[176:177], v[208:209], v[96:97], v[176:177]
v_pk_fma_f32 v[178:179], v[210:211], v[98:99], v[178:179]
v_pk_fma_f32 v[180:181], v[212:213], v[100:101], v[180:181]
v_pk_fma_f32 v[182:183], v[214:215], v[102:103], v[182:183]
v_pk_fma_f32 v[184:185], v[216:217], v[104:105], v[184:185]
v_pk_fma_f32 v[186:187], v[218:219], v[106:107], v[186:187]
v_pk_fma_f32 v[188:189], v[220:221], v[108:109], v[188:189]
v_pk_fma_f32 v[190:191], v[222:223], v[110:111], v[190:191]
v_cvt_pk_bf16_f32 v64, v176, v177
v_cvt_pk_bf16_f32 v65, v178, v179
v_cvt_pk_bf16_f32 v66, v180, v181
v_cvt_pk_bf16_f32 v67, v182, v183
v_cvt_pk_bf16_f32 v68, v184, v185
v_cvt_pk_bf16_f32 v69, v186, v187
v_cvt_pk_bf16_f32 v70, v188, v189
v_cvt_pk_bf16_f32 v71, v190, v191
buffer_store_dwordx2 v[64:65], v50, s[96:99], 0 offen offset:0
buffer_store_dwordx2 v[66:67], v50, s[96:99], s79 offen offset:0
buffer_store_dwordx2 v[68:69], v50, s[96:99], 0 offen offset:32
buffer_store_dwordx2 v[70:71], v50, s[96:99], s79 offen offset:32
s_branch .Lkf_end
; ---- spf
.Lkf_spf:
s_mov_b32 s4, 0x40000
.Lkf_spfl:
buffer_load_dwordx2 v[46:47], v58, s[24:27], s22 offen offset:0 sc1
s_waitcnt vmcnt(0)
v_cmp_eq_u64_e32 vcc, s[94:95], v[46:47]
s_nop 0
s_and_b32 s83, vcc_lo, 15
s_cmp_eq_u32 s83, 15
s_cbranch_scc1 .Lkf_o2
s_sub_u32 s4, s4, 1
s_cbranch_scc0 .Lkf_spfl
s_branch .Lkf_o2
; ---- sph
.Lkf_sph:
s_mov_b32 s4, 0x40000
.Lkf_sphl:
buffer_load_dwordx2 v[46:47], v58, s[24:27], s82 offen offset:0 sc1
s_waitcnt vmcnt(0)
v_cmp_eq_u64_e32 vcc, s[94:95], v[46:47]
s_nop 0
s_and_b32 s83, vcc_lo, 0xff
s_cmp_eq_u32 s83, 0xff
s_cbranch_scc1 .Lkf_eb1
s_sub_u32 s4, s4, 1
s_cbranch_scc0 .Lkf_sphl
s_branch .Lkf_eb1
; ---- spq
.Lkf_spq:
s_mov_b32 s4, 0x40000
.Lkf_spql:
buffer_load_dwordx2 v[46:47], v58, s[24:27], s100 offen offset:0 sc1
s_waitcnt vmcnt(0)
v_cmp_eq_u64_e32 vcc, s[94:95], v[46:47]
s_nop 0
s_and_b32 s83, vcc_lo, 15
s_cmp_eq_u32 s83, 15
s_cbranch_scc1 .Lkf_o1b
s_sub_u32 s4, s4, 1
s_cbranch_scc0 .Lkf_spql
s_branch .Lkf_o1b
; ---- spo
.Lkf_spo:
s_mov_b32 s4, 0x40000
.Lkf_spol:
buffer_load_dwordx2 v[46:47], v58, s[24:27], s82 offen offset:0 sc1
s_waitcnt vmcnt(0)
v_cmp_eq_u64_e32 vcc, s[94:95], v[46:47]
s_nop 0
s_and_b32 s83, vcc_lo, 15
s_cmp_eq_u32 s83, 15
s_cbranch_scc1 .Lkf_o1
s_sub_u32 s4, s4, 1
s_cbranch_scc0 .Lkf_spol
s_branch .Lkf_o1
; ---- slow0
.Lkf_slow:
s_waitcnt vmcnt(0)
s_waitcnt lgkmcnt(0)
v_and_b32_e32 v46, 0xf0, v29
v_lshlrev_b32_e32 v46, 4, v46
v_lshrrev_b32_e32 v47, 8, v29
v_lshl_add_u32 v46, v47, 4, v46
s_add_u32 s79, s68, 0xc000
s_add_u32 s80, s71, 0xffffc000
v_add_u32_e32 v64, s80, v0
v_add_u32_e32 v65, s80, v1
v_add_u32_e32 v66, s80, v2
v_add_u32_e32 v67, s80, v3
v_add_u32_e32 v68, s80, v4
v_add_u32_e32 v69, s80, v5
v_add_u32_e32 v70, s80, v6
v_add_u32_e32 v71, s80, v7
v_mov_b32_e32 v72, 0xff61b1e6
v_mov_b32_e32 v30, 0
v_mov_b32_e32 v73, 0xff61b1e6
v_mov_b32_e32 v31, 0
v_mov_b32_e32 v74, 0xff61b1e6
v_mov_b32_e32 v32, 0
v_mov_b32_e32 v75, 0xff61b1e6
v_mov_b32_e32 v33, 0
v_accvgpr_write_b32 a0, 0
v_accvgpr_write_b32 a1, 0
v_accvgpr_write_b32 a2, 0
v_accvgpr_write_b32 a3, 0
v_accvgpr_write_b32 a4, 0
v_accvgpr_write_b32 a5, 0
v_accvgpr_write_b32 a6, 0
v_accvgpr_write_b32 a7, 0
v_accvgpr_write_b32 a8, 0
v_accvgpr_write_b32 a9, 0
v_accvgpr_write_b32 a10, 0
v_accvgpr_write_b32 a11, 0
v_accvgpr_write_b32 a12, 0
v_accvgpr_write_b32 a13, 0
v_accvgpr_write_b32 a14, 0
v_accvgpr_write_b32 a15, 0
v_accvgpr_write_b32 a16, 0
v_accvgpr_write_b32 a17, 0
v_accvgpr_write_b32 a18, 0
v_accvgpr_write_b32 a19, 0
v_accvgpr_write_b32 a20, 0
v_accvgpr_write_b32 a21, 0
v_accvgpr_write_b32 a22, 0
v_accvgpr_write_b32 a23, 0
v_accvgpr_write_b32 a24, 0
v_accvgpr_write_b32 a25, 0
v_accvgpr_write_b32 a26, 0
v_accvgpr_write_b32 a27, 0
v_accvgpr_write_b32 a28, 0
v_accvgpr_write_b32 a29, 0
v_accvgpr_write_b32 a30, 0
v_accvgpr_write_b32 a31, 0
v_accvgpr_write_b32 a32, 0
v_accvgpr_write_b32 a33, 0
v_accvgpr_write_b32 a34, 0
v_accvgpr_write_b32 a35, 0
v_accvgpr_write_b32 a36, 0
v_accvgpr_write_b32 a37, 0
v_accvgpr_write_b32 a38, 0
v_accvgpr_write_b32 a39, 0
v_accvgpr_write_b32 a40, 0
v_accvgpr_write_b32 a41, 0
v_accvgpr_write_b32 a42, 0
v_accvgpr_write_b32 a43, 0
v_accvgpr_write_b32 a44, 0
v_accvgpr_write_b32 a45, 0
v_accvgpr_write_b32 a46, 0
v_accvgpr_write_b32 a47, 0
v_accvgpr_write_b32 a48, 0
v_accvgpr_write_b32 a49, 0
v_accvgpr_write_b32 a50, 0
v_accvgpr_write_b32 a51, 0
v_accvgpr_write_b32 a52, 0
v_accvgpr_write_b32 a53, 0
v_accvgpr_write_b32 a54, 0
v_accvgpr_write_b32 a55, 0
v_accvgpr_write_b32 a56, 0
v_accvgpr_write_b32 a57, 0
v_accvgpr_write_b32 a58, 0
v_accvgpr_write_b32 a59, 0
v_accvgpr_write_b32 a60, 0
v_accvgpr_write_b32 a61, 0
v_accvgpr_write_b32 a62, 0
v_accvgpr_write_b32 a63, 0
v_accvgpr_write_b32 a64, 0
v_accvgpr_write_b32 a65, 0
v_accvgpr_write_b32 a66, 0
v_accvgpr_write_b32 a67, 0
v_accvgpr_write_b32 a68, 0
v_accvgpr_write_b32 a69, 0
v_accvgpr_write_b32 a70, 0
v_accvgpr_write_b32 a71, 0
v_accvgpr_write_b32 a72, 0
v_accvgpr_write_b32 a73, 0
v_accvgpr_write_b32 a74, 0
v_accvgpr_write_b32 a75, 0
v_accvgpr_write_b32 a76, 0
v_accvgpr_write_b32 a77, 0
v_accvgpr_write_b32 a78, 0
v_accvgpr_write_b32 a79, 0
v_accvgpr_write_b32 a80, 0
v_accvgpr_write_b32 a81, 0
v_accvgpr_write_b32 a82, 0
v_accvgpr_write_b32 a83, 0
v_accvgpr_write_b32 a84, 0
v_accvgpr_write_b32 a85, 0
v_accvgpr_write_b32 a86, 0
v_accvgpr_write_b32 a87, 0
v_accvgpr_write_b32 a88, 0
v_accvgpr_write_b32 a89, 0
v_accvgpr_write_b32 a90, 0
v_accvgpr_write_b32 a91, 0
v_accvgpr_write_b32 a92, 0
v_accvgpr_write_b32 a93, 0
v_accvgpr_write_b32 a94, 0
v_accvgpr_write_b32 a95, 0
v_accvgpr_write_b32 a96, 0
v_accvgpr_write_b32 a97, 0
v_accvgpr_write_b32 a98, 0
v_accvgpr_write_b32 a99, 0
v_accvgpr_write_b32 a100, 0
v_accvgpr_write_b32 a101, 0
v_accvgpr_write_b32 a102, 0
v_accvgpr_write_b32 a103, 0
v_accvgpr_write_b32 a104, 0
v_accvgpr_write_b32 a105, 0
v_accvgpr_write_b32 a106, 0
v_accvgpr_write_b32 a107, 0
v_accvgpr_write_b32 a108, 0
v_accvgpr_write_b32 a109, 0
v_accvgpr_write_b32 a110, 0
v_accvgpr_write_b32 a111, 0
v_accvgpr_write_b32 a112, 0
v_accvgpr_write_b32 a113, 0
v_accvgpr_write_b32 a114, 0
v_accvgpr_write_b32 a115, 0
v_accvgpr_write_b32 a116, 0
v_accvgpr_write_b32 a117, 0
v_accvgpr_write_b32 a118, 0
v_accvgpr_write_b32 a119, 0
v_accvgpr_write_b32 a120, 0
v_accvgpr_write_b32 a121, 0
v_accvgpr_write_b32 a122, 0
v_accvgpr_write_b32 a123, 0
v_accvgpr_write_b32 a124, 0
v_accvgpr_write_b32 a125, 0
v_accvgpr_write_b32 a126, 0
v_accvgpr_write_b32 a127, 0
s_mov_b32 s76, 0
s_mov_b32 s78, 63
; ---- slow1
.Lkf_sl:
s_add_u32 s77, s76, 0x1000
buffer_load_dwordx4 a[128:131], v46, s[36:39], s76 offen offset:0
buffer_load_dwordx4 a[132:135], v46, s[36:39], s76 offen offset:64
buffer_load_dwordx4 a[136:139], v46, s[36:39], s76 offen offset:128
buffer_load_dwordx4 a[140:143], v46, s[36:39], s76 offen offset:192
buffer_load_dwordx4 a[144:147], v46, s[36:39], s77 offen offset:0
buffer_load_dwordx4 a[148:151], v46, s[36:39], s77 offen offset:64
buffer_load_dwordx4 a[152:155], v46, s[36:39], s77 offen offset:128
buffer_load_dwordx4 a[156:159], v46, s[36:39], s77 offen offset:192
s_mov_b32 m0, s79
s_nop 0
buffer_load_dwordx4 v24, s[40:43], s76 offen offset:0 lds
buffer_load_dwordx4 v25, s[40:43], s76 offen offset:1024 lds
buffer_load_dwordx4 v24, s[40:43], s76 offen offset:2048 lds
buffer_load_dwordx4 v25, s[40:43], s76 offen offset:3072 lds
s_add_u32 m0, s79, 0xc000
s_nop 0
buffer_load_dwordx4 v24, s[40:43], s77 offen offset:0 lds
buffer_load_dwordx4 v25, s[40:43], s77 offen offset:1024 lds
buffer_load_dwordx4 v24, s[40:43], s77 offen offset:2048 lds
buffer_load_dwordx4 v25, s[40:43], s77 offen offset:3072 lds
s_waitcnt vmcnt(0)
ds_read_b64_tr_b16 a[160:161], v64 offset:0
ds_read_b64_tr_b16 a[162:163], v64 offset:49152
ds_read_b64_tr_b16 a[164:165], v65 offset:0
ds_read_b64_tr_b16 a[166:167], v65 offset:49152
ds_read_b64_tr_b16 a[168:169], v66 offset:0
ds_read_b64_tr_b16 a[170:171], v66 offset:49152
ds_read_b64_tr_b16 a[172:173], v67 offset:0
ds_read_b64_tr_b16 a[174:175], v67 offset:49152
ds_read_b64_tr_b16 a[176:177], v68 offset:0
ds_read_b64_tr_b16 a[178:179], v68 offset:49152
ds_read_b64_tr_b16 a[180:181], v69 offset:0
ds_read_b64_tr_b16 a[182:183], v69 offset:49152
ds_read_b64_tr_b16 a[184:185], v70 offset:0
ds_read_b64_tr_b16 a[186:187], v70 offset:49152
ds_read_b64_tr_b16 a[188:189], v71 offset:0
ds_read_b64_tr_b16 a[190:191], v71 offset:49152
v_mfma_f32_16x16x32_bf16 v[224:227], a[128:131], v[128:131], 0
v_mfma_f32_16x16x32_bf16 v[224:227], a[132:135], v[132:135], v[224:227]
v_mfma_f32_16x16x32_bf16 v[224:227], a[136:139], v[136:139], v[224:227]
v_mfma_f32_16x16x32_bf16 v[224:227], a[140:143], v[140:143], v[224:227]
v_mfma_f32_16x16x32_bf16 v[228:231], a[128:131], v[144:147], 0
v_mfma_f32_16x16x32_bf16 v[228:231], a[132:135], v[148:151], v[228:231]
v_mfma_f32_16x16x32_bf16 v[228:231], a[136:139], v[152:155], v[228:231]
v_mfma_f32_16x16x32_bf16 v[228:231], a[140:143], v[156:159], v[228:231]
v_mfma_f32_16x16x32_bf16 v[232:235], a[128:131], v[160:163], 0
v_mfma_f32_16x16x32_bf16 v[232:235], a[132:135], v[164:167], v[232:235]
v_mfma_f32_16x16x32_bf16 v[232:235], a[136:139], v[168:171], v[232:235]
v_mfma_f32_16x16x32_bf16 v[232:235], a[140:143], v[172:175], v[232:235]
v_mfma_f32_16x16x32_bf16 v[236:239], a[128:131], v[176:179], 0
v_mfma_f32_16x16x32_bf16 v[236:239], a[132:135], v[180:183], v[236:239]
v_mfma_f32_16x16x32_bf16 v[236:239], a[136:139], v[184:187], v[236:239]
v_mfma_f32_16x16x32_bf16 v[236:239], a[140:143], v[188:191], v[236:239]
v_mfma_f32_16x16x32_bf16 v[240:243], a[144:147], v[128:131], 0
v_mfma_f32_16x16x32_bf16 v[240:243], a[148:151], v[132:135], v[240:243]
v_mfma_f32_16x16x32_bf16 v[240:243], a[152:155], v[136:139], v[240:243]
v_mfma_f32_16x16x32_bf16 v[240:243], a[156:159], v[140:143], v[240:243]
v_mfma_f32_16x16x32_bf16 v[244:247], a[144:147], v[144:147], 0
v_mfma_f32_16x16x32_bf16 v[244:247], a[148:151], v[148:151], v[244:247]
v_mfma_f32_16x16x32_bf16 v[244:247], a[152:155], v[152:155], v[244:247]
v_mfma_f32_16x16x32_bf16 v[244:247], a[156:159], v[156:159], v[244:247]
v_mfma_f32_16x16x32_bf16 v[248:251], a[144:147], v[160:163], 0
v_mfma_f32_16x16x32_bf16 v[248:251], a[148:151], v[164:167], v[248:251]
v_mfma_f32_16x16x32_bf16 v[248:251], a[152:155], v[168:171], v[248:251]
v_mfma_f32_16x16x32_bf16 v[248:251], a[156:159], v[172:175], v[248:251]
v_mfma_f32_16x16x32_bf16 v[252:255], a[144:147], v[176:179], 0
v_mfma_f32_16x16x32_bf16 v[252:255], a[148:151], v[180:183], v[252:255]
v_mfma_f32_16x16x32_bf16 v[252:255], a[152:155], v[184:187], v[252:255]
v_mfma_f32_16x16x32_bf16 v[252:255], a[156:159], v[188:191], v[252:255]
v_max3_f32 v76, v224, v225, v72
v_max3_f32 v76, v226, v227, v76
v_max3_f32 v76, v240, v241, v76
v_max3_f32 v76, v242, v243, v76
v_max3_f32 v77, v228, v229, v73
v_max3_f32 v77, v230, v231, v77
v_max3_f32 v77, v244, v245, v77
v_max3_f32 v77, v246, v247, v77
v_max3_f32 v78, v232, v233, v74
v_max3_f32 v78, v234, v235, v78
v_max3_f32 v78, v248, v249, v78
v_max3_f32 v78, v250, v251, v78
v_max3_f32 v79, v236, v237, v75
v_max3_f32 v79, v238, v239, v79
v_max3_f32 v79, v252, v253, v79
v_max3_f32 v79, v254, v255, v79
v_mov_b32_e32 v38, v76
v_mov_b32_e32 v39, v77
v_mov_b32_e32 v40, v78
v_mov_b32_e32 v41, v79
v_permlane16_swap_b32_e32 v76, v38
v_permlane16_swap_b32_e32 v77, v39
v_permlane16_swap_b32_e32 v78, v40
v_permlane16_swap_b32_e32 v79, v41
v_max_f32_e32 v76, v76, v38
v_max_f32_e32 v77, v77, v39
v_max_f32_e32 v78, v78, v40
v_max_f32_e32 v79, v79, v41
v_mov_b32_e32 v38, v76
v_mov_b32_e32 v39, v77
v_mov_b32_e32 v40, v78
v_mov_b32_e32 v41, v79
v_permlane32_swap_b32_e32 v76, v38
v_permlane32_swap_b32_e32 v77, v39
v_permlane32_swap_b32_e32 v78, v40
v_permlane32_swap_b32_e32 v79, v41
v_max_f32_e32 v76, v76, v38
v_max_f32_e32 v77, v77, v39
v_max_f32_e32 v78, v78, v40
v_max_f32_e32 v79, v79, v41
v_sub_f32_e32 v42, v72, v76
v_exp_f32_e32 v42, v42
v_mov_b32_e32 v72, v76
v_mul_f32_e32 v30, v30, v42
v_sub_f32_e32 v224, v224, v76
v_sub_f32_e32 v225, v225, v76
v_sub_f32_e32 v226, v226, v76
v_sub_f32_e32 v227, v227, v76
v_exp_f32_e32 v224, v224
v_exp_f32_e32 v225, v225
v_exp_f32_e32 v226, v226
v_exp_f32_e32 v227, v227
v_add_f32_e32 v30, v30, v224
v_add_f32_e32 v30, v30, v225
v_add_f32_e32 v30, v30, v226
v_add_f32_e32 v30, v30, v227
v_cvt_pk_bf16_f32 v192, v224, v225
v_cvt_pk_bf16_f32 v193, v226, v227
v_sub_f32_e32 v240, v240, v76
v_sub_f32_e32 v241, v241, v76
v_sub_f32_e32 v242, v242, v76
v_sub_f32_e32 v243, v243, v76
v_exp_f32_e32 v240, v240
v_exp_f32_e32 v241, v241
v_exp_f32_e32 v242, v242
v_exp_f32_e32 v243, v243
v_add_f32_e32 v30, v30, v240
v_add_f32_e32 v30, v30, v241
v_add_f32_e32 v30, v30, v242
v_add_f32_e32 v30, v30, v243
v_cvt_pk_bf16_f32 v194, v240, v241
v_cvt_pk_bf16_f32 v195, v242, v243
v_sub_f32_e32 v43, v73, v77
v_exp_f32_e32 v43, v43
v_mov_b32_e32 v73, v77
v_mul_f32_e32 v31, v31, v43
v_sub_f32_e32 v228, v228, v77
v_sub_f32_e32 v229, v229, v77
v_sub_f32_e32 v230, v230, v77
v_sub_f32_e32 v231, v231, v77
v_exp_f32_e32 v228, v228
v_exp_f32_e32 v229, v229
v_exp_f32_e32 v230, v230
v_exp_f32_e32 v231, v231
v_add_f32_e32 v31, v31, v228
v_add_f32_e32 v31, v31, v229
v_add_f32_e32 v31, v31, v230
v_add_f32_e32 v31, v31, v231
v_cvt_pk_bf16_f32 v200, v228, v229
v_cvt_pk_bf16_f32 v201, v230, v231
v_sub_f32_e32 v244, v244, v77
v_sub_f32_e32 v245, v245, v77
v_sub_f32_e32 v246, v246, v77
v_sub_f32_e32 v247, v247, v77
v_exp_f32_e32 v244, v244
v_exp_f32_e32 v245, v245
v_exp_f32_e32 v246, v246
v_exp_f32_e32 v247, v247
v_add_f32_e32 v31, v31, v244
v_add_f32_e32 v31, v31, v245
v_add_f32_e32 v31, v31, v246
v_add_f32_e32 v31, v31, v247
v_cvt_pk_bf16_f32 v202, v244, v245
v_cvt_pk_bf16_f32 v203, v246, v247
v_sub_f32_e32 v44, v74, v78
v_exp_f32_e32 v44, v44
v_mov_b32_e32 v74, v78
v_mul_f32_e32 v32, v32, v44
v_sub_f32_e32 v232, v232, v78
v_sub_f32_e32 v233, v233, v78
v_sub_f32_e32 v234, v234, v78
v_sub_f32_e32 v235, v235, v78
v_exp_f32_e32 v232, v232
v_exp_f32_e32 v233, v233
v_exp_f32_e32 v234, v234
v_exp_f32_e32 v235, v235
v_add_f32_e32 v32, v32, v232
v_add_f32_e32 v32, v32, v233
v_add_f32_e32 v32, v32, v234
v_add_f32_e32 v32, v32, v235
v_cvt_pk_bf16_f32 v208, v232, v233
v_cvt_pk_bf16_f32 v209, v234, v235
v_sub_f32_e32 v248, v248, v78
v_sub_f32_e32 v249, v249, v78
v_sub_f32_e32 v250, v250, v78
v_sub_f32_e32 v251, v251, v78
v_exp_f32_e32 v248, v248
v_exp_f32_e32 v249, v249
v_exp_f32_e32 v250, v250
v_exp_f32_e32 v251, v251
v_add_f32_e32 v32, v32, v248
v_add_f32_e32 v32, v32, v249
v_add_f32_e32 v32, v32, v250
v_add_f32_e32 v32, v32, v251
v_cvt_pk_bf16_f32 v210, v248, v249
v_cvt_pk_bf16_f32 v211, v250, v251
v_sub_f32_e32 v45, v75, v79
v_exp_f32_e32 v45, v45
v_mov_b32_e32 v75, v79
v_mul_f32_e32 v33, v33, v45
v_sub_f32_e32 v236, v236, v79
v_sub_f32_e32 v237, v237, v79
v_sub_f32_e32 v238, v238, v79
v_sub_f32_e32 v239, v239, v79
v_exp_f32_e32 v236, v236
v_exp_f32_e32 v237, v237
v_exp_f32_e32 v238, v238
v_exp_f32_e32 v239, v239
v_add_f32_e32 v33, v33, v236
v_add_f32_e32 v33, v33, v237
v_add_f32_e32 v33, v33, v238
v_add_f32_e32 v33, v33, v239
v_cvt_pk_bf16_f32 v216, v236, v237
v_cvt_pk_bf16_f32 v217, v238, v239
v_sub_f32_e32 v252, v252, v79
v_sub_f32_e32 v253, v253, v79
v_sub_f32_e32 v254, v254, v79
v_sub_f32_e32 v255, v255, v79
v_exp_f32_e32 v252, v252
v_exp_f32_e32 v253, v253
v_exp_f32_e32 v254, v254
v_exp_f32_e32 v255, v255
v_add_f32_e32 v33, v33, v252
v_add_f32_e32 v33, v33, v253
v_add_f32_e32 v33, v33, v254
v_add_f32_e32 v33, v33, v255
v_cvt_pk_bf16_f32 v218, v252, v253
v_cvt_pk_bf16_f32 v219, v254, v255
v_accvgpr_read_b32 v196, a0
v_accvgpr_read_b32 v197, a1
v_accvgpr_read_b32 v198, a2
v_accvgpr_read_b32 v199, a3
v_accvgpr_read_b32 v204, a16
v_accvgpr_read_b32 v205, a17
v_accvgpr_read_b32 v206, a18
v_accvgpr_read_b32 v207, a19
v_accvgpr_read_b32 v212, a32
v_accvgpr_read_b32 v213, a33
v_accvgpr_read_b32 v214, a34
v_accvgpr_read_b32 v215, a35
v_accvgpr_read_b32 v220, a48
v_accvgpr_read_b32 v221, a49
v_accvgpr_read_b32 v222, a50
v_accvgpr_read_b32 v223, a51
v_mul_f32_e32 v196, v196, v42
v_mul_f32_e32 v197, v197, v42
v_mul_f32_e32 v198, v198, v42
v_mul_f32_e32 v199, v199, v42
v_mul_f32_e32 v204, v204, v42
v_mul_f32_e32 v205, v205, v42
v_mul_f32_e32 v206, v206, v42
v_mul_f32_e32 v207, v207, v42
v_mul_f32_e32 v212, v212, v42
v_mul_f32_e32 v213, v213, v42
v_mul_f32_e32 v214, v214, v42
v_mul_f32_e32 v215, v215, v42
v_mul_f32_e32 v220, v220, v42
v_mul_f32_e32 v221, v221, v42
v_mul_f32_e32 v222, v222, v42
v_mul_f32_e32 v223, v223, v42
v_accvgpr_write_b32 a0, v196
v_accvgpr_write_b32 a1, v197
v_accvgpr_write_b32 a2, v198
v_accvgpr_write_b32 a3, v199
v_accvgpr_write_b32 a16, v204
v_accvgpr_write_b32 a17, v205
v_accvgpr_write_b32 a18, v206
v_accvgpr_write_b32 a19, v207
v_accvgpr_write_b32 a32, v212
v_accvgpr_write_b32 a33, v213
v_accvgpr_write_b32 a34, v214
v_accvgpr_write_b32 a35, v215
v_accvgpr_write_b32 a48, v220
v_accvgpr_write_b32 a49, v221
v_accvgpr_write_b32 a50, v222
v_accvgpr_write_b32 a51, v223
v_accvgpr_read_b32 v196, a4
v_accvgpr_read_b32 v197, a5
v_accvgpr_read_b32 v198, a6
v_accvgpr_read_b32 v199, a7
v_accvgpr_read_b32 v204, a20
v_accvgpr_read_b32 v205, a21
v_accvgpr_read_b32 v206, a22
v_accvgpr_read_b32 v207, a23
v_accvgpr_read_b32 v212, a36
v_accvgpr_read_b32 v213, a37
v_accvgpr_read_b32 v214, a38
v_accvgpr_read_b32 v215, a39
v_accvgpr_read_b32 v220, a52
v_accvgpr_read_b32 v221, a53
v_accvgpr_read_b32 v222, a54
v_accvgpr_read_b32 v223, a55
v_mul_f32_e32 v196, v196, v43
v_mul_f32_e32 v197, v197, v43
v_mul_f32_e32 v198, v198, v43
v_mul_f32_e32 v199, v199, v43
v_mul_f32_e32 v204, v204, v43
v_mul_f32_e32 v205, v205, v43
v_mul_f32_e32 v206, v206, v43
v_mul_f32_e32 v207, v207, v43
v_mul_f32_e32 v212, v212, v43
v_mul_f32_e32 v213, v213, v43
v_mul_f32_e32 v214, v214, v43
v_mul_f32_e32 v215, v215, v43
v_mul_f32_e32 v220, v220, v43
v_mul_f32_e32 v221, v221, v43
v_mul_f32_e32 v222, v222, v43
v_mul_f32_e32 v223, v223, v43
v_accvgpr_write_b32 a4, v196
v_accvgpr_write_b32 a5, v197
v_accvgpr_write_b32 a6, v198
v_accvgpr_write_b32 a7, v199
v_accvgpr_write_b32 a20, v204
v_accvgpr_write_b32 a21, v205
v_accvgpr_write_b32 a22, v206
v_accvgpr_write_b32 a23, v207
v_accvgpr_write_b32 a36, v212
v_accvgpr_write_b32 a37, v213
v_accvgpr_write_b32 a38, v214
v_accvgpr_write_b32 a39, v215
v_accvgpr_write_b32 a52, v220
v_accvgpr_write_b32 a53, v221
v_accvgpr_write_b32 a54, v222
v_accvgpr_write_b32 a55, v223
v_accvgpr_read_b32 v196, a8
v_accvgpr_read_b32 v197, a9
v_accvgpr_read_b32 v198, a10
v_accvgpr_read_b32 v199, a11
v_accvgpr_read_b32 v204, a24
v_accvgpr_read_b32 v205, a25
v_accvgpr_read_b32 v206, a26
v_accvgpr_read_b32 v207, a27
v_accvgpr_read_b32 v212, a40
v_accvgpr_read_b32 v213, a41
v_accvgpr_read_b32 v214, a42
v_accvgpr_read_b32 v215, a43
v_accvgpr_read_b32 v220, a56
v_accvgpr_read_b32 v221, a57
v_accvgpr_read_b32 v222, a58
v_accvgpr_read_b32 v223, a59
v_mul_f32_e32 v196, v196, v44
v_mul_f32_e32 v197, v197, v44
v_mul_f32_e32 v198, v198, v44
v_mul_f32_e32 v199, v199, v44
v_mul_f32_e32 v204, v204, v44
v_mul_f32_e32 v205, v205, v44
v_mul_f32_e32 v206, v206, v44
v_mul_f32_e32 v207, v207, v44
v_mul_f32_e32 v212, v212, v44
v_mul_f32_e32 v213, v213, v44
v_mul_f32_e32 v214, v214, v44
v_mul_f32_e32 v215, v215, v44
v_mul_f32_e32 v220, v220, v44
v_mul_f32_e32 v221, v221, v44
v_mul_f32_e32 v222, v222, v44
v_mul_f32_e32 v223, v223, v44
v_accvgpr_write_b32 a8, v196
v_accvgpr_write_b32 a9, v197
v_accvgpr_write_b32 a10, v198
v_accvgpr_write_b32 a11, v199
v_accvgpr_write_b32 a24, v204
v_accvgpr_write_b32 a25, v205
v_accvgpr_write_b32 a26, v206
v_accvgpr_write_b32 a27, v207
v_accvgpr_write_b32 a40, v212
v_accvgpr_write_b32 a41, v213
v_accvgpr_write_b32 a42, v214
v_accvgpr_write_b32 a43, v215
v_accvgpr_write_b32 a56, v220
v_accvgpr_write_b32 a57, v221
v_accvgpr_write_b32 a58, v222
v_accvgpr_write_b32 a59, v223
v_accvgpr_read_b32 v196, a12
v_accvgpr_read_b32 v197, a13
v_accvgpr_read_b32 v198, a14
v_accvgpr_read_b32 v199, a15
v_accvgpr_read_b32 v204, a28
v_accvgpr_read_b32 v205, a29
v_accvgpr_read_b32 v206, a30
v_accvgpr_read_b32 v207, a31
v_accvgpr_read_b32 v212, a44
v_accvgpr_read_b32 v213, a45
v_accvgpr_read_b32 v214, a46
v_accvgpr_read_b32 v215, a47
v_accvgpr_read_b32 v220, a60
v_accvgpr_read_b32 v221, a61
v_accvgpr_read_b32 v222, a62
v_accvgpr_read_b32 v223, a63
v_mul_f32_e32 v196, v196, v45
v_mul_f32_e32 v197, v197, v45
v_mul_f32_e32 v198, v198, v45
v_mul_f32_e32 v199, v199, v45
v_mul_f32_e32 v204, v204, v45
v_mul_f32_e32 v205, v205, v45
v_mul_f32_e32 v206, v206, v45
v_mul_f32_e32 v207, v207, v45
v_mul_f32_e32 v212, v212, v45
v_mul_f32_e32 v213, v213, v45
v_mul_f32_e32 v214, v214, v45
v_mul_f32_e32 v215, v215, v45
v_mul_f32_e32 v220, v220, v45
v_mul_f32_e32 v221, v221, v45
v_mul_f32_e32 v222, v222, v45
v_mul_f32_e32 v223, v223, v45
v_accvgpr_write_b32 a12, v196
v_accvgpr_write_b32 a13, v197
v_accvgpr_write_b32 a14, v198
v_accvgpr_write_b32 a15, v199
v_accvgpr_write_b32 a28, v204
v_accvgpr_write_b32 a29, v205
v_accvgpr_write_b32 a30, v206
v_accvgpr_write_b32 a31, v207
v_accvgpr_write_b32 a44, v212
v_accvgpr_write_b32 a45, v213
v_accvgpr_write_b32 a46, v214
v_accvgpr_write_b32 a47, v215
v_accvgpr_write_b32 a60, v220
v_accvgpr_write_b32 a61, v221
v_accvgpr_write_b32 a62, v222
v_accvgpr_write_b32 a63, v223
v_accvgpr_read_b32 v196, a64
v_accvgpr_read_b32 v197, a65
v_accvgpr_read_b32 v198, a66
v_accvgpr_read_b32 v199, a67
v_accvgpr_read_b32 v204, a80
v_accvgpr_read_b32 v205, a81
v_accvgpr_read_b32 v206, a82
v_accvgpr_read_b32 v207, a83
v_accvgpr_read_b32 v212, a96
v_accvgpr_read_b32 v213, a97
v_accvgpr_read_b32 v214, a98
v_accvgpr_read_b32 v215, a99
v_accvgpr_read_b32 v220, a112
v_accvgpr_read_b32 v221, a113
v_accvgpr_read_b32 v222, a114
v_accvgpr_read_b32 v223, a115
v_mul_f32_e32 v196, v196, v42
v_mul_f32_e32 v197, v197, v42
v_mul_f32_e32 v198, v198, v42
v_mul_f32_e32 v199, v199, v42
v_mul_f32_e32 v204, v204, v42
v_mul_f32_e32 v205, v205, v42
v_mul_f32_e32 v206, v206, v42
v_mul_f32_e32 v207, v207, v42
v_mul_f32_e32 v212, v212, v42
v_mul_f32_e32 v213, v213, v42
v_mul_f32_e32 v214, v214, v42
v_mul_f32_e32 v215, v215, v42
v_mul_f32_e32 v220, v220, v42
v_mul_f32_e32 v221, v221, v42
v_mul_f32_e32 v222, v222, v42
v_mul_f32_e32 v223, v223, v42
v_accvgpr_write_b32 a64, v196
v_accvgpr_write_b32 a65, v197
v_accvgpr_write_b32 a66, v198
v_accvgpr_write_b32 a67, v199
v_accvgpr_write_b32 a80, v204
v_accvgpr_write_b32 a81, v205
v_accvgpr_write_b32 a82, v206
v_accvgpr_write_b32 a83, v207
v_accvgpr_write_b32 a96, v212
v_accvgpr_write_b32 a97, v213
v_accvgpr_write_b32 a98, v214
v_accvgpr_write_b32 a99, v215
v_accvgpr_write_b32 a112, v220
v_accvgpr_write_b32 a113, v221
v_accvgpr_write_b32 a114, v222
v_accvgpr_write_b32 a115, v223
v_accvgpr_read_b32 v196, a68
v_accvgpr_read_b32 v197, a69
v_accvgpr_read_b32 v198, a70
v_accvgpr_read_b32 v199, a71
v_accvgpr_read_b32 v204, a84
v_accvgpr_read_b32 v205, a85
v_accvgpr_read_b32 v206, a86
v_accvgpr_read_b32 v207, a87
v_accvgpr_read_b32 v212, a100
v_accvgpr_read_b32 v213, a101
v_accvgpr_read_b32 v214, a102
v_accvgpr_read_b32 v215, a103
v_accvgpr_read_b32 v220, a116
v_accvgpr_read_b32 v221, a117
v_accvgpr_read_b32 v222, a118
v_accvgpr_read_b32 v223, a119
v_mul_f32_e32 v196, v196, v43
v_mul_f32_e32 v197, v197, v43
v_mul_f32_e32 v198, v198, v43
v_mul_f32_e32 v199, v199, v43
v_mul_f32_e32 v204, v204, v43
v_mul_f32_e32 v205, v205, v43
v_mul_f32_e32 v206, v206, v43
v_mul_f32_e32 v207, v207, v43
v_mul_f32_e32 v212, v212, v43
v_mul_f32_e32 v213, v213, v43
v_mul_f32_e32 v214, v214, v43
v_mul_f32_e32 v215, v215, v43
v_mul_f32_e32 v220, v220, v43
v_mul_f32_e32 v221, v221, v43
v_mul_f32_e32 v222, v222, v43
v_mul_f32_e32 v223, v223, v43
v_accvgpr_write_b32 a68, v196
v_accvgpr_write_b32 a69, v197
v_accvgpr_write_b32 a70, v198
v_accvgpr_write_b32 a71, v199
v_accvgpr_write_b32 a84, v204
v_accvgpr_write_b32 a85, v205
v_accvgpr_write_b32 a86, v206
v_accvgpr_write_b32 a87, v207
v_accvgpr_write_b32 a100, v212
v_accvgpr_write_b32 a101, v213
v_accvgpr_write_b32 a102, v214
v_accvgpr_write_b32 a103, v215
v_accvgpr_write_b32 a116, v220
v_accvgpr_write_b32 a117, v221
v_accvgpr_write_b32 a118, v222
v_accvgpr_write_b32 a119, v223
v_accvgpr_read_b32 v196, a72
v_accvgpr_read_b32 v197, a73
v_accvgpr_read_b32 v198, a74
v_accvgpr_read_b32 v199, a75
v_accvgpr_read_b32 v204, a88
v_accvgpr_read_b32 v205, a89
v_accvgpr_read_b32 v206, a90
v_accvgpr_read_b32 v207, a91
v_accvgpr_read_b32 v212, a104
v_accvgpr_read_b32 v213, a105
v_accvgpr_read_b32 v214, a106
v_accvgpr_read_b32 v215, a107
v_accvgpr_read_b32 v220, a120
v_accvgpr_read_b32 v221, a121
v_accvgpr_read_b32 v222, a122
v_accvgpr_read_b32 v223, a123
v_mul_f32_e32 v196, v196, v44
v_mul_f32_e32 v197, v197, v44
v_mul_f32_e32 v198, v198, v44
v_mul_f32_e32 v199, v199, v44
v_mul_f32_e32 v204, v204, v44
v_mul_f32_e32 v205, v205, v44
v_mul_f32_e32 v206, v206, v44
v_mul_f32_e32 v207, v207, v44
v_mul_f32_e32 v212, v212, v44
v_mul_f32_e32 v213, v213, v44
v_mul_f32_e32 v214, v214, v44
v_mul_f32_e32 v215, v215, v44
v_mul_f32_e32 v220, v220, v44
v_mul_f32_e32 v221, v221, v44
v_mul_f32_e32 v222, v222, v44
v_mul_f32_e32 v223, v223, v44
v_accvgpr_write_b32 a72, v196
v_accvgpr_write_b32 a73, v197
v_accvgpr_write_b32 a74, v198
v_accvgpr_write_b32 a75, v199
v_accvgpr_write_b32 a88, v204
v_accvgpr_write_b32 a89, v205
v_accvgpr_write_b32 a90, v206
v_accvgpr_write_b32 a91, v207
v_accvgpr_write_b32 a104, v212
v_accvgpr_write_b32 a105, v213
v_accvgpr_write_b32 a106, v214
v_accvgpr_write_b32 a107, v215
v_accvgpr_write_b32 a120, v220
v_accvgpr_write_b32 a121, v221
v_accvgpr_write_b32 a122, v222
v_accvgpr_write_b32 a123, v223
v_accvgpr_read_b32 v196, a76
v_accvgpr_read_b32 v197, a77
v_accvgpr_read_b32 v198, a78
v_accvgpr_read_b32 v199, a79
v_accvgpr_read_b32 v204, a92
v_accvgpr_read_b32 v205, a93
v_accvgpr_read_b32 v206, a94
v_accvgpr_read_b32 v207, a95
v_accvgpr_read_b32 v212, a108
v_accvgpr_read_b32 v213, a109
v_accvgpr_read_b32 v214, a110
v_accvgpr_read_b32 v215, a111
v_accvgpr_read_b32 v220, a124
v_accvgpr_read_b32 v221, a125
v_accvgpr_read_b32 v222, a126
v_accvgpr_read_b32 v223, a127
v_mul_f32_e32 v196, v196, v45
v_mul_f32_e32 v197, v197, v45
v_mul_f32_e32 v198, v198, v45
v_mul_f32_e32 v199, v199, v45
v_mul_f32_e32 v204, v204, v45
v_mul_f32_e32 v205, v205, v45
v_mul_f32_e32 v206, v206, v45
v_mul_f32_e32 v207, v207, v45
v_mul_f32_e32 v212, v212, v45
v_mul_f32_e32 v213, v213, v45
v_mul_f32_e32 v214, v214, v45
v_mul_f32_e32 v215, v215, v45
v_mul_f32_e32 v220, v220, v45
v_mul_f32_e32 v221, v221, v45
v_mul_f32_e32 v222, v222, v45
v_mul_f32_e32 v223, v223, v45
v_accvgpr_write_b32 a76, v196
v_accvgpr_write_b32 a77, v197
v_accvgpr_write_b32 a78, v198
v_accvgpr_write_b32 a79, v199
v_accvgpr_write_b32 a92, v204
v_accvgpr_write_b32 a93, v205
v_accvgpr_write_b32 a94, v206
v_accvgpr_write_b32 a95, v207
v_accvgpr_write_b32 a108, v212
v_accvgpr_write_b32 a109, v213
v_accvgpr_write_b32 a110, v214
v_accvgpr_write_b32 a111, v215
v_accvgpr_write_b32 a124, v220
v_accvgpr_write_b32 a125, v221
v_accvgpr_write_b32 a126, v222
v_accvgpr_write_b32 a127, v223
s_waitcnt lgkmcnt(0)
v_mfma_f32_16x16x32_bf16 a[0:3], a[160:163], v[192:195], a[0:3]
v_mfma_f32_16x16x32_bf16 a[4:7], a[160:163], v[200:203], a[4:7]
v_mfma_f32_16x16x32_bf16 a[8:11], a[160:163], v[208:211], a[8:11]
v_mfma_f32_16x16x32_bf16 a[12:15], a[160:163], v[216:219], a[12:15]
v_mfma_f32_16x16x32_bf16 a[16:19], a[164:167], v[192:195], a[16:19]
v_mfma_f32_16x16x32_bf16 a[20:23], a[164:167], v[200:203], a[20:23]
v_mfma_f32_16x16x32_bf16 a[24:27], a[164:167], v[208:211], a[24:27]
v_mfma_f32_16x16x32_bf16 a[28:31], a[164:167], v[216:219], a[28:31]
v_mfma_f32_16x16x32_bf16 a[32:35], a[168:171], v[192:195], a[32:35]
v_mfma_f32_16x16x32_bf16 a[36:39], a[168:171], v[200:203], a[36:39]
v_mfma_f32_16x16x32_bf16 a[40:43], a[168:171], v[208:211], a[40:43]
v_mfma_f32_16x16x32_bf16 a[44:47], a[168:171], v[216:219], a[44:47]
v_mfma_f32_16x16x32_bf16 a[48:51], a[172:175], v[192:195], a[48:51]
v_mfma_f32_16x16x32_bf16 a[52:55], a[172:175], v[200:203], a[52:55]
v_mfma_f32_16x16x32_bf16 a[56:59], a[172:175], v[208:211], a[56:59]
v_mfma_f32_16x16x32_bf16 a[60:63], a[172:175], v[216:219], a[60:63]
v_mfma_f32_16x16x32_bf16 a[64:67], a[176:179], v[192:195], a[64:67]
v_mfma_f32_16x16x32_bf16 a[68:71], a[176:179], v[200:203], a[68:71]
v_mfma_f32_16x16x32_bf16 a[72:75], a[176:179], v[208:211], a[72:75]
v_mfma_f32_16x16x32_bf16 a[76:79], a[176:179], v[216:219], a[76:79]
v_mfma_f32_16x16x32_bf16 a[80:83], a[180:183], v[192:195], a[80:83]
v_mfma_f32_16x16x32_bf16 a[84:87], a[180:183], v[200:203], a[84:87]
v_mfma_f32_16x16x32_bf16 a[88:91], a[180:183], v[208:211], a[88:91]
v_mfma_f32_16x16x32_bf16 a[92:95], a[180:183], v[216:219], a[92:95]
v_mfma_f32_16x16x32_bf16 a[96:99], a[184:187], v[192:195], a[96:99]
v_mfma_f32_16x16x32_bf16 a[100:103], a[184:187], v[200:203], a[100:103]
v_mfma_f32_16x16x32_bf16 a[104:107], a[184:187], v[208:211], a[104:107]
v_mfma_f32_16x16x32_bf16 a[108:111], a[184:187], v[216:219], a[108:111]
v_mfma_f32_16x16x32_bf16 a[112:115], a[188:191], v[192:195], a[112:115]
v_mfma_f32_16x16x32_bf16 a[116:119], a[188:191], v[200:203], a[116:119]
v_mfma_f32_16x16x32_bf16 a[120:123], a[188:191], v[208:211], a[120:123]
v_mfma_f32_16x16x32_bf16 a[124:127], a[188:191], v[216:219], a[124:127]
s_add_u32 s76, s76, 0x2000
s_sub_u32 s78, s78, 1
s_cbranch_scc0 .Lkf_sl
; ---- slow2
v_mov_b32_e32 v64, v30
v_mov_b32_e32 v65, v31
v_mov_b32_e32 v66, v32
v_mov_b32_e32 v67, v33
v_mov_b32_e32 v68, v64
v_mov_b32_e32 v69, v65
v_mov_b32_e32 v70, v66
v_mov_b32_e32 v71, v67
v_permlane16_swap_b32_e32 v64, v68
v_permlane16_swap_b32_e32 v65, v69
v_permlane16_swap_b32_e32 v66, v70
v_permlane16_swap_b32_e32 v67, v71
v_add_f32_e32 v64, v64, v68
v_add_f32_e32 v65, v65, v69
v_add_f32_e32 v66, v66, v70
v_add_f32_e32 v67, v67, v71
v_mov_b32_e32 v68, v64
v_mov_b32_e32 v69, v65
v_mov_b32_e32 v70, v66
v_mov_b32_e32 v71, v67
v_permlane32_swap_b32_e32 v64, v68
v_permlane32_swap_b32_e32 v65, v69
v_permlane32_swap_b32_e32 v66, v70
v_permlane32_swap_b32_e32 v67, v71
v_add_f32_e32 v64, v64, v68
v_add_f32_e32 v65, v65, v69
v_add_f32_e32 v66, v66, v70
v_add_f32_e32 v67, v67, v71
s_branch .Lkf_epi
; ---- end
.Lkf_end:

)ASM"
      : "+{s[68:71]}"(sc), "+{s[44:47]}"(sWQ), "+{s[48:51]}"(sWG), "+{s[52:55]}"(sWU), "+{s[56:59]}"(sX), "+{s[64:67]}"(sH),
        "+{s[96:99]}"(sXW), "+{v[60:61]}"(e0), "+{v[64:67]}"(x0), "+{v[68:71]}"(x1), "+{v[44:47]}"(cd), "+{v[40:43]}"(cw)
      : "{v[0:7]}"(vva), "{v[8:11]}"(vka), "{v[12:15]}"(vqa), "{v[16:19]}"(vga), "{v[20:23]}"(vkd), "{v[24:27]}"(vm), "{v[28:29]}"(vn),
        "{v[56:59]}"(vf), "{v49}"(pk),
        "{s[36:39]}"(srdK), "{s[40:43]}"(srdV), "{s[60:63]}"(srdO), "{s[24:27]}"(srdF), "{s[8:11]}"(srdPO), "{s[6:7]}"(pfd), "{s[72:75]}"(sd), "{s[100:101]}"(se)
      : "v30","v31","v32","v33","v34","v35","v36","v37","v38","v39",
        "v48",
        "v50","v51","v52","v53","v54","v55",
        "v62","v63",
        "v72","v73","v74","v75","v76","v77","v78","v79","v80","v81","v82","v83","v84","v85","v86","v87","v88","v89","v90","v91","v92","v93","v94","v95","v96","v97","v98","v99","v100","v101","v102","v103","v104","v105","v106","v107","v108","v109","v110","v111","v112","v113","v114","v115","v116","v117","v118","v119","v120","v121","v122","v123","v124","v125","v126","v127","v128","v129","v130","v131","v132","v133","v134","v135","v136","v137","v138","v139","v140","v141","v142","v143","v144","v145","v146","v147","v148","v149","v150","v151","v152","v153","v154","v155","v156","v157","v158","v159","v160","v161","v162","v163","v164","v165","v166","v167","v168","v169","v170","v171","v172","v173","v174","v175","v176","v177","v178","v179","v180","v181","v182","v183","v184","v185","v186","v187","v188","v189","v190","v191","v192","v193","v194","v195","v196","v197","v198","v199","v200","v201","v202","v203","v204","v205","v206","v207","v208","v209","v210","v211","v212","v213","v214","v215","v216","v217","v218","v219","v220","v221","v222","v223","v224","v225","v226","v227","v228","v229","v230","v231","v232","v233","v234","v235","v236","v237","v238","v239","v240","v241","v242","v243","v244","v245","v246","v247","v248","v249","v250","v251","v252","v253","v254","v255",
        "a0","a1","a2","a3","a4","a5","a6","a7","a8","a9","a10","a11","a12","a13","a14","a15","a16","a17","a18","a19","a20","a21","a22","a23","a24","a25","a26","a27","a28","a29","a30","a31","a32","a33","a34","a35","a36","a37","a38","a39","a40","a41","a42","a43","a44","a45","a46","a47","a48","a49","a50","a51","a52","a53","a54","a55","a56","a57","a58","a59","a60","a61","a62","a63","a64","a65","a66","a67","a68","a69","a70","a71","a72","a73","a74","a75","a76","a77","a78","a79","a80","a81","a82","a83","a84","a85","a86","a87","a88","a89","a90","a91","a92","a93","a94","a95","a96","a97","a98","a99","a100","a101","a102","a103","a104","a105","a106","a107","a108","a109","a110","a111","a112","a113","a114","a115","a116","a117","a118","a119","a120","a121","a122","a123","a124","a125","a126","a127","a128","a129","a130","a131","a132","a133","a134","a135","a136","a137","a138","a139","a140","a141","a142","a143","a144","a145","a146","a147","a148","a149","a150","a151","a152","a153","a154","a155","a156","a157","a158","a159","a160","a161","a162","a163","a164","a165","a166","a167","a168","a169","a170","a171","a172","a173","a174","a175","a176","a177","a178","a179","a180","a181","a182","a183","a184","a185","a186","a187","a188","a189","a190","a191","a192","a193","a194","a195","a196","a197","a198","a199","a200","a201","a202","a203","a204","a205","a206","a207","a208","a209","a210","a211","a212","a213","a214","a215","a216","a217","a218","a219","a220","a221","a222","a223","a224","a225","a226","a227","a228","a229","a230","a231","a232","a233","a234","a235","a236","a237","a238","a239","a240","a241","a242","a243","a244","a245","a246","a247","a248","a249","a250","a251","a252","a253","a254","a255",
        "s4","s5",
        "s12","s13","s14","s15","s16","s17","s18","s19","s20","s21","s22","s23",
        "s28","s29","s30","s31",
        "s76","s77","s78","s79","s80","s81","s82","s83","s84","s85","s86","s87","s88","s89","s90","s91","s92","s93","s94","s95", "vcc", "scc", "memory");
}
}

extern "C" void kernel_launch(void* const* d_in, const int* in_sizes, int n_in, void* d_out, int out_size, void* d_ws, size_t ws_size,
                              hipStream_t stream) {
  fused_block<<<256, 256, 0, stream>>>((const u16*)d_in[0], (const u16*)d_in[1], (const u16*)d_in[2], (const u16*)d_in[3], (const u16*)d_in[4],
                                       (const u16*)d_in[6], (const u16*)d_in[7], (const u16*)d_in[8], (u16*)d_out, (char*)d_ws);
}
```
